```python
import torch

def _softmax(x, axis=-1):
    return torch.softmax(x, dim=axis)

def _gelu(x):
    return torch.nn.functional.gelu(x, approximate='tanh')

def _silu(x):
    return torch.nn.functional.silu(x)

def _sigmoid(x):
    return torch.sigmoid(x)

def _layernorm(x, axis=-1, eps=1e-05):
    m = x.mean(dim=axis, keepdim=True)
    v = x.var(dim=axis, keepdim=True, unbiased=False)
    return (x - m) / torch.sqrt(v + eps)

def _scatter_add(tgt, idx, src, axis=0):
    return tgt.clone().index_add_(axis, idx, src)

def _segment_sum(src, seg, n):
    out = torch.zeros((n,) + src.shape[1:], dtype=src.dtype)
    return out.index_add_(0, seg, src)

def _pad(x, pw):
    flat = tuple((v for pair in reversed(pw) for v in pair))
    return torch.nn.functional.pad(x, flat)
kernel_name = 'sequential_preLN__memory_aug_recurrent_mem_tr__swiglu_ffn__prompt'

def setup_inputs():

    _w = lambda a: (lambda t: t.to(torch.bfloat16) if t.is_floating_point() else t)(torch.as_tensor(a))

    def _A_setup():
        B, S, Dm = (1, 2048, 576)
        H, Dh = (32, 128)
        M, seg = (8, 32)
        seg = min(seg, S)
        S = S // seg * seg
        return {'x': _w((lambda *s: torch.randn(*s, dtype=torch.bfloat16) * 0.05)(B, S, Dm)), 'mem0': _w((lambda *s: torch.randn(*s, dtype=torch.bfloat16) * 0.2)(B, M, Dm)), 'Wq': _w((lambda *s: torch.randn(*s, dtype=torch.bfloat16) * 1.6)(Dm, H, Dh) * Dm ** (-0.5)), 'Wk': _w((lambda *s: torch.randn(*s, dtype=torch.bfloat16) * 1.6)(Dm, H, Dh) * Dm ** (-0.5)), 'Wv': _w((lambda *s: torch.randn(*s, dtype=torch.bfloat16) * 0.2)(Dm, H, Dh) * Dm ** (-0.5)), 'Wo': _w((lambda *s: torch.randn(*s, dtype=torch.bfloat16) * 0.2)(H, Dh, Dm) * (H * Dh) ** (-0.5)), 'seg_len': seg}
    _A_in = _A_setup()

    def _B_setup():
        B, S, D, F = (1, 2048, 576, 4608)
        return {'x': _w((lambda *s: torch.randn(*s, dtype=torch.bfloat16) * 0.05)(B, S, D)), 'w_gate': _w((lambda *s: torch.randn(*s, dtype=torch.bfloat16) * 0.05)(D, F)), 'w_up': _w((lambda *s: torch.randn(*s, dtype=torch.bfloat16) * 0.05)(D, F)), 'w_down': _w((lambda *s: torch.randn(*s, dtype=torch.bfloat16) * 0.05)(F, D))}
    _B_in = _B_setup()
    return {'A_x': _A_in['x'], 'A_mem0': _A_in['mem0'], 'A_Wq': _A_in['Wq'], 'A_Wk': _A_in['Wk'], 'A_Wv': _A_in['Wv'], 'A_Wo': _A_in['Wo'], 'A_seg_len': _A_in['seg_len'], 'B_x': _B_in['x'], 'B_w_gate': _B_in['w_gate'], 'B_w_up': _B_in['w_up'], 'B_w_down': _B_in['w_down']}

def reference(A_x, A_mem0, A_Wq, A_Wk, A_Wv, A_Wo, A_seg_len, B_x, B_w_gate, B_w_up, B_w_down):

    def _A_ref(x, mem0, Wq, Wk, Wv, Wo, seg_len):
        B, S, Dm = x.shape
        H, Dh = (Wq.shape[1], Wq.shape[2])
        M = mem0.shape[1]
        scale = Dh ** (-0.5)
        n_seg = S // seg_len
        L = M + seg_len
        idx = torch.arange(L)
        mask = (idx[:, None] < M) | (idx[:, None] >= idx[None, :])
        mem = mem0
        outs = []
        for s in range(n_seg):
            xs = x[:, s * seg_len:(s + 1) * seg_len, :].clone()
            h = torch.concatenate([mem, xs], axis=1)
            q = torch.einsum('btd,dhe->bhte', h, Wq)
            k = torch.einsum('btd,dhe->bhte', h, Wk)
            v = torch.einsum('btd,dhe->bhte', h, Wv)
            scores = torch.einsum('bhqe,bhke->bhqk', q, k) * scale
            scores = torch.where(mask[None, None], scores, -1000000000.0)
            p = _softmax(scores, axis=-1)
            o = torch.einsum('bhqk,bhke->bhqe', p, v)
            o = torch.einsum('bhte,hed->btd', o, Wo)
            mem = o[:, :M, :].clone()
            outs.append(o[:, M:, :])
        return torch.concatenate(outs, axis=1)

    def _B_ref(x, w_gate, w_up, w_down):
        g = _silu(x @ w_gate)
        u = x @ w_up
        return g * u @ w_down
    x = A_x
    h = x + _A_ref(_layernorm(x, axis=-1), A_mem0, A_Wq, A_Wk, A_Wv, A_Wo, A_seg_len)
    h2 = h + _B_ref(_layernorm(h, axis=-1), B_w_gate, B_w_up, B_w_down)
    return h2
```

```cpp
#include <hip/hip_runtime.h>
#include <hip/hip_bf16.h>
#include <stdint.h>

typedef unsigned v4u __attribute__((ext_vector_type(4)));

__device__ const unsigned short table[256] = {0, 1, 2, 192, 448, 466, 449, 467, 3, 4, 5, 193, 450, 468, 451, 469, 6, 7, 8, 194, 452, 470, 453, 471, 9, 10, 11, 195, 454, 472, 455, 473, 12, 13, 14, 196, 456, 474, 457, 475, 15, 16, 17, 197, 458, 476, 459, 477, 18, 19, 20, 198, 460, 478, 461, 479, 21, 22, 23, 199, 462, 480, 463, 481, 24, 25, 26, 200, 464, 482, 465, 483, 27, 28, 29, 201, 320, 338, 321, 339, 30, 31, 72, 202, 322, 340, 323, 341, 64, 68, 73, 203, 324, 342, 325, 343, 65, 69, 74, 204, 326, 344, 327, 345, 66, 70, 75, 205, 328, 346, 329, 347, 67, 71, 84, 206, 330, 348, 331, 349, 76, 80, 85, 207, 332, 350, 333, 351, 77, 81, 86, 268, 334, 352, 335, 353, 78, 82, 87, 269, 336, 354, 337, 355, 79, 83, 96, 270, 384, 393, 402, 411, 88, 92, 97, 271, 385, 394, 403, 412, 89, 93, 98, 272, 386, 395, 404, 413, 90, 94, 99, 273, 387, 396, 405, 414, 91, 95, 258, 274, 388, 397, 406, 415, 256, 257, 261, 275, 389, 398, 407, 416, 259, 260, 264, 276, 390, 399, 408, 417, 262, 263, 267, 277, 391, 400, 409, 418, 265, 266, 138, 278, 392, 401, 410, 419, 128, 133, 139, 279, 208, 212, 216, 220, 129, 134, 140, 280, 209, 213, 217, 221, 130, 135, 141, 281, 210, 214, 218, 222, 131, 136, 142, 282, 211, 215, 219, 223, 132, 137, 143, 283, 284, 285, 286, 287};

__global__ void __launch_bounds__(256) rmt_init(char* wsb) {
  const unsigned b = blockIdx.x;
  if (b >= 32) {
    const unsigned i = (b - 32) * 256 + threadIdx.x;
    if (i < 1327104 / 16) ((v4u*)wsb)[i] = (v4u){~0u, ~0u, ~0u, ~0u};

    if (1) {
      const char* code; unsigned len, rel;
      asm volatile("s_getpc_b64 %0\nLinitpc:\ns_mov_b32 %1, Lcode1-Lcode0\ns_mov_b32 %2, Lcode0-Linitpc" : "=s"(code), "=s"(len), "=s"(rel));
      code += rel;
      const unsigned off = ((b - 32) >> 3) * 4096 + threadIdx.x * 16;
      if (b < 40) { unsigned t = table[threadIdx.x]; asm volatile("" :: "v"(t)); }
      if (off < len) { v4u t = *(const v4u*)(code + off); asm volatile("" :: "v"(t)); }
    }
    return;
  }
  if ((b & 7) != 0 || threadIdx.x != 0) return;
  char* base = wsb + (7u << 18) + (b >> 3) * 4096;
  unsigned best = ~0u;
  for (int k = 0; k < 2; ++k) __hip_atomic_store((unsigned*)(base + k * 1024), 0u, __ATOMIC_RELAXED, __HIP_MEMORY_SCOPE_AGENT);
  asm volatile("s_waitcnt vmcnt(0)" ::: "memory");
  for (int k = 0; k < 2; ++k) {
    unsigned long long t0, t1; unsigned v; const char* p = base + k * 1024;
    asm volatile("s_memtime %0\n\ts_waitcnt lgkmcnt(0)\n\tglobal_load_dword %2, %3, off sc1\n\ts_waitcnt vmcnt(0)\n\ts_memtime %1\n\ts_waitcnt lgkmcnt(0)"
                 : "=&s"(t0), "=&s"(t1), "=&v"(v) : "v"(p) : "memory");
    const unsigned d = (unsigned)(t1 - t0);
    if (d < best) best = d;
  }
  ((unsigned*)(wsb + 1327104))[b >> 3] = best;
}

__global__ void __launch_bounds__(256, 1) rmt_main(const void* x, const void* mem0, const void* wq, const void* wk, const void* wv, const void* wo,
                                                   const void* wg, const void* wu, const void* wd, void* out, void* wsb) {
  const int b = blockIdx.x;
  const int e = __builtin_amdgcn_readfirstlane((int)table[b]);
  const int role = e >> 6, idx = e & 63;
  unsigned tid = threadIdx.x;
  asm volatile(
"Lcode0:\n"
"s_mov_b64 s[16:17], %0\n"
"s_mov_b32 s18, %1\n"
"s_mov_b32 s19, %2\n"
"s_mov_b32 s52, %3\n"
"v_lshrrev_b32 v1, 6, %4\n"
"v_and_b32 v0, 63, %4\n"
"v_readfirstlane_b32 s20, v1\n"
"s_memrealtime s[48:49]\n"
"s_load_dwordx2 s[24:25], s[16:17], 80\n"
"s_mov_b32 s26, 175112192\n"
"s_mov_b32 s27, 0x00020000\n"
"s_waitcnt lgkmcnt(0)\n"
"s_and_b32 s25, s25, 0xffff\n"
"s_add_u32 s23, s48, 2000000\n"
"s_load_dwordx2 s[36:37], s[16:17], 80\n"
"s_waitcnt lgkmcnt(0)\n"
"s_add_u32 s36, s36, 1327104\n"
"s_addc_u32 s37, s37, 0\n"
"s_load_dwordx4 s[40:43], s[36:37], 0\n"
"s_waitcnt lgkmcnt(0)\n"
"s_add_u32 s40, s40, s41\n"
"s_add_u32 s42, s42, s43\n"
"s_cmp_le_u32 s40, s42\n"
"s_cselect_b32 s22, 0, 8192\n"
"s_getpc_b64 s[50:51]\n"
"Lsledref:\n"
"s_add_u32 s50, s50, Lsled_end-Lsledref\n"
"s_addc_u32 s51, s51, 0\n"
"s_cmp_eq_u32 s18, 0\n"
"s_cbranch_scc1 JLA\n"
"s_cmp_eq_u32 s18, 1\n"
"s_cbranch_scc1 JLB\n"
"s_cmp_eq_u32 s18, 2\n"
"s_cbranch_scc1 JLN\n"
"s_cmp_eq_u32 s18, 3\n"
"s_cbranch_scc1 JLX1\n"
"s_cmp_eq_u32 s18, 4\n"
"s_cbranch_scc1 JLX2\n"
"s_cmp_eq_u32 s18, 5\n"
"s_cbranch_scc1 JLP2\n"
"s_cmp_eq_u32 s18, 6\n"
"s_cbranch_scc1 JLP3\n"
"s_cmp_eq_u32 s18, 7\n"
"s_cbranch_scc1 JLP4\n"
"s_endpgm\n"
"JLA:\n"
"s_getpc_b64 s[36:37]\n"
"JLA_r:\n"
"s_add_u32 s36, s36, LA-JLA_r\n"
"s_addc_u32 s37, s37, 0\n"
"s_setpc_b64 s[36:37]\n"
"JLB:\n"
"s_getpc_b64 s[36:37]\n"
"JLB_r:\n"
"s_add_u32 s36, s36, LB-JLB_r\n"
"s_addc_u32 s37, s37, 0\n"
"s_setpc_b64 s[36:37]\n"
"JLN:\n"
"s_getpc_b64 s[36:37]\n"
"JLN_r:\n"
"s_add_u32 s36, s36, LN-JLN_r\n"
"s_addc_u32 s37, s37, 0\n"
"s_setpc_b64 s[36:37]\n"
"JLX1:\n"
"s_getpc_b64 s[36:37]\n"
"JLX1_r:\n"
"s_add_u32 s36, s36, LX1-JLX1_r\n"
"s_addc_u32 s37, s37, 0\n"
"s_setpc_b64 s[36:37]\n"
"JLX2:\n"
"s_getpc_b64 s[36:37]\n"
"JLX2_r:\n"
"s_add_u32 s36, s36, LX2-JLX2_r\n"
"s_addc_u32 s37, s37, 0\n"
"s_setpc_b64 s[36:37]\n"
"JLP2:\n"
"s_getpc_b64 s[36:37]\n"
"JLP2_r:\n"
"s_add_u32 s36, s36, LP2-JLP2_r\n"
"s_addc_u32 s37, s37, 0\n"
"s_setpc_b64 s[36:37]\n"
"JLP3:\n"
"s_getpc_b64 s[36:37]\n"
"JLP3_r:\n"
"s_add_u32 s36, s36, LP3-JLP3_r\n"
"s_addc_u32 s37, s37, 0\n"
"s_setpc_b64 s[36:37]\n"
"JLP4:\n"
"s_getpc_b64 s[36:37]\n"
"JLP4_r:\n"
"s_add_u32 s36, s36, LP4-JLP4_r\n"
"s_addc_u32 s37, s37, 0\n"
"s_setpc_b64 s[36:37]\n"
"Lsled:\n"
"s_nop 0\n"
"s_nop 0\n"
"s_nop 0\n"
"s_nop 0\n"
"s_nop 0\n"
"s_nop 0\n"
"s_nop 0\n"
"s_nop 0\n"
"s_nop 0\n"
"s_nop 0\n"
"s_nop 0\n"
"s_nop 0\n"
"s_nop 0\n"
"s_nop 0\n"
"s_nop 0\n"
"s_nop 0\n"
"s_nop 0\n"
"s_nop 0\n"
"s_nop 0\n"
"s_nop 0\n"
"s_nop 0\n"
"s_nop 0\n"
"s_nop 0\n"
"s_nop 0\n"
"s_nop 0\n"
"s_nop 0\n"
"s_nop 0\n"
"s_nop 0\n"
"s_nop 0\n"
"s_nop 0\n"
"s_nop 0\n"
"s_nop 0\n"
"s_nop 0\n"
"s_nop 0\n"
"s_nop 0\n"
"s_nop 0\n"
"s_nop 0\n"
"s_nop 0\n"
"s_nop 0\n"
"s_nop 0\n"
"s_nop 0\n"
"s_nop 0\n"
"s_nop 0\n"
"s_nop 0\n"
"s_nop 0\n"
"s_nop 0\n"
"s_nop 0\n"
"s_nop 0\n"
"s_nop 0\n"
"s_nop 0\n"
"s_nop 0\n"
"s_nop 0\n"
"s_nop 0\n"
"s_nop 0\n"
"s_nop 0\n"
"s_nop 0\n"
"s_nop 0\n"
"s_nop 0\n"
"s_nop 0\n"
"s_nop 0\n"
"s_nop 0\n"
"s_nop 0\n"
"s_nop 0\n"
"s_nop 0\n"
"s_nop 0\n"
"s_nop 0\n"
"s_nop 0\n"
"s_nop 0\n"
"s_nop 0\n"
"s_nop 0\n"
"s_nop 0\n"
"s_nop 0\n"
"s_nop 0\n"
"s_nop 0\n"
"s_nop 0\n"
"s_nop 0\n"
"s_nop 0\n"
"s_nop 0\n"
"s_nop 0\n"
"s_nop 0\n"
"s_nop 0\n"
"s_nop 0\n"
"s_nop 0\n"
"s_nop 0\n"
"s_nop 0\n"
"s_nop 0\n"
"s_nop 0\n"
"s_nop 0\n"
"s_nop 0\n"
"s_nop 0\n"
"s_nop 0\n"
"s_nop 0\n"
"s_nop 0\n"
"s_nop 0\n"
"s_nop 0\n"
"s_nop 0\n"
"Lsled_end:\n"
"s_setpc_b64 s[46:47]\n"
"LA:\n"
"v_and_b32 v12, 15, v0\n"
"v_mov_b32 v1, 0x4444\n"
"v_mov_b32 v13, 0xeeee\n"
"v_and_b32 v15, 1, v0\n"
"v_cmp_eq_u32 vcc, 1, v15\n"
"s_nop 1\n"
"v_cndmask_b32 v1, v1, v13, vcc\n"
"v_lshlrev_b32 v8, 4, v0\n"
"v_lshlrev_b32 v7, 3, v0\n"
"s_lshl_b32 s36, s20, 11\n"
"v_add_u32 v3, s36, v8\n"
"s_lshl_b32 s36, s20, 8\n"
"v_lshlrev_b32 v4, 2, v0\n"
"v_add_u32 v4, s36, v4\n"
"s_mul_i32 s36, s20, 24576\n"
"s_add_u32 s36, s36, 40960\n"
"v_add_u32 v5, s36, v8\n"
"v_lshrrev_b32 v13, 5, v0\n"
"v_lshlrev_b32 v2, 9, v13\n"
"v_bfe_u32 v13, v0, 2, 2\n"
"v_lshl_add_u32 v2, v13, 7, v2\n"
"v_and_b32 v13, 3, v0\n"
"v_lshl_add_u32 v2, v13, 4, v2\n"
"v_bfe_u32 v13, v0, 4, 1\n"
"v_lshl_add_u32 v2, v13, 6, v2\n"
"v_lshlrev_b32 v6, 5, v12\n"
"v_lshrrev_b32 v13, 4, v0\n"
"v_lshl_add_u32 v6, v13, 3, v6\n"
"s_lshl_b32 s36, s20, 10\n"
"v_add_u32 v6, s36, v6\n"
"v_lshlrev_b32 v9, 4, v13\n"
"v_lshlrev_b32 v10, 7, v13\n"
"v_bfe_u32 v13, v0, 3, 1\n"
"v_lshl_add_u32 v9, v13, 10, v9\n"
"v_bfe_u32 v13, v0, 2, 1\n"
"v_lshl_add_u32 v9, v13, 7, v9\n"
"v_and_b32 v13, 1, v0\n"
"v_lshl_add_u32 v9, v13, 6, v9\n"
"v_lshl_add_u32 v10, v13, 4, v10\n"
"v_bfe_u32 v13, v0, 2, 2\n"
"v_lshl_add_u32 v10, v13, 5, v10\n"
"s_lshr_b32 s36, s19, 2\n"
"s_lshl_b32 s36, s36, 14\n"
"s_and_b32 s37, s19, 3\n"
"s_lshl_b32 s37, s37, 11\n"
"s_add_u32 s36, s36, s37\n"
"s_lshl_b32 s37, s20, 8\n"
"s_add_u32 s36, s36, s37\n"
"s_add_u32 s36, s36, s22\n"
"v_add_u32 v9, s36, v9\n"
"s_lshl_b32 s36, s20, 9\n"
"s_add_u32 s36, s36, 2048\n"
"v_add_u32 v11, s36, v7\n"
"v_mov_b32 v15, 2146435072\n"
"v_and_b32 v13, 2, v0\n"
"v_cmp_eq_u32 vcc, 0, v13\n"
"s_nop 1\n"
"v_cndmask_b32 v9, v9, v15, vcc\n"
"v_cndmask_b32 v10, v15, v10, vcc\n"
"v_lshrrev_b32 v13, 4, v0\n"
"v_cmp_eq_u32 vcc, s20, v13\n"
"s_nop 1\n"
"v_cndmask_b32 v10, v15, v10, vcc\n"
"s_mov_b32 s70, 0x3e0293ee\n"
"s_mov_b32 s71, 0x3e0293ee\n"
"s_lshl_b32 s36, s19, 7\n"
"s_xor_b32 s36, s36, s22\n"
"s_xor_b32 s18, s36, 1212416\n"
"s_mov_b32 s68, -1\n"
"v_cmp_eq_u32 vcc, 0, v0\n"
"s_nop 1\n"
"v_cndmask_b32 v15, v15, 0, vcc\n"
"v_lshrrev_b32 v208, 4, v0\n"
"v_mul_u32_u24 v204, 8192, v208\n"
"v_and_b32 v208, 15, v0\n"
"v_lshl_add_u32 v204, v208, 4, v204\n"
"s_mul_i32 s36, s20, 131072\n"
"v_add_u32 v204, s36, v204\n"
"v_lshlrev_b32 v205, 4, v0\n"
"s_mul_i32 s36, s20, 4096\n"
"s_add_u32 s36, s36, 0\n"
"v_add_u32 v205, s36, v205\n"
"v_bfe_u32 v208, v0, 4, 1\n"
"v_lshlrev_b32 v208, 6, v208\n"
"v_xor_b32 v205, v205, v208\n"
"v_xor_b32 v206, 128, v205\n"
"v_lshrrev_b32 v208, 5, v0\n"
"v_lshlrev_b32 v207, 12, v208\n"
"v_bfe_u32 v208, v0, 3, 1\n"
"v_lshl_add_u32 v207, v208, 11, v207\n"
"v_bfe_u32 v208, v0, 4, 1\n"
"v_lshl_add_u32 v207, v208, 10, v207\n"
"v_bfe_u32 v208, v0, 2, 1\n"
"v_lshl_add_u32 v207, v208, 8, v207\n"
"v_bfe_u32 v208, v0, 2, 2\n"
"v_xor_b32 v208, s20, v208\n"
"v_lshl_add_u32 v207, v208, 6, v207\n"
"v_and_b32 v208, 3, v0\n"
"v_lshl_add_u32 v207, v208, 4, v207\n"
"v_add_u32 v207, 0, v207\n"
"Lce_1:\n"
"s_memtime s[48:49]\n"
"s_memrealtime s[36:37]\n"
"s_nop 3\n"
"s_memrealtime s[38:39]\n"
"s_nop 3\n"
"s_memrealtime s[40:41]\n"
"s_nop 3\n"
"s_memrealtime s[42:43]\n"
"s_nop 3\n"
"s_memrealtime s[44:45]\n"
"s_nop 3\n"
"s_memrealtime s[46:47]\n"
"s_waitcnt lgkmcnt(0)\n"
"s_cmp_eq_u32 s46, s36\n"
"s_cbranch_scc1 Lce_1\n"
"s_cmp_eq_u32 s38, s36\n"
"s_cselect_b32 s37, 20, 0\n"
"s_add_u32 s48, s48, s37\n"
"s_cmp_eq_u32 s40, s36\n"
"s_cselect_b32 s37, 20, 0\n"
"s_add_u32 s48, s48, s37\n"
"s_cmp_eq_u32 s42, s36\n"
"s_cselect_b32 s37, 20, 0\n"
"s_add_u32 s48, s48, s37\n"
"s_cmp_eq_u32 s44, s36\n"
"s_cselect_b32 s37, 20, 0\n"
"s_add_u32 s48, s48, s37\n"
"s_mul_i32 s40, s46, 24\n"
"s_sub_u32 s40, s40, s48\n"
"s_mov_b32 s75, s40\n"
"s_mov_b32 s62, s48\n"
"s_load_dwordx2 s[28:29], s[16:17], 16\n"
"s_mov_b32 s30, 4718592\n"
"s_mov_b32 s31, 0x00020000\n"
"s_waitcnt lgkmcnt(0)\n"
"s_and_b32 s29, s29, 0xffff\n"
"s_load_dwordx2 s[32:33], s[16:17], 24\n"
"s_mov_b32 s34, 4718592\n"
"s_mov_b32 s35, 0x00020000\n"
"s_waitcnt lgkmcnt(0)\n"
"s_and_b32 s33, s33, 0xffff\n"
"s_load_dwordx2 s[64:65], s[16:17], 32\n"
"s_mov_b32 s66, 4718592\n"
"s_mov_b32 s67, 0x00020000\n"
"s_waitcnt lgkmcnt(0)\n"
"s_and_b32 s65, s65, 0xffff\n"
"s_lshl_b32 s59, s19, 8\n"
"s_lshl_b32 s60, s19, 8\n"
"s_lshl_b32 s61, s19, 8\n"
"buffer_load_dwordx4 v[104:107], v204, s[28:31], s59 offen nt\n"
"s_add_u32 s59, s59, 32768\n"
"buffer_load_dwordx4 v[108:111], v204, s[28:31], s59 offen nt\n"
"s_add_u32 s59, s59, 32768\n"
"buffer_load_dwordx4 v[120:123], v204, s[28:31], s59 offen nt\n"
"s_add_u32 s59, s59, 32768\n"
"buffer_load_dwordx4 v[124:127], v204, s[28:31], s59 offen nt\n"
"s_add_u32 s59, s59, 425984\n"
"buffer_load_dwordx4 v[136:139], v204, s[28:31], s59 offen nt\n"
"s_add_u32 s59, s59, 32768\n"
"buffer_load_dwordx4 v[140:143], v204, s[28:31], s59 offen nt\n"
"s_add_u32 s59, s59, 32768\n"
"buffer_load_dwordx4 v[152:155], v204, s[28:31], s59 offen nt\n"
"s_add_u32 s59, s59, 32768\n"
"buffer_load_dwordx4 v[156:159], v204, s[28:31], s59 offen nt\n"
"s_add_u32 s59, s59, 425984\n"
"buffer_load_dwordx4 a[0:3], v204, s[28:31], s59 offen nt\n"
"s_add_u32 s59, s59, 32768\n"
"buffer_load_dwordx4 a[4:7], v204, s[28:31], s59 offen nt\n"
"s_add_u32 s59, s59, 32768\n"
"buffer_load_dwordx4 a[8:11], v204, s[28:31], s59 offen nt\n"
"s_add_u32 s59, s59, 32768\n"
"buffer_load_dwordx4 a[12:15], v204, s[28:31], s59 offen nt\n"
"s_add_u32 s59, s59, 425984\n"
"buffer_load_dwordx4 a[32:35], v204, s[28:31], s59 offen nt\n"
"s_add_u32 s59, s59, 32768\n"
"buffer_load_dwordx4 a[36:39], v204, s[28:31], s59 offen nt\n"
"s_add_u32 s59, s59, 32768\n"
"buffer_load_dwordx4 a[40:43], v204, s[28:31], s59 offen nt\n"
"s_add_u32 s59, s59, 32768\n"
"buffer_load_dwordx4 a[44:47], v204, s[28:31], s59 offen nt\n"
"s_add_u32 s59, s59, 425984\n"
"s_waitcnt vmcnt(12)\n"
"ds_write_b128 v205, v[104:107] offset:0\n"
"ds_write_b128 v205, v[108:111] offset:1024\n"
"ds_write_b128 v206, v[120:123] offset:2048\n"
"ds_write_b128 v206, v[124:127] offset:3072\n"
"s_waitcnt lgkmcnt(0)\n"
"s_barrier\n"
"ds_read_b64_tr_b16 v[104:105], v207 offset:0\n"
"ds_read_b64_tr_b16 v[106:107], v207 offset:512\n"
"ds_read_b64_tr_b16 v[108:109], v207 offset:8192\n"
"ds_read_b64_tr_b16 v[110:111], v207 offset:8704\n"
"ds_read_b64_tr_b16 v[120:121], v207 offset:8\n"
"ds_read_b64_tr_b16 v[122:123], v207 offset:520\n"
"ds_read_b64_tr_b16 v[124:125], v207 offset:8200\n"
"ds_read_b64_tr_b16 v[126:127], v207 offset:8712\n"
"buffer_load_dwordx4 a[64:67], v204, s[28:31], s59 offen nt\n"
"s_add_u32 s59, s59, 32768\n"
"buffer_load_dwordx4 a[68:71], v204, s[28:31], s59 offen nt\n"
"s_add_u32 s59, s59, 32768\n"
"buffer_load_dwordx4 a[72:75], v204, s[28:31], s59 offen nt\n"
"s_add_u32 s59, s59, 32768\n"
"buffer_load_dwordx4 a[76:79], v204, s[28:31], s59 offen nt\n"
"s_add_u32 s59, s59, 425984\n"
"s_waitcnt vmcnt(12)\n"
"ds_write_b128 v205, v[136:139] offset:16384\n"
"ds_write_b128 v205, v[140:143] offset:17408\n"
"ds_write_b128 v206, v[152:155] offset:18432\n"
"ds_write_b128 v206, v[156:159] offset:19456\n"
"s_waitcnt lgkmcnt(0)\n"
"ds_write_b128 v5, v[104:107] offset:0\n"
"ds_write_b128 v5, v[108:111] offset:1024\n"
"ds_write_b128 v5, v[120:123] offset:4096\n"
"ds_write_b128 v5, v[124:127] offset:5120\n"
"s_barrier\n"
"ds_read_b64_tr_b16 v[136:137], v207 offset:16384\n"
"ds_read_b64_tr_b16 v[138:139], v207 offset:16896\n"
"ds_read_b64_tr_b16 v[140:141], v207 offset:24576\n"
"ds_read_b64_tr_b16 v[142:143], v207 offset:25088\n"
"ds_read_b64_tr_b16 v[152:153], v207 offset:16392\n"
"ds_read_b64_tr_b16 v[154:155], v207 offset:16904\n"
"ds_read_b64_tr_b16 v[156:157], v207 offset:24584\n"
"ds_read_b64_tr_b16 v[158:159], v207 offset:25096\n"
"buffer_load_dwordx4 a[96:99], v204, s[28:31], s59 offen nt\n"
"s_add_u32 s59, s59, 32768\n"
"buffer_load_dwordx4 a[100:103], v204, s[28:31], s59 offen nt\n"
"s_add_u32 s59, s59, 32768\n"
"buffer_load_dwordx4 a[104:107], v204, s[28:31], s59 offen nt\n"
"s_add_u32 s59, s59, 32768\n"
"buffer_load_dwordx4 a[108:111], v204, s[28:31], s59 offen nt\n"
"s_add_u32 s59, s59, 425984\n"
"s_waitcnt vmcnt(12)\n"
"ds_write_b128 v205, a[0:3] offset:0\n"
"ds_write_b128 v205, a[4:7] offset:1024\n"
"ds_write_b128 v206, a[8:11] offset:2048\n"
"ds_write_b128 v206, a[12:15] offset:3072\n"
"s_waitcnt lgkmcnt(0)\n"
"ds_write_b128 v5, v[136:139] offset:8192\n"
"ds_write_b128 v5, v[140:143] offset:9216\n"
"ds_write_b128 v5, v[152:155] offset:12288\n"
"ds_write_b128 v5, v[156:159] offset:13312\n"
"s_barrier\n"
"ds_read_b64_tr_b16 a[0:1], v207 offset:0\n"
"ds_read_b64_tr_b16 a[2:3], v207 offset:512\n"
"ds_read_b64_tr_b16 a[4:5], v207 offset:8192\n"
"ds_read_b64_tr_b16 a[6:7], v207 offset:8704\n"
"ds_read_b64_tr_b16 a[8:9], v207 offset:8\n"
"ds_read_b64_tr_b16 a[10:11], v207 offset:520\n"
"ds_read_b64_tr_b16 a[12:13], v207 offset:8200\n"
"ds_read_b64_tr_b16 a[14:15], v207 offset:8712\n"
"buffer_load_dwordx4 a[128:131], v204, s[28:31], s59 offen nt\n"
"s_add_u32 s59, s59, 32768\n"
"buffer_load_dwordx4 a[132:135], v204, s[28:31], s59 offen nt\n"
"s_add_u32 s59, s59, 32768\n"
"buffer_load_dwordx4 a[136:139], v204, s[28:31], s59 offen nt\n"
"s_add_u32 s59, s59, 32768\n"
"buffer_load_dwordx4 a[140:143], v204, s[28:31], s59 offen nt\n"
"s_add_u32 s59, s59, 425984\n"
"s_waitcnt vmcnt(12)\n"
"ds_write_b128 v205, a[32:35] offset:16384\n"
"ds_write_b128 v205, a[36:39] offset:17408\n"
"ds_write_b128 v206, a[40:43] offset:18432\n"
"ds_write_b128 v206, a[44:47] offset:19456\n"
"s_waitcnt lgkmcnt(0)\n"
"s_barrier\n"
"ds_read_b64_tr_b16 a[32:33], v207 offset:16384\n"
"ds_read_b64_tr_b16 a[34:35], v207 offset:16896\n"
"ds_read_b64_tr_b16 a[36:37], v207 offset:24576\n"
"ds_read_b64_tr_b16 a[38:39], v207 offset:25088\n"
"ds_read_b64_tr_b16 a[40:41], v207 offset:16392\n"
"ds_read_b64_tr_b16 a[42:43], v207 offset:16904\n"
"ds_read_b64_tr_b16 a[44:45], v207 offset:24584\n"
"ds_read_b64_tr_b16 a[46:47], v207 offset:25096\n"
"buffer_load_dwordx4 a[160:163], v204, s[28:31], s59 offen nt\n"
"s_add_u32 s59, s59, 32768\n"
"buffer_load_dwordx4 a[164:167], v204, s[28:31], s59 offen nt\n"
"s_add_u32 s59, s59, 32768\n"
"buffer_load_dwordx4 a[168:171], v204, s[28:31], s59 offen nt\n"
"s_add_u32 s59, s59, 32768\n"
"buffer_load_dwordx4 a[172:175], v204, s[28:31], s59 offen nt\n"
"s_add_u32 s59, s59, 425984\n"
"s_waitcnt vmcnt(12)\n"
"ds_write_b128 v205, a[64:67] offset:0\n"
"ds_write_b128 v205, a[68:71] offset:1024\n"
"ds_write_b128 v206, a[72:75] offset:2048\n"
"ds_write_b128 v206, a[76:79] offset:3072\n"
"s_waitcnt lgkmcnt(0)\n"
"s_barrier\n"
"ds_read_b64_tr_b16 a[64:65], v207 offset:0\n"
"ds_read_b64_tr_b16 a[66:67], v207 offset:512\n"
"ds_read_b64_tr_b16 a[68:69], v207 offset:8192\n"
"ds_read_b64_tr_b16 a[70:71], v207 offset:8704\n"
"ds_read_b64_tr_b16 a[72:73], v207 offset:8\n"
"ds_read_b64_tr_b16 a[74:75], v207 offset:520\n"
"ds_read_b64_tr_b16 a[76:77], v207 offset:8200\n"
"ds_read_b64_tr_b16 a[78:79], v207 offset:8712\n"
"buffer_load_dwordx4 v[168:171], v204, s[28:31], s59 offen nt\n"
"s_add_u32 s59, s59, 32768\n"
"buffer_load_dwordx4 v[172:175], v204, s[28:31], s59 offen nt\n"
"s_add_u32 s59, s59, 32768\n"
"buffer_load_dwordx4 v[176:179], v204, s[28:31], s59 offen nt\n"
"s_add_u32 s59, s59, 32768\n"
"buffer_load_dwordx4 v[180:183], v204, s[28:31], s59 offen nt\n"
"s_add_u32 s59, s59, 425984\n"
"s_waitcnt vmcnt(12)\n"
"ds_write_b128 v205, a[96:99] offset:16384\n"
"ds_write_b128 v205, a[100:103] offset:17408\n"
"ds_write_b128 v206, a[104:107] offset:18432\n"
"ds_write_b128 v206, a[108:111] offset:19456\n"
"s_waitcnt lgkmcnt(0)\n"
"s_barrier\n"
"ds_read_b64_tr_b16 a[96:97], v207 offset:16384\n"
"ds_read_b64_tr_b16 a[98:99], v207 offset:16896\n"
"ds_read_b64_tr_b16 a[100:101], v207 offset:24576\n"
"ds_read_b64_tr_b16 a[102:103], v207 offset:25088\n"
"ds_read_b64_tr_b16 a[104:105], v207 offset:16392\n"
"ds_read_b64_tr_b16 a[106:107], v207 offset:16904\n"
"ds_read_b64_tr_b16 a[108:109], v207 offset:24584\n"
"ds_read_b64_tr_b16 a[110:111], v207 offset:25096\n"
"buffer_load_dwordx4 v[112:115], v204, s[32:35], s60 offen nt\n"
"s_add_u32 s60, s60, 32768\n"
"buffer_load_dwordx4 v[116:119], v204, s[32:35], s60 offen nt\n"
"s_add_u32 s60, s60, 32768\n"
"buffer_load_dwordx4 v[128:131], v204, s[32:35], s60 offen nt\n"
"s_add_u32 s60, s60, 32768\n"
"buffer_load_dwordx4 v[132:135], v204, s[32:35], s60 offen nt\n"
"s_add_u32 s60, s60, 425984\n"
"s_waitcnt vmcnt(12)\n"
"ds_write_b128 v205, a[128:131] offset:0\n"
"ds_write_b128 v205, a[132:135] offset:1024\n"
"ds_write_b128 v206, a[136:139] offset:2048\n"
"ds_write_b128 v206, a[140:143] offset:3072\n"
"s_waitcnt lgkmcnt(0)\n"
"s_barrier\n"
"ds_read_b64_tr_b16 a[128:129], v207 offset:0\n"
"ds_read_b64_tr_b16 a[130:131], v207 offset:512\n"
"ds_read_b64_tr_b16 a[132:133], v207 offset:8192\n"
"ds_read_b64_tr_b16 a[134:135], v207 offset:8704\n"
"ds_read_b64_tr_b16 a[136:137], v207 offset:8\n"
"ds_read_b64_tr_b16 a[138:139], v207 offset:520\n"
"ds_read_b64_tr_b16 a[140:141], v207 offset:8200\n"
"ds_read_b64_tr_b16 a[142:143], v207 offset:8712\n"
"buffer_load_dwordx4 v[144:147], v204, s[32:35], s60 offen nt\n"
"s_add_u32 s60, s60, 32768\n"
"buffer_load_dwordx4 v[148:151], v204, s[32:35], s60 offen nt\n"
"s_add_u32 s60, s60, 32768\n"
"buffer_load_dwordx4 v[160:163], v204, s[32:35], s60 offen nt\n"
"s_add_u32 s60, s60, 32768\n"
"buffer_load_dwordx4 v[164:167], v204, s[32:35], s60 offen nt\n"
"s_add_u32 s60, s60, 425984\n"
"s_waitcnt vmcnt(12)\n"
"ds_write_b128 v205, a[160:163] offset:16384\n"
"ds_write_b128 v205, a[164:167] offset:17408\n"
"ds_write_b128 v206, a[168:171] offset:18432\n"
"ds_write_b128 v206, a[172:175] offset:19456\n"
"s_waitcnt lgkmcnt(0)\n"
"s_barrier\n"
"ds_read_b64_tr_b16 a[160:161], v207 offset:16384\n"
"ds_read_b64_tr_b16 a[162:163], v207 offset:16896\n"
"ds_read_b64_tr_b16 a[164:165], v207 offset:24576\n"
"ds_read_b64_tr_b16 a[166:167], v207 offset:25088\n"
"ds_read_b64_tr_b16 a[168:169], v207 offset:16392\n"
"ds_read_b64_tr_b16 a[170:171], v207 offset:16904\n"
"ds_read_b64_tr_b16 a[172:173], v207 offset:24584\n"
"ds_read_b64_tr_b16 a[174:175], v207 offset:25096\n"
"buffer_load_dwordx4 a[16:19], v204, s[32:35], s60 offen nt\n"
"s_add_u32 s60, s60, 32768\n"
"buffer_load_dwordx4 a[20:23], v204, s[32:35], s60 offen nt\n"
"s_add_u32 s60, s60, 32768\n"
"buffer_load_dwordx4 a[24:27], v204, s[32:35], s60 offen nt\n"
"s_add_u32 s60, s60, 32768\n"
"buffer_load_dwordx4 a[28:31], v204, s[32:35], s60 offen nt\n"
"s_add_u32 s60, s60, 425984\n"
"s_waitcnt vmcnt(12)\n"
"ds_write_b128 v205, v[168:171] offset:0\n"
"ds_write_b128 v205, v[172:175] offset:1024\n"
"ds_write_b128 v206, v[176:179] offset:2048\n"
"ds_write_b128 v206, v[180:183] offset:3072\n"
"s_waitcnt lgkmcnt(0)\n"
"s_barrier\n"
"ds_read_b64_tr_b16 v[168:169], v207 offset:0\n"
"ds_read_b64_tr_b16 v[170:171], v207 offset:512\n"
"ds_read_b64_tr_b16 v[172:173], v207 offset:8192\n"
"ds_read_b64_tr_b16 v[174:175], v207 offset:8704\n"
"ds_read_b64_tr_b16 v[176:177], v207 offset:8\n"
"ds_read_b64_tr_b16 v[178:179], v207 offset:520\n"
"ds_read_b64_tr_b16 v[180:181], v207 offset:8200\n"
"ds_read_b64_tr_b16 v[182:183], v207 offset:8712\n"
"buffer_load_dwordx4 a[48:51], v204, s[32:35], s60 offen nt\n"
"s_add_u32 s60, s60, 32768\n"
"buffer_load_dwordx4 a[52:55], v204, s[32:35], s60 offen nt\n"
"s_add_u32 s60, s60, 32768\n"
"buffer_load_dwordx4 a[56:59], v204, s[32:35], s60 offen nt\n"
"s_add_u32 s60, s60, 32768\n"
"buffer_load_dwordx4 a[60:63], v204, s[32:35], s60 offen nt\n"
"s_add_u32 s60, s60, 425984\n"
"s_waitcnt vmcnt(12)\n"
"ds_write_b128 v205, v[112:115] offset:16384\n"
"ds_write_b128 v205, v[116:119] offset:17408\n"
"ds_write_b128 v206, v[128:131] offset:18432\n"
"ds_write_b128 v206, v[132:135] offset:19456\n"
"s_waitcnt lgkmcnt(0)\n"
"ds_write_b128 v5, v[168:171] offset:16384\n"
"ds_write_b128 v5, v[172:175] offset:17408\n"
"ds_write_b128 v5, v[176:179] offset:20480\n"
"ds_write_b128 v5, v[180:183] offset:21504\n"
"s_barrier\n"
"ds_read_b64_tr_b16 v[112:113], v207 offset:16384\n"
"ds_read_b64_tr_b16 v[114:115], v207 offset:16896\n"
"ds_read_b64_tr_b16 v[116:117], v207 offset:24576\n"
"ds_read_b64_tr_b16 v[118:119], v207 offset:25088\n"
"ds_read_b64_tr_b16 v[128:129], v207 offset:16392\n"
"ds_read_b64_tr_b16 v[130:131], v207 offset:16904\n"
"ds_read_b64_tr_b16 v[132:133], v207 offset:24584\n"
"ds_read_b64_tr_b16 v[134:135], v207 offset:25096\n"
"buffer_load_dwordx4 a[80:83], v204, s[32:35], s60 offen nt\n"
"s_add_u32 s60, s60, 32768\n"
"buffer_load_dwordx4 a[84:87], v204, s[32:35], s60 offen nt\n"
"s_add_u32 s60, s60, 32768\n"
"buffer_load_dwordx4 a[88:91], v204, s[32:35], s60 offen nt\n"
"s_add_u32 s60, s60, 32768\n"
"buffer_load_dwordx4 a[92:95], v204, s[32:35], s60 offen nt\n"
"s_add_u32 s60, s60, 425984\n"
"s_waitcnt vmcnt(12)\n"
"ds_write_b128 v205, v[144:147] offset:0\n"
"ds_write_b128 v205, v[148:151] offset:1024\n"
"ds_write_b128 v206, v[160:163] offset:2048\n"
"ds_write_b128 v206, v[164:167] offset:3072\n"
"s_waitcnt lgkmcnt(0)\n"
"ds_write_b128 v5, v[112:115] offset:2048\n"
"ds_write_b128 v5, v[116:119] offset:3072\n"
"ds_write_b128 v5, v[128:131] offset:6144\n"
"ds_write_b128 v5, v[132:135] offset:7168\n"
"s_barrier\n"
"ds_read_b64_tr_b16 v[144:145], v207 offset:0\n"
"ds_read_b64_tr_b16 v[146:147], v207 offset:512\n"
"ds_read_b64_tr_b16 v[148:149], v207 offset:8192\n"
"ds_read_b64_tr_b16 v[150:151], v207 offset:8704\n"
"ds_read_b64_tr_b16 v[160:161], v207 offset:8\n"
"ds_read_b64_tr_b16 v[162:163], v207 offset:520\n"
"ds_read_b64_tr_b16 v[164:165], v207 offset:8200\n"
"ds_read_b64_tr_b16 v[166:167], v207 offset:8712\n"
"buffer_load_dwordx4 a[112:115], v204, s[32:35], s60 offen nt\n"
"s_add_u32 s60, s60, 32768\n"
"buffer_load_dwordx4 a[116:119], v204, s[32:35], s60 offen nt\n"
"s_add_u32 s60, s60, 32768\n"
"buffer_load_dwordx4 a[120:123], v204, s[32:35], s60 offen nt\n"
"s_add_u32 s60, s60, 32768\n"
"buffer_load_dwordx4 a[124:127], v204, s[32:35], s60 offen nt\n"
"s_add_u32 s60, s60, 425984\n"
"s_waitcnt vmcnt(12)\n"
"ds_write_b128 v205, a[16:19] offset:16384\n"
"ds_write_b128 v205, a[20:23] offset:17408\n"
"ds_write_b128 v206, a[24:27] offset:18432\n"
"ds_write_b128 v206, a[28:31] offset:19456\n"
"s_waitcnt lgkmcnt(0)\n"
"ds_write_b128 v5, v[144:147] offset:10240\n"
"ds_write_b128 v5, v[148:151] offset:11264\n"
"ds_write_b128 v5, v[160:163] offset:14336\n"
"ds_write_b128 v5, v[164:167] offset:15360\n"
"s_barrier\n"
"ds_read_b64_tr_b16 a[16:17], v207 offset:16384\n"
"ds_read_b64_tr_b16 a[18:19], v207 offset:16896\n"
"ds_read_b64_tr_b16 a[20:21], v207 offset:24576\n"
"ds_read_b64_tr_b16 a[22:23], v207 offset:25088\n"
"ds_read_b64_tr_b16 a[24:25], v207 offset:16392\n"
"ds_read_b64_tr_b16 a[26:27], v207 offset:16904\n"
"ds_read_b64_tr_b16 a[28:29], v207 offset:24584\n"
"ds_read_b64_tr_b16 a[30:31], v207 offset:25096\n"
"buffer_load_dwordx4 a[144:147], v204, s[32:35], s60 offen nt\n"
"s_add_u32 s60, s60, 32768\n"
"buffer_load_dwordx4 a[148:151], v204, s[32:35], s60 offen nt\n"
"s_add_u32 s60, s60, 32768\n"
"buffer_load_dwordx4 a[152:155], v204, s[32:35], s60 offen nt\n"
"s_add_u32 s60, s60, 32768\n"
"buffer_load_dwordx4 a[156:159], v204, s[32:35], s60 offen nt\n"
"s_add_u32 s60, s60, 425984\n"
"s_waitcnt vmcnt(12)\n"
"ds_write_b128 v205, a[48:51] offset:0\n"
"ds_write_b128 v205, a[52:55] offset:1024\n"
"ds_write_b128 v206, a[56:59] offset:2048\n"
"ds_write_b128 v206, a[60:63] offset:3072\n"
"s_waitcnt lgkmcnt(0)\n"
"s_barrier\n"
"ds_read_b64_tr_b16 a[48:49], v207 offset:0\n"
"ds_read_b64_tr_b16 a[50:51], v207 offset:512\n"
"ds_read_b64_tr_b16 a[52:53], v207 offset:8192\n"
"ds_read_b64_tr_b16 a[54:55], v207 offset:8704\n"
"ds_read_b64_tr_b16 a[56:57], v207 offset:8\n"
"ds_read_b64_tr_b16 a[58:59], v207 offset:520\n"
"ds_read_b64_tr_b16 a[60:61], v207 offset:8200\n"
"ds_read_b64_tr_b16 a[62:63], v207 offset:8712\n"
"buffer_load_dwordx4 a[176:179], v204, s[32:35], s60 offen nt\n"
"s_add_u32 s60, s60, 32768\n"
"buffer_load_dwordx4 a[180:183], v204, s[32:35], s60 offen nt\n"
"s_add_u32 s60, s60, 32768\n"
"buffer_load_dwordx4 a[184:187], v204, s[32:35], s60 offen nt\n"
"s_add_u32 s60, s60, 32768\n"
"buffer_load_dwordx4 a[188:191], v204, s[32:35], s60 offen nt\n"
"s_add_u32 s60, s60, 425984\n"
"s_waitcnt vmcnt(12)\n"
"ds_write_b128 v205, a[80:83] offset:16384\n"
"ds_write_b128 v205, a[84:87] offset:17408\n"
"ds_write_b128 v206, a[88:91] offset:18432\n"
"ds_write_b128 v206, a[92:95] offset:19456\n"
"s_waitcnt lgkmcnt(0)\n"
"s_barrier\n"
"ds_read_b64_tr_b16 a[80:81], v207 offset:16384\n"
"ds_read_b64_tr_b16 a[82:83], v207 offset:16896\n"
"ds_read_b64_tr_b16 a[84:85], v207 offset:24576\n"
"ds_read_b64_tr_b16 a[86:87], v207 offset:25088\n"
"ds_read_b64_tr_b16 a[88:89], v207 offset:16392\n"
"ds_read_b64_tr_b16 a[90:91], v207 offset:16904\n"
"ds_read_b64_tr_b16 a[92:93], v207 offset:24584\n"
"ds_read_b64_tr_b16 a[94:95], v207 offset:25096\n"
"buffer_load_dwordx4 v[184:187], v204, s[32:35], s60 offen nt\n"
"s_add_u32 s60, s60, 32768\n"
"buffer_load_dwordx4 v[188:191], v204, s[32:35], s60 offen nt\n"
"s_add_u32 s60, s60, 32768\n"
"buffer_load_dwordx4 v[192:195], v204, s[32:35], s60 offen nt\n"
"s_add_u32 s60, s60, 32768\n"
"buffer_load_dwordx4 v[196:199], v204, s[32:35], s60 offen nt\n"
"s_add_u32 s60, s60, 425984\n"
"s_waitcnt vmcnt(12)\n"
"ds_write_b128 v205, a[112:115] offset:0\n"
"ds_write_b128 v205, a[116:119] offset:1024\n"
"ds_write_b128 v206, a[120:123] offset:2048\n"
"ds_write_b128 v206, a[124:127] offset:3072\n"
"s_waitcnt lgkmcnt(0)\n"
"s_barrier\n"
"ds_read_b64_tr_b16 a[112:113], v207 offset:0\n"
"ds_read_b64_tr_b16 a[114:115], v207 offset:512\n"
"ds_read_b64_tr_b16 a[116:117], v207 offset:8192\n"
"ds_read_b64_tr_b16 a[118:119], v207 offset:8704\n"
"ds_read_b64_tr_b16 a[120:121], v207 offset:8\n"
"ds_read_b64_tr_b16 a[122:123], v207 offset:520\n"
"ds_read_b64_tr_b16 a[124:125], v207 offset:8200\n"
"ds_read_b64_tr_b16 a[126:127], v207 offset:8712\n"
"buffer_load_dwordx4 a[192:195], v204, s[64:67], s61 offen nt\n"
"s_add_u32 s61, s61, 32768\n"
"buffer_load_dwordx4 a[196:199], v204, s[64:67], s61 offen nt\n"
"s_add_u32 s61, s61, 32768\n"
"buffer_load_dwordx4 a[200:203], v204, s[64:67], s61 offen nt\n"
"s_add_u32 s61, s61, 32768\n"
"buffer_load_dwordx4 a[204:207], v204, s[64:67], s61 offen nt\n"
"s_add_u32 s61, s61, 425984\n"
"s_waitcnt vmcnt(12)\n"
"ds_write_b128 v205, a[144:147] offset:16384\n"
"ds_write_b128 v205, a[148:151] offset:17408\n"
"ds_write_b128 v206, a[152:155] offset:18432\n"
"ds_write_b128 v206, a[156:159] offset:19456\n"
"s_waitcnt lgkmcnt(0)\n"
"s_barrier\n"
"ds_read_b64_tr_b16 a[144:145], v207 offset:16384\n"
"ds_read_b64_tr_b16 a[146:147], v207 offset:16896\n"
"ds_read_b64_tr_b16 a[148:149], v207 offset:24576\n"
"ds_read_b64_tr_b16 a[150:151], v207 offset:25088\n"
"ds_read_b64_tr_b16 a[152:153], v207 offset:16392\n"
"ds_read_b64_tr_b16 a[154:155], v207 offset:16904\n"
"ds_read_b64_tr_b16 a[156:157], v207 offset:24584\n"
"ds_read_b64_tr_b16 a[158:159], v207 offset:25096\n"
"buffer_load_dwordx4 a[208:211], v204, s[64:67], s61 offen nt\n"
"s_add_u32 s61, s61, 32768\n"
"buffer_load_dwordx4 a[212:215], v204, s[64:67], s61 offen nt\n"
"s_add_u32 s61, s61, 32768\n"
"buffer_load_dwordx4 a[216:219], v204, s[64:67], s61 offen nt\n"
"s_add_u32 s61, s61, 32768\n"
"buffer_load_dwordx4 a[220:223], v204, s[64:67], s61 offen nt\n"
"s_add_u32 s61, s61, 425984\n"
"s_waitcnt vmcnt(12)\n"
"ds_write_b128 v205, a[176:179] offset:0\n"
"ds_write_b128 v205, a[180:183] offset:1024\n"
"ds_write_b128 v206, a[184:187] offset:2048\n"
"ds_write_b128 v206, a[188:191] offset:3072\n"
"s_waitcnt lgkmcnt(0)\n"
"s_barrier\n"
"ds_read_b64_tr_b16 a[176:177], v207 offset:0\n"
"ds_read_b64_tr_b16 a[178:179], v207 offset:512\n"
"ds_read_b64_tr_b16 a[180:181], v207 offset:8192\n"
"ds_read_b64_tr_b16 a[182:183], v207 offset:8704\n"
"ds_read_b64_tr_b16 a[184:185], v207 offset:8\n"
"ds_read_b64_tr_b16 a[186:187], v207 offset:520\n"
"ds_read_b64_tr_b16 a[188:189], v207 offset:8200\n"
"ds_read_b64_tr_b16 a[190:191], v207 offset:8712\n"
"buffer_load_dwordx4 a[224:227], v204, s[64:67], s61 offen nt\n"
"s_add_u32 s61, s61, 32768\n"
"buffer_load_dwordx4 a[228:231], v204, s[64:67], s61 offen nt\n"
"s_add_u32 s61, s61, 32768\n"
"buffer_load_dwordx4 a[232:235], v204, s[64:67], s61 offen nt\n"
"s_add_u32 s61, s61, 32768\n"
"buffer_load_dwordx4 a[236:239], v204, s[64:67], s61 offen nt\n"
"s_add_u32 s61, s61, 425984\n"
"s_waitcnt vmcnt(12)\n"
"ds_write_b128 v205, v[184:187] offset:16384\n"
"ds_write_b128 v205, v[188:191] offset:17408\n"
"ds_write_b128 v206, v[192:195] offset:18432\n"
"ds_write_b128 v206, v[196:199] offset:19456\n"
"s_waitcnt lgkmcnt(0)\n"
"s_barrier\n"
"ds_read_b64_tr_b16 v[184:185], v207 offset:16384\n"
"ds_read_b64_tr_b16 v[186:187], v207 offset:16896\n"
"ds_read_b64_tr_b16 v[188:189], v207 offset:24576\n"
"ds_read_b64_tr_b16 v[190:191], v207 offset:25088\n"
"ds_read_b64_tr_b16 v[192:193], v207 offset:16392\n"
"ds_read_b64_tr_b16 v[194:195], v207 offset:16904\n"
"ds_read_b64_tr_b16 v[196:197], v207 offset:24584\n"
"ds_read_b64_tr_b16 v[198:199], v207 offset:25096\n"
"buffer_load_dwordx4 a[240:243], v204, s[64:67], s61 offen nt\n"
"s_add_u32 s61, s61, 32768\n"
"buffer_load_dwordx4 a[244:247], v204, s[64:67], s61 offen nt\n"
"s_add_u32 s61, s61, 32768\n"
"buffer_load_dwordx4 a[248:251], v204, s[64:67], s61 offen nt\n"
"s_add_u32 s61, s61, 32768\n"
"buffer_load_dwordx4 a[252:255], v204, s[64:67], s61 offen nt\n"
"s_add_u32 s61, s61, 425984\n"
"s_waitcnt vmcnt(12)\n"
"ds_write_b128 v205, a[192:195] offset:0\n"
"ds_write_b128 v205, a[196:199] offset:1024\n"
"ds_write_b128 v206, a[200:203] offset:2048\n"
"ds_write_b128 v206, a[204:207] offset:3072\n"
"s_waitcnt lgkmcnt(0)\n"
"ds_write_b128 v5, v[184:187] offset:18432\n"
"ds_write_b128 v5, v[188:191] offset:19456\n"
"ds_write_b128 v5, v[192:195] offset:22528\n"
"ds_write_b128 v5, v[196:199] offset:23552\n"
"s_barrier\n"
"ds_read_b64_tr_b16 a[192:193], v207 offset:0\n"
"ds_read_b64_tr_b16 a[194:195], v207 offset:512\n"
"ds_read_b64_tr_b16 a[196:197], v207 offset:8192\n"
"ds_read_b64_tr_b16 a[198:199], v207 offset:8704\n"
"ds_read_b64_tr_b16 a[200:201], v207 offset:8\n"
"ds_read_b64_tr_b16 a[202:203], v207 offset:520\n"
"ds_read_b64_tr_b16 a[204:205], v207 offset:8200\n"
"ds_read_b64_tr_b16 a[206:207], v207 offset:8712\n"
"buffer_load_dwordx4 v[24:27], v204, s[64:67], s61 offen nt\n"
"s_add_u32 s61, s61, 32768\n"
"buffer_load_dwordx4 v[28:31], v204, s[64:67], s61 offen nt\n"
"s_add_u32 s61, s61, 32768\n"
"buffer_load_dwordx4 v[32:35], v204, s[64:67], s61 offen nt\n"
"s_add_u32 s61, s61, 32768\n"
"buffer_load_dwordx4 v[36:39], v204, s[64:67], s61 offen nt\n"
"s_add_u32 s61, s61, 425984\n"
"s_waitcnt vmcnt(12)\n"
"ds_write_b128 v205, a[208:211] offset:16384\n"
"ds_write_b128 v205, a[212:215] offset:17408\n"
"ds_write_b128 v206, a[216:219] offset:18432\n"
"ds_write_b128 v206, a[220:223] offset:19456\n"
"s_waitcnt lgkmcnt(0)\n"
"s_barrier\n"
"ds_read_b64_tr_b16 a[208:209], v207 offset:16384\n"
"ds_read_b64_tr_b16 a[210:211], v207 offset:16896\n"
"ds_read_b64_tr_b16 a[212:213], v207 offset:24576\n"
"ds_read_b64_tr_b16 a[214:215], v207 offset:25088\n"
"ds_read_b64_tr_b16 a[216:217], v207 offset:16392\n"
"ds_read_b64_tr_b16 a[218:219], v207 offset:16904\n"
"ds_read_b64_tr_b16 a[220:221], v207 offset:24584\n"
"ds_read_b64_tr_b16 a[222:223], v207 offset:25096\n"
"buffer_load_dwordx4 v[40:43], v204, s[64:67], s61 offen nt\n"
"s_add_u32 s61, s61, 32768\n"
"buffer_load_dwordx4 v[44:47], v204, s[64:67], s61 offen nt\n"
"s_add_u32 s61, s61, 32768\n"
"buffer_load_dwordx4 v[48:51], v204, s[64:67], s61 offen nt\n"
"s_add_u32 s61, s61, 32768\n"
"buffer_load_dwordx4 v[52:55], v204, s[64:67], s61 offen nt\n"
"s_add_u32 s61, s61, 425984\n"
"s_waitcnt vmcnt(12)\n"
"ds_write_b128 v205, a[224:227] offset:0\n"
"ds_write_b128 v205, a[228:231] offset:1024\n"
"ds_write_b128 v206, a[232:235] offset:2048\n"
"ds_write_b128 v206, a[236:239] offset:3072\n"
"s_waitcnt lgkmcnt(0)\n"
"s_barrier\n"
"ds_read_b64_tr_b16 a[224:225], v207 offset:0\n"
"ds_read_b64_tr_b16 a[226:227], v207 offset:512\n"
"ds_read_b64_tr_b16 a[228:229], v207 offset:8192\n"
"ds_read_b64_tr_b16 a[230:231], v207 offset:8704\n"
"ds_read_b64_tr_b16 a[232:233], v207 offset:8\n"
"ds_read_b64_tr_b16 a[234:235], v207 offset:520\n"
"ds_read_b64_tr_b16 a[236:237], v207 offset:8200\n"
"ds_read_b64_tr_b16 a[238:239], v207 offset:8712\n"
"buffer_load_dwordx4 v[56:59], v204, s[64:67], s61 offen nt\n"
"s_add_u32 s61, s61, 32768\n"
"buffer_load_dwordx4 v[60:63], v204, s[64:67], s61 offen nt\n"
"s_add_u32 s61, s61, 32768\n"
"buffer_load_dwordx4 v[64:67], v204, s[64:67], s61 offen nt\n"
"s_add_u32 s61, s61, 32768\n"
"buffer_load_dwordx4 v[68:71], v204, s[64:67], s61 offen nt\n"
"s_add_u32 s61, s61, 425984\n"
"s_waitcnt vmcnt(12)\n"
"ds_write_b128 v205, a[240:243] offset:16384\n"
"ds_write_b128 v205, a[244:247] offset:17408\n"
"ds_write_b128 v206, a[248:251] offset:18432\n"
"ds_write_b128 v206, a[252:255] offset:19456\n"
"s_waitcnt lgkmcnt(0)\n"
"s_barrier\n"
"ds_read_b64_tr_b16 a[240:241], v207 offset:16384\n"
"ds_read_b64_tr_b16 a[242:243], v207 offset:16896\n"
"ds_read_b64_tr_b16 a[244:245], v207 offset:24576\n"
"ds_read_b64_tr_b16 a[246:247], v207 offset:25088\n"
"ds_read_b64_tr_b16 a[248:249], v207 offset:16392\n"
"ds_read_b64_tr_b16 a[250:251], v207 offset:16904\n"
"ds_read_b64_tr_b16 a[252:253], v207 offset:24584\n"
"ds_read_b64_tr_b16 a[254:255], v207 offset:25096\n"
"buffer_load_dwordx4 v[72:75], v204, s[64:67], s61 offen nt\n"
"s_add_u32 s61, s61, 32768\n"
"buffer_load_dwordx4 v[76:79], v204, s[64:67], s61 offen nt\n"
"s_add_u32 s61, s61, 32768\n"
"buffer_load_dwordx4 v[80:83], v204, s[64:67], s61 offen nt\n"
"s_add_u32 s61, s61, 32768\n"
"buffer_load_dwordx4 v[84:87], v204, s[64:67], s61 offen nt\n"
"s_add_u32 s61, s61, 425984\n"
"s_waitcnt vmcnt(12)\n"
"ds_write_b128 v205, v[24:27] offset:0\n"
"ds_write_b128 v205, v[28:31] offset:1024\n"
"ds_write_b128 v206, v[32:35] offset:2048\n"
"ds_write_b128 v206, v[36:39] offset:3072\n"
"s_waitcnt lgkmcnt(0)\n"
"s_barrier\n"
"ds_read_b64_tr_b16 v[24:25], v207 offset:0\n"
"ds_read_b64_tr_b16 v[26:27], v207 offset:512\n"
"ds_read_b64_tr_b16 v[28:29], v207 offset:8192\n"
"ds_read_b64_tr_b16 v[30:31], v207 offset:8704\n"
"ds_read_b64_tr_b16 v[32:33], v207 offset:8\n"
"ds_read_b64_tr_b16 v[34:35], v207 offset:520\n"
"ds_read_b64_tr_b16 v[36:37], v207 offset:8200\n"
"ds_read_b64_tr_b16 v[38:39], v207 offset:8712\n"
"buffer_load_dwordx4 v[88:91], v204, s[64:67], s61 offen nt\n"
"s_add_u32 s61, s61, 32768\n"
"buffer_load_dwordx4 v[92:95], v204, s[64:67], s61 offen nt\n"
"s_add_u32 s61, s61, 32768\n"
"buffer_load_dwordx4 v[96:99], v204, s[64:67], s61 offen nt\n"
"s_add_u32 s61, s61, 32768\n"
"buffer_load_dwordx4 v[100:103], v204, s[64:67], s61 offen nt\n"
"s_add_u32 s61, s61, 425984\n"
"s_waitcnt vmcnt(12)\n"
"ds_write_b128 v205, v[40:43] offset:16384\n"
"ds_write_b128 v205, v[44:47] offset:17408\n"
"ds_write_b128 v206, v[48:51] offset:18432\n"
"ds_write_b128 v206, v[52:55] offset:19456\n"
"s_waitcnt lgkmcnt(0)\n"
"s_barrier\n"
"ds_read_b64_tr_b16 v[40:41], v207 offset:16384\n"
"ds_read_b64_tr_b16 v[42:43], v207 offset:16896\n"
"ds_read_b64_tr_b16 v[44:45], v207 offset:24576\n"
"ds_read_b64_tr_b16 v[46:47], v207 offset:25088\n"
"ds_read_b64_tr_b16 v[48:49], v207 offset:16392\n"
"ds_read_b64_tr_b16 v[50:51], v207 offset:16904\n"
"ds_read_b64_tr_b16 v[52:53], v207 offset:24584\n"
"ds_read_b64_tr_b16 v[54:55], v207 offset:25096\n"
"s_waitcnt vmcnt(8)\n"
"ds_write_b128 v205, v[56:59] offset:0\n"
"ds_write_b128 v205, v[60:63] offset:1024\n"
"ds_write_b128 v206, v[64:67] offset:2048\n"
"ds_write_b128 v206, v[68:71] offset:3072\n"
"s_waitcnt lgkmcnt(0)\n"
"s_barrier\n"
"ds_read_b64_tr_b16 v[56:57], v207 offset:0\n"
"ds_read_b64_tr_b16 v[58:59], v207 offset:512\n"
"ds_read_b64_tr_b16 v[60:61], v207 offset:8192\n"
"ds_read_b64_tr_b16 v[62:63], v207 offset:8704\n"
"ds_read_b64_tr_b16 v[64:65], v207 offset:8\n"
"ds_read_b64_tr_b16 v[66:67], v207 offset:520\n"
"ds_read_b64_tr_b16 v[68:69], v207 offset:8200\n"
"ds_read_b64_tr_b16 v[70:71], v207 offset:8712\n"
"s_waitcnt vmcnt(4)\n"
"ds_write_b128 v205, v[72:75] offset:16384\n"
"ds_write_b128 v205, v[76:79] offset:17408\n"
"ds_write_b128 v206, v[80:83] offset:18432\n"
"ds_write_b128 v206, v[84:87] offset:19456\n"
"s_waitcnt lgkmcnt(0)\n"
"s_barrier\n"
"ds_read_b64_tr_b16 v[72:73], v207 offset:16384\n"
"ds_read_b64_tr_b16 v[74:75], v207 offset:16896\n"
"ds_read_b64_tr_b16 v[76:77], v207 offset:24576\n"
"ds_read_b64_tr_b16 v[78:79], v207 offset:25088\n"
"ds_read_b64_tr_b16 v[80:81], v207 offset:16392\n"
"ds_read_b64_tr_b16 v[82:83], v207 offset:16904\n"
"ds_read_b64_tr_b16 v[84:85], v207 offset:24584\n"
"ds_read_b64_tr_b16 v[86:87], v207 offset:25096\n"
"s_waitcnt vmcnt(0)\n"
"ds_write_b128 v205, v[88:91] offset:0\n"
"ds_write_b128 v205, v[92:95] offset:1024\n"
"ds_write_b128 v206, v[96:99] offset:2048\n"
"ds_write_b128 v206, v[100:103] offset:3072\n"
"s_waitcnt lgkmcnt(0)\n"
"s_barrier\n"
"ds_read_b64_tr_b16 v[88:89], v207 offset:0\n"
"ds_read_b64_tr_b16 v[90:91], v207 offset:512\n"
"ds_read_b64_tr_b16 v[92:93], v207 offset:8192\n"
"ds_read_b64_tr_b16 v[94:95], v207 offset:8704\n"
"ds_read_b64_tr_b16 v[96:97], v207 offset:8\n"
"ds_read_b64_tr_b16 v[98:99], v207 offset:520\n"
"ds_read_b64_tr_b16 v[100:101], v207 offset:8200\n"
"ds_read_b64_tr_b16 v[102:103], v207 offset:8712\n"
"s_waitcnt lgkmcnt(0)\n"
"s_barrier\n"
"s_load_dwordx2 s[32:33], s[16:17], 8\n"
"s_mov_b32 s34, 9216\n"
"s_mov_b32 s35, 0x00020000\n"
"s_waitcnt lgkmcnt(0)\n"
"s_and_b32 s33, s33, 0xffff\n"
"v_and_b32 v230, 31, v0\n"
"v_lshlrev_b32 v230, 7, v230\n"
"s_mov_b32 s63, 0\n"
"v_min_u32 v4, 35, v0\n"
"v_lshlrev_b32 v4, 7, v4\n"
"s_lshl_b32 s72, s19, 7\n"
"s_xor_b32 s72, s72, s22\n"
"s_xor_b32 s72, s72, 1228800\n"
"s_cmp_eq_u32 s20, 0\n"
"s_cselect_b32 s72, s72, 2146435072\n"
"v_mov_b32 v214, 0x3f803f80\n"
"v_mov_b32 v208, 0\n"
"v_mov_b32 v215, 0x3f803f80\n"
"v_mov_b32 v209, 0\n"
"v_mov_b32 v216, 0x3f803f80\n"
"v_mov_b32 v210, 0\n"
"v_mov_b32 v217, 0x3f803f80\n"
"v_mov_b32 v211, 0\n"
"s_mov_b32 s21, 0\n"
"s_lshl_b32 s69, s19, 15\n"
"s_add_u32 s69, s69, 2097152\n"
"s_mov_b32 s74, 0\n"
"s_add_u32 s53, s22, 983040\n"
"s_add_u32 s56, s22, 1081344\n"
"s_add_u32 s57, s22, 1146880\n"
"s_lshl_b32 s36, s19, 7\n"
"s_add_u32 s58, s22, s36\n"
"s_add_u32 s58, s58, 1081344\n"
"s_mov_b32 s73, 2146435072\n"
"s_lshl_b32 s60, s19, 12\n"
"s_add_u32 s60, s60, 148897792\n"
"s_mov_b32 s59, 2146435072\n"
"Lce_2:\n"
"s_memtime s[48:49]\n"
"s_memrealtime s[36:37]\n"
"s_nop 3\n"
"s_memrealtime s[38:39]\n"
"s_nop 3\n"
"s_memrealtime s[40:41]\n"
"s_nop 3\n"
"s_memrealtime s[42:43]\n"
"s_nop 3\n"
"s_memrealtime s[44:45]\n"
"s_nop 3\n"
"s_memrealtime s[46:47]\n"
"s_waitcnt lgkmcnt(0)\n"
"s_cmp_eq_u32 s46, s36\n"
"s_cbranch_scc1 Lce_2\n"
"s_cmp_eq_u32 s38, s36\n"
"s_cselect_b32 s37, 20, 0\n"
"s_add_u32 s48, s48, s37\n"
"s_cmp_eq_u32 s40, s36\n"
"s_cselect_b32 s37, 20, 0\n"
"s_add_u32 s48, s48, s37\n"
"s_cmp_eq_u32 s42, s36\n"
"s_cselect_b32 s37, 20, 0\n"
"s_add_u32 s48, s48, s37\n"
"s_cmp_eq_u32 s44, s36\n"
"s_cselect_b32 s37, 20, 0\n"
"s_add_u32 s48, s48, s37\n"
"s_mul_i32 s40, s46, 24\n"
"s_sub_u32 s40, s40, s48\n"
"s_sub_u32 s38, s40, s75\n"
"s_sub_u32 s39, s48, s62\n"
"v_cvt_f32_i32 v12, s38\n"
"v_cvt_f32_u32 v13, s39\n"
"v_rcp_f32 v13, v13\n"
"v_mul_f32 v12, 0x47800000, v12\n"
"v_mul_f32 v12, v12, v13\n"
"v_cvt_i32_f32 v12, v12\n"
"s_nop 0\n"
"v_readfirstlane_b32 s83, v12\n"
"s_max_i32 s83, s83, -2000\n"
"s_min_i32 s83, s83, 30000\n"
"s_mov_b32 s75, s40\n"
"s_mov_b32 s62, s48\n"
"s_mov_b32 s76, 1800\n"
"s_mov_b32 s65, 200\n"
"s_mov_b32 s16, 0\n"
"s_branch LA_pre\n"
".p2align 6\n"
"LA_loop:\n"
"s_cmp_eq_u32 s21, 64\n"
"s_cbranch_scc1 LA_st\n"
"LA_pre:\n"
"s_mov_b32 s73, 2146435072\n"
"s_cmp_lg_u32 s20, 3\n"
"s_cbranch_scc1 Lcw_3\n"
"s_add_u32 s73, s21, 1\n"
"s_and_b32 s73, s73, 3\n"
"s_lshl_b32 s73, s73, 14\n"
"s_add_u32 s73, s73, s58\n"
"v_mov_b32 v12, 0\n"
"buffer_store_dword v12, v15, s[24:27], s73 offen sc1\n"
"s_add_u32 s73, s21, 0\n"
"s_and_b32 s73, s73, 3\n"
"s_lshl_b32 s73, s73, 14\n"
"s_add_u32 s73, s73, s58\n"
"Lcw_3:\n"
"s_add_u32 s64, s21, 3\n"
"s_and_b32 s64, s64, 3\n"
"s_lshl_b32 s64, s64, 14\n"
"s_add_u32 s64, s64, s57\n"
"Lx1_5:\n"
"s_cmp_gt_i32 s68, s21\n"
"s_cbranch_scc1 Lx1_6\n"
"v_mov_b32 v12, 0\n"
"buffer_load_dword v12, v12, s[24:27], s18 offen sc1\n"
"s_waitcnt vmcnt(0)\n"
"v_readfirstlane_b32 s68, v12\n"
"s_cmp_gt_i32 s68, s21\n"
"s_cbranch_scc1 Lx1_6\n"
"s_sleep 4\n"
"s_memrealtime s[48:49]\n"
"s_waitcnt lgkmcnt(0)\n"
"s_sub_u32 s48, s48, s23\n"
"s_cmp_gt_i32 s48, 0\n"
"s_cbranch_scc1 LA_end\n"
"s_branch Lx1_5\n"
"Lx1_6:\n"
"s_mov_b32 s36, s69\n"
"s_lshr_b32 s43, s36, 8\n"
"s_xor_b32 s43, s43, s22\n"
"s_and_b32 s43, s43, 8192\n"
"s_or_b32 s36, s36, s43\n"
"s_add_u32 s37, s36, 16384\n"
"buffer_load_dwordx4 v[172:175], v3, s[24:27], s36 offen sc1\n"
"buffer_load_dwordx4 v[176:179], v3, s[24:27], s36 offen offset:1024 sc1\n"
"buffer_load_dwordx4 v[16:19], v3, s[24:27], s37 offen sc1\n"
"buffer_load_dwordx4 v[20:23], v3, s[24:27], s37 offen offset:1024 sc1\n"
"s_add_u32 s69, s69, 1048576\n"
"v_mov_b32 v220, 0\n"
"buffer_load_dword v220, v220, s[24:27], s18 offen sc1\n"
"LA_st:\n"
"v_mov_b32 v168, -1\n"
"v_mov_b32 v169, -1\n"
"v_mov_b32 v170, -1\n"
"v_mov_b32 v171, -1\n"
"s_mov_b32 s67, s74\n"
"s_add_u32 s74, s74, 131072\n"
"s_cmp_eq_u32 s74, 393216\n"
"s_cselect_b32 s74, 0, s74\n"
"buffer_store_dwordx4 v[168:171], v9, s[24:27], s74 offen sc1\n"
"s_add_u32 s36, s74, 393216\n"
"buffer_store_dwordx4 v[168:171], v9, s[24:27], s36 offen sc1\n"
"s_lshl_b32 s59, s21, 17\n"
"s_add_u32 s59, s59, s60\n"
"s_cmp_eq_u32 s21, 64\n"
"s_cbranch_scc1 LA_fin\n"
"ds_read_b128 v[136:139], v5 offset:8192\n"
"ds_read_b128 v[140:143], v5 offset:9216\n"
"ds_read_b128 v[144:147], v5 offset:10240\n"
"ds_read_b128 v[148:151], v5 offset:11264\n"
"ds_read_b128 v[152:155], v5 offset:12288\n"
"ds_read_b128 v[156:159], v5 offset:13312\n"
"ds_read_b128 v[160:163], v5 offset:14336\n"
"ds_read_b128 v[164:167], v5 offset:15360\n"
"s_add_u32 s53, s53, 98304\n"
"s_cmp_eq_u32 s53, s56\n"
"s_cselect_b32 s36, 294912, 0\n"
"s_sub_u32 s53, s53, s36\n"
"s_add_u32 s55, s53, 16384\n"
"s_add_u32 s54, s53, 4096\n"
"s_waitcnt lgkmcnt(4)\n"
"ds_read_b128 v[104:107], v5 offset:0\n"
"ds_read_b128 v[108:111], v5 offset:1024\n"
"ds_read_b128 v[112:115], v5 offset:2048\n"
"ds_read_b128 v[116:119], v5 offset:3072\n"
"ds_read_b128 v[120:123], v5 offset:4096\n"
"ds_read_b128 v[124:127], v5 offset:5120\n"
"ds_read_b128 v[128:131], v5 offset:6144\n"
"ds_read_b128 v[132:135], v5 offset:7168\n"
"s_cmp_lg_u32 s20, 3\n"
"s_cbranch_scc1 Lcw_7\n"
"s_cmp_eq_u32 s21, 0\n"
"s_cbranch_scc1 Ls0_9\n"
"s_add_u32 s36, s66, 300\n"
"Lwu_10:\n"
"s_memtime s[48:49]\n"
"s_waitcnt lgkmcnt(0)\n"
"s_sub_u32 s44, s36, s48\n"
"s_cmp_lt_i32 s44, 384\n"
"s_cbranch_scc1 Lwu_11\n"
"s_cmp_lt_i32 s44, 534\n"
"s_cbranch_scc1 Lwu_10\n"
"s_cmp_gt_i32 s44, 100000\n"
"s_cbranch_scc1 Lwu_12\n"
"s_sleep 1\n"
"s_branch Lwu_10\n"
"Lwu_11:\n"
"s_sub_i32 s44, s44, 60\n"
"s_cmp_lt_i32 s44, 4\n"
"s_cbranch_scc1 Lwu_12\n"
"s_and_b32 s44, s44, 0xfffffffc\n"
"s_sub_u32 s44, s50, s44\n"
"s_subb_u32 s45, s51, 0\n"
"s_swappc_b64 s[46:47], s[44:45]\n"
"Lwu_12:\n"
"buffer_load_dword v218, v4, s[24:27], s64 offen sc1\n"
"Ls0_9:\n"
"Lce_13:\n"
"s_memtime s[48:49]\n"
"s_memrealtime s[36:37]\n"
"s_nop 3\n"
"s_memrealtime s[38:39]\n"
"s_nop 3\n"
"s_memrealtime s[40:41]\n"
"s_nop 3\n"
"s_memrealtime s[42:43]\n"
"s_nop 3\n"
"s_memrealtime s[44:45]\n"
"s_nop 3\n"
"s_memrealtime s[46:47]\n"
"s_waitcnt lgkmcnt(0)\n"
"s_cmp_eq_u32 s46, s36\n"
"s_cbranch_scc1 Lce_13\n"
"s_cmp_eq_u32 s38, s36\n"
"s_cselect_b32 s37, 20, 0\n"
"s_add_u32 s48, s48, s37\n"
"s_cmp_eq_u32 s40, s36\n"
"s_cselect_b32 s37, 20, 0\n"
"s_add_u32 s48, s48, s37\n"
"s_cmp_eq_u32 s42, s36\n"
"s_cselect_b32 s37, 20, 0\n"
"s_add_u32 s48, s48, s37\n"
"s_cmp_eq_u32 s44, s36\n"
"s_cselect_b32 s37, 20, 0\n"
"s_add_u32 s48, s48, s37\n"
"s_mul_i32 s40, s46, 24\n"
"s_sub_u32 s40, s40, s48\n"
"s_sub_u32 s39, s48, s62\n"
"s_min_u32 s39, s39, 60000\n"
"s_mul_i32 s39, s39, s83\n"
"s_ashr_i32 s39, s39, 16\n"
"s_add_u32 s75, s75, s39\n"
"s_sub_u32 s40, s40, s75\n"
"s_ashr_i32 s39, s40, 1\n"
"s_add_u32 s75, s75, s39\n"
"s_max_i32 s40, s40, -1000\n"
"s_min_i32 s40, s40, 1000\n"
"s_mul_i32 s40, s40, 26\n"
"s_ashr_i32 s40, s40, 3\n"
"s_add_u32 s83, s83, s40\n"
"s_max_i32 s83, s83, -2000\n"
"s_min_i32 s83, s83, 30000\n"
"s_mov_b32 s62, s48\n"
"s_add_u32 s36, s62, 3600\n"
"s_sub_u32 s39, s36, s62\n"
"s_mul_i32 s39, s39, s83\n"
"s_ashr_i32 s39, s39, 16\n"
"s_add_u32 s39, s39, s75\n"
"s_add_u32 s37, s39, s36\n"
"s_sub_u32 s37, s37, s36\n"
"v_mov_b32 v12, s37\n"
"v_mov_b32 v13, 8\n"
"ds_write_b32 v13, v12\n"
"s_waitcnt vmcnt(0)\n"
"ds_write_b128 v3, v[172:175] offset:9216\n"
"ds_write_b128 v3, v[176:179] offset:10240\n"
"s_barrier\n"
"s_cmp_eq_u32 s21, 0\n"
"s_cbranch_scc1 Ls0_8\n"
"s_branch Lst_16\n"
"Lst_14:\n"
"buffer_load_dword v218, v4, s[24:27], s64 offen sc1\n"
"s_waitcnt vmcnt(0)\n"
"Lst_16:\n"
"v_lshrrev_b32 v13, 24, v218\n"
"v_cmp_ne_u32 vcc, s21, v13\n"
"s_cbranch_vccz Lst_15\n"
"s_add_u32 s47, s47, 1\n"
"s_sleep 2\n"
"s_memrealtime s[48:49]\n"
"s_waitcnt lgkmcnt(0)\n"
"s_sub_u32 s48, s48, s23\n"
"s_cmp_gt_i32 s48, 0\n"
"s_cbranch_scc1 LA_end\n"
"s_branch Lst_14\n"
"Lst_15:\n"
"s_add_u32 s36, s62, s75\n"
"v_subrev_u32 v218, s36, v218\n"
"v_bfe_i32 v218, v218, 0, 24\n"
"s_nop 1\n"
"v_max_i32_dpp v218, v218, v218 row_shr:1 row_mask:0xf bank_mask:0xf\n"
"s_nop 1\n"
"v_max_i32_dpp v218, v218, v218 row_shr:2 row_mask:0xf bank_mask:0xf\n"
"s_nop 1\n"
"v_max_i32_dpp v218, v218, v218 row_shr:4 row_mask:0xf bank_mask:0xf\n"
"s_nop 1\n"
"v_max_i32_dpp v218, v218, v218 row_shr:8 row_mask:0xf bank_mask:0xf\n"
"s_nop 0\n"
"v_readlane_b32 s36, v218, 15\n"
"v_readlane_b32 s37, v218, 31\n"
"v_readlane_b32 s38, v218, 47\n"
"s_max_i32 s36, s36, s38\n"
"s_max_i32 s36, s36, s37\n"
"s_max_i32 s36, s36, -60000\n"
"s_min_i32 s36, s36, 60000\n"
"s_add_u32 s43, s36, s62\n"
"s_add_u32 s43, s43, s75\n"
"s_mul_i32 s37, s36, s83\n"
"s_ashr_i32 s37, s37, 16\n"
"s_mul_i32 s38, s37, s83\n"
"s_ashr_i32 s38, s38, 16\n"
"s_sub_u32 s36, s36, s37\n"
"s_add_u32 s36, s36, s38\n"
"s_add_u32 s78, s36, s62\n"
"v_mov_b32 v12, s78\n"
"v_mov_b32 v13, s43\n"
"v_mov_b32 v218, 0\n"
"ds_write_b64 v218, v[12:13]\n"
"s_branch Ls0_8\n"
"Lcw_7:\n"
"s_waitcnt vmcnt(0)\n"
"ds_write_b128 v3, v[172:175] offset:9216\n"
"ds_write_b128 v3, v[176:179] offset:10240\n"
"v_mov_b32 v222, s21\n"
"s_barrier\n"
"buffer_store_dword v222, v15, s[24:27], s72 offen sc1\n"
"Ls0_8:\n"
"s_cmp_eq_u32 s21, 63\n"
"s_cselect_b32 s16, 1, 0\n"
"v_mov_b32 v13, 0\n"
"v_readfirstlane_b32 s68, v220\n"
"s_lshl_b32 s37, s20, 4\n"
"s_add_u32 s37, s37, s65\n"
"s_waitcnt lgkmcnt(0)\n"
"s_barrier\n"
"ds_read_b128 v[222:225], v13\n"
"s_add_u32 s77, s21, 0\n"
"s_and_b32 s77, s77, 3\n"
"s_lshl_b32 s77, s77, 14\n"
"s_add_u32 s77, s77, s58\n"
"s_add_u32 s82, s21, 1\n"
"s_lshl_b32 s82, s82, 24\n"
"s_waitcnt lgkmcnt(0)\n"
"v_readfirstlane_b32 s81, v224\n"
"v_readfirstlane_b32 s36, v222\n"
"s_add_u32 s81, s81, s76\n"
"s_cmp_eq_u32 s21, 0\n"
"s_cbranch_scc1 LA_first\n"
"s_add_u32 s36, s36, s37\n"
"s_mov_b32 s61, s36\n"
"Lwu_17:\n"
"s_memtime s[48:49]\n"
"s_waitcnt lgkmcnt(0)\n"
"s_sub_u32 s44, s36, s48\n"
"s_cmp_lt_i32 s44, 384\n"
"s_cbranch_scc1 Lwu_18\n"
"s_cmp_lt_i32 s44, 534\n"
"s_cbranch_scc1 Lwu_17\n"
"s_cmp_gt_i32 s44, 100000\n"
"s_cbranch_scc1 Lwu_19\n"
"s_sleep 1\n"
"s_branch Lwu_17\n"
"Lwu_18:\n"
"s_sub_i32 s44, s44, 60\n"
"s_cmp_lt_i32 s44, 4\n"
"s_cbranch_scc1 Lwu_19\n"
"s_and_b32 s44, s44, 0xfffffffc\n"
"s_sub_u32 s44, s50, s44\n"
"s_subb_u32 s45, s51, 0\n"
"s_swappc_b64 s[46:47], s[44:45]\n"
"Lwu_19:\n"
"buffer_load_dwordx4 v[168:171], v2, s[24:27], s53 offen offset:0 sc1\n"
"v_mov_b64 v[232:233], 0\n"
"v_mov_b64 v[234:235], 0\n"
"v_mov_b64 v[236:237], 0\n"
"v_mov_b64 v[238:239], 0\n"
"v_mov_b64 v[240:241], 0\n"
"v_mov_b64 v[242:243], 0\n"
"v_mov_b64 v[244:245], 0\n"
"v_mov_b64 v[246:247], 0\n"
"v_mov_b64 v[248:249], 0\n"
"v_mov_b64 v[250:251], 0\n"
"buffer_load_dwordx4 v[172:175], v2, s[24:27], s53 offen offset:1024 sc1\n"
"v_mov_b64 v[252:253], 0\n"
"v_mov_b64 v[254:255], 0\n"
"s_nop 7\n"
"buffer_load_dwordx4 v[176:179], v2, s[24:27], s53 offen offset:2048 sc1\n"
"s_nop 9\n"
"buffer_load_dwordx4 v[180:183], v2, s[24:27], s53 offen offset:3072 sc1\n"
"s_nop 9\n"
"buffer_load_dwordx4 v[184:187], v2, s[24:27], s54 offen offset:0 sc1\n"
"s_nop 9\n"
"buffer_load_dwordx4 v[188:191], v2, s[24:27], s54 offen offset:1024 sc1\n"
"s_nop 9\n"
"buffer_load_dwordx4 v[192:195], v2, s[24:27], s54 offen offset:2048 sc1\n"
"s_nop 9\n"
"buffer_load_dwordx4 v[196:199], v2, s[24:27], s54 offen offset:3072 sc1\n"
"s_waitcnt vmcnt(7)\n"
"buffer_load_dwordx4 v[200:203], v2, s[24:27], s55 offen offset:0 sc1\n"
"LA_QK:\n"
"s_memtime s[78:79]\n"
"v_smfmac_f32_16x16x64_bf16 v[232:235], v[168:171], v[104:111], v1\n"
"v_max3_u32 v204, v168, v169, v170\n"
"v_smfmac_f32_16x16x64_bf16 v[240:243], v[168:171], v[112:119], v1\n"
"v_mov_b32 v205, v171\n"
"v_smfmac_f32_16x16x64_bf16 v[236:239], v[168:171], v[120:127], v1\n"
"v_smfmac_f32_16x16x64_bf16 v[244:247], v[168:171], v[128:135], v1\n"
"s_waitcnt vmcnt(7)\n"
"s_waitcnt lgkmcnt(0)\n"
"v_smfmac_f32_16x16x64_bf16 v[232:235], v[172:175], v[136:143], v1\n"
"v_max3_u32 v204, v204, v172, v173\n"
"s_add_u32 s36, s78, s81\n"
"s_and_b32 s36, s36, 0xffffff\n"
"v_smfmac_f32_16x16x64_bf16 v[240:243], v[172:175], v[144:151], v1\n"
"v_max3_u32 v205, v205, v174, v175\n"
"s_or_b32 s36, s36, s82\n"
"v_smfmac_f32_16x16x64_bf16 v[236:239], v[172:175], v[152:159], v1\n"
"v_max_u32 v12, v204, v205\n"
"v_smfmac_f32_16x16x64_bf16 v[244:247], v[172:175], v[160:167], v1\n"
"v_cmp_eq_u32 vcc, -1, v12\n"
"s_waitcnt vmcnt(6)\n"
"v_smfmac_f32_16x16x64_bf16 v[232:235], v[176:179], a[0:7], v1\n"
"v_max3_u32 v204, v204, v176, v177\n"
"v_mov_b32 v221, s36\n"
"v_smfmac_f32_16x16x64_bf16 v[240:243], v[176:179], a[16:23], v1\n"
"v_max3_u32 v205, v205, v178, v179\n"
"s_cbranch_vccnz LA_fail\n"
"buffer_store_dword v221, v15, s[24:27], s73 offen sc1\n"
"v_smfmac_f32_16x16x64_bf16 v[236:239], v[176:179], a[8:15], v1\n"
"ds_read_b128 v[104:107], v5 offset:16384\n"
"v_smfmac_f32_16x16x64_bf16 v[244:247], v[176:179], a[24:31], v1\n"
"s_waitcnt vmcnt(6)\n"
"v_smfmac_f32_16x16x64_bf16 v[232:235], v[180:183], a[32:39], v1\n"
"ds_read_b128 v[108:111], v5 offset:17408\n"
"v_max3_u32 v204, v204, v180, v181\n"
"v_smfmac_f32_16x16x64_bf16 v[240:243], v[180:183], a[48:55], v1\n"
"ds_read_b128 v[112:115], v5 offset:18432\n"
"v_max3_u32 v205, v205, v182, v183\n"
"v_smfmac_f32_16x16x64_bf16 v[236:239], v[180:183], a[40:47], v1\n"
"ds_read_b128 v[116:119], v5 offset:19456\n"
"v_smfmac_f32_16x16x64_bf16 v[244:247], v[180:183], a[56:63], v1\n"
"s_waitcnt vmcnt(5)\n"
"v_smfmac_f32_16x16x64_bf16 v[232:235], v[184:187], a[64:71], v1\n"
"ds_read_b128 v[120:123], v5 offset:20480\n"
"v_max3_u32 v204, v204, v184, v185\n"
"v_smfmac_f32_16x16x64_bf16 v[240:243], v[184:187], a[80:87], v1\n"
"ds_read_b128 v[124:127], v5 offset:21504\n"
"v_max3_u32 v205, v205, v186, v187\n"
"v_smfmac_f32_16x16x64_bf16 v[236:239], v[184:187], a[72:79], v1\n"
"ds_read_b128 v[128:131], v5 offset:22528\n"
"v_smfmac_f32_16x16x64_bf16 v[244:247], v[184:187], a[88:95], v1\n"
"s_waitcnt vmcnt(4)\n"
"v_smfmac_f32_16x16x64_bf16 v[232:235], v[188:191], a[96:103], v1\n"
"ds_read_b128 v[132:135], v5 offset:23552\n"
"v_max3_u32 v204, v204, v188, v189\n"
"v_smfmac_f32_16x16x64_bf16 v[240:243], v[188:191], a[112:119], v1\n"
"ds_read_b128 v[136:139], v8 offset:9216\n"
"v_max3_u32 v205, v205, v190, v191\n"
"v_smfmac_f32_16x16x64_bf16 v[236:239], v[188:191], a[104:111], v1\n"
"ds_read_b128 v[140:143], v8 offset:10240\n"
"v_smfmac_f32_16x16x64_bf16 v[244:247], v[188:191], a[120:127], v1\n"
"s_waitcnt vmcnt(3)\n"
"v_smfmac_f32_16x16x64_bf16 v[232:235], v[192:195], a[128:135], v1\n"
"ds_read_b128 v[144:147], v8 offset:11264\n"
"v_max3_u32 v204, v204, v192, v193\n"
"v_smfmac_f32_16x16x64_bf16 v[240:243], v[192:195], a[144:151], v1\n"
"ds_read_b128 v[148:151], v8 offset:12288\n"
"v_max3_u32 v205, v205, v194, v195\n"
"v_smfmac_f32_16x16x64_bf16 v[236:239], v[192:195], a[136:143], v1\n"
"ds_read_b128 v[152:155], v8 offset:13312\n"
"v_smfmac_f32_16x16x64_bf16 v[244:247], v[192:195], a[152:159], v1\n"
"s_waitcnt vmcnt(2)\n"
"v_smfmac_f32_16x16x64_bf16 v[232:235], v[196:199], a[160:167], v1\n"
"ds_read_b128 v[156:159], v8 offset:14336\n"
"v_max3_u32 v204, v204, v196, v197\n"
"v_smfmac_f32_16x16x64_bf16 v[240:243], v[196:199], a[176:183], v1\n"
"ds_read_b128 v[160:163], v8 offset:15360\n"
"v_max3_u32 v205, v205, v198, v199\n"
"v_smfmac_f32_16x16x64_bf16 v[236:239], v[196:199], a[168:175], v1\n"
"ds_read_b128 v[164:167], v8 offset:16384\n"
"v_smfmac_f32_16x16x64_bf16 v[244:247], v[196:199], a[184:191], v1\n"
"s_waitcnt vmcnt(1)\n"
"s_waitcnt lgkmcnt(8)\n"
"v_smfmac_f32_16x16x64_bf16 v[232:235], v[200:203], v[104:111], v1\n"
"v_max3_u32 v204, v204, v200, v201\n"
"v_smfmac_f32_16x16x64_bf16 v[240:243], v[200:203], v[112:119], v1\n"
"v_max3_u32 v205, v205, v202, v203\n"
"v_smfmac_f32_16x16x64_bf16 v[236:239], v[200:203], v[120:127], v1\n"
"v_max_u32 v12, v204, v205\n"
"v_smfmac_f32_16x16x64_bf16 v[244:247], v[200:203], v[128:135], v1\n"
"v_cmp_eq_u32 vcc, -1, v12\n"
"v_smfmac_f32_16x16x64_bf16 v[248:251], v[168:171], a[192:199], v1\n"
"v_smfmac_f32_16x16x64_bf16 v[252:255], v[168:171], a[200:207], v1\n"
"s_cbranch_vccnz LA_fail\n"
"v_add_f32 v240, v240, v241\n"
"v_add_f32 v242, v242, v243\n"
"v_add_f32 v232, v232, v233\n"
"v_add_f32 v234, v234, v235\n"
"v_cvt_pk_bf16_f32 v204, v240, v242\n"
"v_cvt_pk_bf16_f32 v205, v232, v234\n"
"ds_write_b64 v6, v[204:205] offset:17408\n"
"v_add_f32 v244, v244, v245\n"
"v_add_f32 v246, v246, v247\n"
"v_add_f32 v236, v236, v237\n"
"v_add_f32 v238, v238, v239\n"
"v_cvt_pk_bf16_f32 v206, v244, v246\n"
"v_cvt_pk_bf16_f32 v207, v236, v238\n"
"ds_write_b64 v6, v[206:207] offset:17920\n"
"v_smfmac_f32_16x16x64_bf16 v[248:251], v[172:175], a[208:215], v1\n"
"v_smfmac_f32_16x16x64_bf16 v[252:255], v[172:175], a[216:223], v1\n"
"v_smfmac_f32_16x16x64_bf16 v[248:251], v[176:179], a[224:231], v1\n"
"v_smfmac_f32_16x16x64_bf16 v[252:255], v[176:179], a[232:239], v1\n"
"v_smfmac_f32_16x16x64_bf16 v[248:251], v[180:183], a[240:247], v1\n"
"v_smfmac_f32_16x16x64_bf16 v[252:255], v[180:183], a[248:255], v1\n"
"s_waitcnt lgkmcnt(0)\n"
"s_barrier\n"
"ds_read_b64_tr_b16 v[104:105], v7 offset:17408\n"
"v_smfmac_f32_16x16x64_bf16 v[248:251], v[184:187], v[24:31], v1\n"
"s_cmp_lg_u32 s16, 0\n"
"s_cbranch_scc1 LA_ann\n"
"LA_annr:\n"
"ds_read_b64_tr_b16 v[106:107], v7 offset:17920\n"
"v_smfmac_f32_16x16x64_bf16 v[252:255], v[184:187], v[32:39], v1\n"
"ds_read_b64_tr_b16 v[108:109], v7 offset:18432\n"
"v_smfmac_f32_16x16x64_bf16 v[248:251], v[188:191], v[40:47], v1\n"
"ds_read_b64_tr_b16 v[110:111], v7 offset:18944\n"
"v_smfmac_f32_16x16x64_bf16 v[252:255], v[188:191], v[48:55], v1\n"
"ds_read_b64_tr_b16 v[112:113], v7 offset:19456\n"
"v_smfmac_f32_16x16x64_bf16 v[248:251], v[192:195], v[56:63], v1\n"
"ds_read_b64_tr_b16 v[114:115], v7 offset:19968\n"
"v_smfmac_f32_16x16x64_bf16 v[252:255], v[192:195], v[64:71], v1\n"
"ds_read_b64_tr_b16 v[116:117], v7 offset:20480\n"
"v_smfmac_f32_16x16x64_bf16 v[248:251], v[196:199], v[72:79], v1\n"
"ds_read_b64_tr_b16 v[118:119], v7 offset:20992\n"
"v_smfmac_f32_16x16x64_bf16 v[252:255], v[196:199], v[80:87], v1\n"
"s_waitcnt lgkmcnt(6)\n"
"v_mfma_f32_16x16x32_bf16 v[128:131], v[104:107], v[104:107], 0\n"
"buffer_store_dwordx4 v[104:107], v10, s[24:27], s59 offen offset:0 sc1\n"
"v_mfma_f32_16x16x32_bf16 v[120:123], v[136:139], v[104:107], 0\n"
"v_mfma_f32_16x16x32_bf16 v[124:127], v[152:155], v[104:107], 0\n"
"s_waitcnt lgkmcnt(4)\n"
"v_mfma_f32_16x16x32_bf16 v[128:131], v[108:111], v[108:111], v[128:131]\n"
"buffer_store_dwordx4 v[108:111], v10, s[24:27], s59 offen offset:512 sc1\n"
"v_mfma_f32_16x16x32_bf16 v[120:123], v[140:143], v[108:111], v[120:123]\n"
"v_mfma_f32_16x16x32_bf16 v[124:127], v[156:159], v[108:111], v[124:127]\n"
"s_waitcnt lgkmcnt(2)\n"
"v_mfma_f32_16x16x32_bf16 v[128:131], v[112:115], v[112:115], v[128:131]\n"
"buffer_store_dwordx4 v[112:115], v10, s[24:27], s59 offen offset:1024 sc1\n"
"v_mfma_f32_16x16x32_bf16 v[120:123], v[144:147], v[112:115], v[120:123]\n"
"v_mfma_f32_16x16x32_bf16 v[124:127], v[160:163], v[112:115], v[124:127]\n"
"s_waitcnt lgkmcnt(0)\n"
"v_mfma_f32_16x16x32_bf16 v[128:131], v[116:119], v[116:119], v[128:131]\n"
"buffer_store_dwordx4 v[116:119], v10, s[24:27], s59 offen offset:1536 sc1\n"
"v_mfma_f32_16x16x32_bf16 v[120:123], v[148:151], v[116:119], v[120:123]\n"
"v_mfma_f32_16x16x32_bf16 v[124:127], v[164:167], v[116:119], v[124:127]\n"
"v_smfmac_f32_16x16x64_bf16 v[248:251], v[200:203], v[88:95], v1\n"
"v_smfmac_f32_16x16x64_bf16 v[252:255], v[200:203], v[96:103], v1\n"
"s_nop 4\n"
"v_max3_f32 v12, v128, v129, v120\n"
"v_max3_f32 v12, v12, v121, v122\n"
"v_max3_f32 v12, v12, v123, v124\n"
"v_max3_f32 v12, v12, v125, v126\n"
"v_max_f32 v12, v12, v127\n"
"v_mov_b32 v13, v12\n"
"v_add_f32 v248, v248, v249\n"
"v_add_f32 v250, v250, v251\n"
"v_permlane32_swap_b32 v12, v13\n"
"v_max_f32 v12, v12, v13\n"
"v_mov_b32 v13, v12\n"
"v_add_f32 v252, v252, v253\n"
"v_add_f32 v254, v254, v255\n"
"v_permlane16_swap_b32 v12, v13\n"
"v_max_f32 v12, v12, v13\n"
"v_mul_f32 v12, s70, v12\n"
"v_pk_fma_f32 v[128:129], v[128:129], s[70:71], v[12:13] op_sel_hi:[1,1,0] neg_lo:[0,0,1] neg_hi:[0,0,1]\n"
"v_pk_fma_f32 v[120:121], v[120:121], s[70:71], v[12:13] op_sel_hi:[1,1,0] neg_lo:[0,0,1] neg_hi:[0,0,1]\n"
"v_pk_fma_f32 v[122:123], v[122:123], s[70:71], v[12:13] op_sel_hi:[1,1,0] neg_lo:[0,0,1] neg_hi:[0,0,1]\n"
"v_pk_fma_f32 v[124:125], v[124:125], s[70:71], v[12:13] op_sel_hi:[1,1,0] neg_lo:[0,0,1] neg_hi:[0,0,1]\n"
"v_pk_fma_f32 v[126:127], v[126:127], s[70:71], v[12:13] op_sel_hi:[1,1,0] neg_lo:[0,0,1] neg_hi:[0,0,1]\n"
"v_exp_f32 v128, v128\n"
"v_exp_f32 v129, v129\n"
"v_cvt_pk_bf16_f32 v212, v248, v250\n"
"v_cvt_pk_bf16_f32 v208, v128, v129\n"
"v_cvt_pk_bf16_f32 v211, v128, v129\n"
"v_cvt_pk_bf16_f32 v213, v252, v254\n"
"v_exp_f32 v120, v120\n"
"v_exp_f32 v121, v121\n"
"v_mfma_f32_16x16x16_bf16 v[218:221], v[214:215], v[208:209], 0\n"
"v_exp_f32 v122, v122\n"
"v_mfma_f32_16x16x16_bf16 v[222:225], v[212:213], v[208:209], 0\n"
"v_exp_f32 v123, v123\n"
"v_mfma_f32_16x16x16_bf16 v[226:229], v[212:213], v[210:211], 0\n"
"v_exp_f32 v124, v124\n"
"v_exp_f32 v125, v125\n"
"v_exp_f32 v126, v126\n"
"v_exp_f32 v127, v127\n"
"v_cvt_pk_bf16_f32 v204, v120, v121\n"
"v_cvt_pk_bf16_f32 v205, v122, v123\n"
"v_cvt_pk_bf16_f32 v206, v124, v125\n"
"v_cvt_pk_bf16_f32 v207, v126, v127\n"
"s_nop 1\n"
"v_mfma_f32_16x16x32_bf16 v[218:221], v[214:217], v[204:207], v[218:221]\n"
"v_mfma_f32_16x16x32_bf16 v[222:225], v[16:19], v[204:207], v[222:225]\n"
"v_mfma_f32_16x16x32_bf16 v[226:229], v[20:23], v[204:207], v[226:229]\n"
"s_nop 5\n"
"v_rcp_f32 v12, v218\n"
"s_nop 0\n"
"v_pk_mul_f32 v[222:223], v[222:223], v[12:13] op_sel_hi:[1,0]\n"
"v_pk_mul_f32 v[224:225], v[224:225], v[12:13] op_sel_hi:[1,0]\n"
"v_pk_mul_f32 v[226:227], v[226:227], v[12:13] op_sel_hi:[1,0]\n"
"v_pk_mul_f32 v[228:229], v[228:229], v[12:13] op_sel_hi:[1,0]\n"
"v_cvt_pk_bf16_f32 v204, v222, v223\n"
"v_cvt_pk_bf16_f32 v205, v224, v225\n"
"v_cvt_pk_bf16_f32 v206, v226, v227\n"
"v_cvt_pk_bf16_f32 v207, v228, v229\n"
"buffer_store_dwordx4 v[204:207], v9, s[24:27], s67 offen sc1\n"
"s_memtime s[48:49]\n"
"s_add_u32 s36, s67, 393216\n"
"buffer_store_dwordx4 v[204:207], v9, s[24:27], s36 offen sc1\n"
"buffer_store_dwordx2 v[212:213], v11, s[24:27], s59 offen sc1\n"
"s_waitcnt lgkmcnt(0)\n"
"s_mov_b32 s66, s48\n"
"s_sub_u32 s36, s66, s78\n"
"s_sub_i32 s36, s36, s76\n"
"s_ashr_i32 s36, s36, 1\n"
"s_max_i32 s36, s36, -100\n"
"s_min_i32 s36, s36, 60\n"
"s_cmp_eq_u32 s16, 0\n"
"s_cselect_b32 s36, s36, 0\n"
"s_mov_b32 s16, 0\n"
"s_add_i32 s76, s76, s36\n"
"s_add_u32 s21, s21, 1\n"
"s_branch LA_loop\n"
"LA_fail:\n"
"s_add_u32 s63, s63, 1\n"
"s_add_u32 s65, s65, 0\n"
"s_min_u32 s65, s65, 900\n"
"s_waitcnt lgkmcnt(0)\n"
"s_mov_b32 s73, 2146435072\n"
"s_mov_b32 s16, 1\n"
"s_add_u32 s36, s53, 32768\n"
"s_add_u32 s37, s54, 32768\n"
"s_add_u32 s38, s55, 32768\n"
"buffer_load_dwordx4 v[168:171], v2, s[24:27], s36 offen offset:0 sc1\n"
"buffer_load_dwordx4 v[172:175], v2, s[24:27], s36 offen offset:1024 sc1\n"
"buffer_load_dwordx4 v[176:179], v2, s[24:27], s36 offen offset:2048 sc1\n"
"buffer_load_dwordx4 v[180:183], v2, s[24:27], s36 offen offset:3072 sc1\n"
"buffer_load_dwordx4 v[184:187], v2, s[24:27], s37 offen offset:0 sc1\n"
"buffer_load_dwordx4 v[188:191], v2, s[24:27], s37 offen offset:1024 sc1\n"
"buffer_load_dwordx4 v[192:195], v2, s[24:27], s37 offen offset:2048 sc1\n"
"buffer_load_dwordx4 v[196:199], v2, s[24:27], s37 offen offset:3072 sc1\n"
"buffer_load_dwordx4 v[200:203], v2, s[24:27], s38 offen offset:0 sc1\n"
"v_mov_b32 v12, 0\n"
"buffer_store_dword v12, v15, s[24:27], s77 offen sc1\n"
"ds_read_b128 v[104:107], v5 offset:0\n"
"ds_read_b128 v[108:111], v5 offset:1024\n"
"ds_read_b128 v[112:115], v5 offset:2048\n"
"ds_read_b128 v[116:119], v5 offset:3072\n"
"ds_read_b128 v[120:123], v5 offset:4096\n"
"ds_read_b128 v[124:127], v5 offset:5120\n"
"ds_read_b128 v[128:131], v5 offset:6144\n"
"ds_read_b128 v[132:135], v5 offset:7168\n"
"s_waitcnt lgkmcnt(0)\n"
"ds_read_b128 v[136:139], v5 offset:8192\n"
"ds_read_b128 v[140:143], v5 offset:9216\n"
"ds_read_b128 v[144:147], v5 offset:10240\n"
"ds_read_b128 v[148:151], v5 offset:11264\n"
"ds_read_b128 v[152:155], v5 offset:12288\n"
"ds_read_b128 v[156:159], v5 offset:13312\n"
"ds_read_b128 v[160:163], v5 offset:14336\n"
"ds_read_b128 v[164:167], v5 offset:15360\n"
"s_waitcnt vmcnt(1)\n"
"v_max3_u32 v204, v168, v169, v170\n"
"v_mov_b32 v205, v171\n"
"v_max3_u32 v204, v204, v172, v173\n"
"v_max3_u32 v205, v205, v174, v175\n"
"v_max3_u32 v204, v204, v176, v177\n"
"v_max3_u32 v205, v205, v178, v179\n"
"v_max3_u32 v204, v204, v180, v181\n"
"v_max3_u32 v205, v205, v182, v183\n"
"v_max3_u32 v204, v204, v184, v185\n"
"v_max3_u32 v205, v205, v186, v187\n"
"v_max3_u32 v204, v204, v188, v189\n"
"v_max3_u32 v205, v205, v190, v191\n"
"v_max3_u32 v204, v204, v192, v193\n"
"v_max3_u32 v205, v205, v194, v195\n"
"v_max3_u32 v204, v204, v196, v197\n"
"v_max3_u32 v205, v205, v198, v199\n"
"v_max3_u32 v204, v204, v200, v201\n"
"v_max3_u32 v205, v205, v202, v203\n"
"v_max_u32 v204, v204, v205\n"
"v_cmp_eq_u32 vcc, -1, v204\n"
"s_waitcnt lgkmcnt(0)\n"
"s_cbranch_vccz LA_redo\n"
"Lst_20:\n"
"buffer_load_dword v218, v4, s[24:27], s64 offen sc1\n"
"s_waitcnt vmcnt(0)\n"
"Lst_22:\n"
"v_lshrrev_b32 v13, 24, v218\n"
"v_cmp_ne_u32 vcc, s21, v13\n"
"s_cbranch_vccz Lst_21\n"
"s_sleep 2\n"
"s_memrealtime s[48:49]\n"
"s_waitcnt lgkmcnt(0)\n"
"s_sub_u32 s48, s48, s23\n"
"s_cmp_gt_i32 s48, 0\n"
"s_cbranch_scc1 LA_end\n"
"s_branch Lst_20\n"
"Lst_21:\n"
"v_sub_u32 v218, v218, v223\n"
"v_bfe_i32 v218, v218, 0, 24\n"
"s_nop 1\n"
"v_max_i32_dpp v218, v218, v218 row_shr:1 row_mask:0xf bank_mask:0xf\n"
"s_nop 1\n"
"v_max_i32_dpp v218, v218, v218 row_shr:2 row_mask:0xf bank_mask:0xf\n"
"s_nop 1\n"
"v_max_i32_dpp v218, v218, v218 row_shr:4 row_mask:0xf bank_mask:0xf\n"
"s_nop 1\n"
"v_max_i32_dpp v218, v218, v218 row_shr:8 row_mask:0xf bank_mask:0xf\n"
"s_nop 0\n"
"v_readlane_b32 s36, v218, 15\n"
"v_readlane_b32 s37, v218, 31\n"
"v_readlane_b32 s38, v218, 47\n"
"s_max_i32 s36, s36, s38\n"
"s_max_i32 s36, s36, s37\n"
"s_max_i32 s36, s36, 0\n"
"s_min_i32 s36, s36, 20000\n"
"s_add_u32 s36, s36, s61\n"
"s_add_u32 s36, s36, 100\n"
"Lwu_23:\n"
"s_memtime s[48:49]\n"
"s_waitcnt lgkmcnt(0)\n"
"s_sub_u32 s44, s36, s48\n"
"s_cmp_lt_i32 s44, 384\n"
"s_cbranch_scc1 Lwu_24\n"
"s_cmp_lt_i32 s44, 534\n"
"s_cbranch_scc1 Lwu_23\n"
"s_cmp_gt_i32 s44, 100000\n"
"s_cbranch_scc1 Lwu_25\n"
"s_sleep 1\n"
"s_branch Lwu_23\n"
"Lwu_24:\n"
"s_sub_i32 s44, s44, 60\n"
"s_cmp_lt_i32 s44, 4\n"
"s_cbranch_scc1 Lwu_25\n"
"s_and_b32 s44, s44, 0xfffffffc\n"
"s_sub_u32 s44, s50, s44\n"
"s_subb_u32 s45, s51, 0\n"
"s_swappc_b64 s[46:47], s[44:45]\n"
"Lwu_25:\n"
"LA_poll:\n"
"s_add_u32 s36, s53, 65536\n"
"s_add_u32 s37, s54, 65536\n"
"s_add_u32 s38, s55, 65536\n"
"buffer_load_dwordx4 v[168:171], v2, s[24:27], s36 offen offset:0 sc1\n"
"buffer_load_dwordx4 v[172:175], v2, s[24:27], s36 offen offset:1024 sc1\n"
"buffer_load_dwordx4 v[176:179], v2, s[24:27], s36 offen offset:2048 sc1\n"
"buffer_load_dwordx4 v[180:183], v2, s[24:27], s36 offen offset:3072 sc1\n"
"buffer_load_dwordx4 v[184:187], v2, s[24:27], s37 offen offset:0 sc1\n"
"buffer_load_dwordx4 v[188:191], v2, s[24:27], s37 offen offset:1024 sc1\n"
"buffer_load_dwordx4 v[192:195], v2, s[24:27], s37 offen offset:2048 sc1\n"
"buffer_load_dwordx4 v[196:199], v2, s[24:27], s37 offen offset:3072 sc1\n"
"buffer_load_dwordx4 v[200:203], v2, s[24:27], s38 offen offset:0 sc1\n"
"s_waitcnt vmcnt(0)\n"
"v_max3_u32 v204, v168, v169, v170\n"
"v_mov_b32 v205, v171\n"
"v_max3_u32 v204, v204, v172, v173\n"
"v_max3_u32 v205, v205, v174, v175\n"
"v_max3_u32 v204, v204, v176, v177\n"
"v_max3_u32 v205, v205, v178, v179\n"
"v_max3_u32 v204, v204, v180, v181\n"
"v_max3_u32 v205, v205, v182, v183\n"
"v_max3_u32 v204, v204, v184, v185\n"
"v_max3_u32 v205, v205, v186, v187\n"
"v_max3_u32 v204, v204, v188, v189\n"
"v_max3_u32 v205, v205, v190, v191\n"
"v_max3_u32 v204, v204, v192, v193\n"
"v_max3_u32 v205, v205, v194, v195\n"
"v_max3_u32 v204, v204, v196, v197\n"
"v_max3_u32 v205, v205, v198, v199\n"
"v_max3_u32 v204, v204, v200, v201\n"
"v_max3_u32 v205, v205, v202, v203\n"
"v_max_u32 v204, v204, v205\n"
"v_cmp_eq_u32 vcc, -1, v204\n"
"s_waitcnt lgkmcnt(0)\n"
"s_cbranch_vccz LA_redo\n"
"s_sleep 2\n"
"s_memrealtime s[48:49]\n"
"s_waitcnt lgkmcnt(0)\n"
"s_sub_u32 s48, s48, s23\n"
"s_cmp_gt_i32 s48, 0\n"
"s_cbranch_scc1 LA_end\n"
"s_branch LA_poll\n"
"LA_last:\n"
"ds_read_b64_tr_b16 v[106:107], v7 offset:17920\n"
"ds_read_b64_tr_b16 v[108:109], v7 offset:18432\n"
"ds_read_b64_tr_b16 v[110:111], v7 offset:18944\n"
"ds_read_b64_tr_b16 v[112:113], v7 offset:19456\n"
"ds_read_b64_tr_b16 v[114:115], v7 offset:19968\n"
"ds_read_b64_tr_b16 v[116:117], v7 offset:20480\n"
"ds_read_b64_tr_b16 v[118:119], v7 offset:20992\n"
"v_smfmac_f32_16x16x64_bf16 v[252:255], v[184:187], v[32:39], v1\n"
"v_smfmac_f32_16x16x64_bf16 v[248:251], v[188:191], v[40:47], v1\n"
"v_smfmac_f32_16x16x64_bf16 v[252:255], v[188:191], v[48:55], v1\n"
"v_smfmac_f32_16x16x64_bf16 v[248:251], v[192:195], v[56:63], v1\n"
"v_smfmac_f32_16x16x64_bf16 v[252:255], v[192:195], v[64:71], v1\n"
"v_smfmac_f32_16x16x64_bf16 v[248:251], v[196:199], v[72:79], v1\n"
"v_smfmac_f32_16x16x64_bf16 v[252:255], v[196:199], v[80:87], v1\n"
"v_smfmac_f32_16x16x64_bf16 v[248:251], v[200:203], v[88:95], v1\n"
"v_smfmac_f32_16x16x64_bf16 v[252:255], v[200:203], v[96:103], v1\n"
"s_waitcnt lgkmcnt(0)\n"
"buffer_store_dwordx4 v[104:107], v10, s[24:27], s59 offen offset:0 sc1\n"
"buffer_store_dwordx4 v[108:111], v10, s[24:27], s59 offen offset:512 sc1\n"
"buffer_store_dwordx4 v[112:115], v10, s[24:27], s59 offen offset:1024 sc1\n"
"buffer_store_dwordx4 v[116:119], v10, s[24:27], s59 offen offset:1536 sc1\n"
"s_nop 1\n"
"v_add_f32 v248, v248, v249\n"
"v_add_f32 v250, v250, v251\n"
"v_cvt_pk_bf16_f32 v212, v248, v250\n"
"v_add_f32 v252, v252, v253\n"
"v_add_f32 v254, v254, v255\n"
"v_cvt_pk_bf16_f32 v213, v252, v254\n"
"buffer_store_dwordx2 v[212:213], v11, s[24:27], s59 offen sc1\n"
"s_mov_b32 s21, 64\n"
"s_branch LA_fin\n"
"LA_ann:\n"
"s_cmp_eq_u32 s21, 63\n"
"s_cbranch_scc1 LA_last\n"
"s_memtime s[48:49]\n"
"s_waitcnt lgkmcnt(0)\n"
"s_sub_u32 s78, s48, 700\n"
"s_add_u32 s36, s78, s81\n"
"s_and_b32 s36, s36, 0xffffff\n"
"s_or_b32 s36, s36, s82\n"
"v_mov_b32 v221, s36\n"
"buffer_store_dword v221, v15, s[24:27], s77 offen sc1\n"
"s_branch LA_annr\n"
"LA_first:\n"
"v_bfe_u32 v12, v0, 1, 3\n"
"v_mul_u32_u24 v12, 1152, v12\n"
"v_lshrrev_b32 v13, 4, v0\n"
"v_lshl_add_u32 v12, v13, 5, v12\n"
"v_and_b32 v13, 1, v0\n"
"v_lshl_add_u32 v12, v13, 4, v12\n"
"buffer_load_dwordx4 v[168:171], v12, s[32:35], 0 offen offset:0\n"
"buffer_load_dwordx4 v[172:175], v12, s[32:35], 0 offen offset:128\n"
"buffer_load_dwordx4 v[176:179], v12, s[32:35], 0 offen offset:256\n"
"buffer_load_dwordx4 v[180:183], v12, s[32:35], 0 offen offset:384\n"
"buffer_load_dwordx4 v[184:187], v12, s[32:35], 0 offen offset:512\n"
"buffer_load_dwordx4 v[188:191], v12, s[32:35], 0 offen offset:640\n"
"buffer_load_dwordx4 v[192:195], v12, s[32:35], 0 offen offset:768\n"
"buffer_load_dwordx4 v[196:199], v12, s[32:35], 0 offen offset:896\n"
"buffer_load_dwordx4 v[200:203], v12, s[32:35], 0 offen offset:1024\n"
"s_waitcnt vmcnt(0)\n"
"LA_redo:\n"
"v_mov_b64 v[232:233], 0\n"
"v_mov_b64 v[234:235], 0\n"
"v_mov_b64 v[236:237], 0\n"
"v_mov_b64 v[238:239], 0\n"
"v_mov_b64 v[240:241], 0\n"
"v_mov_b64 v[242:243], 0\n"
"v_mov_b64 v[244:245], 0\n"
"v_mov_b64 v[246:247], 0\n"
"v_mov_b64 v[248:249], 0\n"
"v_mov_b64 v[250:251], 0\n"
"v_mov_b64 v[252:253], 0\n"
"v_mov_b64 v[254:255], 0\n"
"s_branch LA_QK\n"
"LA_fin:\n"
"v_mov_b32 v222, s21\n"
"s_waitcnt vmcnt(0)\n"
"s_barrier\n"
"buffer_store_dword v222, v15, s[24:27], s72 offen sc1\n"
"LA_end:\n"
"s_endpgm\n"
"LB:\n"
"s_and_b32 s61, s19, 1\n"
"s_lshr_b32 s62, s19, 1\n"
"s_mov_b32 s54, 0xffff0000\n"
"s_mov_b32 s55, 0xffff0000\n"
"v_and_b32 v6, 1, v0\n"
"v_mov_b32 v1, 0x4444\n"
"v_mov_b32 v7, 0xeeee\n"
"v_cmp_eq_u32 vcc, 1, v6\n"
"s_nop 1\n"
"v_cndmask_b32 v1, v1, v7, vcc\n"
"v_lshlrev_b32 v4, 3, v0\n"
"v_mov_b32 v8, -1\n"
"v_lshlrev_b32 v2, 4, v0\n"
"s_lshl_b32 s36, s61, 10\n"
"s_add_u32 s36, s36, s22\n"
"s_lshl_b32 s37, s20, 15\n"
"s_add_u32 s36, s36, s37\n"
"v_add_u32 v2, s36, v2\n"
"v_lshrrev_b32 v7, 4, v0\n"
"v_mul_u32_u24 v3, 128, v7\n"
"v_bfe_u32 v7, v0, 2, 2\n"
"v_lshl_add_u32 v3, v7, 5, v3\n"
"v_and_b32 v7, 3, v0\n"
"v_lshl_add_u32 v3, v7, 2, v3\n"
"v_lshlrev_b32 v4, 3, v7\n"
"s_mul_i32 s36, s20, 512\n"
"v_add_u32 v3, s36, v3\n"
"v_lshrrev_b32 v7, 5, v0\n"
"v_mul_u32_u24 v7, 256, v7\n"
"v_add_u32 v4, v4, v7\n"
"v_bfe_u32 v7, v0, 3, 1\n"
"v_mul_u32_u24 v7, 128, v7\n"
"v_add_u32 v4, v4, v7\n"
"v_bfe_u32 v7, v0, 4, 1\n"
"v_lshl_add_u32 v4, v7, 6, v4\n"
"v_bfe_u32 v7, v0, 2, 1\n"
"v_lshl_add_u32 v4, v7, 5, v4\n"
"s_and_b32 s36, s19, 31\n"
"s_lshl_b32 s36, s36, 8\n"
"s_add_u32 s36, s36, s22\n"
"v_lshlrev_b32 v5, 2, v0\n"
"v_add_u32 v5, s36, v5\n"
"s_mov_b32 s56, 125\n"
"s_mov_b32 s18, 1600\n"
"s_mov_b32 s60, 0\n"
"v_and_b32 v9, 31, v0\n"
"v_lshlrev_b32 v9, 7, v9\n"
"v_mov_b32 v10, 2146435072\n"
"v_cmp_eq_u32 vcc, 0, v0\n"
"s_nop 1\n"
"v_cndmask_b32 v10, v10, 0, vcc\n"
"v_mov_b32 v11, 3584\n"
"Lce_26:\n"
"s_memtime s[48:49]\n"
"s_memrealtime s[36:37]\n"
"s_nop 3\n"
"s_memrealtime s[38:39]\n"
"s_nop 3\n"
"s_memrealtime s[40:41]\n"
"s_nop 3\n"
"s_memrealtime s[42:43]\n"
"s_nop 3\n"
"s_memrealtime s[44:45]\n"
"s_nop 3\n"
"s_memrealtime s[46:47]\n"
"s_waitcnt lgkmcnt(0)\n"
"s_cmp_eq_u32 s46, s36\n"
"s_cbranch_scc1 Lce_26\n"
"s_cmp_eq_u32 s38, s36\n"
"s_cselect_b32 s37, 20, 0\n"
"s_add_u32 s48, s48, s37\n"
"s_cmp_eq_u32 s40, s36\n"
"s_cselect_b32 s37, 20, 0\n"
"s_add_u32 s48, s48, s37\n"
"s_cmp_eq_u32 s42, s36\n"
"s_cselect_b32 s37, 20, 0\n"
"s_add_u32 s48, s48, s37\n"
"s_cmp_eq_u32 s44, s36\n"
"s_cselect_b32 s37, 20, 0\n"
"s_add_u32 s48, s48, s37\n"
"s_mul_i32 s40, s46, 24\n"
"s_sub_u32 s40, s40, s48\n"
"s_mov_b32 s76, s40\n"
"s_mov_b32 s79, s48\n"
"s_lshl_b32 s36, s20, 12\n"
"s_add_u32 s36, s36, 4096\n"
"v_lshlrev_b32 v65, 4, v0\n"
"v_add_u32 v65, s36, v65\n"
"v_lshrrev_b32 v64, 5, v0\n"
"v_lshlrev_b32 v66, 10, v64\n"
"v_bfe_u32 v64, v0, 3, 1\n"
"v_lshl_add_u32 v66, v64, 9, v66\n"
"v_bfe_u32 v64, v0, 4, 1\n"
"v_lshl_add_u32 v66, v64, 8, v66\n"
"v_bfe_u32 v64, v0, 2, 1\n"
"v_lshl_add_u32 v66, v64, 6, v66\n"
"v_and_b32 v64, 3, v0\n"
"v_lshl_add_u32 v66, v64, 4, v66\n"
"v_add_u32 v66, s36, v66\n"
"v_lshrrev_b32 v64, 4, v0\n"
"v_lshlrev_b32 v67, 9, v64\n"
"v_bfe_u32 v64, v0, 2, 2\n"
"v_lshl_add_u32 v67, v64, 6, v67\n"
"v_and_b32 v64, 3, v0\n"
"v_lshl_add_u32 v67, v64, 4, v67\n"
"v_add_u32 v67, s36, v67\n"
"v_lshrrev_b32 v69, 2, v0\n"
"v_mul_u32_u24 v68, 1152, v69\n"
"v_and_b32 v69, 3, v0\n"
"v_lshl_add_u32 v68, v69, 4, v68\n"
"s_load_dwordx2 s[28:29], s[16:17], 40\n"
"s_mov_b32 s30, 4718592\n"
"s_mov_b32 s31, 0x00020000\n"
"s_waitcnt lgkmcnt(0)\n"
"s_and_b32 s29, s29, 0xffff\n"
"s_mul_i32 s80, s20, 1179648\n"
"s_lshl_b32 s36, s62, 6\n"
"s_add_u32 s80, s80, s36\n"
"buffer_load_dwordx4 a[0:3], v68, s[28:31], s80 offen nt\n"
"s_add_u32 s80, s80, 36864\n"
"buffer_load_dwordx4 a[4:7], v68, s[28:31], s80 offen nt\n"
"s_add_u32 s80, s80, 36864\n"
"buffer_load_dwordx4 a[8:11], v68, s[28:31], s80 offen nt\n"
"s_add_u32 s80, s80, 36864\n"
"buffer_load_dwordx4 a[12:15], v68, s[28:31], s80 offen nt\n"
"s_sub_u32 s80, s80, 92160\n"
"buffer_load_dwordx4 a[16:19], v68, s[28:31], s80 offen nt\n"
"s_add_u32 s80, s80, 36864\n"
"buffer_load_dwordx4 a[20:23], v68, s[28:31], s80 offen nt\n"
"s_add_u32 s80, s80, 36864\n"
"buffer_load_dwordx4 a[24:27], v68, s[28:31], s80 offen nt\n"
"s_add_u32 s80, s80, 36864\n"
"buffer_load_dwordx4 a[28:31], v68, s[28:31], s80 offen nt\n"
"s_add_u32 s80, s80, 18432\n"
"buffer_load_dwordx4 a[32:35], v68, s[28:31], s80 offen nt\n"
"s_add_u32 s80, s80, 36864\n"
"buffer_load_dwordx4 a[36:39], v68, s[28:31], s80 offen nt\n"
"s_add_u32 s80, s80, 36864\n"
"buffer_load_dwordx4 a[40:43], v68, s[28:31], s80 offen nt\n"
"s_add_u32 s80, s80, 36864\n"
"buffer_load_dwordx4 a[44:47], v68, s[28:31], s80 offen nt\n"
"s_sub_u32 s80, s80, 92160\n"
"buffer_load_dwordx4 a[48:51], v68, s[28:31], s80 offen nt\n"
"s_add_u32 s80, s80, 36864\n"
"buffer_load_dwordx4 a[52:55], v68, s[28:31], s80 offen nt\n"
"s_add_u32 s80, s80, 36864\n"
"buffer_load_dwordx4 a[56:59], v68, s[28:31], s80 offen nt\n"
"s_add_u32 s80, s80, 36864\n"
"buffer_load_dwordx4 a[60:63], v68, s[28:31], s80 offen nt\n"
"s_add_u32 s80, s80, 18432\n"
"buffer_load_dwordx4 a[64:67], v68, s[28:31], s80 offen nt\n"
"s_add_u32 s80, s80, 36864\n"
"buffer_load_dwordx4 a[68:71], v68, s[28:31], s80 offen nt\n"
"s_add_u32 s80, s80, 36864\n"
"buffer_load_dwordx4 a[72:75], v68, s[28:31], s80 offen nt\n"
"s_add_u32 s80, s80, 36864\n"
"buffer_load_dwordx4 a[76:79], v68, s[28:31], s80 offen nt\n"
"s_sub_u32 s80, s80, 92160\n"
"buffer_load_dwordx4 a[80:83], v68, s[28:31], s80 offen nt\n"
"s_add_u32 s80, s80, 36864\n"
"buffer_load_dwordx4 a[84:87], v68, s[28:31], s80 offen nt\n"
"s_add_u32 s80, s80, 36864\n"
"buffer_load_dwordx4 a[88:91], v68, s[28:31], s80 offen nt\n"
"s_add_u32 s80, s80, 36864\n"
"buffer_load_dwordx4 a[92:95], v68, s[28:31], s80 offen nt\n"
"s_add_u32 s80, s80, 18432\n"
"s_waitcnt vmcnt(20)\n"
"ds_write_b128 v65, a[0:3] offset:0\n"
"ds_write_b128 v65, a[4:7] offset:1024\n"
"ds_write_b128 v65, a[8:11] offset:2048\n"
"ds_write_b128 v65, a[12:15] offset:3072\n"
"ds_read_b64_tr_b16 a[0:1], v66 offset:0\n"
"ds_read_b64_tr_b16 a[2:3], v66 offset:128\n"
"ds_read_b64_tr_b16 a[4:5], v66 offset:2048\n"
"ds_read_b64_tr_b16 a[6:7], v66 offset:2176\n"
"ds_read_b64_tr_b16 a[8:9], v66 offset:8\n"
"ds_read_b64_tr_b16 a[10:11], v66 offset:136\n"
"ds_read_b64_tr_b16 a[12:13], v66 offset:2056\n"
"ds_read_b64_tr_b16 a[14:15], v66 offset:2184\n"
"buffer_load_dwordx4 a[96:99], v68, s[28:31], s80 offen nt\n"
"s_add_u32 s80, s80, 36864\n"
"buffer_load_dwordx4 a[100:103], v68, s[28:31], s80 offen nt\n"
"s_add_u32 s80, s80, 36864\n"
"buffer_load_dwordx4 a[104:107], v68, s[28:31], s80 offen nt\n"
"s_add_u32 s80, s80, 36864\n"
"buffer_load_dwordx4 a[108:111], v68, s[28:31], s80 offen nt\n"
"s_sub_u32 s80, s80, 92160\n"
"s_waitcnt vmcnt(20)\n"
"ds_write_b128 v65, a[16:19] offset:0\n"
"ds_write_b128 v65, a[20:23] offset:1024\n"
"ds_write_b128 v65, a[24:27] offset:2048\n"
"ds_write_b128 v65, a[28:31] offset:3072\n"
"ds_read_b64_tr_b16 a[16:17], v66 offset:0\n"
"ds_read_b64_tr_b16 a[18:19], v66 offset:128\n"
"ds_read_b64_tr_b16 a[20:21], v66 offset:2048\n"
"ds_read_b64_tr_b16 a[22:23], v66 offset:2176\n"
"ds_read_b64_tr_b16 a[24:25], v66 offset:8\n"
"ds_read_b64_tr_b16 a[26:27], v66 offset:136\n"
"ds_read_b64_tr_b16 a[28:29], v66 offset:2056\n"
"ds_read_b64_tr_b16 a[30:31], v66 offset:2184\n"
"buffer_load_dwordx4 a[112:115], v68, s[28:31], s80 offen nt\n"
"s_add_u32 s80, s80, 36864\n"
"buffer_load_dwordx4 a[116:119], v68, s[28:31], s80 offen nt\n"
"s_add_u32 s80, s80, 36864\n"
"buffer_load_dwordx4 a[120:123], v68, s[28:31], s80 offen nt\n"
"s_add_u32 s80, s80, 36864\n"
"buffer_load_dwordx4 a[124:127], v68, s[28:31], s80 offen nt\n"
"s_add_u32 s80, s80, 18432\n"
"s_waitcnt vmcnt(20)\n"
"ds_write_b128 v65, a[32:35] offset:0\n"
"ds_write_b128 v65, a[36:39] offset:1024\n"
"ds_write_b128 v65, a[40:43] offset:2048\n"
"ds_write_b128 v65, a[44:47] offset:3072\n"
"ds_read_b64_tr_b16 a[32:33], v66 offset:0\n"
"ds_read_b64_tr_b16 a[34:35], v66 offset:128\n"
"ds_read_b64_tr_b16 a[36:37], v66 offset:2048\n"
"ds_read_b64_tr_b16 a[38:39], v66 offset:2176\n"
"ds_read_b64_tr_b16 a[40:41], v66 offset:8\n"
"ds_read_b64_tr_b16 a[42:43], v66 offset:136\n"
"ds_read_b64_tr_b16 a[44:45], v66 offset:2056\n"
"ds_read_b64_tr_b16 a[46:47], v66 offset:2184\n"
"buffer_load_dwordx4 a[128:131], v68, s[28:31], s80 offen nt\n"
"s_add_u32 s80, s80, 36864\n"
"buffer_load_dwordx4 a[132:135], v68, s[28:31], s80 offen nt\n"
"s_add_u32 s80, s80, 36864\n"
"buffer_load_dwordx4 a[136:139], v68, s[28:31], s80 offen nt\n"
"s_add_u32 s80, s80, 36864\n"
"buffer_load_dwordx4 a[140:143], v68, s[28:31], s80 offen nt\n"
"s_sub_u32 s80, s80, 92160\n"
"s_waitcnt vmcnt(20)\n"
"ds_write_b128 v65, a[48:51] offset:0\n"
"ds_write_b128 v65, a[52:55] offset:1024\n"
"ds_write_b128 v65, a[56:59] offset:2048\n"
"ds_write_b128 v65, a[60:63] offset:3072\n"
"ds_read_b64_tr_b16 a[48:49], v66 offset:0\n"
"ds_read_b64_tr_b16 a[50:51], v66 offset:128\n"
"ds_read_b64_tr_b16 a[52:53], v66 offset:2048\n"
"ds_read_b64_tr_b16 a[54:55], v66 offset:2176\n"
"ds_read_b64_tr_b16 a[56:57], v66 offset:8\n"
"ds_read_b64_tr_b16 a[58:59], v66 offset:136\n"
"ds_read_b64_tr_b16 a[60:61], v66 offset:2056\n"
"ds_read_b64_tr_b16 a[62:63], v66 offset:2184\n"
"buffer_load_dwordx4 a[144:147], v68, s[28:31], s80 offen nt\n"
"s_add_u32 s80, s80, 36864\n"
"buffer_load_dwordx4 a[148:151], v68, s[28:31], s80 offen nt\n"
"s_add_u32 s80, s80, 36864\n"
"buffer_load_dwordx4 a[152:155], v68, s[28:31], s80 offen nt\n"
"s_add_u32 s80, s80, 36864\n"
"buffer_load_dwordx4 a[156:159], v68, s[28:31], s80 offen nt\n"
"s_add_u32 s80, s80, 18432\n"
"s_waitcnt vmcnt(20)\n"
"ds_write_b128 v65, a[64:67] offset:0\n"
"ds_write_b128 v65, a[68:71] offset:1024\n"
"ds_write_b128 v65, a[72:75] offset:2048\n"
"ds_write_b128 v65, a[76:79] offset:3072\n"
"ds_read_b64_tr_b16 a[64:65], v66 offset:0\n"
"ds_read_b64_tr_b16 a[66:67], v66 offset:128\n"
"ds_read_b64_tr_b16 a[68:69], v66 offset:2048\n"
"ds_read_b64_tr_b16 a[70:71], v66 offset:2176\n"
"ds_read_b64_tr_b16 a[72:73], v66 offset:8\n"
"ds_read_b64_tr_b16 a[74:75], v66 offset:136\n"
"ds_read_b64_tr_b16 a[76:77], v66 offset:2056\n"
"ds_read_b64_tr_b16 a[78:79], v66 offset:2184\n"
"buffer_load_dwordx4 a[160:163], v68, s[28:31], s80 offen nt\n"
"s_add_u32 s80, s80, 36864\n"
"buffer_load_dwordx4 a[164:167], v68, s[28:31], s80 offen nt\n"
"s_add_u32 s80, s80, 36864\n"
"buffer_load_dwordx4 a[168:171], v68, s[28:31], s80 offen nt\n"
"s_add_u32 s80, s80, 36864\n"
"buffer_load_dwordx4 a[172:175], v68, s[28:31], s80 offen nt\n"
"s_sub_u32 s80, s80, 92160\n"
"s_waitcnt vmcnt(20)\n"
"ds_write_b128 v65, a[80:83] offset:0\n"
"ds_write_b128 v65, a[84:87] offset:1024\n"
"ds_write_b128 v65, a[88:91] offset:2048\n"
"ds_write_b128 v65, a[92:95] offset:3072\n"
"ds_read_b64_tr_b16 a[80:81], v66 offset:0\n"
"ds_read_b64_tr_b16 a[82:83], v66 offset:128\n"
"ds_read_b64_tr_b16 a[84:85], v66 offset:2048\n"
"ds_read_b64_tr_b16 a[86:87], v66 offset:2176\n"
"ds_read_b64_tr_b16 a[88:89], v66 offset:8\n"
"ds_read_b64_tr_b16 a[90:91], v66 offset:136\n"
"ds_read_b64_tr_b16 a[92:93], v66 offset:2056\n"
"ds_read_b64_tr_b16 a[94:95], v66 offset:2184\n"
"buffer_load_dwordx4 a[176:179], v68, s[28:31], s80 offen nt\n"
"s_add_u32 s80, s80, 36864\n"
"buffer_load_dwordx4 a[180:183], v68, s[28:31], s80 offen nt\n"
"s_add_u32 s80, s80, 36864\n"
"buffer_load_dwordx4 a[184:187], v68, s[28:31], s80 offen nt\n"
"s_add_u32 s80, s80, 36864\n"
"buffer_load_dwordx4 a[188:191], v68, s[28:31], s80 offen nt\n"
"s_add_u32 s80, s80, 18432\n"
"s_waitcnt vmcnt(20)\n"
"ds_write_b128 v65, a[96:99] offset:0\n"
"ds_write_b128 v65, a[100:103] offset:1024\n"
"ds_write_b128 v65, a[104:107] offset:2048\n"
"ds_write_b128 v65, a[108:111] offset:3072\n"
"ds_read_b64_tr_b16 a[96:97], v66 offset:0\n"
"ds_read_b64_tr_b16 a[98:99], v66 offset:128\n"
"ds_read_b64_tr_b16 a[100:101], v66 offset:2048\n"
"ds_read_b64_tr_b16 a[102:103], v66 offset:2176\n"
"ds_read_b64_tr_b16 a[104:105], v66 offset:8\n"
"ds_read_b64_tr_b16 a[106:107], v66 offset:136\n"
"ds_read_b64_tr_b16 a[108:109], v66 offset:2056\n"
"ds_read_b64_tr_b16 a[110:111], v66 offset:2184\n"
"buffer_load_dwordx4 a[192:195], v68, s[28:31], s80 offen nt\n"
"s_add_u32 s80, s80, 36864\n"
"buffer_load_dwordx4 a[196:199], v68, s[28:31], s80 offen nt\n"
"s_add_u32 s80, s80, 36864\n"
"buffer_load_dwordx4 a[200:203], v68, s[28:31], s80 offen nt\n"
"s_add_u32 s80, s80, 36864\n"
"buffer_load_dwordx4 a[204:207], v68, s[28:31], s80 offen nt\n"
"s_sub_u32 s80, s80, 92160\n"
"s_waitcnt vmcnt(20)\n"
"ds_write_b128 v65, a[112:115] offset:0\n"
"ds_write_b128 v65, a[116:119] offset:1024\n"
"ds_write_b128 v65, a[120:123] offset:2048\n"
"ds_write_b128 v65, a[124:127] offset:3072\n"
"ds_read_b64_tr_b16 a[112:113], v66 offset:0\n"
"ds_read_b64_tr_b16 a[114:115], v66 offset:128\n"
"ds_read_b64_tr_b16 a[116:117], v66 offset:2048\n"
"ds_read_b64_tr_b16 a[118:119], v66 offset:2176\n"
"ds_read_b64_tr_b16 a[120:121], v66 offset:8\n"
"ds_read_b64_tr_b16 a[122:123], v66 offset:136\n"
"ds_read_b64_tr_b16 a[124:125], v66 offset:2056\n"
"ds_read_b64_tr_b16 a[126:127], v66 offset:2184\n"
"buffer_load_dwordx4 a[208:211], v68, s[28:31], s80 offen nt\n"
"s_add_u32 s80, s80, 36864\n"
"buffer_load_dwordx4 a[212:215], v68, s[28:31], s80 offen nt\n"
"s_add_u32 s80, s80, 36864\n"
"buffer_load_dwordx4 a[216:219], v68, s[28:31], s80 offen nt\n"
"s_add_u32 s80, s80, 36864\n"
"buffer_load_dwordx4 a[220:223], v68, s[28:31], s80 offen nt\n"
"s_add_u32 s80, s80, 18432\n"
"s_waitcnt vmcnt(20)\n"
"ds_write_b128 v65, a[128:131] offset:0\n"
"ds_write_b128 v65, a[132:135] offset:1024\n"
"ds_write_b128 v65, a[136:139] offset:2048\n"
"ds_write_b128 v65, a[140:143] offset:3072\n"
"ds_read_b64_tr_b16 a[128:129], v66 offset:0\n"
"ds_read_b64_tr_b16 a[130:131], v66 offset:128\n"
"ds_read_b64_tr_b16 a[132:133], v66 offset:2048\n"
"ds_read_b64_tr_b16 a[134:135], v66 offset:2176\n"
"ds_read_b64_tr_b16 a[136:137], v66 offset:8\n"
"ds_read_b64_tr_b16 a[138:139], v66 offset:136\n"
"ds_read_b64_tr_b16 a[140:141], v66 offset:2056\n"
"ds_read_b64_tr_b16 a[142:143], v66 offset:2184\n"
"buffer_load_dwordx4 a[224:227], v68, s[28:31], s80 offen nt\n"
"s_add_u32 s80, s80, 36864\n"
"buffer_load_dwordx4 a[228:231], v68, s[28:31], s80 offen nt\n"
"s_add_u32 s80, s80, 36864\n"
"buffer_load_dwordx4 a[232:235], v68, s[28:31], s80 offen nt\n"
"s_add_u32 s80, s80, 36864\n"
"buffer_load_dwordx4 a[236:239], v68, s[28:31], s80 offen nt\n"
"s_sub_u32 s80, s80, 92160\n"
"s_waitcnt vmcnt(20)\n"
"ds_write_b128 v65, a[144:147] offset:0\n"
"ds_write_b128 v65, a[148:151] offset:1024\n"
"ds_write_b128 v65, a[152:155] offset:2048\n"
"ds_write_b128 v65, a[156:159] offset:3072\n"
"ds_read_b64_tr_b16 a[144:145], v66 offset:0\n"
"ds_read_b64_tr_b16 a[146:147], v66 offset:128\n"
"ds_read_b64_tr_b16 a[148:149], v66 offset:2048\n"
"ds_read_b64_tr_b16 a[150:151], v66 offset:2176\n"
"ds_read_b64_tr_b16 a[152:153], v66 offset:8\n"
"ds_read_b64_tr_b16 a[154:155], v66 offset:136\n"
"ds_read_b64_tr_b16 a[156:157], v66 offset:2056\n"
"ds_read_b64_tr_b16 a[158:159], v66 offset:2184\n"
"buffer_load_dwordx4 a[240:243], v68, s[28:31], s80 offen nt\n"
"s_add_u32 s80, s80, 36864\n"
"buffer_load_dwordx4 a[244:247], v68, s[28:31], s80 offen nt\n"
"s_add_u32 s80, s80, 36864\n"
"buffer_load_dwordx4 a[248:251], v68, s[28:31], s80 offen nt\n"
"s_add_u32 s80, s80, 36864\n"
"buffer_load_dwordx4 a[252:255], v68, s[28:31], s80 offen nt\n"
"s_add_u32 s80, s80, 18432\n"
"s_waitcnt vmcnt(20)\n"
"ds_write_b128 v65, a[160:163] offset:0\n"
"ds_write_b128 v65, a[164:167] offset:1024\n"
"ds_write_b128 v65, a[168:171] offset:2048\n"
"ds_write_b128 v65, a[172:175] offset:3072\n"
"ds_read_b64_tr_b16 a[160:161], v66 offset:0\n"
"ds_read_b64_tr_b16 a[162:163], v66 offset:128\n"
"ds_read_b64_tr_b16 a[164:165], v66 offset:2048\n"
"ds_read_b64_tr_b16 a[166:167], v66 offset:2176\n"
"ds_read_b64_tr_b16 a[168:169], v66 offset:8\n"
"ds_read_b64_tr_b16 a[170:171], v66 offset:136\n"
"ds_read_b64_tr_b16 a[172:173], v66 offset:2056\n"
"ds_read_b64_tr_b16 a[174:175], v66 offset:2184\n"
"s_waitcnt vmcnt(16)\n"
"ds_write_b128 v65, a[176:179] offset:0\n"
"ds_write_b128 v65, a[180:183] offset:1024\n"
"ds_write_b128 v65, a[184:187] offset:2048\n"
"ds_write_b128 v65, a[188:191] offset:3072\n"
"ds_read_b64_tr_b16 a[176:177], v66 offset:0\n"
"ds_read_b64_tr_b16 a[178:179], v66 offset:128\n"
"ds_read_b64_tr_b16 a[180:181], v66 offset:2048\n"
"ds_read_b64_tr_b16 a[182:183], v66 offset:2176\n"
"ds_read_b64_tr_b16 a[184:185], v66 offset:8\n"
"ds_read_b64_tr_b16 a[186:187], v66 offset:136\n"
"ds_read_b64_tr_b16 a[188:189], v66 offset:2056\n"
"ds_read_b64_tr_b16 a[190:191], v66 offset:2184\n"
"s_waitcnt vmcnt(12)\n"
"ds_write_b128 v65, a[192:195] offset:0\n"
"ds_write_b128 v65, a[196:199] offset:1024\n"
"ds_write_b128 v65, a[200:203] offset:2048\n"
"ds_write_b128 v65, a[204:207] offset:3072\n"
"ds_read_b64_tr_b16 a[192:193], v66 offset:0\n"
"ds_read_b64_tr_b16 a[194:195], v66 offset:128\n"
"ds_read_b64_tr_b16 a[196:197], v66 offset:2048\n"
"ds_read_b64_tr_b16 a[198:199], v66 offset:2176\n"
"ds_read_b64_tr_b16 a[200:201], v66 offset:8\n"
"ds_read_b64_tr_b16 a[202:203], v66 offset:136\n"
"ds_read_b64_tr_b16 a[204:205], v66 offset:2056\n"
"ds_read_b64_tr_b16 a[206:207], v66 offset:2184\n"
"s_waitcnt vmcnt(8)\n"
"ds_write_b128 v65, a[208:211] offset:0\n"
"ds_write_b128 v65, a[212:215] offset:1024\n"
"ds_write_b128 v65, a[216:219] offset:2048\n"
"ds_write_b128 v65, a[220:223] offset:3072\n"
"ds_read_b64_tr_b16 a[208:209], v66 offset:0\n"
"ds_read_b64_tr_b16 a[210:211], v66 offset:128\n"
"ds_read_b64_tr_b16 a[212:213], v66 offset:2048\n"
"ds_read_b64_tr_b16 a[214:215], v66 offset:2176\n"
"ds_read_b64_tr_b16 a[216:217], v66 offset:8\n"
"ds_read_b64_tr_b16 a[218:219], v66 offset:136\n"
"ds_read_b64_tr_b16 a[220:221], v66 offset:2056\n"
"ds_read_b64_tr_b16 a[222:223], v66 offset:2184\n"
"s_waitcnt vmcnt(4)\n"
"ds_write_b128 v65, a[224:227] offset:0\n"
"ds_write_b128 v65, a[228:231] offset:1024\n"
"ds_write_b128 v65, a[232:235] offset:2048\n"
"ds_write_b128 v65, a[236:239] offset:3072\n"
"ds_read_b64_tr_b16 a[224:225], v66 offset:0\n"
"ds_read_b64_tr_b16 a[226:227], v66 offset:128\n"
"ds_read_b64_tr_b16 a[228:229], v66 offset:2048\n"
"ds_read_b64_tr_b16 a[230:231], v66 offset:2176\n"
"ds_read_b64_tr_b16 a[232:233], v66 offset:8\n"
"ds_read_b64_tr_b16 a[234:235], v66 offset:136\n"
"ds_read_b64_tr_b16 a[236:237], v66 offset:2056\n"
"ds_read_b64_tr_b16 a[238:239], v66 offset:2184\n"
"s_waitcnt vmcnt(0)\n"
"ds_write_b128 v65, a[240:243] offset:0\n"
"ds_write_b128 v65, a[244:247] offset:1024\n"
"ds_write_b128 v65, a[248:251] offset:2048\n"
"ds_write_b128 v65, a[252:255] offset:3072\n"
"ds_read_b64_tr_b16 a[240:241], v66 offset:0\n"
"ds_read_b64_tr_b16 a[242:243], v66 offset:128\n"
"ds_read_b64_tr_b16 a[244:245], v66 offset:2048\n"
"ds_read_b64_tr_b16 a[246:247], v66 offset:2176\n"
"ds_read_b64_tr_b16 a[248:249], v66 offset:8\n"
"ds_read_b64_tr_b16 a[250:251], v66 offset:136\n"
"ds_read_b64_tr_b16 a[252:253], v66 offset:2056\n"
"ds_read_b64_tr_b16 a[254:255], v66 offset:2184\n"
"s_waitcnt lgkmcnt(0)\n"
"s_mov_b32 s21, 0\n"
"s_mov_b32 s58, 0\n"
"s_mov_b32 s59, 0\n"
"Lce_27:\n"
"s_memtime s[48:49]\n"
"s_memrealtime s[36:37]\n"
"s_nop 3\n"
"s_memrealtime s[38:39]\n"
"s_nop 3\n"
"s_memrealtime s[40:41]\n"
"s_nop 3\n"
"s_memrealtime s[42:43]\n"
"s_nop 3\n"
"s_memrealtime s[44:45]\n"
"s_nop 3\n"
"s_memrealtime s[46:47]\n"
"s_waitcnt lgkmcnt(0)\n"
"s_cmp_eq_u32 s46, s36\n"
"s_cbranch_scc1 Lce_27\n"
"s_cmp_eq_u32 s38, s36\n"
"s_cselect_b32 s37, 20, 0\n"
"s_add_u32 s48, s48, s37\n"
"s_cmp_eq_u32 s40, s36\n"
"s_cselect_b32 s37, 20, 0\n"
"s_add_u32 s48, s48, s37\n"
"s_cmp_eq_u32 s42, s36\n"
"s_cselect_b32 s37, 20, 0\n"
"s_add_u32 s48, s48, s37\n"
"s_cmp_eq_u32 s44, s36\n"
"s_cselect_b32 s37, 20, 0\n"
"s_add_u32 s48, s48, s37\n"
"s_mul_i32 s40, s46, 24\n"
"s_sub_u32 s40, s40, s48\n"
"s_sub_u32 s38, s40, s76\n"
"s_sub_u32 s39, s48, s79\n"
"v_cvt_f32_i32 v6, s38\n"
"v_cvt_f32_u32 v7, s39\n"
"v_rcp_f32 v7, v7\n"
"v_mul_f32 v6, 0x47800000, v6\n"
"v_mul_f32 v6, v6, v7\n"
"v_cvt_i32_f32 v6, v6\n"
"s_nop 0\n"
"v_readfirstlane_b32 s81, v6\n"
"s_max_i32 s81, s81, -2000\n"
"s_min_i32 s81, s81, 30000\n"
"s_mov_b32 s76, s40\n"
"s_mov_b32 s79, s48\n"
"s_mov_b32 s83, 1350\n"
"s_add_u32 s53, s22, 1081344\n"
"s_cmp_lt_u32 s19, 32\n"
"s_cselect_b32 s36, 0, 16384\n"
"s_mul_i32 s72, s20, 32768\n"
"s_add_u32 s72, s72, s36\n"
"s_add_u32 s72, s72, 786432\n"
"s_mov_b32 s62, 98304\n"
"s_mul_i32 s63, s62, 3\n"
"s_add_u32 s63, s63, s72\n"
"s_cmp_lt_u32 s20, 3\n"
"s_cselect_b32 s72, s72, 2146435072\n"
"s_cselect_b32 s62, s62, 0\n"
"s_lshl_b32 s16, s19, 7\n"
"s_add_u32 s16, s16, s22\n"
"s_add_u32 s16, s16, 1146880\n"
".p2align 6\n"
"LB_loop:\n"
"s_add_u32 s64, s58, 0\n"
"s_add_u32 s65, s58, 2048\n"
"s_add_u32 s66, s58, 4096\n"
"s_add_u32 s67, s58, 6144\n"
"s_add_u32 s68, s58, 16384\n"
"s_add_u32 s69, s58, 18432\n"
"s_add_u32 s70, s58, 20480\n"
"s_add_u32 s71, s58, 22528\n"
"s_mov_b32 s74, s72\n"
"s_add_u32 s72, s72, s62\n"
"s_mul_i32 s36, s62, 3\n"
"s_cmp_eq_u32 s72, s63\n"
"s_cselect_b32 s36, s36, 0\n"
"s_sub_u32 s72, s72, s36\n"
"s_cmp_lg_u32 s20, 3\n"
"s_cbranch_scc1 Lst_29\n"
"s_add_u32 s42, s21, 1\n"
"s_and_b32 s42, s42, 3\n"
"s_lshl_b32 s42, s42, 14\n"
"s_add_u32 s42, s42, s16\n"
"v_mov_b32 v6, 0\n"
"buffer_store_dword v6, v10, s[24:27], s42 offen sc1\n"
"s_add_u32 s77, s21, 0\n"
"s_and_b32 s77, s77, 3\n"
"s_lshl_b32 s77, s77, 14\n"
"s_add_u32 s77, s77, s53\n"
"Lce_30:\n"
"s_memtime s[48:49]\n"
"s_memrealtime s[36:37]\n"
"s_nop 3\n"
"s_memrealtime s[38:39]\n"
"s_nop 3\n"
"s_memrealtime s[40:41]\n"
"s_nop 3\n"
"s_memrealtime s[42:43]\n"
"s_nop 3\n"
"s_memrealtime s[44:45]\n"
"s_nop 3\n"
"s_memrealtime s[46:47]\n"
"s_waitcnt lgkmcnt(0)\n"
"s_cmp_eq_u32 s46, s36\n"
"s_cbranch_scc1 Lce_30\n"
"s_cmp_eq_u32 s38, s36\n"
"s_cselect_b32 s37, 20, 0\n"
"s_add_u32 s48, s48, s37\n"
"s_cmp_eq_u32 s40, s36\n"
"s_cselect_b32 s37, 20, 0\n"
"s_add_u32 s48, s48, s37\n"
"s_cmp_eq_u32 s42, s36\n"
"s_cselect_b32 s37, 20, 0\n"
"s_add_u32 s48, s48, s37\n"
"s_cmp_eq_u32 s44, s36\n"
"s_cselect_b32 s37, 20, 0\n"
"s_add_u32 s48, s48, s37\n"
"s_mul_i32 s40, s46, 24\n"
"s_sub_u32 s40, s40, s48\n"
"s_sub_u32 s39, s48, s79\n"
"s_min_u32 s39, s39, 60000\n"
"s_mul_i32 s39, s39, s81\n"
"s_ashr_i32 s39, s39, 16\n"
"s_add_u32 s76, s76, s39\n"
"s_sub_u32 s40, s40, s76\n"
"s_ashr_i32 s39, s40, 1\n"
"s_add_u32 s76, s76, s39\n"
"s_max_i32 s40, s40, -1000\n"
"s_min_i32 s40, s40, 1000\n"
"s_mul_i32 s40, s40, 26\n"
"s_ashr_i32 s40, s40, 3\n"
"s_add_u32 s81, s81, s40\n"
"s_max_i32 s81, s81, -2000\n"
"s_min_i32 s81, s81, 30000\n"
"s_mov_b32 s79, s48\n"
"s_mov_b32 s78, 0\n"
"s_cmp_eq_u32 s21, 0\n"
"s_cbranch_scc1 Lst_28\n"
"s_add_u32 s36, s57, s83\n"
"Lwu_31:\n"
"s_memtime s[48:49]\n"
"s_waitcnt lgkmcnt(0)\n"
"s_sub_u32 s44, s36, s48\n"
"s_cmp_lt_i32 s44, 384\n"
"s_cbranch_scc1 Lwu_32\n"
"s_cmp_lt_i32 s44, 534\n"
"s_cbranch_scc1 Lwu_31\n"
"s_cmp_gt_i32 s44, 100000\n"
"s_cbranch_scc1 Lwu_33\n"
"s_sleep 1\n"
"s_branch Lwu_31\n"
"Lwu_32:\n"
"s_sub_i32 s44, s44, 60\n"
"s_cmp_lt_i32 s44, 4\n"
"s_cbranch_scc1 Lwu_33\n"
"s_and_b32 s44, s44, 0xfffffffc\n"
"s_sub_u32 s44, s50, s44\n"
"s_subb_u32 s45, s51, 0\n"
"s_swappc_b64 s[46:47], s[44:45]\n"
"Lwu_33:\n"
"Lst_28:\n"
"s_add_u32 s42, s21, 1\n"
"Lst_34:\n"
"buffer_load_dword v6, v9, s[24:27], s77 offen sc1\n"
"s_waitcnt vmcnt(0)\n"
"Lst_36:\n"
"v_lshrrev_b32 v7, 24, v6\n"
"v_cmp_ne_u32 vcc, s42, v7\n"
"s_cbranch_vccz Lst_35\n"
"s_add_u32 s78, s78, 1\n"
"s_sleep 3\n"
"s_memrealtime s[48:49]\n"
"s_waitcnt lgkmcnt(0)\n"
"s_sub_u32 s48, s48, s23\n"
"s_cmp_gt_i32 s48, 0\n"
"s_cbranch_scc1 LB_end\n"
"s_branch Lst_34\n"
"Lst_35:\n"
"s_add_u32 s36, s79, s76\n"
"v_subrev_u32 v6, s36, v6\n"
"v_bfe_i32 v6, v6, 0, 24\n"
"s_nop 1\n"
"v_max_i32_dpp v6, v6, v6 row_shr:1 row_mask:0xf bank_mask:0xf\n"
"s_nop 1\n"
"v_max_i32_dpp v6, v6, v6 row_shr:2 row_mask:0xf bank_mask:0xf\n"
"s_nop 1\n"
"v_max_i32_dpp v6, v6, v6 row_shr:4 row_mask:0xf bank_mask:0xf\n"
"s_nop 1\n"
"v_max_i32_dpp v6, v6, v6 row_shr:8 row_mask:0xf bank_mask:0xf\n"
"s_nop 0\n"
"v_readlane_b32 s36, v6, 15\n"
"v_readlane_b32 s37, v6, 31\n"
"s_max_i32 s36, s36, s37\n"
"s_max_i32 s36, s36, -60000\n"
"s_min_i32 s36, s36, 60000\n"
"s_add_u32 s43, s36, s79\n"
"s_add_u32 s43, s43, s76\n"
"s_mul_i32 s37, s36, s81\n"
"s_ashr_i32 s37, s37, 16\n"
"s_mul_i32 s38, s37, s81\n"
"s_ashr_i32 s38, s38, 16\n"
"s_sub_u32 s36, s36, s37\n"
"s_add_u32 s36, s36, s38\n"
"s_add_u32 s82, s36, s79\n"
"s_memtime s[48:49]\n"
"v_mov_b32 v7, s43\n"
"s_waitcnt lgkmcnt(0)\n"
"s_add_u32 s48, s48, 260\n"
"s_sub_u32 s48, s48, s56\n"
"s_sub_u32 s36, s82, s48\n"
"s_cmp_lt_i32 s36, 0\n"
"s_cselect_b32 s82, s48, s82\n"
"v_mov_b32 v6, s82\n"
"s_add_u32 s36, s82, s56\n"
"s_add_u32 s36, s36, s18\n"
"s_sub_u32 s39, s36, s79\n"
"s_mul_i32 s39, s39, s81\n"
"s_ashr_i32 s39, s39, 16\n"
"s_add_u32 s39, s39, s76\n"
"s_add_u32 s37, s39, s36\n"
"s_sub_u32 s38, s37, s36\n"
"v_mov_b32 v64, s38\n"
"ds_write_b64 v11, v[6:7]\n"
"ds_write_b32 v11, v64 offset:8\n"
"s_add_u32 s40, s21, 0\n"
"s_and_b32 s40, s40, 3\n"
"s_lshl_b32 s40, s40, 14\n"
"s_add_u32 s40, s40, s16\n"
"s_and_b32 s41, s37, 0xffffff\n"
"s_add_u32 s39, s21, 1\n"
"s_lshl_b32 s39, s39, 24\n"
"s_or_b32 s41, s41, s39\n"
"v_mov_b32 v65, s41\n"
"buffer_store_dword v65, v10, s[24:27], s40 offen sc1\n"
"Lst_29:\n"
"s_waitcnt lgkmcnt(0)\n"
"s_barrier\n"
"ds_read_b128 v[76:79], v11\n"
"s_lshl_b32 s37, s20, 4\n"
"s_add_u32 s37, s37, s56\n"
"s_waitcnt lgkmcnt(0)\n"
"v_readfirstlane_b32 s82, v76\n"
"s_add_u32 s36, s82, s37\n"
"s_mov_b32 s73, s36\n"
"Lwu_37:\n"
"s_memtime s[48:49]\n"
"s_waitcnt lgkmcnt(0)\n"
"s_sub_u32 s44, s36, s48\n"
"s_cmp_lt_i32 s44, 384\n"
"s_cbranch_scc1 Lwu_38\n"
"s_cmp_lt_i32 s44, 534\n"
"s_cbranch_scc1 Lwu_37\n"
"s_cmp_gt_i32 s44, 100000\n"
"s_cbranch_scc1 Lwu_39\n"
"s_sleep 1\n"
"s_branch Lwu_37\n"
"Lwu_38:\n"
"s_sub_i32 s44, s44, 60\n"
"s_cmp_lt_i32 s44, 4\n"
"s_cbranch_scc1 Lwu_39\n"
"s_and_b32 s44, s44, 0xfffffffc\n"
"s_sub_u32 s44, s50, s44\n"
"s_subb_u32 s45, s51, 0\n"
"s_swappc_b64 s[46:47], s[44:45]\n"
"Lwu_39:\n"
"buffer_load_dwordx4 v[16:19], v2, s[24:27], s64 offen sc1\n"
"v_mov_b64 v[48:49], 0\n"
"v_mov_b64 v[50:51], 0\n"
"v_mov_b64 v[52:53], 0\n"
"v_mov_b64 v[54:55], 0\n"
"v_mov_b64 v[56:57], 0\n"
"v_mov_b64 v[58:59], 0\n"
"v_mov_b64 v[60:61], 0\n"
"v_mov_b64 v[62:63], 0\n"
"s_nop 1\n"
"buffer_load_dwordx4 v[20:23], v2, s[24:27], s65 offen sc1\n"
"s_nop 9\n"
"buffer_load_dwordx4 v[24:27], v2, s[24:27], s66 offen sc1\n"
"s_nop 9\n"
"buffer_load_dwordx4 v[28:31], v2, s[24:27], s67 offen sc1\n"
"s_nop 9\n"
"buffer_load_dwordx4 v[32:35], v2, s[24:27], s68 offen sc1\n"
"s_nop 9\n"
"buffer_load_dwordx4 v[36:39], v2, s[24:27], s69 offen sc1\n"
"s_nop 9\n"
"buffer_load_dwordx4 v[40:43], v2, s[24:27], s70 offen sc1\n"
"s_nop 9\n"
"buffer_load_dwordx4 v[44:47], v2, s[24:27], s71 offen sc1\n"
"s_waitcnt vmcnt(7)\n"
"v_smfmac_f32_16x16x64_bf16 v[48:51], v[16:19], a[0:7], v1\n"
"v_max3_u32 v6, v16, v17, v18\n"
"v_smfmac_f32_16x16x64_bf16 v[56:59], v[16:19], a[16:23], v1\n"
"v_mov_b32 v7, v19\n"
"v_smfmac_f32_16x16x64_bf16 v[52:55], v[16:19], a[8:15], v1\n"
"v_smfmac_f32_16x16x64_bf16 v[60:63], v[16:19], a[24:31], v1\n"
"s_waitcnt vmcnt(6)\n"
"v_smfmac_f32_16x16x64_bf16 v[48:51], v[20:23], a[32:39], v1\n"
"v_max3_u32 v6, v6, v20, v21\n"
"v_smfmac_f32_16x16x64_bf16 v[56:59], v[20:23], a[48:55], v1\n"
"v_max3_u32 v7, v7, v22, v23\n"
"v_smfmac_f32_16x16x64_bf16 v[52:55], v[20:23], a[40:47], v1\n"
"v_max_u32 v6, v6, v7\n"
"v_smfmac_f32_16x16x64_bf16 v[60:63], v[20:23], a[56:63], v1\n"
"v_cmp_eq_u32 s[54:55], -1, v6\n"
"s_waitcnt vmcnt(5)\n"
"v_smfmac_f32_16x16x64_bf16 v[48:51], v[24:27], a[64:71], v1\n"
"v_max3_u32 v6, v24, v25, v26\n"
"v_smfmac_f32_16x16x64_bf16 v[56:59], v[24:27], a[80:87], v1\n"
"v_mov_b32 v7, v27\n"
"v_smfmac_f32_16x16x64_bf16 v[52:55], v[24:27], a[72:79], v1\n"
"s_cmp_lg_u64 s[54:55], 0\n"
"s_cbranch_scc1 LB_m\n"
"LB_r:\n"
"v_smfmac_f32_16x16x64_bf16 v[60:63], v[24:27], a[88:95], v1\n"
"s_waitcnt vmcnt(4)\n"
"v_smfmac_f32_16x16x64_bf16 v[48:51], v[28:31], a[96:103], v1\n"
"v_max3_u32 v6, v6, v28, v29\n"
"v_smfmac_f32_16x16x64_bf16 v[56:59], v[28:31], a[112:119], v1\n"
"v_max3_u32 v7, v7, v30, v31\n"
"v_smfmac_f32_16x16x64_bf16 v[52:55], v[28:31], a[104:111], v1\n"
"v_smfmac_f32_16x16x64_bf16 v[60:63], v[28:31], a[120:127], v1\n"
"s_waitcnt vmcnt(3)\n"
"v_smfmac_f32_16x16x64_bf16 v[48:51], v[32:35], a[128:135], v1\n"
"v_max3_u32 v6, v6, v32, v33\n"
"v_smfmac_f32_16x16x64_bf16 v[56:59], v[32:35], a[144:151], v1\n"
"v_max3_u32 v7, v7, v34, v35\n"
"v_smfmac_f32_16x16x64_bf16 v[52:55], v[32:35], a[136:143], v1\n"
"v_smfmac_f32_16x16x64_bf16 v[60:63], v[32:35], a[152:159], v1\n"
"s_waitcnt vmcnt(2)\n"
"v_smfmac_f32_16x16x64_bf16 v[48:51], v[36:39], a[160:167], v1\n"
"v_max3_u32 v6, v6, v36, v37\n"
"v_smfmac_f32_16x16x64_bf16 v[56:59], v[36:39], a[176:183], v1\n"
"v_max3_u32 v7, v7, v38, v39\n"
"v_smfmac_f32_16x16x64_bf16 v[52:55], v[36:39], a[168:175], v1\n"
"v_smfmac_f32_16x16x64_bf16 v[60:63], v[36:39], a[184:191], v1\n"
"s_waitcnt vmcnt(1)\n"
"v_smfmac_f32_16x16x64_bf16 v[48:51], v[40:43], a[192:199], v1\n"
"v_max3_u32 v6, v6, v40, v41\n"
"v_smfmac_f32_16x16x64_bf16 v[56:59], v[40:43], a[208:215], v1\n"
"v_max3_u32 v7, v7, v42, v43\n"
"v_smfmac_f32_16x16x64_bf16 v[52:55], v[40:43], a[200:207], v1\n"
"v_smfmac_f32_16x16x64_bf16 v[60:63], v[40:43], a[216:223], v1\n"
"s_waitcnt vmcnt(0)\n"
"v_smfmac_f32_16x16x64_bf16 v[48:51], v[44:47], a[224:231], v1\n"
"v_max3_u32 v6, v6, v44, v45\n"
"v_smfmac_f32_16x16x64_bf16 v[56:59], v[44:47], a[240:247], v1\n"
"v_max3_u32 v7, v7, v46, v47\n"
"v_smfmac_f32_16x16x64_bf16 v[52:55], v[44:47], a[232:239], v1\n"
"v_max_u32 v6, v6, v7\n"
"v_smfmac_f32_16x16x64_bf16 v[60:63], v[44:47], a[248:255], v1\n"
"v_cmp_eq_u32 vcc, -1, v6\n"
"s_or_b64 vcc, vcc, s[54:55]\n"
"s_cbranch_vccnz LB_fix\n"
"LB_red:\n"
"v_add_f32 v48, v48, v49\n"
"v_add_f32 v58, v58, v59\n"
"v_add_f32 v48, v48, v58\n"
"v_add_f32 v52, v52, v53\n"
"s_nop 0\n"
"v_add_f32 v62, v62, v63\n"
"v_add_f32 v52, v52, v62\n"
"ds_write2_b32 v3, v48, v52 offset0:0 offset1:4\n"
"s_waitcnt lgkmcnt(0)\n"
"s_barrier\n"
"ds_read_b64 v[64:65], v4 offset:0\n"
"ds_read_b64 v[66:67], v4 offset:512\n"
"ds_read_b64 v[68:69], v4 offset:1024\n"
"ds_read_b64 v[70:71], v4 offset:1536\n"
"s_waitcnt lgkmcnt(2)\n"
"v_pk_add_f32 v[64:65], v[64:65], v[66:67]\n"
"s_waitcnt lgkmcnt(0)\n"
"v_pk_add_f32 v[68:69], v[68:69], v[70:71]\n"
"v_pk_add_f32 v[64:65], v[64:65], v[68:69]\n"
"v_cvt_pk_bf16_f32 v64, v64, v65\n"
"buffer_store_dword v64, v5, s[24:27], s72 offen sc1\n"
"s_memtime s[48:49]\n"
"s_waitcnt lgkmcnt(0)\n"
"s_mov_b32 s57, s48\n"
"buffer_store_dword v8, v5, s[24:27], s74 offen sc1\n"
"s_sub_u32 s36, s57, s82\n"
"s_sub_u32 s36, s36, s56\n"
"s_sub_u32 s36, s36, s18\n"
"s_ashr_i32 s36, s36, 1\n"
"s_max_i32 s36, s36, -60\n"
"s_min_i32 s36, s36, 40\n"
"s_add_u32 s18, s18, s36\n"
"s_add_u32 s58, s58, 131072\n"
"s_cmp_eq_u32 s58, 393216\n"
"s_cselect_b32 s58, 0, s58\n"
"s_sub_i32 s56, s56, 0\n"
"s_add_u32 s21, s21, 1\n"
"s_cmp_lt_u32 s21, 63\n"
"s_cbranch_scc1 LB_loop\n"
"LB_end:\n"
"s_endpgm\n"
"LB_m:\n"
"s_add_u32 s36, s64, 393216\n"
"buffer_load_dwordx4 v[16:19], v2, s[24:27], s36 offen sc1\n"
"s_add_u32 s36, s65, 393216\n"
"buffer_load_dwordx4 v[20:23], v2, s[24:27], s36 offen sc1\n"
"v_readfirstlane_b32 s37, v78\n"
"s_add_u32 s37, s37, s73\n"
"s_add_u32 s37, s37, s18\n"
"s_add_u32 s37, s37, 900\n"
"s_add_u32 s40, s21, 0\n"
"s_and_b32 s40, s40, 3\n"
"s_lshl_b32 s40, s40, 14\n"
"s_add_u32 s40, s40, s16\n"
"s_and_b32 s41, s37, 0xffffff\n"
"s_add_u32 s39, s21, 1\n"
"s_lshl_b32 s39, s39, 24\n"
"s_or_b32 s41, s41, s39\n"
"v_mov_b32 v65, s41\n"
"buffer_store_dword v65, v10, s[24:27], s40 offen sc1\n"
"s_branch LB_r\n"
"LB_fix:\n"
"s_mov_b32 s75, 1\n"
"s_add_u32 s60, s60, 1\n"
"v_mov_b64 v[48:49], 0\n"
"v_mov_b64 v[50:51], 0\n"
"v_mov_b64 v[52:53], 0\n"
"v_mov_b64 v[54:55], 0\n"
"v_mov_b64 v[56:57], 0\n"
"v_mov_b64 v[58:59], 0\n"
"v_mov_b64 v[60:61], 0\n"
"v_mov_b64 v[62:63], 0\n"
"s_waitcnt vmcnt(0)\n"
"v_max3_u32 v6, v16, v17, v18\n"
"v_mov_b32 v7, v19\n"
"v_max3_u32 v6, v6, v20, v21\n"
"v_max3_u32 v7, v7, v22, v23\n"
"v_max3_u32 v6, v6, v24, v25\n"
"v_max3_u32 v7, v7, v26, v27\n"
"v_max3_u32 v6, v6, v28, v29\n"
"v_max3_u32 v7, v7, v30, v31\n"
"v_max3_u32 v6, v6, v32, v33\n"
"v_max3_u32 v7, v7, v34, v35\n"
"v_max3_u32 v6, v6, v36, v37\n"
"v_max3_u32 v7, v7, v38, v39\n"
"v_max3_u32 v6, v6, v40, v41\n"
"v_max3_u32 v7, v7, v42, v43\n"
"v_max3_u32 v6, v6, v44, v45\n"
"v_max3_u32 v7, v7, v46, v47\n"
"v_max_u32 v6, v6, v7\n"
"v_cmp_eq_u32 vcc, -1, v6\n"
"s_cbranch_vccz LB_redo\n"
"LB_fail:\n"
"s_add_u32 s56, s56, 0\n"
"s_min_u32 s56, s56, 900\n"
"s_add_u32 s42, s21, 0\n"
"s_and_b32 s42, s42, 3\n"
"s_lshl_b32 s42, s42, 14\n"
"s_add_u32 s42, s42, s53\n"
"v_mov_b32 v65, 0\n"
"s_add_u32 s40, s21, 0\n"
"s_and_b32 s40, s40, 3\n"
"s_lshl_b32 s40, s40, 14\n"
"s_add_u32 s40, s40, s16\n"
"buffer_store_dword v65, v10, s[24:27], s40 offen sc1\n"
"s_add_u32 s64, s64, 393216\n"
"s_add_u32 s65, s65, 393216\n"
"s_add_u32 s66, s66, 393216\n"
"s_add_u32 s67, s67, 393216\n"
"s_add_u32 s68, s68, 393216\n"
"s_add_u32 s69, s69, 393216\n"
"s_add_u32 s70, s70, 393216\n"
"s_add_u32 s71, s71, 393216\n"
"s_add_u32 s43, s21, 1\n"
"Lst_40:\n"
"buffer_load_dword v6, v9, s[24:27], s42 offen sc1\n"
"s_waitcnt vmcnt(0)\n"
"Lst_42:\n"
"v_lshrrev_b32 v7, 24, v6\n"
"v_cmp_ne_u32 vcc, s43, v7\n"
"s_cbranch_vccz Lst_41\n"
"s_sleep 2\n"
"s_memrealtime s[48:49]\n"
"s_waitcnt lgkmcnt(0)\n"
"s_sub_u32 s48, s48, s23\n"
"s_cmp_gt_i32 s48, 0\n"
"s_cbranch_scc1 LB_end\n"
"s_branch Lst_40\n"
"Lst_41:\n"
"v_sub_u32 v6, v6, v77\n"
"v_bfe_i32 v6, v6, 0, 24\n"
"s_nop 1\n"
"v_max_i32_dpp v6, v6, v6 row_shr:1 row_mask:0xf bank_mask:0xf\n"
"s_nop 1\n"
"v_max_i32_dpp v6, v6, v6 row_shr:2 row_mask:0xf bank_mask:0xf\n"
"s_nop 1\n"
"v_max_i32_dpp v6, v6, v6 row_shr:4 row_mask:0xf bank_mask:0xf\n"
"s_nop 1\n"
"v_max_i32_dpp v6, v6, v6 row_shr:8 row_mask:0xf bank_mask:0xf\n"
"s_nop 0\n"
"v_readlane_b32 s36, v6, 15\n"
"v_readlane_b32 s37, v6, 31\n"
"s_max_i32 s36, s36, s37\n"
"s_max_i32 s36, s36, 0\n"
"s_min_i32 s36, s36, 20000\n"
"s_add_u32 s36, s36, s73\n"
"s_add_u32 s36, s36, 100\n"
"s_memtime s[48:49]\n"
"s_waitcnt lgkmcnt(0)\n"
"s_sub_u32 s37, s36, s48\n"
"s_cmp_lt_i32 s37, 0\n"
"s_cselect_b32 s36, s48, s36\n"
"v_readfirstlane_b32 s37, v78\n"
"s_add_u32 s37, s37, s36\n"
"s_add_u32 s37, s37, s18\n"
"s_add_u32 s37, s37, 600\n"
"s_add_u32 s40, s21, 0\n"
"s_and_b32 s40, s40, 3\n"
"s_lshl_b32 s40, s40, 14\n"
"s_add_u32 s40, s40, s16\n"
"s_and_b32 s41, s37, 0xffffff\n"
"s_add_u32 s39, s21, 1\n"
"s_lshl_b32 s39, s39, 24\n"
"s_or_b32 s41, s41, s39\n"
"v_mov_b32 v65, s41\n"
"buffer_store_dword v65, v10, s[24:27], s40 offen sc1\n"
"Lwu_43:\n"
"s_memtime s[48:49]\n"
"s_waitcnt lgkmcnt(0)\n"
"s_sub_u32 s44, s36, s48\n"
"s_cmp_lt_i32 s44, 384\n"
"s_cbranch_scc1 Lwu_44\n"
"s_cmp_lt_i32 s44, 534\n"
"s_cbranch_scc1 Lwu_43\n"
"s_cmp_gt_i32 s44, 100000\n"
"s_cbranch_scc1 Lwu_45\n"
"s_sleep 1\n"
"s_branch Lwu_43\n"
"Lwu_44:\n"
"s_sub_i32 s44, s44, 60\n"
"s_cmp_lt_i32 s44, 4\n"
"s_cbranch_scc1 Lwu_45\n"
"s_and_b32 s44, s44, 0xfffffffc\n"
"s_sub_u32 s44, s50, s44\n"
"s_subb_u32 s45, s51, 0\n"
"s_swappc_b64 s[46:47], s[44:45]\n"
"Lwu_45:\n"
"LB_slow:\n"
"buffer_load_dwordx4 v[16:19], v2, s[24:27], s64 offen sc1\n"
"buffer_load_dwordx4 v[20:23], v2, s[24:27], s65 offen sc1\n"
"buffer_load_dwordx4 v[24:27], v2, s[24:27], s66 offen sc1\n"
"buffer_load_dwordx4 v[28:31], v2, s[24:27], s67 offen sc1\n"
"buffer_load_dwordx4 v[32:35], v2, s[24:27], s68 offen sc1\n"
"buffer_load_dwordx4 v[36:39], v2, s[24:27], s69 offen sc1\n"
"buffer_load_dwordx4 v[40:43], v2, s[24:27], s70 offen sc1\n"
"buffer_load_dwordx4 v[44:47], v2, s[24:27], s71 offen sc1\n"
"s_waitcnt vmcnt(0)\n"
"v_max3_u32 v6, v16, v17, v18\n"
"v_mov_b32 v7, v19\n"
"v_max3_u32 v6, v6, v20, v21\n"
"v_max3_u32 v7, v7, v22, v23\n"
"v_max3_u32 v6, v6, v24, v25\n"
"v_max3_u32 v7, v7, v26, v27\n"
"v_max3_u32 v6, v6, v28, v29\n"
"v_max3_u32 v7, v7, v30, v31\n"
"v_max3_u32 v6, v6, v32, v33\n"
"v_max3_u32 v7, v7, v34, v35\n"
"v_max3_u32 v6, v6, v36, v37\n"
"v_max3_u32 v7, v7, v38, v39\n"
"v_max3_u32 v6, v6, v40, v41\n"
"v_max3_u32 v7, v7, v42, v43\n"
"v_max3_u32 v6, v6, v44, v45\n"
"v_max3_u32 v7, v7, v46, v47\n"
"v_max_u32 v6, v6, v7\n"
"v_cmp_eq_u32 vcc, -1, v6\n"
"s_cbranch_vccz LB_redo\n"
"s_sleep 2\n"
"s_memrealtime s[48:49]\n"
"s_waitcnt lgkmcnt(0)\n"
"s_sub_u32 s48, s48, s23\n"
"s_cmp_gt_i32 s48, 0\n"
"s_cbranch_scc1 LB_end\n"
"s_branch LB_slow\n"
"LB_redo:\n"
"v_smfmac_f32_16x16x64_bf16 v[48:51], v[16:19], a[0:7], v1\n"
"v_smfmac_f32_16x16x64_bf16 v[56:59], v[16:19], a[16:23], v1\n"
"v_smfmac_f32_16x16x64_bf16 v[52:55], v[16:19], a[8:15], v1\n"
"v_smfmac_f32_16x16x64_bf16 v[60:63], v[16:19], a[24:31], v1\n"
"v_smfmac_f32_16x16x64_bf16 v[48:51], v[20:23], a[32:39], v1\n"
"v_smfmac_f32_16x16x64_bf16 v[56:59], v[20:23], a[48:55], v1\n"
"v_smfmac_f32_16x16x64_bf16 v[52:55], v[20:23], a[40:47], v1\n"
"v_smfmac_f32_16x16x64_bf16 v[60:63], v[20:23], a[56:63], v1\n"
"v_smfmac_f32_16x16x64_bf16 v[48:51], v[24:27], a[64:71], v1\n"
"v_smfmac_f32_16x16x64_bf16 v[56:59], v[24:27], a[80:87], v1\n"
"v_smfmac_f32_16x16x64_bf16 v[52:55], v[24:27], a[72:79], v1\n"
"v_smfmac_f32_16x16x64_bf16 v[60:63], v[24:27], a[88:95], v1\n"
"v_smfmac_f32_16x16x64_bf16 v[48:51], v[28:31], a[96:103], v1\n"
"v_smfmac_f32_16x16x64_bf16 v[56:59], v[28:31], a[112:119], v1\n"
"v_smfmac_f32_16x16x64_bf16 v[52:55], v[28:31], a[104:111], v1\n"
"v_smfmac_f32_16x16x64_bf16 v[60:63], v[28:31], a[120:127], v1\n"
"v_smfmac_f32_16x16x64_bf16 v[48:51], v[32:35], a[128:135], v1\n"
"v_smfmac_f32_16x16x64_bf16 v[56:59], v[32:35], a[144:151], v1\n"
"v_smfmac_f32_16x16x64_bf16 v[52:55], v[32:35], a[136:143], v1\n"
"v_smfmac_f32_16x16x64_bf16 v[60:63], v[32:35], a[152:159], v1\n"
"v_smfmac_f32_16x16x64_bf16 v[48:51], v[36:39], a[160:167], v1\n"
"v_smfmac_f32_16x16x64_bf16 v[56:59], v[36:39], a[176:183], v1\n"
"v_smfmac_f32_16x16x64_bf16 v[52:55], v[36:39], a[168:175], v1\n"
"v_smfmac_f32_16x16x64_bf16 v[60:63], v[36:39], a[184:191], v1\n"
"v_smfmac_f32_16x16x64_bf16 v[48:51], v[40:43], a[192:199], v1\n"
"v_smfmac_f32_16x16x64_bf16 v[56:59], v[40:43], a[208:215], v1\n"
"v_smfmac_f32_16x16x64_bf16 v[52:55], v[40:43], a[200:207], v1\n"
"v_smfmac_f32_16x16x64_bf16 v[60:63], v[40:43], a[216:223], v1\n"
"v_smfmac_f32_16x16x64_bf16 v[48:51], v[44:47], a[224:231], v1\n"
"v_smfmac_f32_16x16x64_bf16 v[56:59], v[44:47], a[240:247], v1\n"
"v_smfmac_f32_16x16x64_bf16 v[52:55], v[44:47], a[232:239], v1\n"
"v_smfmac_f32_16x16x64_bf16 v[60:63], v[44:47], a[248:255], v1\n"
"s_nop 3\n"
"s_branch LB_red\n"
"LN:\n"
"s_load_dwordx2 s[32:33], s[16:17], 0\n"
"s_mov_b32 s34, 2359296\n"
"s_mov_b32 s35, 0x00020000\n"
"s_waitcnt lgkmcnt(0)\n"
"s_and_b32 s33, s33, 0xffff\n"
"v_mov_b32 v3, 2146435072\n"
"v_cmp_eq_u32 vcc, 0, v0\n"
"s_nop 1\n"
"v_cndmask_b32 v3, v3, 0, vcc\n"
"v_and_b32 v6, 15, v0\n"
"v_mul_u32_u24 v1, 1152, v6\n"
"v_lshrrev_b32 v6, 4, v0\n"
"v_lshl_add_u32 v1, v6, 4, v1\n"
"v_lshlrev_b32 v2, 4, v0\n"
"s_mov_b32 s54, 0x3f803f80\n"
"s_mov_b32 s55, 0xffff0000\n"
"s_mov_b32 s21, 0\n"
"LN_loop:\n"
"s_lshl_b32 s53, s21, 6\n"
"s_lshl_b32 s36, s19, 2\n"
"s_add_u32 s53, s53, s36\n"
"s_add_u32 s53, s53, s20\n"
"s_mul_i32 s36, s53, 18432\n"
"buffer_load_dwordx4 v[16:19], v1, s[32:35], s36 offen offset:0\n"
"buffer_load_dwordx4 v[20:23], v1, s[32:35], s36 offen offset:64\n"
"buffer_load_dwordx4 v[24:27], v1, s[32:35], s36 offen offset:128\n"
"buffer_load_dwordx4 v[28:31], v1, s[32:35], s36 offen offset:192\n"
"buffer_load_dwordx4 v[32:35], v1, s[32:35], s36 offen offset:256\n"
"buffer_load_dwordx4 v[36:39], v1, s[32:35], s36 offen offset:320\n"
"buffer_load_dwordx4 v[40:43], v1, s[32:35], s36 offen offset:384\n"
"buffer_load_dwordx4 v[44:47], v1, s[32:35], s36 offen offset:448\n"
"buffer_load_dwordx4 v[48:51], v1, s[32:35], s36 offen offset:512\n"
"buffer_load_dwordx4 v[52:55], v1, s[32:35], s36 offen offset:576\n"
"buffer_load_dwordx4 v[56:59], v1, s[32:35], s36 offen offset:640\n"
"buffer_load_dwordx4 v[60:63], v1, s[32:35], s36 offen offset:704\n"
"buffer_load_dwordx4 v[64:67], v1, s[32:35], s36 offen offset:768\n"
"buffer_load_dwordx4 v[68:71], v1, s[32:35], s36 offen offset:832\n"
"buffer_load_dwordx4 v[72:75], v1, s[32:35], s36 offen offset:896\n"
"buffer_load_dwordx4 v[76:79], v1, s[32:35], s36 offen offset:960\n"
"buffer_load_dwordx4 v[80:83], v1, s[32:35], s36 offen offset:1024\n"
"buffer_load_dwordx4 v[84:87], v1, s[32:35], s36 offen offset:1088\n"
"v_mov_b32 v4, 0\n"
"v_mov_b32 v5, 0\n"
"v_mov_b32 v7, s54\n"
"s_waitcnt vmcnt(17)\n"
"v_dot2_f32_bf16 v5, v16, v7, v5\n"
"v_dot2_f32_bf16 v4, v17, v7, v4\n"
"v_dot2_f32_bf16 v5, v18, v7, v5\n"
"v_dot2_f32_bf16 v4, v19, v7, v4\n"
"s_waitcnt vmcnt(16)\n"
"v_dot2_f32_bf16 v5, v20, v7, v5\n"
"v_dot2_f32_bf16 v4, v21, v7, v4\n"
"v_dot2_f32_bf16 v5, v22, v7, v5\n"
"v_dot2_f32_bf16 v4, v23, v7, v4\n"
"s_waitcnt vmcnt(15)\n"
"v_dot2_f32_bf16 v5, v24, v7, v5\n"
"v_dot2_f32_bf16 v4, v25, v7, v4\n"
"v_dot2_f32_bf16 v5, v26, v7, v5\n"
"v_dot2_f32_bf16 v4, v27, v7, v4\n"
"s_waitcnt vmcnt(14)\n"
"v_dot2_f32_bf16 v5, v28, v7, v5\n"
"v_dot2_f32_bf16 v4, v29, v7, v4\n"
"v_dot2_f32_bf16 v5, v30, v7, v5\n"
"v_dot2_f32_bf16 v4, v31, v7, v4\n"
"s_waitcnt vmcnt(13)\n"
"v_dot2_f32_bf16 v5, v32, v7, v5\n"
"v_dot2_f32_bf16 v4, v33, v7, v4\n"
"v_dot2_f32_bf16 v5, v34, v7, v5\n"
"v_dot2_f32_bf16 v4, v35, v7, v4\n"
"s_waitcnt vmcnt(12)\n"
"v_dot2_f32_bf16 v5, v36, v7, v5\n"
"v_dot2_f32_bf16 v4, v37, v7, v4\n"
"v_dot2_f32_bf16 v5, v38, v7, v5\n"
"v_dot2_f32_bf16 v4, v39, v7, v4\n"
"s_waitcnt vmcnt(11)\n"
"v_dot2_f32_bf16 v5, v40, v7, v5\n"
"v_dot2_f32_bf16 v4, v41, v7, v4\n"
"v_dot2_f32_bf16 v5, v42, v7, v5\n"
"v_dot2_f32_bf16 v4, v43, v7, v4\n"
"s_waitcnt vmcnt(10)\n"
"v_dot2_f32_bf16 v5, v44, v7, v5\n"
"v_dot2_f32_bf16 v4, v45, v7, v4\n"
"v_dot2_f32_bf16 v5, v46, v7, v5\n"
"v_dot2_f32_bf16 v4, v47, v7, v4\n"
"s_waitcnt vmcnt(9)\n"
"v_dot2_f32_bf16 v5, v48, v7, v5\n"
"v_dot2_f32_bf16 v4, v49, v7, v4\n"
"v_dot2_f32_bf16 v5, v50, v7, v5\n"
"v_dot2_f32_bf16 v4, v51, v7, v4\n"
"s_waitcnt vmcnt(8)\n"
"v_dot2_f32_bf16 v5, v52, v7, v5\n"
"v_dot2_f32_bf16 v4, v53, v7, v4\n"
"v_dot2_f32_bf16 v5, v54, v7, v5\n"
"v_dot2_f32_bf16 v4, v55, v7, v4\n"
"s_waitcnt vmcnt(7)\n"
"v_dot2_f32_bf16 v5, v56, v7, v5\n"
"v_dot2_f32_bf16 v4, v57, v7, v4\n"
"v_dot2_f32_bf16 v5, v58, v7, v5\n"
"v_dot2_f32_bf16 v4, v59, v7, v4\n"
"s_waitcnt vmcnt(6)\n"
"v_dot2_f32_bf16 v5, v60, v7, v5\n"
"v_dot2_f32_bf16 v4, v61, v7, v4\n"
"v_dot2_f32_bf16 v5, v62, v7, v5\n"
"v_dot2_f32_bf16 v4, v63, v7, v4\n"
"s_waitcnt vmcnt(5)\n"
"v_dot2_f32_bf16 v5, v64, v7, v5\n"
"v_dot2_f32_bf16 v4, v65, v7, v4\n"
"v_dot2_f32_bf16 v5, v66, v7, v5\n"
"v_dot2_f32_bf16 v4, v67, v7, v4\n"
"s_waitcnt vmcnt(4)\n"
"v_dot2_f32_bf16 v5, v68, v7, v5\n"
"v_dot2_f32_bf16 v4, v69, v7, v4\n"
"v_dot2_f32_bf16 v5, v70, v7, v5\n"
"v_dot2_f32_bf16 v4, v71, v7, v4\n"
"s_waitcnt vmcnt(3)\n"
"v_dot2_f32_bf16 v5, v72, v7, v5\n"
"v_dot2_f32_bf16 v4, v73, v7, v4\n"
"v_dot2_f32_bf16 v5, v74, v7, v5\n"
"v_dot2_f32_bf16 v4, v75, v7, v4\n"
"s_waitcnt vmcnt(2)\n"
"v_dot2_f32_bf16 v5, v76, v7, v5\n"
"v_dot2_f32_bf16 v4, v77, v7, v4\n"
"v_dot2_f32_bf16 v5, v78, v7, v5\n"
"v_dot2_f32_bf16 v4, v79, v7, v4\n"
"s_waitcnt vmcnt(1)\n"
"v_dot2_f32_bf16 v5, v80, v7, v5\n"
"v_dot2_f32_bf16 v4, v81, v7, v4\n"
"v_dot2_f32_bf16 v5, v82, v7, v5\n"
"v_dot2_f32_bf16 v4, v83, v7, v4\n"
"s_waitcnt vmcnt(0)\n"
"v_dot2_f32_bf16 v5, v84, v7, v5\n"
"v_dot2_f32_bf16 v4, v85, v7, v4\n"
"v_dot2_f32_bf16 v5, v86, v7, v5\n"
"v_dot2_f32_bf16 v4, v87, v7, v4\n"
"s_nop 2\n"
"v_add_f32 v4, v4, v5\n"
"v_mov_b32 v6, v4\n"
"s_nop 1\n"
"v_permlane32_swap_b32 v4, v6\n"
"v_add_f32 v4, v4, v6\n"
"v_mov_b32 v6, v4\n"
"s_nop 1\n"
"v_permlane16_swap_b32 v4, v6\n"
"v_add_f32 v4, v4, v6\n"
"v_mul_f32 v8, 0x3ae38e39, v4\n"
"v_mov_b64 v[12:13], 0\n"
"v_lshlrev_b32 v88, 16, v16\n"
"v_and_b32 v89, s55, v16\n"
"v_pk_add_f32 v[88:89], v[88:89], v[8:9] op_sel_hi:[1,0] neg_lo:[0,1] neg_hi:[0,1]\n"
"v_pk_fma_f32 v[12:13], v[88:89], v[88:89], v[12:13]\n"
"v_lshlrev_b32 v90, 16, v17\n"
"v_and_b32 v91, s55, v17\n"
"v_pk_add_f32 v[90:91], v[90:91], v[8:9] op_sel_hi:[1,0] neg_lo:[0,1] neg_hi:[0,1]\n"
"v_pk_fma_f32 v[12:13], v[90:91], v[90:91], v[12:13]\n"
"v_lshlrev_b32 v92, 16, v18\n"
"v_and_b32 v93, s55, v18\n"
"v_pk_add_f32 v[92:93], v[92:93], v[8:9] op_sel_hi:[1,0] neg_lo:[0,1] neg_hi:[0,1]\n"
"v_pk_fma_f32 v[12:13], v[92:93], v[92:93], v[12:13]\n"
"v_lshlrev_b32 v94, 16, v19\n"
"v_and_b32 v95, s55, v19\n"
"v_pk_add_f32 v[94:95], v[94:95], v[8:9] op_sel_hi:[1,0] neg_lo:[0,1] neg_hi:[0,1]\n"
"v_pk_fma_f32 v[12:13], v[94:95], v[94:95], v[12:13]\n"
"v_lshlrev_b32 v96, 16, v20\n"
"v_and_b32 v97, s55, v20\n"
"v_pk_add_f32 v[96:97], v[96:97], v[8:9] op_sel_hi:[1,0] neg_lo:[0,1] neg_hi:[0,1]\n"
"v_pk_fma_f32 v[12:13], v[96:97], v[96:97], v[12:13]\n"
"v_lshlrev_b32 v98, 16, v21\n"
"v_and_b32 v99, s55, v21\n"
"v_pk_add_f32 v[98:99], v[98:99], v[8:9] op_sel_hi:[1,0] neg_lo:[0,1] neg_hi:[0,1]\n"
"v_pk_fma_f32 v[12:13], v[98:99], v[98:99], v[12:13]\n"
"v_lshlrev_b32 v100, 16, v22\n"
"v_and_b32 v101, s55, v22\n"
"v_pk_add_f32 v[100:101], v[100:101], v[8:9] op_sel_hi:[1,0] neg_lo:[0,1] neg_hi:[0,1]\n"
"v_pk_fma_f32 v[12:13], v[100:101], v[100:101], v[12:13]\n"
"v_lshlrev_b32 v102, 16, v23\n"
"v_and_b32 v103, s55, v23\n"
"v_pk_add_f32 v[102:103], v[102:103], v[8:9] op_sel_hi:[1,0] neg_lo:[0,1] neg_hi:[0,1]\n"
"v_pk_fma_f32 v[12:13], v[102:103], v[102:103], v[12:13]\n"
"v_lshlrev_b32 v104, 16, v24\n"
"v_and_b32 v105, s55, v24\n"
"v_pk_add_f32 v[104:105], v[104:105], v[8:9] op_sel_hi:[1,0] neg_lo:[0,1] neg_hi:[0,1]\n"
"v_pk_fma_f32 v[12:13], v[104:105], v[104:105], v[12:13]\n"
"v_lshlrev_b32 v106, 16, v25\n"
"v_and_b32 v107, s55, v25\n"
"v_pk_add_f32 v[106:107], v[106:107], v[8:9] op_sel_hi:[1,0] neg_lo:[0,1] neg_hi:[0,1]\n"
"v_pk_fma_f32 v[12:13], v[106:107], v[106:107], v[12:13]\n"
"v_lshlrev_b32 v108, 16, v26\n"
"v_and_b32 v109, s55, v26\n"
"v_pk_add_f32 v[108:109], v[108:109], v[8:9] op_sel_hi:[1,0] neg_lo:[0,1] neg_hi:[0,1]\n"
"v_pk_fma_f32 v[12:13], v[108:109], v[108:109], v[12:13]\n"
"v_lshlrev_b32 v110, 16, v27\n"
"v_and_b32 v111, s55, v27\n"
"v_pk_add_f32 v[110:111], v[110:111], v[8:9] op_sel_hi:[1,0] neg_lo:[0,1] neg_hi:[0,1]\n"
"v_pk_fma_f32 v[12:13], v[110:111], v[110:111], v[12:13]\n"
"v_lshlrev_b32 v112, 16, v28\n"
"v_and_b32 v113, s55, v28\n"
"v_pk_add_f32 v[112:113], v[112:113], v[8:9] op_sel_hi:[1,0] neg_lo:[0,1] neg_hi:[0,1]\n"
"v_pk_fma_f32 v[12:13], v[112:113], v[112:113], v[12:13]\n"
"v_lshlrev_b32 v114, 16, v29\n"
"v_and_b32 v115, s55, v29\n"
"v_pk_add_f32 v[114:115], v[114:115], v[8:9] op_sel_hi:[1,0] neg_lo:[0,1] neg_hi:[0,1]\n"
"v_pk_fma_f32 v[12:13], v[114:115], v[114:115], v[12:13]\n"
"v_lshlrev_b32 v116, 16, v30\n"
"v_and_b32 v117, s55, v30\n"
"v_pk_add_f32 v[116:117], v[116:117], v[8:9] op_sel_hi:[1,0] neg_lo:[0,1] neg_hi:[0,1]\n"
"v_pk_fma_f32 v[12:13], v[116:117], v[116:117], v[12:13]\n"
"v_lshlrev_b32 v118, 16, v31\n"
"v_and_b32 v119, s55, v31\n"
"v_pk_add_f32 v[118:119], v[118:119], v[8:9] op_sel_hi:[1,0] neg_lo:[0,1] neg_hi:[0,1]\n"
"v_pk_fma_f32 v[12:13], v[118:119], v[118:119], v[12:13]\n"
"v_lshlrev_b32 v120, 16, v32\n"
"v_and_b32 v121, s55, v32\n"
"v_pk_add_f32 v[120:121], v[120:121], v[8:9] op_sel_hi:[1,0] neg_lo:[0,1] neg_hi:[0,1]\n"
"v_pk_fma_f32 v[12:13], v[120:121], v[120:121], v[12:13]\n"
"v_lshlrev_b32 v122, 16, v33\n"
"v_and_b32 v123, s55, v33\n"
"v_pk_add_f32 v[122:123], v[122:123], v[8:9] op_sel_hi:[1,0] neg_lo:[0,1] neg_hi:[0,1]\n"
"v_pk_fma_f32 v[12:13], v[122:123], v[122:123], v[12:13]\n"
"v_lshlrev_b32 v124, 16, v34\n"
"v_and_b32 v125, s55, v34\n"
"v_pk_add_f32 v[124:125], v[124:125], v[8:9] op_sel_hi:[1,0] neg_lo:[0,1] neg_hi:[0,1]\n"
"v_pk_fma_f32 v[12:13], v[124:125], v[124:125], v[12:13]\n"
"v_lshlrev_b32 v126, 16, v35\n"
"v_and_b32 v127, s55, v35\n"
"v_pk_add_f32 v[126:127], v[126:127], v[8:9] op_sel_hi:[1,0] neg_lo:[0,1] neg_hi:[0,1]\n"
"v_pk_fma_f32 v[12:13], v[126:127], v[126:127], v[12:13]\n"
"v_lshlrev_b32 v128, 16, v36\n"
"v_and_b32 v129, s55, v36\n"
"v_pk_add_f32 v[128:129], v[128:129], v[8:9] op_sel_hi:[1,0] neg_lo:[0,1] neg_hi:[0,1]\n"
"v_pk_fma_f32 v[12:13], v[128:129], v[128:129], v[12:13]\n"
"v_lshlrev_b32 v130, 16, v37\n"
"v_and_b32 v131, s55, v37\n"
"v_pk_add_f32 v[130:131], v[130:131], v[8:9] op_sel_hi:[1,0] neg_lo:[0,1] neg_hi:[0,1]\n"
"v_pk_fma_f32 v[12:13], v[130:131], v[130:131], v[12:13]\n"
"v_lshlrev_b32 v132, 16, v38\n"
"v_and_b32 v133, s55, v38\n"
"v_pk_add_f32 v[132:133], v[132:133], v[8:9] op_sel_hi:[1,0] neg_lo:[0,1] neg_hi:[0,1]\n"
"v_pk_fma_f32 v[12:13], v[132:133], v[132:133], v[12:13]\n"
"v_lshlrev_b32 v134, 16, v39\n"
"v_and_b32 v135, s55, v39\n"
"v_pk_add_f32 v[134:135], v[134:135], v[8:9] op_sel_hi:[1,0] neg_lo:[0,1] neg_hi:[0,1]\n"
"v_pk_fma_f32 v[12:13], v[134:135], v[134:135], v[12:13]\n"
"v_lshlrev_b32 v136, 16, v40\n"
"v_and_b32 v137, s55, v40\n"
"v_pk_add_f32 v[136:137], v[136:137], v[8:9] op_sel_hi:[1,0] neg_lo:[0,1] neg_hi:[0,1]\n"
"v_pk_fma_f32 v[12:13], v[136:137], v[136:137], v[12:13]\n"
"v_lshlrev_b32 v138, 16, v41\n"
"v_and_b32 v139, s55, v41\n"
"v_pk_add_f32 v[138:139], v[138:139], v[8:9] op_sel_hi:[1,0] neg_lo:[0,1] neg_hi:[0,1]\n"
"v_pk_fma_f32 v[12:13], v[138:139], v[138:139], v[12:13]\n"
"v_lshlrev_b32 v140, 16, v42\n"
"v_and_b32 v141, s55, v42\n"
"v_pk_add_f32 v[140:141], v[140:141], v[8:9] op_sel_hi:[1,0] neg_lo:[0,1] neg_hi:[0,1]\n"
"v_pk_fma_f32 v[12:13], v[140:141], v[140:141], v[12:13]\n"
"v_lshlrev_b32 v142, 16, v43\n"
"v_and_b32 v143, s55, v43\n"
"v_pk_add_f32 v[142:143], v[142:143], v[8:9] op_sel_hi:[1,0] neg_lo:[0,1] neg_hi:[0,1]\n"
"v_pk_fma_f32 v[12:13], v[142:143], v[142:143], v[12:13]\n"
"v_lshlrev_b32 v144, 16, v44\n"
"v_and_b32 v145, s55, v44\n"
"v_pk_add_f32 v[144:145], v[144:145], v[8:9] op_sel_hi:[1,0] neg_lo:[0,1] neg_hi:[0,1]\n"
"v_pk_fma_f32 v[12:13], v[144:145], v[144:145], v[12:13]\n"
"v_lshlrev_b32 v146, 16, v45\n"
"v_and_b32 v147, s55, v45\n"
"v_pk_add_f32 v[146:147], v[146:147], v[8:9] op_sel_hi:[1,0] neg_lo:[0,1] neg_hi:[0,1]\n"
"v_pk_fma_f32 v[12:13], v[146:147], v[146:147], v[12:13]\n"
"v_lshlrev_b32 v148, 16, v46\n"
"v_and_b32 v149, s55, v46\n"
"v_pk_add_f32 v[148:149], v[148:149], v[8:9] op_sel_hi:[1,0] neg_lo:[0,1] neg_hi:[0,1]\n"
"v_pk_fma_f32 v[12:13], v[148:149], v[148:149], v[12:13]\n"
"v_lshlrev_b32 v150, 16, v47\n"
"v_and_b32 v151, s55, v47\n"
"v_pk_add_f32 v[150:151], v[150:151], v[8:9] op_sel_hi:[1,0] neg_lo:[0,1] neg_hi:[0,1]\n"
"v_pk_fma_f32 v[12:13], v[150:151], v[150:151], v[12:13]\n"
"v_lshlrev_b32 v152, 16, v48\n"
"v_and_b32 v153, s55, v48\n"
"v_pk_add_f32 v[152:153], v[152:153], v[8:9] op_sel_hi:[1,0] neg_lo:[0,1] neg_hi:[0,1]\n"
"v_pk_fma_f32 v[12:13], v[152:153], v[152:153], v[12:13]\n"
"v_lshlrev_b32 v154, 16, v49\n"
"v_and_b32 v155, s55, v49\n"
"v_pk_add_f32 v[154:155], v[154:155], v[8:9] op_sel_hi:[1,0] neg_lo:[0,1] neg_hi:[0,1]\n"
"v_pk_fma_f32 v[12:13], v[154:155], v[154:155], v[12:13]\n"
"v_lshlrev_b32 v156, 16, v50\n"
"v_and_b32 v157, s55, v50\n"
"v_pk_add_f32 v[156:157], v[156:157], v[8:9] op_sel_hi:[1,0] neg_lo:[0,1] neg_hi:[0,1]\n"
"v_pk_fma_f32 v[12:13], v[156:157], v[156:157], v[12:13]\n"
"v_lshlrev_b32 v158, 16, v51\n"
"v_and_b32 v159, s55, v51\n"
"v_pk_add_f32 v[158:159], v[158:159], v[8:9] op_sel_hi:[1,0] neg_lo:[0,1] neg_hi:[0,1]\n"
"v_pk_fma_f32 v[12:13], v[158:159], v[158:159], v[12:13]\n"
"v_lshlrev_b32 v160, 16, v52\n"
"v_and_b32 v161, s55, v52\n"
"v_pk_add_f32 v[160:161], v[160:161], v[8:9] op_sel_hi:[1,0] neg_lo:[0,1] neg_hi:[0,1]\n"
"v_pk_fma_f32 v[12:13], v[160:161], v[160:161], v[12:13]\n"
"v_lshlrev_b32 v162, 16, v53\n"
"v_and_b32 v163, s55, v53\n"
"v_pk_add_f32 v[162:163], v[162:163], v[8:9] op_sel_hi:[1,0] neg_lo:[0,1] neg_hi:[0,1]\n"
"v_pk_fma_f32 v[12:13], v[162:163], v[162:163], v[12:13]\n"
"v_lshlrev_b32 v164, 16, v54\n"
"v_and_b32 v165, s55, v54\n"
"v_pk_add_f32 v[164:165], v[164:165], v[8:9] op_sel_hi:[1,0] neg_lo:[0,1] neg_hi:[0,1]\n"
"v_pk_fma_f32 v[12:13], v[164:165], v[164:165], v[12:13]\n"
"v_lshlrev_b32 v166, 16, v55\n"
"v_and_b32 v167, s55, v55\n"
"v_pk_add_f32 v[166:167], v[166:167], v[8:9] op_sel_hi:[1,0] neg_lo:[0,1] neg_hi:[0,1]\n"
"v_pk_fma_f32 v[12:13], v[166:167], v[166:167], v[12:13]\n"
"v_lshlrev_b32 v168, 16, v56\n"
"v_and_b32 v169, s55, v56\n"
"v_pk_add_f32 v[168:169], v[168:169], v[8:9] op_sel_hi:[1,0] neg_lo:[0,1] neg_hi:[0,1]\n"
"v_pk_fma_f32 v[12:13], v[168:169], v[168:169], v[12:13]\n"
"v_lshlrev_b32 v170, 16, v57\n"
"v_and_b32 v171, s55, v57\n"
"v_pk_add_f32 v[170:171], v[170:171], v[8:9] op_sel_hi:[1,0] neg_lo:[0,1] neg_hi:[0,1]\n"
"v_pk_fma_f32 v[12:13], v[170:171], v[170:171], v[12:13]\n"
"v_lshlrev_b32 v172, 16, v58\n"
"v_and_b32 v173, s55, v58\n"
"v_pk_add_f32 v[172:173], v[172:173], v[8:9] op_sel_hi:[1,0] neg_lo:[0,1] neg_hi:[0,1]\n"
"v_pk_fma_f32 v[12:13], v[172:173], v[172:173], v[12:13]\n"
"v_lshlrev_b32 v174, 16, v59\n"
"v_and_b32 v175, s55, v59\n"
"v_pk_add_f32 v[174:175], v[174:175], v[8:9] op_sel_hi:[1,0] neg_lo:[0,1] neg_hi:[0,1]\n"
"v_pk_fma_f32 v[12:13], v[174:175], v[174:175], v[12:13]\n"
"v_lshlrev_b32 v176, 16, v60\n"
"v_and_b32 v177, s55, v60\n"
"v_pk_add_f32 v[176:177], v[176:177], v[8:9] op_sel_hi:[1,0] neg_lo:[0,1] neg_hi:[0,1]\n"
"v_pk_fma_f32 v[12:13], v[176:177], v[176:177], v[12:13]\n"
"v_lshlrev_b32 v178, 16, v61\n"
"v_and_b32 v179, s55, v61\n"
"v_pk_add_f32 v[178:179], v[178:179], v[8:9] op_sel_hi:[1,0] neg_lo:[0,1] neg_hi:[0,1]\n"
"v_pk_fma_f32 v[12:13], v[178:179], v[178:179], v[12:13]\n"
"v_lshlrev_b32 v180, 16, v62\n"
"v_and_b32 v181, s55, v62\n"
"v_pk_add_f32 v[180:181], v[180:181], v[8:9] op_sel_hi:[1,0] neg_lo:[0,1] neg_hi:[0,1]\n"
"v_pk_fma_f32 v[12:13], v[180:181], v[180:181], v[12:13]\n"
"v_lshlrev_b32 v182, 16, v63\n"
"v_and_b32 v183, s55, v63\n"
"v_pk_add_f32 v[182:183], v[182:183], v[8:9] op_sel_hi:[1,0] neg_lo:[0,1] neg_hi:[0,1]\n"
"v_pk_fma_f32 v[12:13], v[182:183], v[182:183], v[12:13]\n"
"v_lshlrev_b32 v184, 16, v64\n"
"v_and_b32 v185, s55, v64\n"
"v_pk_add_f32 v[184:185], v[184:185], v[8:9] op_sel_hi:[1,0] neg_lo:[0,1] neg_hi:[0,1]\n"
"v_pk_fma_f32 v[12:13], v[184:185], v[184:185], v[12:13]\n"
"v_lshlrev_b32 v186, 16, v65\n"
"v_and_b32 v187, s55, v65\n"
"v_pk_add_f32 v[186:187], v[186:187], v[8:9] op_sel_hi:[1,0] neg_lo:[0,1] neg_hi:[0,1]\n"
"v_pk_fma_f32 v[12:13], v[186:187], v[186:187], v[12:13]\n"
"v_lshlrev_b32 v188, 16, v66\n"
"v_and_b32 v189, s55, v66\n"
"v_pk_add_f32 v[188:189], v[188:189], v[8:9] op_sel_hi:[1,0] neg_lo:[0,1] neg_hi:[0,1]\n"
"v_pk_fma_f32 v[12:13], v[188:189], v[188:189], v[12:13]\n"
"v_lshlrev_b32 v190, 16, v67\n"
"v_and_b32 v191, s55, v67\n"
"v_pk_add_f32 v[190:191], v[190:191], v[8:9] op_sel_hi:[1,0] neg_lo:[0,1] neg_hi:[0,1]\n"
"v_pk_fma_f32 v[12:13], v[190:191], v[190:191], v[12:13]\n"
"v_lshlrev_b32 v192, 16, v68\n"
"v_and_b32 v193, s55, v68\n"
"v_pk_add_f32 v[192:193], v[192:193], v[8:9] op_sel_hi:[1,0] neg_lo:[0,1] neg_hi:[0,1]\n"
"v_pk_fma_f32 v[12:13], v[192:193], v[192:193], v[12:13]\n"
"v_lshlrev_b32 v194, 16, v69\n"
"v_and_b32 v195, s55, v69\n"
"v_pk_add_f32 v[194:195], v[194:195], v[8:9] op_sel_hi:[1,0] neg_lo:[0,1] neg_hi:[0,1]\n"
"v_pk_fma_f32 v[12:13], v[194:195], v[194:195], v[12:13]\n"
"v_lshlrev_b32 v196, 16, v70\n"
"v_and_b32 v197, s55, v70\n"
"v_pk_add_f32 v[196:197], v[196:197], v[8:9] op_sel_hi:[1,0] neg_lo:[0,1] neg_hi:[0,1]\n"
"v_pk_fma_f32 v[12:13], v[196:197], v[196:197], v[12:13]\n"
"v_lshlrev_b32 v198, 16, v71\n"
"v_and_b32 v199, s55, v71\n"
"v_pk_add_f32 v[198:199], v[198:199], v[8:9] op_sel_hi:[1,0] neg_lo:[0,1] neg_hi:[0,1]\n"
"v_pk_fma_f32 v[12:13], v[198:199], v[198:199], v[12:13]\n"
"v_lshlrev_b32 v200, 16, v72\n"
"v_and_b32 v201, s55, v72\n"
"v_pk_add_f32 v[200:201], v[200:201], v[8:9] op_sel_hi:[1,0] neg_lo:[0,1] neg_hi:[0,1]\n"
"v_pk_fma_f32 v[12:13], v[200:201], v[200:201], v[12:13]\n"
"v_lshlrev_b32 v202, 16, v73\n"
"v_and_b32 v203, s55, v73\n"
"v_pk_add_f32 v[202:203], v[202:203], v[8:9] op_sel_hi:[1,0] neg_lo:[0,1] neg_hi:[0,1]\n"
"v_pk_fma_f32 v[12:13], v[202:203], v[202:203], v[12:13]\n"
"v_lshlrev_b32 v204, 16, v74\n"
"v_and_b32 v205, s55, v74\n"
"v_pk_add_f32 v[204:205], v[204:205], v[8:9] op_sel_hi:[1,0] neg_lo:[0,1] neg_hi:[0,1]\n"
"v_pk_fma_f32 v[12:13], v[204:205], v[204:205], v[12:13]\n"
"v_lshlrev_b32 v206, 16, v75\n"
"v_and_b32 v207, s55, v75\n"
"v_pk_add_f32 v[206:207], v[206:207], v[8:9] op_sel_hi:[1,0] neg_lo:[0,1] neg_hi:[0,1]\n"
"v_pk_fma_f32 v[12:13], v[206:207], v[206:207], v[12:13]\n"
"v_lshlrev_b32 v208, 16, v76\n"
"v_and_b32 v209, s55, v76\n"
"v_pk_add_f32 v[208:209], v[208:209], v[8:9] op_sel_hi:[1,0] neg_lo:[0,1] neg_hi:[0,1]\n"
"v_pk_fma_f32 v[12:13], v[208:209], v[208:209], v[12:13]\n"
"v_lshlrev_b32 v210, 16, v77\n"
"v_and_b32 v211, s55, v77\n"
"v_pk_add_f32 v[210:211], v[210:211], v[8:9] op_sel_hi:[1,0] neg_lo:[0,1] neg_hi:[0,1]\n"
"v_pk_fma_f32 v[12:13], v[210:211], v[210:211], v[12:13]\n"
"v_lshlrev_b32 v212, 16, v78\n"
"v_and_b32 v213, s55, v78\n"
"v_pk_add_f32 v[212:213], v[212:213], v[8:9] op_sel_hi:[1,0] neg_lo:[0,1] neg_hi:[0,1]\n"
"v_pk_fma_f32 v[12:13], v[212:213], v[212:213], v[12:13]\n"
"v_lshlrev_b32 v214, 16, v79\n"
"v_and_b32 v215, s55, v79\n"
"v_pk_add_f32 v[214:215], v[214:215], v[8:9] op_sel_hi:[1,0] neg_lo:[0,1] neg_hi:[0,1]\n"
"v_pk_fma_f32 v[12:13], v[214:215], v[214:215], v[12:13]\n"
"v_lshlrev_b32 v216, 16, v80\n"
"v_and_b32 v217, s55, v80\n"
"v_pk_add_f32 v[216:217], v[216:217], v[8:9] op_sel_hi:[1,0] neg_lo:[0,1] neg_hi:[0,1]\n"
"v_pk_fma_f32 v[12:13], v[216:217], v[216:217], v[12:13]\n"
"v_lshlrev_b32 v218, 16, v81\n"
"v_and_b32 v219, s55, v81\n"
"v_pk_add_f32 v[218:219], v[218:219], v[8:9] op_sel_hi:[1,0] neg_lo:[0,1] neg_hi:[0,1]\n"
"v_pk_fma_f32 v[12:13], v[218:219], v[218:219], v[12:13]\n"
"v_lshlrev_b32 v220, 16, v82\n"
"v_and_b32 v221, s55, v82\n"
"v_pk_add_f32 v[220:221], v[220:221], v[8:9] op_sel_hi:[1,0] neg_lo:[0,1] neg_hi:[0,1]\n"
"v_pk_fma_f32 v[12:13], v[220:221], v[220:221], v[12:13]\n"
"v_lshlrev_b32 v222, 16, v83\n"
"v_and_b32 v223, s55, v83\n"
"v_pk_add_f32 v[222:223], v[222:223], v[8:9] op_sel_hi:[1,0] neg_lo:[0,1] neg_hi:[0,1]\n"
"v_pk_fma_f32 v[12:13], v[222:223], v[222:223], v[12:13]\n"
"v_lshlrev_b32 v224, 16, v84\n"
"v_and_b32 v225, s55, v84\n"
"v_pk_add_f32 v[224:225], v[224:225], v[8:9] op_sel_hi:[1,0] neg_lo:[0,1] neg_hi:[0,1]\n"
"v_pk_fma_f32 v[12:13], v[224:225], v[224:225], v[12:13]\n"
"v_lshlrev_b32 v226, 16, v85\n"
"v_and_b32 v227, s55, v85\n"
"v_pk_add_f32 v[226:227], v[226:227], v[8:9] op_sel_hi:[1,0] neg_lo:[0,1] neg_hi:[0,1]\n"
"v_pk_fma_f32 v[12:13], v[226:227], v[226:227], v[12:13]\n"
"v_lshlrev_b32 v228, 16, v86\n"
"v_and_b32 v229, s55, v86\n"
"v_pk_add_f32 v[228:229], v[228:229], v[8:9] op_sel_hi:[1,0] neg_lo:[0,1] neg_hi:[0,1]\n"
"v_pk_fma_f32 v[12:13], v[228:229], v[228:229], v[12:13]\n"
"v_lshlrev_b32 v230, 16, v87\n"
"v_and_b32 v231, s55, v87\n"
"v_pk_add_f32 v[230:231], v[230:231], v[8:9] op_sel_hi:[1,0] neg_lo:[0,1] neg_hi:[0,1]\n"
"v_pk_fma_f32 v[12:13], v[230:231], v[230:231], v[12:13]\n"
"v_add_f32 v4, v12, v13\n"
"v_mov_b32 v6, v4\n"
"s_nop 1\n"
"v_permlane32_swap_b32 v4, v6\n"
"v_add_f32 v4, v4, v6\n"
"v_mov_b32 v6, v4\n"
"s_nop 1\n"
"v_permlane16_swap_b32 v4, v6\n"
"v_add_f32 v4, v4, v6\n"
"v_mov_b32 v6, 0x3727c5ac\n"
"v_fmac_f32 v6, 0x3ae38e39, v4\n"
"v_rsq_f32 v10, v6\n"
"s_mul_i32 s36, s53, 18432\n"
"s_add_u32 s36, s36, 165675008\n"
"v_pk_mul_f32 v[88:89], v[88:89], v[10:11] op_sel_hi:[1,0]\n"
"v_cvt_pk_bf16_f32 v16, v88, v89\n"
"v_pk_mul_f32 v[90:91], v[90:91], v[10:11] op_sel_hi:[1,0]\n"
"v_cvt_pk_bf16_f32 v17, v90, v91\n"
"v_pk_mul_f32 v[92:93], v[92:93], v[10:11] op_sel_hi:[1,0]\n"
"v_cvt_pk_bf16_f32 v18, v92, v93\n"
"v_pk_mul_f32 v[94:95], v[94:95], v[10:11] op_sel_hi:[1,0]\n"
"v_cvt_pk_bf16_f32 v19, v94, v95\n"
"buffer_store_dwordx4 v[16:19], v2, s[24:27], s36 offen offset:0 sc1\n"
"v_pk_mul_f32 v[96:97], v[96:97], v[10:11] op_sel_hi:[1,0]\n"
"v_cvt_pk_bf16_f32 v20, v96, v97\n"
"v_pk_mul_f32 v[98:99], v[98:99], v[10:11] op_sel_hi:[1,0]\n"
"v_cvt_pk_bf16_f32 v21, v98, v99\n"
"v_pk_mul_f32 v[100:101], v[100:101], v[10:11] op_sel_hi:[1,0]\n"
"v_cvt_pk_bf16_f32 v22, v100, v101\n"
"v_pk_mul_f32 v[102:103], v[102:103], v[10:11] op_sel_hi:[1,0]\n"
"v_cvt_pk_bf16_f32 v23, v102, v103\n"
"buffer_store_dwordx4 v[20:23], v2, s[24:27], s36 offen offset:1024 sc1\n"
"v_pk_mul_f32 v[104:105], v[104:105], v[10:11] op_sel_hi:[1,0]\n"
"v_cvt_pk_bf16_f32 v24, v104, v105\n"
"v_pk_mul_f32 v[106:107], v[106:107], v[10:11] op_sel_hi:[1,0]\n"
"v_cvt_pk_bf16_f32 v25, v106, v107\n"
"v_pk_mul_f32 v[108:109], v[108:109], v[10:11] op_sel_hi:[1,0]\n"
"v_cvt_pk_bf16_f32 v26, v108, v109\n"
"v_pk_mul_f32 v[110:111], v[110:111], v[10:11] op_sel_hi:[1,0]\n"
"v_cvt_pk_bf16_f32 v27, v110, v111\n"
"buffer_store_dwordx4 v[24:27], v2, s[24:27], s36 offen offset:2048 sc1\n"
"v_pk_mul_f32 v[112:113], v[112:113], v[10:11] op_sel_hi:[1,0]\n"
"v_cvt_pk_bf16_f32 v28, v112, v113\n"
"v_pk_mul_f32 v[114:115], v[114:115], v[10:11] op_sel_hi:[1,0]\n"
"v_cvt_pk_bf16_f32 v29, v114, v115\n"
"v_pk_mul_f32 v[116:117], v[116:117], v[10:11] op_sel_hi:[1,0]\n"
"v_cvt_pk_bf16_f32 v30, v116, v117\n"
"v_pk_mul_f32 v[118:119], v[118:119], v[10:11] op_sel_hi:[1,0]\n"
"v_cvt_pk_bf16_f32 v31, v118, v119\n"
"buffer_store_dwordx4 v[28:31], v2, s[24:27], s36 offen offset:3072 sc1\n"
"s_add_u32 s36, s36, 4096\n"
"v_pk_mul_f32 v[120:121], v[120:121], v[10:11] op_sel_hi:[1,0]\n"
"v_cvt_pk_bf16_f32 v32, v120, v121\n"
"v_pk_mul_f32 v[122:123], v[122:123], v[10:11] op_sel_hi:[1,0]\n"
"v_cvt_pk_bf16_f32 v33, v122, v123\n"
"v_pk_mul_f32 v[124:125], v[124:125], v[10:11] op_sel_hi:[1,0]\n"
"v_cvt_pk_bf16_f32 v34, v124, v125\n"
"v_pk_mul_f32 v[126:127], v[126:127], v[10:11] op_sel_hi:[1,0]\n"
"v_cvt_pk_bf16_f32 v35, v126, v127\n"
"buffer_store_dwordx4 v[32:35], v2, s[24:27], s36 offen offset:0 sc1\n"
"v_pk_mul_f32 v[128:129], v[128:129], v[10:11] op_sel_hi:[1,0]\n"
"v_cvt_pk_bf16_f32 v36, v128, v129\n"
"v_pk_mul_f32 v[130:131], v[130:131], v[10:11] op_sel_hi:[1,0]\n"
"v_cvt_pk_bf16_f32 v37, v130, v131\n"
"v_pk_mul_f32 v[132:133], v[132:133], v[10:11] op_sel_hi:[1,0]\n"
"v_cvt_pk_bf16_f32 v38, v132, v133\n"
"v_pk_mul_f32 v[134:135], v[134:135], v[10:11] op_sel_hi:[1,0]\n"
"v_cvt_pk_bf16_f32 v39, v134, v135\n"
"buffer_store_dwordx4 v[36:39], v2, s[24:27], s36 offen offset:1024 sc1\n"
"v_pk_mul_f32 v[136:137], v[136:137], v[10:11] op_sel_hi:[1,0]\n"
"v_cvt_pk_bf16_f32 v40, v136, v137\n"
"v_pk_mul_f32 v[138:139], v[138:139], v[10:11] op_sel_hi:[1,0]\n"
"v_cvt_pk_bf16_f32 v41, v138, v139\n"
"v_pk_mul_f32 v[140:141], v[140:141], v[10:11] op_sel_hi:[1,0]\n"
"v_cvt_pk_bf16_f32 v42, v140, v141\n"
"v_pk_mul_f32 v[142:143], v[142:143], v[10:11] op_sel_hi:[1,0]\n"
"v_cvt_pk_bf16_f32 v43, v142, v143\n"
"buffer_store_dwordx4 v[40:43], v2, s[24:27], s36 offen offset:2048 sc1\n"
"v_pk_mul_f32 v[144:145], v[144:145], v[10:11] op_sel_hi:[1,0]\n"
"v_cvt_pk_bf16_f32 v44, v144, v145\n"
"v_pk_mul_f32 v[146:147], v[146:147], v[10:11] op_sel_hi:[1,0]\n"
"v_cvt_pk_bf16_f32 v45, v146, v147\n"
"v_pk_mul_f32 v[148:149], v[148:149], v[10:11] op_sel_hi:[1,0]\n"
"v_cvt_pk_bf16_f32 v46, v148, v149\n"
"v_pk_mul_f32 v[150:151], v[150:151], v[10:11] op_sel_hi:[1,0]\n"
"v_cvt_pk_bf16_f32 v47, v150, v151\n"
"buffer_store_dwordx4 v[44:47], v2, s[24:27], s36 offen offset:3072 sc1\n"
"s_add_u32 s36, s36, 4096\n"
"v_pk_mul_f32 v[152:153], v[152:153], v[10:11] op_sel_hi:[1,0]\n"
"v_cvt_pk_bf16_f32 v48, v152, v153\n"
"v_pk_mul_f32 v[154:155], v[154:155], v[10:11] op_sel_hi:[1,0]\n"
"v_cvt_pk_bf16_f32 v49, v154, v155\n"
"v_pk_mul_f32 v[156:157], v[156:157], v[10:11] op_sel_hi:[1,0]\n"
"v_cvt_pk_bf16_f32 v50, v156, v157\n"
"v_pk_mul_f32 v[158:159], v[158:159], v[10:11] op_sel_hi:[1,0]\n"
"v_cvt_pk_bf16_f32 v51, v158, v159\n"
"buffer_store_dwordx4 v[48:51], v2, s[24:27], s36 offen offset:0 sc1\n"
"v_pk_mul_f32 v[160:161], v[160:161], v[10:11] op_sel_hi:[1,0]\n"
"v_cvt_pk_bf16_f32 v52, v160, v161\n"
"v_pk_mul_f32 v[162:163], v[162:163], v[10:11] op_sel_hi:[1,0]\n"
"v_cvt_pk_bf16_f32 v53, v162, v163\n"
"v_pk_mul_f32 v[164:165], v[164:165], v[10:11] op_sel_hi:[1,0]\n"
"v_cvt_pk_bf16_f32 v54, v164, v165\n"
"v_pk_mul_f32 v[166:167], v[166:167], v[10:11] op_sel_hi:[1,0]\n"
"v_cvt_pk_bf16_f32 v55, v166, v167\n"
"buffer_store_dwordx4 v[52:55], v2, s[24:27], s36 offen offset:1024 sc1\n"
"v_pk_mul_f32 v[168:169], v[168:169], v[10:11] op_sel_hi:[1,0]\n"
"v_cvt_pk_bf16_f32 v56, v168, v169\n"
"v_pk_mul_f32 v[170:171], v[170:171], v[10:11] op_sel_hi:[1,0]\n"
"v_cvt_pk_bf16_f32 v57, v170, v171\n"
"v_pk_mul_f32 v[172:173], v[172:173], v[10:11] op_sel_hi:[1,0]\n"
"v_cvt_pk_bf16_f32 v58, v172, v173\n"
"v_pk_mul_f32 v[174:175], v[174:175], v[10:11] op_sel_hi:[1,0]\n"
"v_cvt_pk_bf16_f32 v59, v174, v175\n"
"buffer_store_dwordx4 v[56:59], v2, s[24:27], s36 offen offset:2048 sc1\n"
"v_pk_mul_f32 v[176:177], v[176:177], v[10:11] op_sel_hi:[1,0]\n"
"v_cvt_pk_bf16_f32 v60, v176, v177\n"
"v_pk_mul_f32 v[178:179], v[178:179], v[10:11] op_sel_hi:[1,0]\n"
"v_cvt_pk_bf16_f32 v61, v178, v179\n"
"v_pk_mul_f32 v[180:181], v[180:181], v[10:11] op_sel_hi:[1,0]\n"
"v_cvt_pk_bf16_f32 v62, v180, v181\n"
"v_pk_mul_f32 v[182:183], v[182:183], v[10:11] op_sel_hi:[1,0]\n"
"v_cvt_pk_bf16_f32 v63, v182, v183\n"
"buffer_store_dwordx4 v[60:63], v2, s[24:27], s36 offen offset:3072 sc1\n"
"s_add_u32 s36, s36, 4096\n"
"v_pk_mul_f32 v[184:185], v[184:185], v[10:11] op_sel_hi:[1,0]\n"
"v_cvt_pk_bf16_f32 v64, v184, v185\n"
"v_pk_mul_f32 v[186:187], v[186:187], v[10:11] op_sel_hi:[1,0]\n"
"v_cvt_pk_bf16_f32 v65, v186, v187\n"
"v_pk_mul_f32 v[188:189], v[188:189], v[10:11] op_sel_hi:[1,0]\n"
"v_cvt_pk_bf16_f32 v66, v188, v189\n"
"v_pk_mul_f32 v[190:191], v[190:191], v[10:11] op_sel_hi:[1,0]\n"
"v_cvt_pk_bf16_f32 v67, v190, v191\n"
"buffer_store_dwordx4 v[64:67], v2, s[24:27], s36 offen offset:0 sc1\n"
"v_pk_mul_f32 v[192:193], v[192:193], v[10:11] op_sel_hi:[1,0]\n"
"v_cvt_pk_bf16_f32 v68, v192, v193\n"
"v_pk_mul_f32 v[194:195], v[194:195], v[10:11] op_sel_hi:[1,0]\n"
"v_cvt_pk_bf16_f32 v69, v194, v195\n"
"v_pk_mul_f32 v[196:197], v[196:197], v[10:11] op_sel_hi:[1,0]\n"
"v_cvt_pk_bf16_f32 v70, v196, v197\n"
"v_pk_mul_f32 v[198:199], v[198:199], v[10:11] op_sel_hi:[1,0]\n"
"v_cvt_pk_bf16_f32 v71, v198, v199\n"
"buffer_store_dwordx4 v[68:71], v2, s[24:27], s36 offen offset:1024 sc1\n"
"v_pk_mul_f32 v[200:201], v[200:201], v[10:11] op_sel_hi:[1,0]\n"
"v_cvt_pk_bf16_f32 v72, v200, v201\n"
"v_pk_mul_f32 v[202:203], v[202:203], v[10:11] op_sel_hi:[1,0]\n"
"v_cvt_pk_bf16_f32 v73, v202, v203\n"
"v_pk_mul_f32 v[204:205], v[204:205], v[10:11] op_sel_hi:[1,0]\n"
"v_cvt_pk_bf16_f32 v74, v204, v205\n"
"v_pk_mul_f32 v[206:207], v[206:207], v[10:11] op_sel_hi:[1,0]\n"
"v_cvt_pk_bf16_f32 v75, v206, v207\n"
"buffer_store_dwordx4 v[72:75], v2, s[24:27], s36 offen offset:2048 sc1\n"
"v_pk_mul_f32 v[208:209], v[208:209], v[10:11] op_sel_hi:[1,0]\n"
"v_cvt_pk_bf16_f32 v76, v208, v209\n"
"v_pk_mul_f32 v[210:211], v[210:211], v[10:11] op_sel_hi:[1,0]\n"
"v_cvt_pk_bf16_f32 v77, v210, v211\n"
"v_pk_mul_f32 v[212:213], v[212:213], v[10:11] op_sel_hi:[1,0]\n"
"v_cvt_pk_bf16_f32 v78, v212, v213\n"
"v_pk_mul_f32 v[214:215], v[214:215], v[10:11] op_sel_hi:[1,0]\n"
"v_cvt_pk_bf16_f32 v79, v214, v215\n"
"buffer_store_dwordx4 v[76:79], v2, s[24:27], s36 offen offset:3072 sc1\n"
"s_add_u32 s36, s36, 4096\n"
"v_pk_mul_f32 v[216:217], v[216:217], v[10:11] op_sel_hi:[1,0]\n"
"v_cvt_pk_bf16_f32 v80, v216, v217\n"
"v_pk_mul_f32 v[218:219], v[218:219], v[10:11] op_sel_hi:[1,0]\n"
"v_cvt_pk_bf16_f32 v81, v218, v219\n"
"v_pk_mul_f32 v[220:221], v[220:221], v[10:11] op_sel_hi:[1,0]\n"
"v_cvt_pk_bf16_f32 v82, v220, v221\n"
"v_pk_mul_f32 v[222:223], v[222:223], v[10:11] op_sel_hi:[1,0]\n"
"v_cvt_pk_bf16_f32 v83, v222, v223\n"
"buffer_store_dwordx4 v[80:83], v2, s[24:27], s36 offen offset:0 sc1\n"
"v_pk_mul_f32 v[224:225], v[224:225], v[10:11] op_sel_hi:[1,0]\n"
"v_cvt_pk_bf16_f32 v84, v224, v225\n"
"v_pk_mul_f32 v[226:227], v[226:227], v[10:11] op_sel_hi:[1,0]\n"
"v_cvt_pk_bf16_f32 v85, v226, v227\n"
"v_pk_mul_f32 v[228:229], v[228:229], v[10:11] op_sel_hi:[1,0]\n"
"v_cvt_pk_bf16_f32 v86, v228, v229\n"
"v_pk_mul_f32 v[230:231], v[230:231], v[10:11] op_sel_hi:[1,0]\n"
"v_cvt_pk_bf16_f32 v87, v230, v231\n"
"buffer_store_dwordx4 v[84:87], v2, s[24:27], s36 offen offset:1024 sc1\n"
"s_waitcnt vmcnt(0)\n"
"s_barrier\n"
"s_cmp_lg_u32 s20, 0\n"
"s_cbranch_scc1 Lpf_46\n"
"s_lshl_b32 s43, s19, 7\n"
"s_xor_b32 s43, s43, s22\n"
"s_xor_b32 s43, s43, 1245184\n"
"s_add_u32 s42, s21, 1\n"
"v_mov_b32 v6, s42\n"
"buffer_store_dword v6, v3, s[24:27], s43 offen sc1\n"
"Lpf_46:\n"
"s_add_u32 s21, s21, 1\n"
"s_cmp_lt_u32 s21, 2\n"
"s_cbranch_scc1 LN_loop\n"
"LN_end:\n"
"s_endpgm\n"
"LX1:\n"
"v_mov_b32 v3, 2146435072\n"
"v_cmp_eq_u32 vcc, 0, v0\n"
"s_nop 1\n"
"v_cndmask_b32 v3, v3, 0, vcc\n"
"v_lshlrev_b32 v2, 4, v0\n"
"s_mul_i32 s36, s20, 9216\n"
"v_add_u32 v1, s36, v2\n"
"s_mov_b32 s55, -1\n"
"v_min_u32 v5, 15, v0\n"
"v_lshlrev_b32 v5, 7, v5\n"
"v_lshrrev_b32 v152, 4, v0\n"
"v_mul_u32_u24 v148, 8192, v152\n"
"v_and_b32 v152, 15, v0\n"
"v_lshl_add_u32 v148, v152, 4, v148\n"
"s_mul_i32 s36, s20, 65536\n"
"v_add_u32 v148, s36, v148\n"
"v_lshlrev_b32 v149, 4, v0\n"
"s_mul_i32 s36, s20, 2048\n"
"s_add_u32 s36, s36, 73728\n"
"v_add_u32 v149, s36, v149\n"
"v_bfe_u32 v152, v0, 4, 2\n"
"v_lshlrev_b32 v152, 6, v152\n"
"v_xor_b32 v149, v149, v152\n"
"v_xor_b32 v150, 128, v149\n"
"v_lshrrev_b32 v152, 4, v0\n"
"v_lshlrev_b32 v151, 11, v152\n"
"v_bfe_u32 v152, v0, 2, 2\n"
"v_lshl_add_u32 v151, v152, 8, v151\n"
"v_bfe_u32 v152, v0, 2, 2\n"
"v_xor_b32 v152, s20, v152\n"
"v_lshl_add_u32 v151, v152, 6, v151\n"
"v_and_b32 v152, 3, v0\n"
"v_lshl_add_u32 v151, v152, 4, v151\n"
"v_add_u32 v151, 73728, v151\n"
"s_load_dwordx2 s[28:29], s[16:17], 24\n"
"s_mov_b32 s30, 4718592\n"
"s_mov_b32 s31, 0x00020000\n"
"s_waitcnt lgkmcnt(0)\n"
"s_and_b32 s29, s29, 0xffff\n"
"s_load_dwordx2 s[32:33], s[16:17], 32\n"
"s_mov_b32 s34, 4718592\n"
"s_mov_b32 s35, 0x00020000\n"
"s_waitcnt lgkmcnt(0)\n"
"s_and_b32 s33, s33, 0xffff\n"
"s_lshl_b32 s60, s19, 8\n"
"s_lshl_b32 s61, s19, 8\n"
"buffer_load_dwordx4 a[8:11], v148, s[32:35], s61 offen nt\n"
"s_add_u32 s61, s61, 32768\n"
"buffer_load_dwordx4 a[12:15], v148, s[32:35], s61 offen nt\n"
"s_add_u32 s61, s61, 229376\n"
"buffer_load_dwordx4 a[24:27], v148, s[32:35], s61 offen nt\n"
"s_add_u32 s61, s61, 32768\n"
"buffer_load_dwordx4 a[28:31], v148, s[32:35], s61 offen nt\n"
"s_add_u32 s61, s61, 229376\n"
"buffer_load_dwordx4 a[40:43], v148, s[32:35], s61 offen nt\n"
"s_add_u32 s61, s61, 32768\n"
"buffer_load_dwordx4 a[44:47], v148, s[32:35], s61 offen nt\n"
"s_add_u32 s61, s61, 229376\n"
"buffer_load_dwordx4 a[56:59], v148, s[32:35], s61 offen nt\n"
"s_add_u32 s61, s61, 32768\n"
"buffer_load_dwordx4 a[60:63], v148, s[32:35], s61 offen nt\n"
"s_add_u32 s61, s61, 229376\n"
"buffer_load_dwordx4 a[72:75], v148, s[32:35], s61 offen nt\n"
"s_add_u32 s61, s61, 32768\n"
"buffer_load_dwordx4 a[76:79], v148, s[32:35], s61 offen nt\n"
"s_add_u32 s61, s61, 229376\n"
"buffer_load_dwordx4 a[88:91], v148, s[32:35], s61 offen nt\n"
"s_add_u32 s61, s61, 32768\n"
"buffer_load_dwordx4 a[92:95], v148, s[32:35], s61 offen nt\n"
"s_add_u32 s61, s61, 229376\n"
"buffer_load_dwordx4 a[104:107], v148, s[32:35], s61 offen nt\n"
"s_add_u32 s61, s61, 32768\n"
"buffer_load_dwordx4 a[108:111], v148, s[32:35], s61 offen nt\n"
"s_add_u32 s61, s61, 229376\n"
"buffer_load_dwordx4 a[120:123], v148, s[32:35], s61 offen nt\n"
"s_add_u32 s61, s61, 32768\n"
"buffer_load_dwordx4 a[124:127], v148, s[32:35], s61 offen nt\n"
"s_add_u32 s61, s61, 229376\n"
"s_waitcnt vmcnt(14)\n"
"ds_write_b128 v149, a[8:11] offset:0\n"
"ds_write_b128 v149, a[12:15] offset:1024\n"
"s_waitcnt lgkmcnt(0)\n"
"s_barrier\n"
"ds_read_b64_tr_b16 a[8:9], v151 offset:0\n"
"ds_read_b64_tr_b16 a[10:11], v151 offset:1024\n"
"ds_read_b64_tr_b16 a[12:13], v151 offset:8\n"
"ds_read_b64_tr_b16 a[14:15], v151 offset:1032\n"
"buffer_load_dwordx4 a[136:139], v148, s[32:35], s61 offen nt\n"
"s_add_u32 s61, s61, 32768\n"
"buffer_load_dwordx4 a[140:143], v148, s[32:35], s61 offen nt\n"
"s_add_u32 s61, s61, 229376\n"
"s_waitcnt vmcnt(14)\n"
"ds_write_b128 v149, a[24:27] offset:8192\n"
"ds_write_b128 v149, a[28:31] offset:9216\n"
"s_waitcnt lgkmcnt(0)\n"
"s_barrier\n"
"ds_read_b64_tr_b16 a[24:25], v151 offset:8192\n"
"ds_read_b64_tr_b16 a[26:27], v151 offset:9216\n"
"ds_read_b64_tr_b16 a[28:29], v151 offset:8200\n"
"ds_read_b64_tr_b16 a[30:31], v151 offset:9224\n"
"buffer_load_dwordx4 a[152:155], v148, s[32:35], s61 offen nt\n"
"s_add_u32 s61, s61, 32768\n"
"buffer_load_dwordx4 a[156:159], v148, s[32:35], s61 offen nt\n"
"s_add_u32 s61, s61, 229376\n"
"s_waitcnt vmcnt(14)\n"
"ds_write_b128 v149, a[40:43] offset:0\n"
"ds_write_b128 v149, a[44:47] offset:1024\n"
"s_waitcnt lgkmcnt(0)\n"
"s_barrier\n"
"ds_read_b64_tr_b16 a[40:41], v151 offset:0\n"
"ds_read_b64_tr_b16 a[42:43], v151 offset:1024\n"
"ds_read_b64_tr_b16 a[44:45], v151 offset:8\n"
"ds_read_b64_tr_b16 a[46:47], v151 offset:1032\n"
"buffer_load_dwordx4 a[168:171], v148, s[32:35], s61 offen nt\n"
"s_add_u32 s61, s61, 32768\n"
"buffer_load_dwordx4 a[172:175], v148, s[32:35], s61 offen nt\n"
"s_add_u32 s61, s61, 229376\n"
"s_waitcnt vmcnt(14)\n"
"ds_write_b128 v149, a[56:59] offset:8192\n"
"ds_write_b128 v149, a[60:63] offset:9216\n"
"s_waitcnt lgkmcnt(0)\n"
"s_barrier\n"
"ds_read_b64_tr_b16 a[56:57], v151 offset:8192\n"
"ds_read_b64_tr_b16 a[58:59], v151 offset:9216\n"
"ds_read_b64_tr_b16 a[60:61], v151 offset:8200\n"
"ds_read_b64_tr_b16 a[62:63], v151 offset:9224\n"
"buffer_load_dwordx4 a[184:187], v148, s[32:35], s61 offen nt\n"
"s_add_u32 s61, s61, 32768\n"
"buffer_load_dwordx4 a[188:191], v148, s[32:35], s61 offen nt\n"
"s_add_u32 s61, s61, 229376\n"
"s_waitcnt vmcnt(14)\n"
"ds_write_b128 v149, a[72:75] offset:0\n"
"ds_write_b128 v149, a[76:79] offset:1024\n"
"s_waitcnt lgkmcnt(0)\n"
"s_barrier\n"
"ds_read_b64_tr_b16 a[72:73], v151 offset:0\n"
"ds_read_b64_tr_b16 a[74:75], v151 offset:1024\n"
"ds_read_b64_tr_b16 a[76:77], v151 offset:8\n"
"ds_read_b64_tr_b16 a[78:79], v151 offset:1032\n"
"buffer_load_dwordx4 a[200:203], v148, s[32:35], s61 offen nt\n"
"s_add_u32 s61, s61, 32768\n"
"buffer_load_dwordx4 a[204:207], v148, s[32:35], s61 offen nt\n"
"s_add_u32 s61, s61, 229376\n"
"s_waitcnt vmcnt(14)\n"
"ds_write_b128 v149, a[88:91] offset:8192\n"
"ds_write_b128 v149, a[92:95] offset:9216\n"
"s_waitcnt lgkmcnt(0)\n"
"s_barrier\n"
"ds_read_b64_tr_b16 a[88:89], v151 offset:8192\n"
"ds_read_b64_tr_b16 a[90:91], v151 offset:9216\n"
"ds_read_b64_tr_b16 a[92:93], v151 offset:8200\n"
"ds_read_b64_tr_b16 a[94:95], v151 offset:9224\n"
"buffer_load_dwordx4 a[216:219], v148, s[32:35], s61 offen nt\n"
"s_add_u32 s61, s61, 32768\n"
"buffer_load_dwordx4 a[220:223], v148, s[32:35], s61 offen nt\n"
"s_add_u32 s61, s61, 229376\n"
"s_waitcnt vmcnt(14)\n"
"ds_write_b128 v149, a[104:107] offset:0\n"
"ds_write_b128 v149, a[108:111] offset:1024\n"
"s_waitcnt lgkmcnt(0)\n"
"s_barrier\n"
"ds_read_b64_tr_b16 a[104:105], v151 offset:0\n"
"ds_read_b64_tr_b16 a[106:107], v151 offset:1024\n"
"ds_read_b64_tr_b16 a[108:109], v151 offset:8\n"
"ds_read_b64_tr_b16 a[110:111], v151 offset:1032\n"
"buffer_load_dwordx4 a[232:235], v148, s[32:35], s61 offen nt\n"
"s_add_u32 s61, s61, 32768\n"
"buffer_load_dwordx4 a[236:239], v148, s[32:35], s61 offen nt\n"
"s_add_u32 s61, s61, 229376\n"
"s_waitcnt vmcnt(14)\n"
"ds_write_b128 v149, a[120:123] offset:8192\n"
"ds_write_b128 v149, a[124:127] offset:9216\n"
"s_waitcnt lgkmcnt(0)\n"
"s_barrier\n"
"ds_read_b64_tr_b16 a[120:121], v151 offset:8192\n"
"ds_read_b64_tr_b16 a[122:123], v151 offset:9216\n"
"ds_read_b64_tr_b16 a[124:125], v151 offset:8200\n"
"ds_read_b64_tr_b16 a[126:127], v151 offset:9224\n"
"buffer_load_dwordx4 a[248:251], v148, s[32:35], s61 offen nt\n"
"s_add_u32 s61, s61, 32768\n"
"buffer_load_dwordx4 a[252:255], v148, s[32:35], s61 offen nt\n"
"s_add_u32 s61, s61, 229376\n"
"s_waitcnt vmcnt(14)\n"
"ds_write_b128 v149, a[136:139] offset:0\n"
"ds_write_b128 v149, a[140:143] offset:1024\n"
"s_waitcnt lgkmcnt(0)\n"
"s_barrier\n"
"ds_read_b64_tr_b16 a[136:137], v151 offset:0\n"
"ds_read_b64_tr_b16 a[138:139], v151 offset:1024\n"
"ds_read_b64_tr_b16 a[140:141], v151 offset:8\n"
"ds_read_b64_tr_b16 a[142:143], v151 offset:1032\n"
"buffer_load_dwordx4 v[24:27], v148, s[32:35], s61 offen nt\n"
"s_add_u32 s61, s61, 32768\n"
"buffer_load_dwordx4 v[28:31], v148, s[32:35], s61 offen nt\n"
"s_add_u32 s61, s61, 229376\n"
"s_waitcnt vmcnt(14)\n"
"ds_write_b128 v149, a[152:155] offset:8192\n"
"ds_write_b128 v149, a[156:159] offset:9216\n"
"s_waitcnt lgkmcnt(0)\n"
"s_barrier\n"
"ds_read_b64_tr_b16 a[152:153], v151 offset:8192\n"
"ds_read_b64_tr_b16 a[154:155], v151 offset:9216\n"
"ds_read_b64_tr_b16 a[156:157], v151 offset:8200\n"
"ds_read_b64_tr_b16 a[158:159], v151 offset:9224\n"
"buffer_load_dwordx4 v[40:43], v148, s[32:35], s61 offen nt\n"
"s_add_u32 s61, s61, 32768\n"
"buffer_load_dwordx4 v[44:47], v148, s[32:35], s61 offen nt\n"
"s_add_u32 s61, s61, 229376\n"
"s_waitcnt vmcnt(14)\n"
"ds_write_b128 v149, a[168:171] offset:0\n"
"ds_write_b128 v149, a[172:175] offset:1024\n"
"s_waitcnt lgkmcnt(0)\n"
"s_barrier\n"
"ds_read_b64_tr_b16 a[168:169], v151 offset:0\n"
"ds_read_b64_tr_b16 a[170:171], v151 offset:1024\n"
"ds_read_b64_tr_b16 a[172:173], v151 offset:8\n"
"ds_read_b64_tr_b16 a[174:175], v151 offset:1032\n"
"buffer_load_dwordx4 a[0:3], v148, s[28:31], s60 offen nt\n"
"s_add_u32 s60, s60, 32768\n"
"buffer_load_dwordx4 a[4:7], v148, s[28:31], s60 offen nt\n"
"s_add_u32 s60, s60, 229376\n"
"s_waitcnt vmcnt(14)\n"
"ds_write_b128 v149, a[184:187] offset:8192\n"
"ds_write_b128 v149, a[188:191] offset:9216\n"
"s_waitcnt lgkmcnt(0)\n"
"s_barrier\n"
"ds_read_b64_tr_b16 a[184:185], v151 offset:8192\n"
"ds_read_b64_tr_b16 a[186:187], v151 offset:9216\n"
"ds_read_b64_tr_b16 a[188:189], v151 offset:8200\n"
"ds_read_b64_tr_b16 a[190:191], v151 offset:9224\n"
"buffer_load_dwordx4 a[16:19], v148, s[28:31], s60 offen nt\n"
"s_add_u32 s60, s60, 32768\n"
"buffer_load_dwordx4 a[20:23], v148, s[28:31], s60 offen nt\n"
"s_add_u32 s60, s60, 229376\n"
"s_waitcnt vmcnt(14)\n"
"ds_write_b128 v149, a[200:203] offset:0\n"
"ds_write_b128 v149, a[204:207] offset:1024\n"
"s_waitcnt lgkmcnt(0)\n"
"s_barrier\n"
"ds_read_b64_tr_b16 a[200:201], v151 offset:0\n"
"ds_read_b64_tr_b16 a[202:203], v151 offset:1024\n"
"ds_read_b64_tr_b16 a[204:205], v151 offset:8\n"
"ds_read_b64_tr_b16 a[206:207], v151 offset:1032\n"
"buffer_load_dwordx4 a[32:35], v148, s[28:31], s60 offen nt\n"
"s_add_u32 s60, s60, 32768\n"
"buffer_load_dwordx4 a[36:39], v148, s[28:31], s60 offen nt\n"
"s_add_u32 s60, s60, 229376\n"
"s_waitcnt vmcnt(14)\n"
"ds_write_b128 v149, a[216:219] offset:8192\n"
"ds_write_b128 v149, a[220:223] offset:9216\n"
"s_waitcnt lgkmcnt(0)\n"
"s_barrier\n"
"ds_read_b64_tr_b16 a[216:217], v151 offset:8192\n"
"ds_read_b64_tr_b16 a[218:219], v151 offset:9216\n"
"ds_read_b64_tr_b16 a[220:221], v151 offset:8200\n"
"ds_read_b64_tr_b16 a[222:223], v151 offset:9224\n"
"buffer_load_dwordx4 a[48:51], v148, s[28:31], s60 offen nt\n"
"s_add_u32 s60, s60, 32768\n"
"buffer_load_dwordx4 a[52:55], v148, s[28:31], s60 offen nt\n"
"s_add_u32 s60, s60, 229376\n"
"s_waitcnt vmcnt(14)\n"
"ds_write_b128 v149, a[232:235] offset:0\n"
"ds_write_b128 v149, a[236:239] offset:1024\n"
"s_waitcnt lgkmcnt(0)\n"
"s_barrier\n"
"ds_read_b64_tr_b16 a[232:233], v151 offset:0\n"
"ds_read_b64_tr_b16 a[234:235], v151 offset:1024\n"
"ds_read_b64_tr_b16 a[236:237], v151 offset:8\n"
"ds_read_b64_tr_b16 a[238:239], v151 offset:1032\n"
"buffer_load_dwordx4 a[64:67], v148, s[28:31], s60 offen nt\n"
"s_add_u32 s60, s60, 32768\n"
"buffer_load_dwordx4 a[68:71], v148, s[28:31], s60 offen nt\n"
"s_add_u32 s60, s60, 229376\n"
"s_waitcnt vmcnt(14)\n"
"ds_write_b128 v149, a[248:251] offset:8192\n"
"ds_write_b128 v149, a[252:255] offset:9216\n"
"s_waitcnt lgkmcnt(0)\n"
"s_barrier\n"
"ds_read_b64_tr_b16 a[248:249], v151 offset:8192\n"
"ds_read_b64_tr_b16 a[250:251], v151 offset:9216\n"
"ds_read_b64_tr_b16 a[252:253], v151 offset:8200\n"
"ds_read_b64_tr_b16 a[254:255], v151 offset:9224\n"
"buffer_load_dwordx4 a[80:83], v148, s[28:31], s60 offen nt\n"
"s_add_u32 s60, s60, 32768\n"
"buffer_load_dwordx4 a[84:87], v148, s[28:31], s60 offen nt\n"
"s_add_u32 s60, s60, 229376\n"
"s_waitcnt vmcnt(14)\n"
"ds_write_b128 v149, v[24:27] offset:0\n"
"ds_write_b128 v149, v[28:31] offset:1024\n"
"s_waitcnt lgkmcnt(0)\n"
"s_barrier\n"
"ds_read_b64_tr_b16 v[24:25], v151 offset:0\n"
"ds_read_b64_tr_b16 v[26:27], v151 offset:1024\n"
"ds_read_b64_tr_b16 v[28:29], v151 offset:8\n"
"ds_read_b64_tr_b16 v[30:31], v151 offset:1032\n"
"buffer_load_dwordx4 a[96:99], v148, s[28:31], s60 offen nt\n"
"s_add_u32 s60, s60, 32768\n"
"buffer_load_dwordx4 a[100:103], v148, s[28:31], s60 offen nt\n"
"s_add_u32 s60, s60, 229376\n"
"s_waitcnt vmcnt(14)\n"
"ds_write_b128 v149, v[40:43] offset:8192\n"
"ds_write_b128 v149, v[44:47] offset:9216\n"
"s_waitcnt lgkmcnt(0)\n"
"s_barrier\n"
"ds_read_b64_tr_b16 v[40:41], v151 offset:8192\n"
"ds_read_b64_tr_b16 v[42:43], v151 offset:9216\n"
"ds_read_b64_tr_b16 v[44:45], v151 offset:8200\n"
"ds_read_b64_tr_b16 v[46:47], v151 offset:9224\n"
"buffer_load_dwordx4 a[112:115], v148, s[28:31], s60 offen nt\n"
"s_add_u32 s60, s60, 32768\n"
"buffer_load_dwordx4 a[116:119], v148, s[28:31], s60 offen nt\n"
"s_add_u32 s60, s60, 229376\n"
"s_waitcnt vmcnt(14)\n"
"ds_write_b128 v149, a[0:3] offset:0\n"
"ds_write_b128 v149, a[4:7] offset:1024\n"
"s_waitcnt lgkmcnt(0)\n"
"s_barrier\n"
"ds_read_b64_tr_b16 a[0:1], v151 offset:0\n"
"ds_read_b64_tr_b16 a[2:3], v151 offset:1024\n"
"ds_read_b64_tr_b16 a[4:5], v151 offset:8\n"
"ds_read_b64_tr_b16 a[6:7], v151 offset:1032\n"
"buffer_load_dwordx4 a[128:131], v148, s[28:31], s60 offen nt\n"
"s_add_u32 s60, s60, 32768\n"
"buffer_load_dwordx4 a[132:135], v148, s[28:31], s60 offen nt\n"
"s_add_u32 s60, s60, 229376\n"
"s_waitcnt vmcnt(14)\n"
"ds_write_b128 v149, a[16:19] offset:8192\n"
"ds_write_b128 v149, a[20:23] offset:9216\n"
"s_waitcnt lgkmcnt(0)\n"
"s_barrier\n"
"ds_read_b64_tr_b16 a[16:17], v151 offset:8192\n"
"ds_read_b64_tr_b16 a[18:19], v151 offset:9216\n"
"ds_read_b64_tr_b16 a[20:21], v151 offset:8200\n"
"ds_read_b64_tr_b16 a[22:23], v151 offset:9224\n"
"buffer_load_dwordx4 a[144:147], v148, s[28:31], s60 offen nt\n"
"s_add_u32 s60, s60, 32768\n"
"buffer_load_dwordx4 a[148:151], v148, s[28:31], s60 offen nt\n"
"s_add_u32 s60, s60, 229376\n"
"s_waitcnt vmcnt(14)\n"
"ds_write_b128 v149, a[32:35] offset:0\n"
"ds_write_b128 v149, a[36:39] offset:1024\n"
"s_waitcnt lgkmcnt(0)\n"
"s_barrier\n"
"ds_read_b64_tr_b16 a[32:33], v151 offset:0\n"
"ds_read_b64_tr_b16 a[34:35], v151 offset:1024\n"
"ds_read_b64_tr_b16 a[36:37], v151 offset:8\n"
"ds_read_b64_tr_b16 a[38:39], v151 offset:1032\n"
"buffer_load_dwordx4 a[160:163], v148, s[28:31], s60 offen nt\n"
"s_add_u32 s60, s60, 32768\n"
"buffer_load_dwordx4 a[164:167], v148, s[28:31], s60 offen nt\n"
"s_add_u32 s60, s60, 229376\n"
"s_waitcnt vmcnt(14)\n"
"ds_write_b128 v149, a[48:51] offset:8192\n"
"ds_write_b128 v149, a[52:55] offset:9216\n"
"s_waitcnt lgkmcnt(0)\n"
"s_barrier\n"
"ds_read_b64_tr_b16 a[48:49], v151 offset:8192\n"
"ds_read_b64_tr_b16 a[50:51], v151 offset:9216\n"
"ds_read_b64_tr_b16 a[52:53], v151 offset:8200\n"
"ds_read_b64_tr_b16 a[54:55], v151 offset:9224\n"
"buffer_load_dwordx4 a[176:179], v148, s[28:31], s60 offen nt\n"
"s_add_u32 s60, s60, 32768\n"
"buffer_load_dwordx4 a[180:183], v148, s[28:31], s60 offen nt\n"
"s_add_u32 s60, s60, 229376\n"
"s_waitcnt vmcnt(14)\n"
"ds_write_b128 v149, a[64:67] offset:0\n"
"ds_write_b128 v149, a[68:71] offset:1024\n"
"s_waitcnt lgkmcnt(0)\n"
"s_barrier\n"
"ds_read_b64_tr_b16 a[64:65], v151 offset:0\n"
"ds_read_b64_tr_b16 a[66:67], v151 offset:1024\n"
"ds_read_b64_tr_b16 a[68:69], v151 offset:8\n"
"ds_read_b64_tr_b16 a[70:71], v151 offset:1032\n"
"buffer_load_dwordx4 a[192:195], v148, s[28:31], s60 offen nt\n"
"s_add_u32 s60, s60, 32768\n"
"buffer_load_dwordx4 a[196:199], v148, s[28:31], s60 offen nt\n"
"s_add_u32 s60, s60, 229376\n"
"s_waitcnt vmcnt(14)\n"
"ds_write_b128 v149, a[80:83] offset:8192\n"
"ds_write_b128 v149, a[84:87] offset:9216\n"
"s_waitcnt lgkmcnt(0)\n"
"s_barrier\n"
"ds_read_b64_tr_b16 a[80:81], v151 offset:8192\n"
"ds_read_b64_tr_b16 a[82:83], v151 offset:9216\n"
"ds_read_b64_tr_b16 a[84:85], v151 offset:8200\n"
"ds_read_b64_tr_b16 a[86:87], v151 offset:9224\n"
"buffer_load_dwordx4 a[208:211], v148, s[28:31], s60 offen nt\n"
"s_add_u32 s60, s60, 32768\n"
"buffer_load_dwordx4 a[212:215], v148, s[28:31], s60 offen nt\n"
"s_add_u32 s60, s60, 229376\n"
"s_waitcnt vmcnt(14)\n"
"ds_write_b128 v149, a[96:99] offset:0\n"
"ds_write_b128 v149, a[100:103] offset:1024\n"
"s_waitcnt lgkmcnt(0)\n"
"s_barrier\n"
"ds_read_b64_tr_b16 a[96:97], v151 offset:0\n"
"ds_read_b64_tr_b16 a[98:99], v151 offset:1024\n"
"ds_read_b64_tr_b16 a[100:101], v151 offset:8\n"
"ds_read_b64_tr_b16 a[102:103], v151 offset:1032\n"
"buffer_load_dwordx4 a[224:227], v148, s[28:31], s60 offen nt\n"
"s_add_u32 s60, s60, 32768\n"
"buffer_load_dwordx4 a[228:231], v148, s[28:31], s60 offen nt\n"
"s_add_u32 s60, s60, 229376\n"
"s_waitcnt vmcnt(14)\n"
"ds_write_b128 v149, a[112:115] offset:8192\n"
"ds_write_b128 v149, a[116:119] offset:9216\n"
"s_waitcnt lgkmcnt(0)\n"
"s_barrier\n"
"ds_read_b64_tr_b16 a[112:113], v151 offset:8192\n"
"ds_read_b64_tr_b16 a[114:115], v151 offset:9216\n"
"ds_read_b64_tr_b16 a[116:117], v151 offset:8200\n"
"ds_read_b64_tr_b16 a[118:119], v151 offset:9224\n"
"buffer_load_dwordx4 a[240:243], v148, s[28:31], s60 offen nt\n"
"s_add_u32 s60, s60, 32768\n"
"buffer_load_dwordx4 a[244:247], v148, s[28:31], s60 offen nt\n"
"s_add_u32 s60, s60, 229376\n"
"s_waitcnt vmcnt(14)\n"
"ds_write_b128 v149, a[128:131] offset:0\n"
"ds_write_b128 v149, a[132:135] offset:1024\n"
"s_waitcnt lgkmcnt(0)\n"
"s_barrier\n"
"ds_read_b64_tr_b16 a[128:129], v151 offset:0\n"
"ds_read_b64_tr_b16 a[130:131], v151 offset:1024\n"
"ds_read_b64_tr_b16 a[132:133], v151 offset:8\n"
"ds_read_b64_tr_b16 a[134:135], v151 offset:1032\n"
"buffer_load_dwordx4 v[16:19], v148, s[28:31], s60 offen nt\n"
"s_add_u32 s60, s60, 32768\n"
"buffer_load_dwordx4 v[20:23], v148, s[28:31], s60 offen nt\n"
"s_add_u32 s60, s60, 229376\n"
"s_waitcnt vmcnt(14)\n"
"ds_write_b128 v149, a[144:147] offset:8192\n"
"ds_write_b128 v149, a[148:151] offset:9216\n"
"s_waitcnt lgkmcnt(0)\n"
"s_barrier\n"
"ds_read_b64_tr_b16 a[144:145], v151 offset:8192\n"
"ds_read_b64_tr_b16 a[146:147], v151 offset:9216\n"
"ds_read_b64_tr_b16 a[148:149], v151 offset:8200\n"
"ds_read_b64_tr_b16 a[150:151], v151 offset:9224\n"
"buffer_load_dwordx4 v[32:35], v148, s[28:31], s60 offen nt\n"
"s_add_u32 s60, s60, 32768\n"
"buffer_load_dwordx4 v[36:39], v148, s[28:31], s60 offen nt\n"
"s_add_u32 s60, s60, 229376\n"
"s_waitcnt vmcnt(14)\n"
"ds_write_b128 v149, a[160:163] offset:0\n"
"ds_write_b128 v149, a[164:167] offset:1024\n"
"s_waitcnt lgkmcnt(0)\n"
"s_barrier\n"
"ds_read_b64_tr_b16 a[160:161], v151 offset:0\n"
"ds_read_b64_tr_b16 a[162:163], v151 offset:1024\n"
"ds_read_b64_tr_b16 a[164:165], v151 offset:8\n"
"ds_read_b64_tr_b16 a[166:167], v151 offset:1032\n"
"s_waitcnt vmcnt(12)\n"
"ds_write_b128 v149, a[176:179] offset:8192\n"
"ds_write_b128 v149, a[180:183] offset:9216\n"
"s_waitcnt lgkmcnt(0)\n"
"s_barrier\n"
"ds_read_b64_tr_b16 a[176:177], v151 offset:8192\n"
"ds_read_b64_tr_b16 a[178:179], v151 offset:9216\n"
"ds_read_b64_tr_b16 a[180:181], v151 offset:8200\n"
"ds_read_b64_tr_b16 a[182:183], v151 offset:9224\n"
"s_waitcnt vmcnt(10)\n"
"ds_write_b128 v149, a[192:195] offset:0\n"
"ds_write_b128 v149, a[196:199] offset:1024\n"
"s_waitcnt lgkmcnt(0)\n"
"s_barrier\n"
"ds_read_b64_tr_b16 a[192:193], v151 offset:0\n"
"ds_read_b64_tr_b16 a[194:195], v151 offset:1024\n"
"ds_read_b64_tr_b16 a[196:197], v151 offset:8\n"
"ds_read_b64_tr_b16 a[198:199], v151 offset:1032\n"
"s_waitcnt vmcnt(8)\n"
"ds_write_b128 v149, a[208:211] offset:8192\n"
"ds_write_b128 v149, a[212:215] offset:9216\n"
"s_waitcnt lgkmcnt(0)\n"
"s_barrier\n"
"ds_read_b64_tr_b16 a[208:209], v151 offset:8192\n"
"ds_read_b64_tr_b16 a[210:211], v151 offset:9216\n"
"ds_read_b64_tr_b16 a[212:213], v151 offset:8200\n"
"ds_read_b64_tr_b16 a[214:215], v151 offset:9224\n"
"s_waitcnt vmcnt(6)\n"
"ds_write_b128 v149, a[224:227] offset:0\n"
"ds_write_b128 v149, a[228:231] offset:1024\n"
"s_waitcnt lgkmcnt(0)\n"
"s_barrier\n"
"ds_read_b64_tr_b16 a[224:225], v151 offset:0\n"
"ds_read_b64_tr_b16 a[226:227], v151 offset:1024\n"
"ds_read_b64_tr_b16 a[228:229], v151 offset:8\n"
"ds_read_b64_tr_b16 a[230:231], v151 offset:1032\n"
"s_waitcnt vmcnt(4)\n"
"ds_write_b128 v149, a[240:243] offset:8192\n"
"ds_write_b128 v149, a[244:247] offset:9216\n"
"s_waitcnt lgkmcnt(0)\n"
"s_barrier\n"
"ds_read_b64_tr_b16 a[240:241], v151 offset:8192\n"
"ds_read_b64_tr_b16 a[242:243], v151 offset:9216\n"
"ds_read_b64_tr_b16 a[244:245], v151 offset:8200\n"
"ds_read_b64_tr_b16 a[246:247], v151 offset:9224\n"
"s_waitcnt vmcnt(2)\n"
"ds_write_b128 v149, v[16:19] offset:0\n"
"ds_write_b128 v149, v[20:23] offset:1024\n"
"s_waitcnt lgkmcnt(0)\n"
"s_barrier\n"
"ds_read_b64_tr_b16 v[16:17], v151 offset:0\n"
"ds_read_b64_tr_b16 v[18:19], v151 offset:1024\n"
"ds_read_b64_tr_b16 v[20:21], v151 offset:8\n"
"ds_read_b64_tr_b16 v[22:23], v151 offset:1032\n"
"s_waitcnt vmcnt(0)\n"
"ds_write_b128 v149, v[32:35] offset:8192\n"
"ds_write_b128 v149, v[36:39] offset:9216\n"
"s_waitcnt lgkmcnt(0)\n"
"s_barrier\n"
"ds_read_b64_tr_b16 v[32:33], v151 offset:8192\n"
"ds_read_b64_tr_b16 v[34:35], v151 offset:9216\n"
"ds_read_b64_tr_b16 v[36:37], v151 offset:8200\n"
"ds_read_b64_tr_b16 v[38:39], v151 offset:9224\n"
"s_waitcnt lgkmcnt(0)\n"
"s_barrier\n"
"s_mov_b32 s21, 0\n"
"s_lshl_b32 s58, s19, 15\n"
"s_add_u32 s58, s58, 2097152\n"
"v_mov_b32 v6, v2\n"
"v_add_u32 v7, 36864, v1\n"
"v_mov_b32 v8, 90112\n"
"v_mov_b32 v9, 0\n"
"v_mov_b32 v10, 1\n"
"ds_write2_b32 v8, v9, v9 offset0:0 offset1:1\n"
"s_waitcnt lgkmcnt(0)\n"
"s_barrier\n"
"s_lshr_b32 s40, s21, 5\n"
"s_cmp_gt_i32 s55, s40\n"
"s_cbranch_scc1 Lln_48\n"
"s_lshr_b32 s41, s21, 1\n"
"s_and_b32 s41, s41, 15\n"
"Lln_47:\n"
"s_xor_b32 s43, s22, 1245184\n"
"buffer_load_dword v4, v5, s[24:27], s43 offen sc1\n"
"s_waitcnt vmcnt(0)\n"
"v_cmp_ge_i32 vcc, s40, v4\n"
"v_readlane_b32 s42, v4, s41\n"
"s_cbranch_vccnz Lln_49\n"
"s_add_u32 s55, s40, 1\n"
"Lln_49:\n"
"s_cmp_gt_i32 s42, s40\n"
"s_cbranch_scc1 Lln_48\n"
"s_sleep 2\n"
"s_memrealtime s[48:49]\n"
"s_waitcnt lgkmcnt(0)\n"
"s_sub_u32 s48, s48, s23\n"
"s_cmp_gt_i32 s48, 0\n"
"s_cbranch_scc1 LX1_end\n"
"s_branch Lln_47\n"
"Lln_48:\n"
"s_mul_i32 s54, s21, 36864\n"
"s_add_u32 s54, s54, 165675008\n"
"buffer_load_dwordx4 v[48:51], v1, s[24:27], s54 offen offset:0 sc1\n"
"buffer_load_dwordx4 v[52:55], v1, s[24:27], s54 offen offset:1024 sc1\n"
"buffer_load_dwordx4 v[56:59], v1, s[24:27], s54 offen offset:2048 sc1\n"
"buffer_load_dwordx4 v[60:63], v1, s[24:27], s54 offen offset:3072 sc1\n"
"s_add_u32 s54, s54, 4096\n"
"buffer_load_dwordx4 v[64:67], v1, s[24:27], s54 offen offset:0 sc1\n"
"buffer_load_dwordx4 v[68:71], v1, s[24:27], s54 offen offset:1024 sc1\n"
"buffer_load_dwordx4 v[72:75], v1, s[24:27], s54 offen offset:2048 sc1\n"
"buffer_load_dwordx4 v[76:79], v1, s[24:27], s54 offen offset:3072 sc1\n"
"s_add_u32 s54, s54, 4096\n"
"buffer_load_dwordx4 v[80:83], v1, s[24:27], s54 offen offset:0 sc1\n"
"s_waitcnt vmcnt(0)\n"
"ds_write_b128 v1, v[48:51] offset:0\n"
"ds_write_b128 v1, v[52:55] offset:1024\n"
"ds_write_b128 v1, v[56:59] offset:2048\n"
"ds_write_b128 v1, v[60:63] offset:3072\n"
"ds_write_b128 v1, v[64:67] offset:4096\n"
"ds_write_b128 v1, v[68:71] offset:5120\n"
"ds_write_b128 v1, v[72:75] offset:6144\n"
"ds_write_b128 v1, v[76:79] offset:7168\n"
"ds_write_b128 v1, v[80:83] offset:8192\n"
"s_waitcnt lgkmcnt(0)\n"
"s_barrier\n"
"ds_read_b128 v[84:87], v6 offset:0\n"
"ds_read_b128 v[88:91], v6 offset:18432\n"
"ds_read_b128 v[92:95], v6 offset:1024\n"
"ds_read_b128 v[96:99], v6 offset:19456\n"
"ds_read_b128 v[100:103], v6 offset:2048\n"
"ds_read_b128 v[104:107], v6 offset:20480\n"
"s_mov_b32 s62, s56\n"
"s_mov_b32 s63, s57\n"
"s_add_u32 s53, s21, 1\n"
"s_min_u32 s53, s53, 63\n"
"s_lshr_b32 s40, s53, 5\n"
"s_cmp_gt_i32 s55, s40\n"
"s_cbranch_scc1 Lln_51\n"
"s_lshr_b32 s41, s53, 1\n"
"s_and_b32 s41, s41, 15\n"
"Lln_50:\n"
"s_xor_b32 s43, s22, 1245184\n"
"buffer_load_dword v4, v5, s[24:27], s43 offen sc1\n"
"s_waitcnt vmcnt(0)\n"
"v_cmp_ge_i32 vcc, s40, v4\n"
"v_readlane_b32 s42, v4, s41\n"
"s_cbranch_vccnz Lln_52\n"
"s_add_u32 s55, s40, 1\n"
"Lln_52:\n"
"s_cmp_gt_i32 s42, s40\n"
"s_cbranch_scc1 Lln_51\n"
"s_sleep 2\n"
"s_memrealtime s[48:49]\n"
"s_waitcnt lgkmcnt(0)\n"
"s_sub_u32 s48, s48, s23\n"
"s_cmp_gt_i32 s48, 0\n"
"s_cbranch_scc1 LX1_end\n"
"s_branch Lln_50\n"
"Lln_51:\n"
"s_mul_i32 s54, s53, 36864\n"
"s_add_u32 s54, s54, 165675008\n"
"s_mov_b32 s56, s58\n"
"s_add_u32 s58, s58, 1048576\n"
"s_lshr_b32 s43, s56, 8\n"
"s_xor_b32 s43, s43, s22\n"
"s_and_b32 s43, s43, 8192\n"
"s_or_b32 s56, s56, s43\n"
"s_lshl_b32 s36, s20, 10\n"
"s_add_u32 s56, s56, s36\n"
"s_add_u32 s57, s56, s36\n"
"s_add_u32 s57, s57, 16384\n"
"s_waitcnt lgkmcnt(4)\n"
"v_mfma_f32_16x16x32_bf16 v[116:119], a[0:3], v[84:87], 0\n"
"ds_read_b128 v[108:111], v6 offset:3072\n"
"v_mfma_f32_16x16x32_bf16 v[132:135], v[84:87], a[8:11], 0\n"
"ds_read_b128 v[112:115], v6 offset:21504\n"
"v_mfma_f32_16x16x32_bf16 v[124:127], a[4:7], v[84:87], 0\n"
"v_mfma_f32_16x16x32_bf16 v[140:143], v[84:87], a[12:15], 0\n"
"v_mfma_f32_16x16x32_bf16 v[120:123], a[0:3], v[88:91], 0\n"
"v_mfma_f32_16x16x32_bf16 v[136:139], v[88:91], a[8:11], 0\n"
"v_mfma_f32_16x16x32_bf16 v[128:131], a[4:7], v[88:91], 0\n"
"v_mfma_f32_16x16x32_bf16 v[144:147], v[88:91], a[12:15], 0\n"
"s_waitcnt lgkmcnt(4)\n"
"v_mfma_f32_16x16x32_bf16 v[116:119], a[16:19], v[92:95], v[116:119]\n"
"ds_read_b128 v[84:87], v6 offset:4096\n"
"v_mfma_f32_16x16x32_bf16 v[132:135], v[92:95], a[24:27], v[132:135]\n"
"ds_read_b128 v[88:91], v6 offset:22528\n"
"v_mfma_f32_16x16x32_bf16 v[124:127], a[20:23], v[92:95], v[124:127]\n"
"v_mfma_f32_16x16x32_bf16 v[140:143], v[92:95], a[28:31], v[140:143]\n"
"v_mfma_f32_16x16x32_bf16 v[120:123], a[16:19], v[96:99], v[120:123]\n"
"v_mfma_f32_16x16x32_bf16 v[136:139], v[96:99], a[24:27], v[136:139]\n"
"v_mfma_f32_16x16x32_bf16 v[128:131], a[20:23], v[96:99], v[128:131]\n"
"v_mfma_f32_16x16x32_bf16 v[144:147], v[96:99], a[28:31], v[144:147]\n"
"s_waitcnt lgkmcnt(4)\n"
"v_mfma_f32_16x16x32_bf16 v[116:119], a[32:35], v[100:103], v[116:119]\n"
"ds_read_b128 v[92:95], v6 offset:5120\n"
"v_mfma_f32_16x16x32_bf16 v[132:135], v[100:103], a[40:43], v[132:135]\n"
"ds_read_b128 v[96:99], v6 offset:23552\n"
"v_mfma_f32_16x16x32_bf16 v[124:127], a[36:39], v[100:103], v[124:127]\n"
"buffer_load_dwordx4 v[48:51], v1, s[24:27], s54 offen offset:0 sc1\n"
"v_mfma_f32_16x16x32_bf16 v[140:143], v[100:103], a[44:47], v[140:143]\n"
"v_mfma_f32_16x16x32_bf16 v[120:123], a[32:35], v[104:107], v[120:123]\n"
"v_mfma_f32_16x16x32_bf16 v[136:139], v[104:107], a[40:43], v[136:139]\n"
"buffer_load_dwordx4 v[52:55], v1, s[24:27], s54 offen offset:1024 sc1\n"
"v_mfma_f32_16x16x32_bf16 v[128:131], a[36:39], v[104:107], v[128:131]\n"
"v_mfma_f32_16x16x32_bf16 v[144:147], v[104:107], a[44:47], v[144:147]\n"
"s_waitcnt lgkmcnt(4)\n"
"v_mfma_f32_16x16x32_bf16 v[116:119], a[48:51], v[108:111], v[116:119]\n"
"ds_read_b128 v[100:103], v6 offset:6144\n"
"v_mfma_f32_16x16x32_bf16 v[132:135], v[108:111], a[56:59], v[132:135]\n"
"ds_read_b128 v[104:107], v6 offset:24576\n"
"v_mfma_f32_16x16x32_bf16 v[124:127], a[52:55], v[108:111], v[124:127]\n"
"buffer_load_dwordx4 v[56:59], v1, s[24:27], s54 offen offset:2048 sc1\n"
"v_mfma_f32_16x16x32_bf16 v[140:143], v[108:111], a[60:63], v[140:143]\n"
"v_mfma_f32_16x16x32_bf16 v[120:123], a[48:51], v[112:115], v[120:123]\n"
"v_mfma_f32_16x16x32_bf16 v[136:139], v[112:115], a[56:59], v[136:139]\n"
"buffer_load_dwordx4 v[60:63], v1, s[24:27], s54 offen offset:3072 sc1\n"
"s_add_u32 s54, s54, 4096\n"
"v_mfma_f32_16x16x32_bf16 v[128:131], a[52:55], v[112:115], v[128:131]\n"
"v_mfma_f32_16x16x32_bf16 v[144:147], v[112:115], a[60:63], v[144:147]\n"
"s_waitcnt lgkmcnt(4)\n"
"v_mfma_f32_16x16x32_bf16 v[116:119], a[64:67], v[84:87], v[116:119]\n"
"ds_read_b128 v[108:111], v6 offset:7168\n"
"v_mfma_f32_16x16x32_bf16 v[132:135], v[84:87], a[72:75], v[132:135]\n"
"ds_read_b128 v[112:115], v6 offset:25600\n"
"v_mfma_f32_16x16x32_bf16 v[124:127], a[68:71], v[84:87], v[124:127]\n"
"buffer_load_dwordx4 v[64:67], v1, s[24:27], s54 offen offset:0 sc1\n"
"v_mfma_f32_16x16x32_bf16 v[140:143], v[84:87], a[76:79], v[140:143]\n"
"v_mfma_f32_16x16x32_bf16 v[120:123], a[64:67], v[88:91], v[120:123]\n"
"v_mfma_f32_16x16x32_bf16 v[136:139], v[88:91], a[72:75], v[136:139]\n"
"buffer_load_dwordx4 v[68:71], v1, s[24:27], s54 offen offset:1024 sc1\n"
"v_mfma_f32_16x16x32_bf16 v[128:131], a[68:71], v[88:91], v[128:131]\n"
"v_mfma_f32_16x16x32_bf16 v[144:147], v[88:91], a[76:79], v[144:147]\n"
"s_waitcnt lgkmcnt(4)\n"
"v_mfma_f32_16x16x32_bf16 v[116:119], a[80:83], v[92:95], v[116:119]\n"
"ds_read_b128 v[84:87], v6 offset:8192\n"
"v_mfma_f32_16x16x32_bf16 v[132:135], v[92:95], a[88:91], v[132:135]\n"
"ds_read_b128 v[88:91], v6 offset:26624\n"
"v_mfma_f32_16x16x32_bf16 v[124:127], a[84:87], v[92:95], v[124:127]\n"
"buffer_load_dwordx4 v[72:75], v1, s[24:27], s54 offen offset:2048 sc1\n"
"v_mfma_f32_16x16x32_bf16 v[140:143], v[92:95], a[92:95], v[140:143]\n"
"v_mfma_f32_16x16x32_bf16 v[120:123], a[80:83], v[96:99], v[120:123]\n"
"v_mfma_f32_16x16x32_bf16 v[136:139], v[96:99], a[88:91], v[136:139]\n"
"buffer_load_dwordx4 v[76:79], v1, s[24:27], s54 offen offset:3072 sc1\n"
"s_add_u32 s54, s54, 4096\n"
"v_mfma_f32_16x16x32_bf16 v[128:131], a[84:87], v[96:99], v[128:131]\n"
"v_mfma_f32_16x16x32_bf16 v[144:147], v[96:99], a[92:95], v[144:147]\n"
"s_waitcnt lgkmcnt(4)\n"
"v_mfma_f32_16x16x32_bf16 v[116:119], a[96:99], v[100:103], v[116:119]\n"
"ds_read_b128 v[92:95], v6 offset:9216\n"
"v_mfma_f32_16x16x32_bf16 v[132:135], v[100:103], a[104:107], v[132:135]\n"
"ds_read_b128 v[96:99], v6 offset:27648\n"
"v_mfma_f32_16x16x32_bf16 v[124:127], a[100:103], v[100:103], v[124:127]\n"
"buffer_load_dwordx4 v[80:83], v1, s[24:27], s54 offen offset:0 sc1\n"
"v_mfma_f32_16x16x32_bf16 v[140:143], v[100:103], a[108:111], v[140:143]\n"
"v_mfma_f32_16x16x32_bf16 v[120:123], a[96:99], v[104:107], v[120:123]\n"
"v_mfma_f32_16x16x32_bf16 v[136:139], v[104:107], a[104:107], v[136:139]\n"
"v_mfma_f32_16x16x32_bf16 v[128:131], a[100:103], v[104:107], v[128:131]\n"
"v_mfma_f32_16x16x32_bf16 v[144:147], v[104:107], a[108:111], v[144:147]\n"
"s_waitcnt lgkmcnt(4)\n"
"v_mfma_f32_16x16x32_bf16 v[116:119], a[112:115], v[108:111], v[116:119]\n"
"ds_read_b128 v[100:103], v6 offset:10240\n"
"v_mfma_f32_16x16x32_bf16 v[132:135], v[108:111], a[120:123], v[132:135]\n"
"ds_read_b128 v[104:107], v6 offset:28672\n"
"v_mfma_f32_16x16x32_bf16 v[124:127], a[116:119], v[108:111], v[124:127]\n"
"v_mfma_f32_16x16x32_bf16 v[140:143], v[108:111], a[124:127], v[140:143]\n"
"v_mfma_f32_16x16x32_bf16 v[120:123], a[112:115], v[112:115], v[120:123]\n"
"v_mfma_f32_16x16x32_bf16 v[136:139], v[112:115], a[120:123], v[136:139]\n"
"v_mfma_f32_16x16x32_bf16 v[128:131], a[116:119], v[112:115], v[128:131]\n"
"v_mfma_f32_16x16x32_bf16 v[144:147], v[112:115], a[124:127], v[144:147]\n"
"s_waitcnt lgkmcnt(4)\n"
"v_mfma_f32_16x16x32_bf16 v[116:119], a[128:131], v[84:87], v[116:119]\n"
"ds_read_b128 v[108:111], v6 offset:11264\n"
"v_mfma_f32_16x16x32_bf16 v[132:135], v[84:87], a[136:139], v[132:135]\n"
"ds_read_b128 v[112:115], v6 offset:29696\n"
"v_mfma_f32_16x16x32_bf16 v[124:127], a[132:135], v[84:87], v[124:127]\n"
"v_mfma_f32_16x16x32_bf16 v[140:143], v[84:87], a[140:143], v[140:143]\n"
"v_mfma_f32_16x16x32_bf16 v[120:123], a[128:131], v[88:91], v[120:123]\n"
"v_mfma_f32_16x16x32_bf16 v[136:139], v[88:91], a[136:139], v[136:139]\n"
"v_mfma_f32_16x16x32_bf16 v[128:131], a[132:135], v[88:91], v[128:131]\n"
"v_mfma_f32_16x16x32_bf16 v[144:147], v[88:91], a[140:143], v[144:147]\n"
"s_waitcnt lgkmcnt(4)\n"
"v_mfma_f32_16x16x32_bf16 v[116:119], a[144:147], v[92:95], v[116:119]\n"
"ds_read_b128 v[84:87], v6 offset:12288\n"
"v_mfma_f32_16x16x32_bf16 v[132:135], v[92:95], a[152:155], v[132:135]\n"
"ds_read_b128 v[88:91], v6 offset:30720\n"
"v_mfma_f32_16x16x32_bf16 v[124:127], a[148:151], v[92:95], v[124:127]\n"
"v_mfma_f32_16x16x32_bf16 v[140:143], v[92:95], a[156:159], v[140:143]\n"
"v_mfma_f32_16x16x32_bf16 v[120:123], a[144:147], v[96:99], v[120:123]\n"
"v_mfma_f32_16x16x32_bf16 v[136:139], v[96:99], a[152:155], v[136:139]\n"
"v_mfma_f32_16x16x32_bf16 v[128:131], a[148:151], v[96:99], v[128:131]\n"
"v_mfma_f32_16x16x32_bf16 v[144:147], v[96:99], a[156:159], v[144:147]\n"
"s_waitcnt lgkmcnt(4)\n"
"v_mfma_f32_16x16x32_bf16 v[116:119], a[160:163], v[100:103], v[116:119]\n"
"ds_read_b128 v[92:95], v6 offset:13312\n"
"v_mfma_f32_16x16x32_bf16 v[132:135], v[100:103], a[168:171], v[132:135]\n"
"ds_read_b128 v[96:99], v6 offset:31744\n"
"v_mfma_f32_16x16x32_bf16 v[124:127], a[164:167], v[100:103], v[124:127]\n"
"v_mfma_f32_16x16x32_bf16 v[140:143], v[100:103], a[172:175], v[140:143]\n"
"v_mfma_f32_16x16x32_bf16 v[120:123], a[160:163], v[104:107], v[120:123]\n"
"v_mfma_f32_16x16x32_bf16 v[136:139], v[104:107], a[168:171], v[136:139]\n"
"v_mfma_f32_16x16x32_bf16 v[128:131], a[164:167], v[104:107], v[128:131]\n"
"v_mfma_f32_16x16x32_bf16 v[144:147], v[104:107], a[172:175], v[144:147]\n"
"s_waitcnt lgkmcnt(4)\n"
"v_mfma_f32_16x16x32_bf16 v[116:119], a[176:179], v[108:111], v[116:119]\n"
"ds_read_b128 v[100:103], v6 offset:14336\n"
"v_mfma_f32_16x16x32_bf16 v[132:135], v[108:111], a[184:187], v[132:135]\n"
"ds_read_b128 v[104:107], v6 offset:32768\n"
"v_mfma_f32_16x16x32_bf16 v[124:127], a[180:183], v[108:111], v[124:127]\n"
"v_mfma_f32_16x16x32_bf16 v[140:143], v[108:111], a[188:191], v[140:143]\n"
"v_mfma_f32_16x16x32_bf16 v[120:123], a[176:179], v[112:115], v[120:123]\n"
"v_mfma_f32_16x16x32_bf16 v[136:139], v[112:115], a[184:187], v[136:139]\n"
"v_mfma_f32_16x16x32_bf16 v[128:131], a[180:183], v[112:115], v[128:131]\n"
"v_mfma_f32_16x16x32_bf16 v[144:147], v[112:115], a[188:191], v[144:147]\n"
"s_waitcnt lgkmcnt(4)\n"
"v_mfma_f32_16x16x32_bf16 v[116:119], a[192:195], v[84:87], v[116:119]\n"
"ds_read_b128 v[108:111], v6 offset:15360\n"
"v_mfma_f32_16x16x32_bf16 v[132:135], v[84:87], a[200:203], v[132:135]\n"
"ds_read_b128 v[112:115], v6 offset:33792\n"
"v_mfma_f32_16x16x32_bf16 v[124:127], a[196:199], v[84:87], v[124:127]\n"
"v_mfma_f32_16x16x32_bf16 v[140:143], v[84:87], a[204:207], v[140:143]\n"
"v_mfma_f32_16x16x32_bf16 v[120:123], a[192:195], v[88:91], v[120:123]\n"
"v_mfma_f32_16x16x32_bf16 v[136:139], v[88:91], a[200:203], v[136:139]\n"
"v_mfma_f32_16x16x32_bf16 v[128:131], a[196:199], v[88:91], v[128:131]\n"
"v_mfma_f32_16x16x32_bf16 v[144:147], v[88:91], a[204:207], v[144:147]\n"
"s_waitcnt lgkmcnt(4)\n"
"s_waitcnt vmcnt(0)\n"
"v_mfma_f32_16x16x32_bf16 v[116:119], a[208:211], v[92:95], v[116:119]\n"
"ds_read_b128 v[84:87], v6 offset:16384\n"
"v_mfma_f32_16x16x32_bf16 v[132:135], v[92:95], a[216:219], v[132:135]\n"
"ds_read_b128 v[88:91], v6 offset:34816\n"
"v_mfma_f32_16x16x32_bf16 v[124:127], a[212:215], v[92:95], v[124:127]\n"
"ds_write_b128 v7, v[48:51] offset:0\n"
"v_mfma_f32_16x16x32_bf16 v[140:143], v[92:95], a[220:223], v[140:143]\n"
"v_mfma_f32_16x16x32_bf16 v[120:123], a[208:211], v[96:99], v[120:123]\n"
"v_mfma_f32_16x16x32_bf16 v[136:139], v[96:99], a[216:219], v[136:139]\n"
"ds_write_b128 v7, v[52:55] offset:1024\n"
"v_mfma_f32_16x16x32_bf16 v[128:131], a[212:215], v[96:99], v[128:131]\n"
"v_mfma_f32_16x16x32_bf16 v[144:147], v[96:99], a[220:223], v[144:147]\n"
"s_waitcnt lgkmcnt(6)\n"
"v_mfma_f32_16x16x32_bf16 v[116:119], a[224:227], v[100:103], v[116:119]\n"
"ds_read_b128 v[92:95], v6 offset:17408\n"
"v_mfma_f32_16x16x32_bf16 v[132:135], v[100:103], a[232:235], v[132:135]\n"
"ds_read_b128 v[96:99], v6 offset:35840\n"
"v_mfma_f32_16x16x32_bf16 v[124:127], a[228:231], v[100:103], v[124:127]\n"
"ds_write_b128 v7, v[56:59] offset:2048\n"
"v_mfma_f32_16x16x32_bf16 v[140:143], v[100:103], a[236:239], v[140:143]\n"
"v_mfma_f32_16x16x32_bf16 v[120:123], a[224:227], v[104:107], v[120:123]\n"
"v_mfma_f32_16x16x32_bf16 v[136:139], v[104:107], a[232:235], v[136:139]\n"
"ds_write_b128 v7, v[60:63] offset:3072\n"
"v_mfma_f32_16x16x32_bf16 v[128:131], a[228:231], v[104:107], v[128:131]\n"
"v_mfma_f32_16x16x32_bf16 v[144:147], v[104:107], a[236:239], v[144:147]\n"
"s_waitcnt lgkmcnt(8)\n"
"v_mfma_f32_16x16x32_bf16 v[116:119], a[240:243], v[108:111], v[116:119]\n"
"v_mfma_f32_16x16x32_bf16 v[132:135], v[108:111], a[248:251], v[132:135]\n"
"v_mfma_f32_16x16x32_bf16 v[124:127], a[244:247], v[108:111], v[124:127]\n"
"ds_write_b128 v7, v[64:67] offset:4096\n"
"v_mfma_f32_16x16x32_bf16 v[140:143], v[108:111], a[252:255], v[140:143]\n"
"v_mfma_f32_16x16x32_bf16 v[120:123], a[240:243], v[112:115], v[120:123]\n"
"v_mfma_f32_16x16x32_bf16 v[136:139], v[112:115], a[248:251], v[136:139]\n"
"ds_write_b128 v7, v[68:71] offset:5120\n"
"v_mfma_f32_16x16x32_bf16 v[128:131], a[244:247], v[112:115], v[128:131]\n"
"v_mfma_f32_16x16x32_bf16 v[144:147], v[112:115], a[252:255], v[144:147]\n"
"s_waitcnt lgkmcnt(8)\n"
"v_mfma_f32_16x16x32_bf16 v[116:119], v[16:19], v[84:87], v[116:119]\n"
"v_mfma_f32_16x16x32_bf16 v[132:135], v[84:87], v[24:27], v[132:135]\n"
"v_mfma_f32_16x16x32_bf16 v[124:127], v[20:23], v[84:87], v[124:127]\n"
"ds_write_b128 v7, v[72:75] offset:6144\n"
"v_mfma_f32_16x16x32_bf16 v[140:143], v[84:87], v[28:31], v[140:143]\n"
"v_mfma_f32_16x16x32_bf16 v[120:123], v[16:19], v[88:91], v[120:123]\n"
"v_mfma_f32_16x16x32_bf16 v[136:139], v[88:91], v[24:27], v[136:139]\n"
"ds_write_b128 v7, v[76:79] offset:7168\n"
"v_mfma_f32_16x16x32_bf16 v[128:131], v[20:23], v[88:91], v[128:131]\n"
"v_mfma_f32_16x16x32_bf16 v[144:147], v[88:91], v[28:31], v[144:147]\n"
"s_waitcnt lgkmcnt(6)\n"
"v_mfma_f32_16x16x32_bf16 v[116:119], v[32:35], v[92:95], v[116:119]\n"
"v_mfma_f32_16x16x32_bf16 v[132:135], v[92:95], v[40:43], v[132:135]\n"
"v_mfma_f32_16x16x32_bf16 v[124:127], v[36:39], v[92:95], v[124:127]\n"
"ds_write_b128 v7, v[80:83] offset:8192\n"
"v_mfma_f32_16x16x32_bf16 v[140:143], v[92:95], v[44:47], v[140:143]\n"
"v_mfma_f32_16x16x32_bf16 v[120:123], v[32:35], v[96:99], v[120:123]\n"
"v_mfma_f32_16x16x32_bf16 v[136:139], v[96:99], v[40:43], v[136:139]\n"
"v_mfma_f32_16x16x32_bf16 v[128:131], v[36:39], v[96:99], v[128:131]\n"
"v_mfma_f32_16x16x32_bf16 v[144:147], v[96:99], v[44:47], v[144:147]\n"
"v_xor_b32 v4, v6, v2\n"
"v_sub_u32 v7, v7, v1\n"
"v_add_u32 v6, v7, v2\n"
"v_add_u32 v7, v4, v1\n"
"s_waitcnt lgkmcnt(0)\n"
"s_barrier\n"
"v_cvt_pk_bf16_f32 v148, v116, v117\n"
"v_cvt_pk_bf16_f32 v149, v118, v119\n"
"v_cvt_pk_bf16_f32 v150, v124, v125\n"
"v_cvt_pk_bf16_f32 v151, v126, v127\n"
"buffer_store_dwordx4 v[148:151], v2, s[24:27], s56 offen sc1\n"
"v_cvt_pk_bf16_f32 v152, v120, v121\n"
"v_cvt_pk_bf16_f32 v153, v122, v123\n"
"v_cvt_pk_bf16_f32 v154, v128, v129\n"
"v_cvt_pk_bf16_f32 v155, v130, v131\n"
"s_add_u32 s56, s56, 4096\n"
"buffer_store_dwordx4 v[152:155], v2, s[24:27], s56 offen sc1\n"
"v_cvt_pk_bf16_f32 v156, v132, v133\n"
"v_cvt_pk_bf16_f32 v157, v134, v135\n"
"v_cvt_pk_bf16_f32 v158, v136, v137\n"
"v_cvt_pk_bf16_f32 v159, v138, v139\n"
"buffer_store_dwordx4 v[156:159], v2, s[24:27], s57 offen offset:0 sc1\n"
"v_cvt_pk_bf16_f32 v160, v140, v141\n"
"v_cvt_pk_bf16_f32 v161, v142, v143\n"
"v_cvt_pk_bf16_f32 v162, v144, v145\n"
"v_cvt_pk_bf16_f32 v163, v146, v147\n"
"buffer_store_dwordx4 v[160:163], v2, s[24:27], s57 offen offset:1024 sc1\n"
"s_waitcnt vmcnt(0)\n"
"s_add_u32 s59, s21, 0\n"
"s_and_b32 s41, s59, 1\n"
"s_lshl_b32 s41, s41, 2\n"
"s_add_u32 s41, s41, 90112\n"
"v_mov_b32 v8, s41\n"
"v_mov_b32 v10, 1\n"
"s_mov_b64 exec, 1\n"
"ds_add_rtn_u32 v9, v8, v10\n"
"s_mov_b64 exec, -1\n"
"s_waitcnt lgkmcnt(0)\n"
"v_readfirstlane_b32 s41, v9\n"
"s_and_b32 s41, s41, 3\n"
"s_cmp_lg_u32 s41, 3\n"
"s_cbranch_scc1 Lar_53\n"
"s_lshl_b32 s43, s19, 7\n"
"s_xor_b32 s43, s43, s22\n"
"s_xor_b32 s43, s43, 1212416\n"
"s_add_u32 s42, s59, 1\n"
"v_mov_b32 v4, s42\n"
"buffer_store_dword v4, v3, s[24:27], s43 offen sc1\n"
"Lpf_54:\n"
"Lar_53:\n"
"s_mov_b32 s21, 1\n"
"ds_read_b128 v[84:87], v6 offset:0\n"
"ds_read_b128 v[88:91], v6 offset:18432\n"
"ds_read_b128 v[92:95], v6 offset:1024\n"
"ds_read_b128 v[96:99], v6 offset:19456\n"
"ds_read_b128 v[100:103], v6 offset:2048\n"
"ds_read_b128 v[104:107], v6 offset:20480\n"
"s_mov_b32 s62, s56\n"
"s_mov_b32 s63, s57\n"
"s_add_u32 s53, s21, 1\n"
"s_min_u32 s53, s53, 63\n"
"s_lshr_b32 s40, s53, 5\n"
"s_cmp_gt_i32 s55, s40\n"
"s_cbranch_scc1 Lln_56\n"
"s_lshr_b32 s41, s53, 1\n"
"s_and_b32 s41, s41, 15\n"
"Lln_55:\n"
"s_xor_b32 s43, s22, 1245184\n"
"buffer_load_dword v4, v5, s[24:27], s43 offen sc1\n"
"s_waitcnt vmcnt(0)\n"
"v_cmp_ge_i32 vcc, s40, v4\n"
"v_readlane_b32 s42, v4, s41\n"
"s_cbranch_vccnz Lln_57\n"
"s_add_u32 s55, s40, 1\n"
"Lln_57:\n"
"s_cmp_gt_i32 s42, s40\n"
"s_cbranch_scc1 Lln_56\n"
"s_sleep 2\n"
"s_memrealtime s[48:49]\n"
"s_waitcnt lgkmcnt(0)\n"
"s_sub_u32 s48, s48, s23\n"
"s_cmp_gt_i32 s48, 0\n"
"s_cbranch_scc1 LX1_end\n"
"s_branch Lln_55\n"
"Lln_56:\n"
"s_mul_i32 s54, s53, 36864\n"
"s_add_u32 s54, s54, 165675008\n"
"s_mov_b32 s56, s58\n"
"s_add_u32 s58, s58, 1048576\n"
"s_lshr_b32 s43, s56, 8\n"
"s_xor_b32 s43, s43, s22\n"
"s_and_b32 s43, s43, 8192\n"
"s_or_b32 s56, s56, s43\n"
"s_lshl_b32 s36, s20, 10\n"
"s_add_u32 s56, s56, s36\n"
"s_add_u32 s57, s56, s36\n"
"s_add_u32 s57, s57, 16384\n"
"s_waitcnt lgkmcnt(4)\n"
"v_mfma_f32_16x16x32_bf16 v[164:167], a[0:3], v[84:87], 0\n"
"ds_read_b128 v[108:111], v6 offset:3072\n"
"v_mfma_f32_16x16x32_bf16 v[180:183], v[84:87], a[8:11], 0\n"
"ds_read_b128 v[112:115], v6 offset:21504\n"
"v_mfma_f32_16x16x32_bf16 v[172:175], a[4:7], v[84:87], 0\n"
"v_mfma_f32_16x16x32_bf16 v[188:191], v[84:87], a[12:15], 0\n"
"v_mfma_f32_16x16x32_bf16 v[168:171], a[0:3], v[88:91], 0\n"
"v_mfma_f32_16x16x32_bf16 v[184:187], v[88:91], a[8:11], 0\n"
"v_mfma_f32_16x16x32_bf16 v[176:179], a[4:7], v[88:91], 0\n"
"v_mfma_f32_16x16x32_bf16 v[192:195], v[88:91], a[12:15], 0\n"
"s_waitcnt lgkmcnt(4)\n"
"v_mfma_f32_16x16x32_bf16 v[164:167], a[16:19], v[92:95], v[164:167]\n"
"ds_read_b128 v[84:87], v6 offset:4096\n"
"v_mfma_f32_16x16x32_bf16 v[180:183], v[92:95], a[24:27], v[180:183]\n"
"ds_read_b128 v[88:91], v6 offset:22528\n"
"v_mfma_f32_16x16x32_bf16 v[172:175], a[20:23], v[92:95], v[172:175]\n"
"v_mfma_f32_16x16x32_bf16 v[188:191], v[92:95], a[28:31], v[188:191]\n"
"v_mfma_f32_16x16x32_bf16 v[168:171], a[16:19], v[96:99], v[168:171]\n"
"v_mfma_f32_16x16x32_bf16 v[184:187], v[96:99], a[24:27], v[184:187]\n"
"v_mfma_f32_16x16x32_bf16 v[176:179], a[20:23], v[96:99], v[176:179]\n"
"v_mfma_f32_16x16x32_bf16 v[192:195], v[96:99], a[28:31], v[192:195]\n"
"s_waitcnt lgkmcnt(4)\n"
"v_mfma_f32_16x16x32_bf16 v[164:167], a[32:35], v[100:103], v[164:167]\n"
"ds_read_b128 v[92:95], v6 offset:5120\n"
"v_mfma_f32_16x16x32_bf16 v[180:183], v[100:103], a[40:43], v[180:183]\n"
"ds_read_b128 v[96:99], v6 offset:23552\n"
"v_mfma_f32_16x16x32_bf16 v[172:175], a[36:39], v[100:103], v[172:175]\n"
"buffer_load_dwordx4 v[48:51], v1, s[24:27], s54 offen offset:0 sc1\n"
"v_mfma_f32_16x16x32_bf16 v[188:191], v[100:103], a[44:47], v[188:191]\n"
"v_mfma_f32_16x16x32_bf16 v[168:171], a[32:35], v[104:107], v[168:171]\n"
"v_mfma_f32_16x16x32_bf16 v[184:187], v[104:107], a[40:43], v[184:187]\n"
"buffer_load_dwordx4 v[52:55], v1, s[24:27], s54 offen offset:1024 sc1\n"
"v_mfma_f32_16x16x32_bf16 v[176:179], a[36:39], v[104:107], v[176:179]\n"
"v_mfma_f32_16x16x32_bf16 v[192:195], v[104:107], a[44:47], v[192:195]\n"
"s_waitcnt lgkmcnt(4)\n"
"v_mfma_f32_16x16x32_bf16 v[164:167], a[48:51], v[108:111], v[164:167]\n"
"ds_read_b128 v[100:103], v6 offset:6144\n"
"v_mfma_f32_16x16x32_bf16 v[180:183], v[108:111], a[56:59], v[180:183]\n"
"ds_read_b128 v[104:107], v6 offset:24576\n"
"v_mfma_f32_16x16x32_bf16 v[172:175], a[52:55], v[108:111], v[172:175]\n"
"buffer_load_dwordx4 v[56:59], v1, s[24:27], s54 offen offset:2048 sc1\n"
"v_mfma_f32_16x16x32_bf16 v[188:191], v[108:111], a[60:63], v[188:191]\n"
"v_mfma_f32_16x16x32_bf16 v[168:171], a[48:51], v[112:115], v[168:171]\n"
"v_mfma_f32_16x16x32_bf16 v[184:187], v[112:115], a[56:59], v[184:187]\n"
"buffer_load_dwordx4 v[60:63], v1, s[24:27], s54 offen offset:3072 sc1\n"
"s_add_u32 s54, s54, 4096\n"
"v_mfma_f32_16x16x32_bf16 v[176:179], a[52:55], v[112:115], v[176:179]\n"
"v_mfma_f32_16x16x32_bf16 v[192:195], v[112:115], a[60:63], v[192:195]\n"
"s_waitcnt lgkmcnt(4)\n"
"v_mfma_f32_16x16x32_bf16 v[164:167], a[64:67], v[84:87], v[164:167]\n"
"ds_read_b128 v[108:111], v6 offset:7168\n"
"v_mfma_f32_16x16x32_bf16 v[180:183], v[84:87], a[72:75], v[180:183]\n"
"ds_read_b128 v[112:115], v6 offset:25600\n"
"v_mfma_f32_16x16x32_bf16 v[172:175], a[68:71], v[84:87], v[172:175]\n"
"buffer_load_dwordx4 v[64:67], v1, s[24:27], s54 offen offset:0 sc1\n"
"v_mfma_f32_16x16x32_bf16 v[188:191], v[84:87], a[76:79], v[188:191]\n"
"v_mfma_f32_16x16x32_bf16 v[168:171], a[64:67], v[88:91], v[168:171]\n"
"v_mfma_f32_16x16x32_bf16 v[184:187], v[88:91], a[72:75], v[184:187]\n"
"buffer_load_dwordx4 v[68:71], v1, s[24:27], s54 offen offset:1024 sc1\n"
"v_mfma_f32_16x16x32_bf16 v[176:179], a[68:71], v[88:91], v[176:179]\n"
"v_mfma_f32_16x16x32_bf16 v[192:195], v[88:91], a[76:79], v[192:195]\n"
"s_waitcnt lgkmcnt(4)\n"
"v_mfma_f32_16x16x32_bf16 v[164:167], a[80:83], v[92:95], v[164:167]\n"
"ds_read_b128 v[84:87], v6 offset:8192\n"
"v_mfma_f32_16x16x32_bf16 v[180:183], v[92:95], a[88:91], v[180:183]\n"
"ds_read_b128 v[88:91], v6 offset:26624\n"
"v_mfma_f32_16x16x32_bf16 v[172:175], a[84:87], v[92:95], v[172:175]\n"
"buffer_load_dwordx4 v[72:75], v1, s[24:27], s54 offen offset:2048 sc1\n"
"v_mfma_f32_16x16x32_bf16 v[188:191], v[92:95], a[92:95], v[188:191]\n"
"v_mfma_f32_16x16x32_bf16 v[168:171], a[80:83], v[96:99], v[168:171]\n"
"v_mfma_f32_16x16x32_bf16 v[184:187], v[96:99], a[88:91], v[184:187]\n"
"buffer_load_dwordx4 v[76:79], v1, s[24:27], s54 offen offset:3072 sc1\n"
"s_add_u32 s54, s54, 4096\n"
"v_mfma_f32_16x16x32_bf16 v[176:179], a[84:87], v[96:99], v[176:179]\n"
"v_mfma_f32_16x16x32_bf16 v[192:195], v[96:99], a[92:95], v[192:195]\n"
"s_waitcnt lgkmcnt(4)\n"
"v_mfma_f32_16x16x32_bf16 v[164:167], a[96:99], v[100:103], v[164:167]\n"
"ds_read_b128 v[92:95], v6 offset:9216\n"
"v_mfma_f32_16x16x32_bf16 v[180:183], v[100:103], a[104:107], v[180:183]\n"
"ds_read_b128 v[96:99], v6 offset:27648\n"
"v_mfma_f32_16x16x32_bf16 v[172:175], a[100:103], v[100:103], v[172:175]\n"
"buffer_load_dwordx4 v[80:83], v1, s[24:27], s54 offen offset:0 sc1\n"
"v_mfma_f32_16x16x32_bf16 v[188:191], v[100:103], a[108:111], v[188:191]\n"
"v_mfma_f32_16x16x32_bf16 v[168:171], a[96:99], v[104:107], v[168:171]\n"
"v_mfma_f32_16x16x32_bf16 v[184:187], v[104:107], a[104:107], v[184:187]\n"
"v_mfma_f32_16x16x32_bf16 v[176:179], a[100:103], v[104:107], v[176:179]\n"
"v_mfma_f32_16x16x32_bf16 v[192:195], v[104:107], a[108:111], v[192:195]\n"
"s_waitcnt lgkmcnt(4)\n"
"v_mfma_f32_16x16x32_bf16 v[164:167], a[112:115], v[108:111], v[164:167]\n"
"ds_read_b128 v[100:103], v6 offset:10240\n"
"v_mfma_f32_16x16x32_bf16 v[180:183], v[108:111], a[120:123], v[180:183]\n"
"ds_read_b128 v[104:107], v6 offset:28672\n"
"v_mfma_f32_16x16x32_bf16 v[172:175], a[116:119], v[108:111], v[172:175]\n"
"v_mfma_f32_16x16x32_bf16 v[188:191], v[108:111], a[124:127], v[188:191]\n"
"v_mfma_f32_16x16x32_bf16 v[168:171], a[112:115], v[112:115], v[168:171]\n"
"v_mfma_f32_16x16x32_bf16 v[184:187], v[112:115], a[120:123], v[184:187]\n"
"v_mfma_f32_16x16x32_bf16 v[176:179], a[116:119], v[112:115], v[176:179]\n"
"v_mfma_f32_16x16x32_bf16 v[192:195], v[112:115], a[124:127], v[192:195]\n"
"s_waitcnt lgkmcnt(4)\n"
"v_mfma_f32_16x16x32_bf16 v[164:167], a[128:131], v[84:87], v[164:167]\n"
"ds_read_b128 v[108:111], v6 offset:11264\n"
"v_mfma_f32_16x16x32_bf16 v[180:183], v[84:87], a[136:139], v[180:183]\n"
"ds_read_b128 v[112:115], v6 offset:29696\n"
"v_mfma_f32_16x16x32_bf16 v[172:175], a[132:135], v[84:87], v[172:175]\n"
"v_mfma_f32_16x16x32_bf16 v[188:191], v[84:87], a[140:143], v[188:191]\n"
"v_mfma_f32_16x16x32_bf16 v[168:171], a[128:131], v[88:91], v[168:171]\n"
"v_mfma_f32_16x16x32_bf16 v[184:187], v[88:91], a[136:139], v[184:187]\n"
"v_mfma_f32_16x16x32_bf16 v[176:179], a[132:135], v[88:91], v[176:179]\n"
"v_mfma_f32_16x16x32_bf16 v[192:195], v[88:91], a[140:143], v[192:195]\n"
"s_waitcnt lgkmcnt(4)\n"
"v_mfma_f32_16x16x32_bf16 v[164:167], a[144:147], v[92:95], v[164:167]\n"
"ds_read_b128 v[84:87], v6 offset:12288\n"
"v_mfma_f32_16x16x32_bf16 v[180:183], v[92:95], a[152:155], v[180:183]\n"
"ds_read_b128 v[88:91], v6 offset:30720\n"
"v_mfma_f32_16x16x32_bf16 v[172:175], a[148:151], v[92:95], v[172:175]\n"
"v_mfma_f32_16x16x32_bf16 v[188:191], v[92:95], a[156:159], v[188:191]\n"
"v_mfma_f32_16x16x32_bf16 v[168:171], a[144:147], v[96:99], v[168:171]\n"
"v_mfma_f32_16x16x32_bf16 v[184:187], v[96:99], a[152:155], v[184:187]\n"
"v_mfma_f32_16x16x32_bf16 v[176:179], a[148:151], v[96:99], v[176:179]\n"
"v_mfma_f32_16x16x32_bf16 v[192:195], v[96:99], a[156:159], v[192:195]\n"
"s_waitcnt lgkmcnt(4)\n"
"v_mfma_f32_16x16x32_bf16 v[164:167], a[160:163], v[100:103], v[164:167]\n"
"ds_read_b128 v[92:95], v6 offset:13312\n"
"v_mfma_f32_16x16x32_bf16 v[180:183], v[100:103], a[168:171], v[180:183]\n"
"ds_read_b128 v[96:99], v6 offset:31744\n"
"v_mfma_f32_16x16x32_bf16 v[172:175], a[164:167], v[100:103], v[172:175]\n"
"v_mfma_f32_16x16x32_bf16 v[188:191], v[100:103], a[172:175], v[188:191]\n"
"v_mfma_f32_16x16x32_bf16 v[168:171], a[160:163], v[104:107], v[168:171]\n"
"v_mfma_f32_16x16x32_bf16 v[184:187], v[104:107], a[168:171], v[184:187]\n"
"v_mfma_f32_16x16x32_bf16 v[176:179], a[164:167], v[104:107], v[176:179]\n"
"v_mfma_f32_16x16x32_bf16 v[192:195], v[104:107], a[172:175], v[192:195]\n"
"s_waitcnt lgkmcnt(4)\n"
"v_mfma_f32_16x16x32_bf16 v[164:167], a[176:179], v[108:111], v[164:167]\n"
"ds_read_b128 v[100:103], v6 offset:14336\n"
"v_mfma_f32_16x16x32_bf16 v[180:183], v[108:111], a[184:187], v[180:183]\n"
"ds_read_b128 v[104:107], v6 offset:32768\n"
"v_mfma_f32_16x16x32_bf16 v[172:175], a[180:183], v[108:111], v[172:175]\n"
"v_mfma_f32_16x16x32_bf16 v[188:191], v[108:111], a[188:191], v[188:191]\n"
"v_mfma_f32_16x16x32_bf16 v[168:171], a[176:179], v[112:115], v[168:171]\n"
"v_mfma_f32_16x16x32_bf16 v[184:187], v[112:115], a[184:187], v[184:187]\n"
"v_mfma_f32_16x16x32_bf16 v[176:179], a[180:183], v[112:115], v[176:179]\n"
"v_mfma_f32_16x16x32_bf16 v[192:195], v[112:115], a[188:191], v[192:195]\n"
"s_waitcnt lgkmcnt(4)\n"
"v_mfma_f32_16x16x32_bf16 v[164:167], a[192:195], v[84:87], v[164:167]\n"
"ds_read_b128 v[108:111], v6 offset:15360\n"
"v_mfma_f32_16x16x32_bf16 v[180:183], v[84:87], a[200:203], v[180:183]\n"
"ds_read_b128 v[112:115], v6 offset:33792\n"
"v_mfma_f32_16x16x32_bf16 v[172:175], a[196:199], v[84:87], v[172:175]\n"
"v_mfma_f32_16x16x32_bf16 v[188:191], v[84:87], a[204:207], v[188:191]\n"
"v_mfma_f32_16x16x32_bf16 v[168:171], a[192:195], v[88:91], v[168:171]\n"
"v_mfma_f32_16x16x32_bf16 v[184:187], v[88:91], a[200:203], v[184:187]\n"
"v_mfma_f32_16x16x32_bf16 v[176:179], a[196:199], v[88:91], v[176:179]\n"
"v_mfma_f32_16x16x32_bf16 v[192:195], v[88:91], a[204:207], v[192:195]\n"
"s_waitcnt lgkmcnt(4)\n"
"s_waitcnt vmcnt(0)\n"
"v_mfma_f32_16x16x32_bf16 v[164:167], a[208:211], v[92:95], v[164:167]\n"
"ds_read_b128 v[84:87], v6 offset:16384\n"
"v_mfma_f32_16x16x32_bf16 v[180:183], v[92:95], a[216:219], v[180:183]\n"
"ds_read_b128 v[88:91], v6 offset:34816\n"
"v_mfma_f32_16x16x32_bf16 v[172:175], a[212:215], v[92:95], v[172:175]\n"
"ds_write_b128 v7, v[48:51] offset:0\n"
"v_mfma_f32_16x16x32_bf16 v[188:191], v[92:95], a[220:223], v[188:191]\n"
"v_mfma_f32_16x16x32_bf16 v[168:171], a[208:211], v[96:99], v[168:171]\n"
"v_mfma_f32_16x16x32_bf16 v[184:187], v[96:99], a[216:219], v[184:187]\n"
"ds_write_b128 v7, v[52:55] offset:1024\n"
"v_mfma_f32_16x16x32_bf16 v[176:179], a[212:215], v[96:99], v[176:179]\n"
"v_mfma_f32_16x16x32_bf16 v[192:195], v[96:99], a[220:223], v[192:195]\n"
"s_waitcnt lgkmcnt(6)\n"
"v_mfma_f32_16x16x32_bf16 v[164:167], a[224:227], v[100:103], v[164:167]\n"
"ds_read_b128 v[92:95], v6 offset:17408\n"
"v_mfma_f32_16x16x32_bf16 v[180:183], v[100:103], a[232:235], v[180:183]\n"
"ds_read_b128 v[96:99], v6 offset:35840\n"
"v_mfma_f32_16x16x32_bf16 v[172:175], a[228:231], v[100:103], v[172:175]\n"
"ds_write_b128 v7, v[56:59] offset:2048\n"
"v_mfma_f32_16x16x32_bf16 v[188:191], v[100:103], a[236:239], v[188:191]\n"
"v_mfma_f32_16x16x32_bf16 v[168:171], a[224:227], v[104:107], v[168:171]\n"
"v_mfma_f32_16x16x32_bf16 v[184:187], v[104:107], a[232:235], v[184:187]\n"
"ds_write_b128 v7, v[60:63] offset:3072\n"
"v_mfma_f32_16x16x32_bf16 v[176:179], a[228:231], v[104:107], v[176:179]\n"
"v_mfma_f32_16x16x32_bf16 v[192:195], v[104:107], a[236:239], v[192:195]\n"
"s_waitcnt lgkmcnt(8)\n"
"v_mfma_f32_16x16x32_bf16 v[164:167], a[240:243], v[108:111], v[164:167]\n"
"v_mfma_f32_16x16x32_bf16 v[180:183], v[108:111], a[248:251], v[180:183]\n"
"v_mfma_f32_16x16x32_bf16 v[172:175], a[244:247], v[108:111], v[172:175]\n"
"ds_write_b128 v7, v[64:67] offset:4096\n"
"v_mfma_f32_16x16x32_bf16 v[188:191], v[108:111], a[252:255], v[188:191]\n"
"v_mfma_f32_16x16x32_bf16 v[168:171], a[240:243], v[112:115], v[168:171]\n"
"v_mfma_f32_16x16x32_bf16 v[184:187], v[112:115], a[248:251], v[184:187]\n"
"ds_write_b128 v7, v[68:71] offset:5120\n"
"v_mfma_f32_16x16x32_bf16 v[176:179], a[244:247], v[112:115], v[176:179]\n"
"v_mfma_f32_16x16x32_bf16 v[192:195], v[112:115], a[252:255], v[192:195]\n"
"s_waitcnt lgkmcnt(8)\n"
"v_mfma_f32_16x16x32_bf16 v[164:167], v[16:19], v[84:87], v[164:167]\n"
"v_mfma_f32_16x16x32_bf16 v[180:183], v[84:87], v[24:27], v[180:183]\n"
"v_mfma_f32_16x16x32_bf16 v[172:175], v[20:23], v[84:87], v[172:175]\n"
"ds_write_b128 v7, v[72:75] offset:6144\n"
"v_mfma_f32_16x16x32_bf16 v[188:191], v[84:87], v[28:31], v[188:191]\n"
"v_mfma_f32_16x16x32_bf16 v[168:171], v[16:19], v[88:91], v[168:171]\n"
"v_mfma_f32_16x16x32_bf16 v[184:187], v[88:91], v[24:27], v[184:187]\n"
"ds_write_b128 v7, v[76:79] offset:7168\n"
"v_mfma_f32_16x16x32_bf16 v[176:179], v[20:23], v[88:91], v[176:179]\n"
"v_mfma_f32_16x16x32_bf16 v[192:195], v[88:91], v[28:31], v[192:195]\n"
"s_waitcnt lgkmcnt(6)\n"
"v_mfma_f32_16x16x32_bf16 v[164:167], v[32:35], v[92:95], v[164:167]\n"
"v_mfma_f32_16x16x32_bf16 v[180:183], v[92:95], v[40:43], v[180:183]\n"
"v_mfma_f32_16x16x32_bf16 v[172:175], v[36:39], v[92:95], v[172:175]\n"
"ds_write_b128 v7, v[80:83] offset:8192\n"
"v_mfma_f32_16x16x32_bf16 v[188:191], v[92:95], v[44:47], v[188:191]\n"
"v_mfma_f32_16x16x32_bf16 v[168:171], v[32:35], v[96:99], v[168:171]\n"
"v_mfma_f32_16x16x32_bf16 v[184:187], v[96:99], v[40:43], v[184:187]\n"
"v_mfma_f32_16x16x32_bf16 v[176:179], v[36:39], v[96:99], v[176:179]\n"
"v_mfma_f32_16x16x32_bf16 v[192:195], v[96:99], v[44:47], v[192:195]\n"
"v_xor_b32 v4, v6, v2\n"
"v_sub_u32 v7, v7, v1\n"
"v_add_u32 v6, v7, v2\n"
"v_add_u32 v7, v4, v1\n"
"s_waitcnt lgkmcnt(0)\n"
"s_barrier\n"
"LX1_loop:\n"
"s_add_u32 s21, s21, 1\n"
"ds_read_b128 v[84:87], v6 offset:0\n"
"ds_read_b128 v[88:91], v6 offset:18432\n"
"ds_read_b128 v[92:95], v6 offset:1024\n"
"ds_read_b128 v[96:99], v6 offset:19456\n"
"ds_read_b128 v[100:103], v6 offset:2048\n"
"ds_read_b128 v[104:107], v6 offset:20480\n"
"s_mov_b32 s62, s56\n"
"s_mov_b32 s63, s57\n"
"s_add_u32 s53, s21, 1\n"
"s_min_u32 s53, s53, 63\n"
"s_lshr_b32 s40, s53, 5\n"
"s_cmp_gt_i32 s55, s40\n"
"s_cbranch_scc1 Lln_59\n"
"s_lshr_b32 s41, s53, 1\n"
"s_and_b32 s41, s41, 15\n"
"Lln_58:\n"
"s_xor_b32 s43, s22, 1245184\n"
"buffer_load_dword v4, v5, s[24:27], s43 offen sc1\n"
"s_waitcnt vmcnt(0)\n"
"v_cmp_ge_i32 vcc, s40, v4\n"
"v_readlane_b32 s42, v4, s41\n"
"s_cbranch_vccnz Lln_60\n"
"s_add_u32 s55, s40, 1\n"
"Lln_60:\n"
"s_cmp_gt_i32 s42, s40\n"
"s_cbranch_scc1 Lln_59\n"
"s_sleep 2\n"
"s_memrealtime s[48:49]\n"
"s_waitcnt lgkmcnt(0)\n"
"s_sub_u32 s48, s48, s23\n"
"s_cmp_gt_i32 s48, 0\n"
"s_cbranch_scc1 LX1_end\n"
"s_branch Lln_58\n"
"Lln_59:\n"
"s_mul_i32 s54, s53, 36864\n"
"s_add_u32 s54, s54, 165675008\n"
"s_mov_b32 s56, s58\n"
"s_add_u32 s58, s58, 1048576\n"
"s_lshr_b32 s43, s56, 8\n"
"s_xor_b32 s43, s43, s22\n"
"s_and_b32 s43, s43, 8192\n"
"s_or_b32 s56, s56, s43\n"
"s_lshl_b32 s36, s20, 10\n"
"s_add_u32 s56, s56, s36\n"
"s_add_u32 s57, s56, s36\n"
"s_add_u32 s57, s57, 16384\n"
"s_waitcnt lgkmcnt(4)\n"
"v_mfma_f32_16x16x32_bf16 v[116:119], a[0:3], v[84:87], 0\n"
"ds_read_b128 v[108:111], v6 offset:3072\n"
"v_cvt_pk_bf16_f32 v148, v164, v165\n"
"v_mfma_f32_16x16x32_bf16 v[132:135], v[84:87], a[8:11], 0\n"
"ds_read_b128 v[112:115], v6 offset:21504\n"
"v_cvt_pk_bf16_f32 v149, v166, v167\n"
"v_mfma_f32_16x16x32_bf16 v[124:127], a[4:7], v[84:87], 0\n"
"v_cvt_pk_bf16_f32 v150, v172, v173\n"
"v_cvt_pk_bf16_f32 v151, v174, v175\n"
"v_mfma_f32_16x16x32_bf16 v[140:143], v[84:87], a[12:15], 0\n"
"buffer_store_dwordx4 v[148:151], v2, s[24:27], s62 offen sc1\n"
"v_cvt_pk_bf16_f32 v152, v168, v169\n"
"v_mfma_f32_16x16x32_bf16 v[120:123], a[0:3], v[88:91], 0\n"
"v_cvt_pk_bf16_f32 v153, v170, v171\n"
"v_cvt_pk_bf16_f32 v154, v176, v177\n"
"v_mfma_f32_16x16x32_bf16 v[136:139], v[88:91], a[8:11], 0\n"
"v_cvt_pk_bf16_f32 v155, v178, v179\n"
"s_add_u32 s62, s62, 4096\n"
"buffer_store_dwordx4 v[152:155], v2, s[24:27], s62 offen sc1\n"
"v_mfma_f32_16x16x32_bf16 v[128:131], a[4:7], v[88:91], 0\n"
"v_cvt_pk_bf16_f32 v156, v180, v181\n"
"v_cvt_pk_bf16_f32 v157, v182, v183\n"
"v_mfma_f32_16x16x32_bf16 v[144:147], v[88:91], a[12:15], 0\n"
"v_cvt_pk_bf16_f32 v158, v184, v185\n"
"v_cvt_pk_bf16_f32 v159, v186, v187\n"
"s_waitcnt lgkmcnt(4)\n"
"v_mfma_f32_16x16x32_bf16 v[116:119], a[16:19], v[92:95], v[116:119]\n"
"ds_read_b128 v[84:87], v6 offset:4096\n"
"buffer_store_dwordx4 v[156:159], v2, s[24:27], s63 offen offset:0 sc1\n"
"v_mfma_f32_16x16x32_bf16 v[132:135], v[92:95], a[24:27], v[132:135]\n"
"ds_read_b128 v[88:91], v6 offset:22528\n"
"v_cvt_pk_bf16_f32 v160, v188, v189\n"
"v_mfma_f32_16x16x32_bf16 v[124:127], a[20:23], v[92:95], v[124:127]\n"
"v_cvt_pk_bf16_f32 v161, v190, v191\n"
"v_cvt_pk_bf16_f32 v162, v192, v193\n"
"v_mfma_f32_16x16x32_bf16 v[140:143], v[92:95], a[28:31], v[140:143]\n"
"v_cvt_pk_bf16_f32 v163, v194, v195\n"
"buffer_store_dwordx4 v[160:163], v2, s[24:27], s63 offen offset:1024 sc1\n"
"v_mfma_f32_16x16x32_bf16 v[120:123], a[16:19], v[96:99], v[120:123]\n"
"v_mfma_f32_16x16x32_bf16 v[136:139], v[96:99], a[24:27], v[136:139]\n"
"v_mfma_f32_16x16x32_bf16 v[128:131], a[20:23], v[96:99], v[128:131]\n"
"v_mfma_f32_16x16x32_bf16 v[144:147], v[96:99], a[28:31], v[144:147]\n"
"s_waitcnt lgkmcnt(4)\n"
"v_mfma_f32_16x16x32_bf16 v[116:119], a[32:35], v[100:103], v[116:119]\n"
"ds_read_b128 v[92:95], v6 offset:5120\n"
"v_mfma_f32_16x16x32_bf16 v[132:135], v[100:103], a[40:43], v[132:135]\n"
"ds_read_b128 v[96:99], v6 offset:23552\n"
"v_mfma_f32_16x16x32_bf16 v[124:127], a[36:39], v[100:103], v[124:127]\n"
"buffer_load_dwordx4 v[48:51], v1, s[24:27], s54 offen offset:0 sc1\n"
"v_mfma_f32_16x16x32_bf16 v[140:143], v[100:103], a[44:47], v[140:143]\n"
"v_mfma_f32_16x16x32_bf16 v[120:123], a[32:35], v[104:107], v[120:123]\n"
"v_mfma_f32_16x16x32_bf16 v[136:139], v[104:107], a[40:43], v[136:139]\n"
"buffer_load_dwordx4 v[52:55], v1, s[24:27], s54 offen offset:1024 sc1\n"
"v_mfma_f32_16x16x32_bf16 v[128:131], a[36:39], v[104:107], v[128:131]\n"
"v_mfma_f32_16x16x32_bf16 v[144:147], v[104:107], a[44:47], v[144:147]\n"
"s_waitcnt lgkmcnt(4)\n"
"v_mfma_f32_16x16x32_bf16 v[116:119], a[48:51], v[108:111], v[116:119]\n"
"ds_read_b128 v[100:103], v6 offset:6144\n"
"v_mfma_f32_16x16x32_bf16 v[132:135], v[108:111], a[56:59], v[132:135]\n"
"ds_read_b128 v[104:107], v6 offset:24576\n"
"v_mfma_f32_16x16x32_bf16 v[124:127], a[52:55], v[108:111], v[124:127]\n"
"buffer_load_dwordx4 v[56:59], v1, s[24:27], s54 offen offset:2048 sc1\n"
"v_mfma_f32_16x16x32_bf16 v[140:143], v[108:111], a[60:63], v[140:143]\n"
"v_mfma_f32_16x16x32_bf16 v[120:123], a[48:51], v[112:115], v[120:123]\n"
"v_mfma_f32_16x16x32_bf16 v[136:139], v[112:115], a[56:59], v[136:139]\n"
"buffer_load_dwordx4 v[60:63], v1, s[24:27], s54 offen offset:3072 sc1\n"
"s_add_u32 s54, s54, 4096\n"
"v_mfma_f32_16x16x32_bf16 v[128:131], a[52:55], v[112:115], v[128:131]\n"
"v_mfma_f32_16x16x32_bf16 v[144:147], v[112:115], a[60:63], v[144:147]\n"
"s_waitcnt lgkmcnt(4)\n"
"v_mfma_f32_16x16x32_bf16 v[116:119], a[64:67], v[84:87], v[116:119]\n"
"ds_read_b128 v[108:111], v6 offset:7168\n"
"v_mfma_f32_16x16x32_bf16 v[132:135], v[84:87], a[72:75], v[132:135]\n"
"ds_read_b128 v[112:115], v6 offset:25600\n"
"v_mfma_f32_16x16x32_bf16 v[124:127], a[68:71], v[84:87], v[124:127]\n"
"buffer_load_dwordx4 v[64:67], v1, s[24:27], s54 offen offset:0 sc1\n"
"v_mfma_f32_16x16x32_bf16 v[140:143], v[84:87], a[76:79], v[140:143]\n"
"v_mfma_f32_16x16x32_bf16 v[120:123], a[64:67], v[88:91], v[120:123]\n"
"v_mfma_f32_16x16x32_bf16 v[136:139], v[88:91], a[72:75], v[136:139]\n"
"buffer_load_dwordx4 v[68:71], v1, s[24:27], s54 offen offset:1024 sc1\n"
"v_mfma_f32_16x16x32_bf16 v[128:131], a[68:71], v[88:91], v[128:131]\n"
"v_mfma_f32_16x16x32_bf16 v[144:147], v[88:91], a[76:79], v[144:147]\n"
"s_waitcnt lgkmcnt(4)\n"
"v_mfma_f32_16x16x32_bf16 v[116:119], a[80:83], v[92:95], v[116:119]\n"
"ds_read_b128 v[84:87], v6 offset:8192\n"
"v_mfma_f32_16x16x32_bf16 v[132:135], v[92:95], a[88:91], v[132:135]\n"
"ds_read_b128 v[88:91], v6 offset:26624\n"
"v_mfma_f32_16x16x32_bf16 v[124:127], a[84:87], v[92:95], v[124:127]\n"
"buffer_load_dwordx4 v[72:75], v1, s[24:27], s54 offen offset:2048 sc1\n"
"v_mfma_f32_16x16x32_bf16 v[140:143], v[92:95], a[92:95], v[140:143]\n"
"v_mfma_f32_16x16x32_bf16 v[120:123], a[80:83], v[96:99], v[120:123]\n"
"v_mfma_f32_16x16x32_bf16 v[136:139], v[96:99], a[88:91], v[136:139]\n"
"buffer_load_dwordx4 v[76:79], v1, s[24:27], s54 offen offset:3072 sc1\n"
"s_add_u32 s54, s54, 4096\n"
"v_mfma_f32_16x16x32_bf16 v[128:131], a[84:87], v[96:99], v[128:131]\n"
"v_mfma_f32_16x16x32_bf16 v[144:147], v[96:99], a[92:95], v[144:147]\n"
"s_waitcnt lgkmcnt(4)\n"
"v_mfma_f32_16x16x32_bf16 v[116:119], a[96:99], v[100:103], v[116:119]\n"
"ds_read_b128 v[92:95], v6 offset:9216\n"
"v_mfma_f32_16x16x32_bf16 v[132:135], v[100:103], a[104:107], v[132:135]\n"
"ds_read_b128 v[96:99], v6 offset:27648\n"
"v_mfma_f32_16x16x32_bf16 v[124:127], a[100:103], v[100:103], v[124:127]\n"
"buffer_load_dwordx4 v[80:83], v1, s[24:27], s54 offen offset:0 sc1\n"
"v_mfma_f32_16x16x32_bf16 v[140:143], v[100:103], a[108:111], v[140:143]\n"
"v_mfma_f32_16x16x32_bf16 v[120:123], a[96:99], v[104:107], v[120:123]\n"
"v_mfma_f32_16x16x32_bf16 v[136:139], v[104:107], a[104:107], v[136:139]\n"
"v_mfma_f32_16x16x32_bf16 v[128:131], a[100:103], v[104:107], v[128:131]\n"
"v_mfma_f32_16x16x32_bf16 v[144:147], v[104:107], a[108:111], v[144:147]\n"
"s_waitcnt lgkmcnt(4)\n"
"v_mfma_f32_16x16x32_bf16 v[116:119], a[112:115], v[108:111], v[116:119]\n"
"ds_read_b128 v[100:103], v6 offset:10240\n"
"v_mfma_f32_16x16x32_bf16 v[132:135], v[108:111], a[120:123], v[132:135]\n"
"ds_read_b128 v[104:107], v6 offset:28672\n"
"v_mfma_f32_16x16x32_bf16 v[124:127], a[116:119], v[108:111], v[124:127]\n"
"v_mfma_f32_16x16x32_bf16 v[140:143], v[108:111], a[124:127], v[140:143]\n"
"v_mfma_f32_16x16x32_bf16 v[120:123], a[112:115], v[112:115], v[120:123]\n"
"v_mfma_f32_16x16x32_bf16 v[136:139], v[112:115], a[120:123], v[136:139]\n"
"v_mfma_f32_16x16x32_bf16 v[128:131], a[116:119], v[112:115], v[128:131]\n"
"v_mfma_f32_16x16x32_bf16 v[144:147], v[112:115], a[124:127], v[144:147]\n"
"s_waitcnt lgkmcnt(4)\n"
"v_mfma_f32_16x16x32_bf16 v[116:119], a[128:131], v[84:87], v[116:119]\n"
"ds_read_b128 v[108:111], v6 offset:11264\n"
"v_mfma_f32_16x16x32_bf16 v[132:135], v[84:87], a[136:139], v[132:135]\n"
"ds_read_b128 v[112:115], v6 offset:29696\n"
"v_mfma_f32_16x16x32_bf16 v[124:127], a[132:135], v[84:87], v[124:127]\n"
"v_mfma_f32_16x16x32_bf16 v[140:143], v[84:87], a[140:143], v[140:143]\n"
"v_mfma_f32_16x16x32_bf16 v[120:123], a[128:131], v[88:91], v[120:123]\n"
"v_mfma_f32_16x16x32_bf16 v[136:139], v[88:91], a[136:139], v[136:139]\n"
"v_mfma_f32_16x16x32_bf16 v[128:131], a[132:135], v[88:91], v[128:131]\n"
"v_mfma_f32_16x16x32_bf16 v[144:147], v[88:91], a[140:143], v[144:147]\n"
"s_waitcnt lgkmcnt(4)\n"
"v_mfma_f32_16x16x32_bf16 v[116:119], a[144:147], v[92:95], v[116:119]\n"
"ds_read_b128 v[84:87], v6 offset:12288\n"
"v_mfma_f32_16x16x32_bf16 v[132:135], v[92:95], a[152:155], v[132:135]\n"
"ds_read_b128 v[88:91], v6 offset:30720\n"
"v_mfma_f32_16x16x32_bf16 v[124:127], a[148:151], v[92:95], v[124:127]\n"
"v_mfma_f32_16x16x32_bf16 v[140:143], v[92:95], a[156:159], v[140:143]\n"
"v_mfma_f32_16x16x32_bf16 v[120:123], a[144:147], v[96:99], v[120:123]\n"
"v_mfma_f32_16x16x32_bf16 v[136:139], v[96:99], a[152:155], v[136:139]\n"
"v_mfma_f32_16x16x32_bf16 v[128:131], a[148:151], v[96:99], v[128:131]\n"
"v_mfma_f32_16x16x32_bf16 v[144:147], v[96:99], a[156:159], v[144:147]\n"
"s_waitcnt lgkmcnt(4)\n"
"v_mfma_f32_16x16x32_bf16 v[116:119], a[160:163], v[100:103], v[116:119]\n"
"ds_read_b128 v[92:95], v6 offset:13312\n"
"v_mfma_f32_16x16x32_bf16 v[132:135], v[100:103], a[168:171], v[132:135]\n"
"ds_read_b128 v[96:99], v6 offset:31744\n"
"v_mfma_f32_16x16x32_bf16 v[124:127], a[164:167], v[100:103], v[124:127]\n"
"v_mfma_f32_16x16x32_bf16 v[140:143], v[100:103], a[172:175], v[140:143]\n"
"v_mfma_f32_16x16x32_bf16 v[120:123], a[160:163], v[104:107], v[120:123]\n"
"v_mfma_f32_16x16x32_bf16 v[136:139], v[104:107], a[168:171], v[136:139]\n"
"v_mfma_f32_16x16x32_bf16 v[128:131], a[164:167], v[104:107], v[128:131]\n"
"v_mfma_f32_16x16x32_bf16 v[144:147], v[104:107], a[172:175], v[144:147]\n"
"s_waitcnt lgkmcnt(4)\n"
"v_mfma_f32_16x16x32_bf16 v[116:119], a[176:179], v[108:111], v[116:119]\n"
"ds_read_b128 v[100:103], v6 offset:14336\n"
"v_mfma_f32_16x16x32_bf16 v[132:135], v[108:111], a[184:187], v[132:135]\n"
"ds_read_b128 v[104:107], v6 offset:32768\n"
"v_mfma_f32_16x16x32_bf16 v[124:127], a[180:183], v[108:111], v[124:127]\n"
"v_mfma_f32_16x16x32_bf16 v[140:143], v[108:111], a[188:191], v[140:143]\n"
"v_mfma_f32_16x16x32_bf16 v[120:123], a[176:179], v[112:115], v[120:123]\n"
"v_mfma_f32_16x16x32_bf16 v[136:139], v[112:115], a[184:187], v[136:139]\n"
"v_mfma_f32_16x16x32_bf16 v[128:131], a[180:183], v[112:115], v[128:131]\n"
"v_mfma_f32_16x16x32_bf16 v[144:147], v[112:115], a[188:191], v[144:147]\n"
"s_waitcnt lgkmcnt(4)\n"
"v_mfma_f32_16x16x32_bf16 v[116:119], a[192:195], v[84:87], v[116:119]\n"
"ds_read_b128 v[108:111], v6 offset:15360\n"
"v_mfma_f32_16x16x32_bf16 v[132:135], v[84:87], a[200:203], v[132:135]\n"
"ds_read_b128 v[112:115], v6 offset:33792\n"
"v_mfma_f32_16x16x32_bf16 v[124:127], a[196:199], v[84:87], v[124:127]\n"
"v_mfma_f32_16x16x32_bf16 v[140:143], v[84:87], a[204:207], v[140:143]\n"
"v_mfma_f32_16x16x32_bf16 v[120:123], a[192:195], v[88:91], v[120:123]\n"
"v_mfma_f32_16x16x32_bf16 v[136:139], v[88:91], a[200:203], v[136:139]\n"
"v_mfma_f32_16x16x32_bf16 v[128:131], a[196:199], v[88:91], v[128:131]\n"
"v_mfma_f32_16x16x32_bf16 v[144:147], v[88:91], a[204:207], v[144:147]\n"
"s_waitcnt lgkmcnt(4)\n"
"s_waitcnt vmcnt(0)\n"
"s_add_u32 s59, s21, -1\n"
"s_and_b32 s41, s59, 1\n"
"s_lshl_b32 s41, s41, 2\n"
"s_add_u32 s41, s41, 90112\n"
"v_mov_b32 v8, s41\n"
"v_mov_b32 v10, 1\n"
"s_mov_b64 exec, 1\n"
"ds_add_rtn_u32 v9, v8, v10\n"
"s_mov_b64 exec, -1\n"
"v_mfma_f32_16x16x32_bf16 v[116:119], a[208:211], v[92:95], v[116:119]\n"
"ds_read_b128 v[84:87], v6 offset:16384\n"
"v_mfma_f32_16x16x32_bf16 v[132:135], v[92:95], a[216:219], v[132:135]\n"
"ds_read_b128 v[88:91], v6 offset:34816\n"
"v_mfma_f32_16x16x32_bf16 v[124:127], a[212:215], v[92:95], v[124:127]\n"
"ds_write_b128 v7, v[48:51] offset:0\n"
"v_mfma_f32_16x16x32_bf16 v[140:143], v[92:95], a[220:223], v[140:143]\n"
"v_mfma_f32_16x16x32_bf16 v[120:123], a[208:211], v[96:99], v[120:123]\n"
"v_mfma_f32_16x16x32_bf16 v[136:139], v[96:99], a[216:219], v[136:139]\n"
"ds_write_b128 v7, v[52:55] offset:1024\n"
"v_mfma_f32_16x16x32_bf16 v[128:131], a[212:215], v[96:99], v[128:131]\n"
"v_mfma_f32_16x16x32_bf16 v[144:147], v[96:99], a[220:223], v[144:147]\n"
"s_waitcnt lgkmcnt(7)\n"
"v_mfma_f32_16x16x32_bf16 v[116:119], a[224:227], v[100:103], v[116:119]\n"
"ds_read_b128 v[92:95], v6 offset:17408\n"
"v_mfma_f32_16x16x32_bf16 v[132:135], v[100:103], a[232:235], v[132:135]\n"
"ds_read_b128 v[96:99], v6 offset:35840\n"
"v_mfma_f32_16x16x32_bf16 v[124:127], a[228:231], v[100:103], v[124:127]\n"
"ds_write_b128 v7, v[56:59] offset:2048\n"
"v_mfma_f32_16x16x32_bf16 v[140:143], v[100:103], a[236:239], v[140:143]\n"
"v_mfma_f32_16x16x32_bf16 v[120:123], a[224:227], v[104:107], v[120:123]\n"
"v_mfma_f32_16x16x32_bf16 v[136:139], v[104:107], a[232:235], v[136:139]\n"
"ds_write_b128 v7, v[60:63] offset:3072\n"
"v_mfma_f32_16x16x32_bf16 v[128:131], a[228:231], v[104:107], v[128:131]\n"
"v_mfma_f32_16x16x32_bf16 v[144:147], v[104:107], a[236:239], v[144:147]\n"
"s_waitcnt lgkmcnt(9)\n"
"v_mfma_f32_16x16x32_bf16 v[116:119], a[240:243], v[108:111], v[116:119]\n"
"v_mfma_f32_16x16x32_bf16 v[132:135], v[108:111], a[248:251], v[132:135]\n"
"v_mfma_f32_16x16x32_bf16 v[124:127], a[244:247], v[108:111], v[124:127]\n"
"ds_write_b128 v7, v[64:67] offset:4096\n"
"v_mfma_f32_16x16x32_bf16 v[140:143], v[108:111], a[252:255], v[140:143]\n"
"v_mfma_f32_16x16x32_bf16 v[120:123], a[240:243], v[112:115], v[120:123]\n"
"v_mfma_f32_16x16x32_bf16 v[136:139], v[112:115], a[248:251], v[136:139]\n"
"ds_write_b128 v7, v[68:71] offset:5120\n"
"v_mfma_f32_16x16x32_bf16 v[128:131], a[244:247], v[112:115], v[128:131]\n"
"v_mfma_f32_16x16x32_bf16 v[144:147], v[112:115], a[252:255], v[144:147]\n"
"s_waitcnt lgkmcnt(8)\n"
"s_waitcnt lgkmcnt(10)\n"
"v_readfirstlane_b32 s41, v9\n"
"s_and_b32 s41, s41, 3\n"
"s_cmp_lg_u32 s41, 3\n"
"s_cbranch_scc1 Lar_61\n"
"s_lshl_b32 s43, s19, 7\n"
"s_xor_b32 s43, s43, s22\n"
"s_xor_b32 s43, s43, 1212416\n"
"s_add_u32 s42, s59, 1\n"
"v_mov_b32 v4, s42\n"
"buffer_store_dword v4, v3, s[24:27], s43 offen sc1\n"
"Lpf_62:\n"
"Lar_61:\n"
"v_mfma_f32_16x16x32_bf16 v[116:119], v[16:19], v[84:87], v[116:119]\n"
"v_mfma_f32_16x16x32_bf16 v[132:135], v[84:87], v[24:27], v[132:135]\n"
"v_mfma_f32_16x16x32_bf16 v[124:127], v[20:23], v[84:87], v[124:127]\n"
"ds_write_b128 v7, v[72:75] offset:6144\n"
"v_mfma_f32_16x16x32_bf16 v[140:143], v[84:87], v[28:31], v[140:143]\n"
"v_mfma_f32_16x16x32_bf16 v[120:123], v[16:19], v[88:91], v[120:123]\n"
"v_mfma_f32_16x16x32_bf16 v[136:139], v[88:91], v[24:27], v[136:139]\n"
"ds_write_b128 v7, v[76:79] offset:7168\n"
"v_mfma_f32_16x16x32_bf16 v[128:131], v[20:23], v[88:91], v[128:131]\n"
"v_mfma_f32_16x16x32_bf16 v[144:147], v[88:91], v[28:31], v[144:147]\n"
"s_waitcnt lgkmcnt(6)\n"
"v_mfma_f32_16x16x32_bf16 v[116:119], v[32:35], v[92:95], v[116:119]\n"
"v_mfma_f32_16x16x32_bf16 v[132:135], v[92:95], v[40:43], v[132:135]\n"
"v_mfma_f32_16x16x32_bf16 v[124:127], v[36:39], v[92:95], v[124:127]\n"
"ds_write_b128 v7, v[80:83] offset:8192\n"
"v_mfma_f32_16x16x32_bf16 v[140:143], v[92:95], v[44:47], v[140:143]\n"
"v_mfma_f32_16x16x32_bf16 v[120:123], v[32:35], v[96:99], v[120:123]\n"
"v_mfma_f32_16x16x32_bf16 v[136:139], v[96:99], v[40:43], v[136:139]\n"
"v_mfma_f32_16x16x32_bf16 v[128:131], v[36:39], v[96:99], v[128:131]\n"
"v_mfma_f32_16x16x32_bf16 v[144:147], v[96:99], v[44:47], v[144:147]\n"
"v_xor_b32 v4, v6, v2\n"
"v_sub_u32 v7, v7, v1\n"
"v_add_u32 v6, v7, v2\n"
"v_add_u32 v7, v4, v1\n"
"s_waitcnt lgkmcnt(0)\n"
"s_barrier\n"
"s_add_u32 s21, s21, 1\n"
"ds_read_b128 v[84:87], v6 offset:0\n"
"ds_read_b128 v[88:91], v6 offset:18432\n"
"ds_read_b128 v[92:95], v6 offset:1024\n"
"ds_read_b128 v[96:99], v6 offset:19456\n"
"ds_read_b128 v[100:103], v6 offset:2048\n"
"ds_read_b128 v[104:107], v6 offset:20480\n"
"s_mov_b32 s62, s56\n"
"s_mov_b32 s63, s57\n"
"s_add_u32 s53, s21, 1\n"
"s_min_u32 s53, s53, 63\n"
"s_lshr_b32 s40, s53, 5\n"
"s_cmp_gt_i32 s55, s40\n"
"s_cbranch_scc1 Lln_64\n"
"s_lshr_b32 s41, s53, 1\n"
"s_and_b32 s41, s41, 15\n"
"Lln_63:\n"
"s_xor_b32 s43, s22, 1245184\n"
"buffer_load_dword v4, v5, s[24:27], s43 offen sc1\n"
"s_waitcnt vmcnt(0)\n"
"v_cmp_ge_i32 vcc, s40, v4\n"
"v_readlane_b32 s42, v4, s41\n"
"s_cbranch_vccnz Lln_65\n"
"s_add_u32 s55, s40, 1\n"
"Lln_65:\n"
"s_cmp_gt_i32 s42, s40\n"
"s_cbranch_scc1 Lln_64\n"
"s_sleep 2\n"
"s_memrealtime s[48:49]\n"
"s_waitcnt lgkmcnt(0)\n"
"s_sub_u32 s48, s48, s23\n"
"s_cmp_gt_i32 s48, 0\n"
"s_cbranch_scc1 LX1_end\n"
"s_branch Lln_63\n"
"Lln_64:\n"
"s_mul_i32 s54, s53, 36864\n"
"s_add_u32 s54, s54, 165675008\n"
"s_mov_b32 s56, s58\n"
"s_add_u32 s58, s58, 1048576\n"
"s_lshr_b32 s43, s56, 8\n"
"s_xor_b32 s43, s43, s22\n"
"s_and_b32 s43, s43, 8192\n"
"s_or_b32 s56, s56, s43\n"
"s_lshl_b32 s36, s20, 10\n"
"s_add_u32 s56, s56, s36\n"
"s_add_u32 s57, s56, s36\n"
"s_add_u32 s57, s57, 16384\n"
"s_waitcnt lgkmcnt(4)\n"
"v_mfma_f32_16x16x32_bf16 v[164:167], a[0:3], v[84:87], 0\n"
"ds_read_b128 v[108:111], v6 offset:3072\n"
"v_cvt_pk_bf16_f32 v148, v116, v117\n"
"v_mfma_f32_16x16x32_bf16 v[180:183], v[84:87], a[8:11], 0\n"
"ds_read_b128 v[112:115], v6 offset:21504\n"
"v_cvt_pk_bf16_f32 v149, v118, v119\n"
"v_mfma_f32_16x16x32_bf16 v[172:175], a[4:7], v[84:87], 0\n"
"v_cvt_pk_bf16_f32 v150, v124, v125\n"
"v_cvt_pk_bf16_f32 v151, v126, v127\n"
"v_mfma_f32_16x16x32_bf16 v[188:191], v[84:87], a[12:15], 0\n"
"buffer_store_dwordx4 v[148:151], v2, s[24:27], s62 offen sc1\n"
"v_cvt_pk_bf16_f32 v152, v120, v121\n"
"v_mfma_f32_16x16x32_bf16 v[168:171], a[0:3], v[88:91], 0\n"
"v_cvt_pk_bf16_f32 v153, v122, v123\n"
"v_cvt_pk_bf16_f32 v154, v128, v129\n"
"v_mfma_f32_16x16x32_bf16 v[184:187], v[88:91], a[8:11], 0\n"
"v_cvt_pk_bf16_f32 v155, v130, v131\n"
"s_add_u32 s62, s62, 4096\n"
"buffer_store_dwordx4 v[152:155], v2, s[24:27], s62 offen sc1\n"
"v_mfma_f32_16x16x32_bf16 v[176:179], a[4:7], v[88:91], 0\n"
"v_cvt_pk_bf16_f32 v156, v132, v133\n"
"v_cvt_pk_bf16_f32 v157, v134, v135\n"
"v_mfma_f32_16x16x32_bf16 v[192:195], v[88:91], a[12:15], 0\n"
"v_cvt_pk_bf16_f32 v158, v136, v137\n"
"v_cvt_pk_bf16_f32 v159, v138, v139\n"
"s_waitcnt lgkmcnt(4)\n"
"v_mfma_f32_16x16x32_bf16 v[164:167], a[16:19], v[92:95], v[164:167]\n"
"ds_read_b128 v[84:87], v6 offset:4096\n"
"buffer_store_dwordx4 v[156:159], v2, s[24:27], s63 offen offset:0 sc1\n"
"v_mfma_f32_16x16x32_bf16 v[180:183], v[92:95], a[24:27], v[180:183]\n"
"ds_read_b128 v[88:91], v6 offset:22528\n"
"v_cvt_pk_bf16_f32 v160, v140, v141\n"
"v_mfma_f32_16x16x32_bf16 v[172:175], a[20:23], v[92:95], v[172:175]\n"
"v_cvt_pk_bf16_f32 v161, v142, v143\n"
"v_cvt_pk_bf16_f32 v162, v144, v145\n"
"v_mfma_f32_16x16x32_bf16 v[188:191], v[92:95], a[28:31], v[188:191]\n"
"v_cvt_pk_bf16_f32 v163, v146, v147\n"
"buffer_store_dwordx4 v[160:163], v2, s[24:27], s63 offen offset:1024 sc1\n"
"v_mfma_f32_16x16x32_bf16 v[168:171], a[16:19], v[96:99], v[168:171]\n"
"v_mfma_f32_16x16x32_bf16 v[184:187], v[96:99], a[24:27], v[184:187]\n"
"v_mfma_f32_16x16x32_bf16 v[176:179], a[20:23], v[96:99], v[176:179]\n"
"v_mfma_f32_16x16x32_bf16 v[192:195], v[96:99], a[28:31], v[192:195]\n"
"s_waitcnt lgkmcnt(4)\n"
"v_mfma_f32_16x16x32_bf16 v[164:167], a[32:35], v[100:103], v[164:167]\n"
"ds_read_b128 v[92:95], v6 offset:5120\n"
"v_mfma_f32_16x16x32_bf16 v[180:183], v[100:103], a[40:43], v[180:183]\n"
"ds_read_b128 v[96:99], v6 offset:23552\n"
"v_mfma_f32_16x16x32_bf16 v[172:175], a[36:39], v[100:103], v[172:175]\n"
"buffer_load_dwordx4 v[48:51], v1, s[24:27], s54 offen offset:0 sc1\n"
"v_mfma_f32_16x16x32_bf16 v[188:191], v[100:103], a[44:47], v[188:191]\n"
"v_mfma_f32_16x16x32_bf16 v[168:171], a[32:35], v[104:107], v[168:171]\n"
"v_mfma_f32_16x16x32_bf16 v[184:187], v[104:107], a[40:43], v[184:187]\n"
"buffer_load_dwordx4 v[52:55], v1, s[24:27], s54 offen offset:1024 sc1\n"
"v_mfma_f32_16x16x32_bf16 v[176:179], a[36:39], v[104:107], v[176:179]\n"
"v_mfma_f32_16x16x32_bf16 v[192:195], v[104:107], a[44:47], v[192:195]\n"
"s_waitcnt lgkmcnt(4)\n"
"v_mfma_f32_16x16x32_bf16 v[164:167], a[48:51], v[108:111], v[164:167]\n"
"ds_read_b128 v[100:103], v6 offset:6144\n"
"v_mfma_f32_16x16x32_bf16 v[180:183], v[108:111], a[56:59], v[180:183]\n"
"ds_read_b128 v[104:107], v6 offset:24576\n"
"v_mfma_f32_16x16x32_bf16 v[172:175], a[52:55], v[108:111], v[172:175]\n"
"buffer_load_dwordx4 v[56:59], v1, s[24:27], s54 offen offset:2048 sc1\n"
"v_mfma_f32_16x16x32_bf16 v[188:191], v[108:111], a[60:63], v[188:191]\n"
"v_mfma_f32_16x16x32_bf16 v[168:171], a[48:51], v[112:115], v[168:171]\n"
"v_mfma_f32_16x16x32_bf16 v[184:187], v[112:115], a[56:59], v[184:187]\n"
"buffer_load_dwordx4 v[60:63], v1, s[24:27], s54 offen offset:3072 sc1\n"
"s_add_u32 s54, s54, 4096\n"
"v_mfma_f32_16x16x32_bf16 v[176:179], a[52:55], v[112:115], v[176:179]\n"
"v_mfma_f32_16x16x32_bf16 v[192:195], v[112:115], a[60:63], v[192:195]\n"
"s_waitcnt lgkmcnt(4)\n"
"v_mfma_f32_16x16x32_bf16 v[164:167], a[64:67], v[84:87], v[164:167]\n"
"ds_read_b128 v[108:111], v6 offset:7168\n"
"v_mfma_f32_16x16x32_bf16 v[180:183], v[84:87], a[72:75], v[180:183]\n"
"ds_read_b128 v[112:115], v6 offset:25600\n"
"v_mfma_f32_16x16x32_bf16 v[172:175], a[68:71], v[84:87], v[172:175]\n"
"buffer_load_dwordx4 v[64:67], v1, s[24:27], s54 offen offset:0 sc1\n"
"v_mfma_f32_16x16x32_bf16 v[188:191], v[84:87], a[76:79], v[188:191]\n"
"v_mfma_f32_16x16x32_bf16 v[168:171], a[64:67], v[88:91], v[168:171]\n"
"v_mfma_f32_16x16x32_bf16 v[184:187], v[88:91], a[72:75], v[184:187]\n"
"buffer_load_dwordx4 v[68:71], v1, s[24:27], s54 offen offset:1024 sc1\n"
"v_mfma_f32_16x16x32_bf16 v[176:179], a[68:71], v[88:91], v[176:179]\n"
"v_mfma_f32_16x16x32_bf16 v[192:195], v[88:91], a[76:79], v[192:195]\n"
"s_waitcnt lgkmcnt(4)\n"
"v_mfma_f32_16x16x32_bf16 v[164:167], a[80:83], v[92:95], v[164:167]\n"
"ds_read_b128 v[84:87], v6 offset:8192\n"
"v_mfma_f32_16x16x32_bf16 v[180:183], v[92:95], a[88:91], v[180:183]\n"
"ds_read_b128 v[88:91], v6 offset:26624\n"
"v_mfma_f32_16x16x32_bf16 v[172:175], a[84:87], v[92:95], v[172:175]\n"
"buffer_load_dwordx4 v[72:75], v1, s[24:27], s54 offen offset:2048 sc1\n"
"v_mfma_f32_16x16x32_bf16 v[188:191], v[92:95], a[92:95], v[188:191]\n"
"v_mfma_f32_16x16x32_bf16 v[168:171], a[80:83], v[96:99], v[168:171]\n"
"v_mfma_f32_16x16x32_bf16 v[184:187], v[96:99], a[88:91], v[184:187]\n"
"buffer_load_dwordx4 v[76:79], v1, s[24:27], s54 offen offset:3072 sc1\n"
"s_add_u32 s54, s54, 4096\n"
"v_mfma_f32_16x16x32_bf16 v[176:179], a[84:87], v[96:99], v[176:179]\n"
"v_mfma_f32_16x16x32_bf16 v[192:195], v[96:99], a[92:95], v[192:195]\n"
"s_waitcnt lgkmcnt(4)\n"
"v_mfma_f32_16x16x32_bf16 v[164:167], a[96:99], v[100:103], v[164:167]\n"
"ds_read_b128 v[92:95], v6 offset:9216\n"
"v_mfma_f32_16x16x32_bf16 v[180:183], v[100:103], a[104:107], v[180:183]\n"
"ds_read_b128 v[96:99], v6 offset:27648\n"
"v_mfma_f32_16x16x32_bf16 v[172:175], a[100:103], v[100:103], v[172:175]\n"
"buffer_load_dwordx4 v[80:83], v1, s[24:27], s54 offen offset:0 sc1\n"
"v_mfma_f32_16x16x32_bf16 v[188:191], v[100:103], a[108:111], v[188:191]\n"
"v_mfma_f32_16x16x32_bf16 v[168:171], a[96:99], v[104:107], v[168:171]\n"
"v_mfma_f32_16x16x32_bf16 v[184:187], v[104:107], a[104:107], v[184:187]\n"
"v_mfma_f32_16x16x32_bf16 v[176:179], a[100:103], v[104:107], v[176:179]\n"
"v_mfma_f32_16x16x32_bf16 v[192:195], v[104:107], a[108:111], v[192:195]\n"
"s_waitcnt lgkmcnt(4)\n"
"v_mfma_f32_16x16x32_bf16 v[164:167], a[112:115], v[108:111], v[164:167]\n"
"ds_read_b128 v[100:103], v6 offset:10240\n"
"v_mfma_f32_16x16x32_bf16 v[180:183], v[108:111], a[120:123], v[180:183]\n"
"ds_read_b128 v[104:107], v6 offset:28672\n"
"v_mfma_f32_16x16x32_bf16 v[172:175], a[116:119], v[108:111], v[172:175]\n"
"v_mfma_f32_16x16x32_bf16 v[188:191], v[108:111], a[124:127], v[188:191]\n"
"v_mfma_f32_16x16x32_bf16 v[168:171], a[112:115], v[112:115], v[168:171]\n"
"v_mfma_f32_16x16x32_bf16 v[184:187], v[112:115], a[120:123], v[184:187]\n"
"v_mfma_f32_16x16x32_bf16 v[176:179], a[116:119], v[112:115], v[176:179]\n"
"v_mfma_f32_16x16x32_bf16 v[192:195], v[112:115], a[124:127], v[192:195]\n"
"s_waitcnt lgkmcnt(4)\n"
"v_mfma_f32_16x16x32_bf16 v[164:167], a[128:131], v[84:87], v[164:167]\n"
"ds_read_b128 v[108:111], v6 offset:11264\n"
"v_mfma_f32_16x16x32_bf16 v[180:183], v[84:87], a[136:139], v[180:183]\n"
"ds_read_b128 v[112:115], v6 offset:29696\n"
"v_mfma_f32_16x16x32_bf16 v[172:175], a[132:135], v[84:87], v[172:175]\n"
"v_mfma_f32_16x16x32_bf16 v[188:191], v[84:87], a[140:143], v[188:191]\n"
"v_mfma_f32_16x16x32_bf16 v[168:171], a[128:131], v[88:91], v[168:171]\n"
"v_mfma_f32_16x16x32_bf16 v[184:187], v[88:91], a[136:139], v[184:187]\n"
"v_mfma_f32_16x16x32_bf16 v[176:179], a[132:135], v[88:91], v[176:179]\n"
"v_mfma_f32_16x16x32_bf16 v[192:195], v[88:91], a[140:143], v[192:195]\n"
"s_waitcnt lgkmcnt(4)\n"
"v_mfma_f32_16x16x32_bf16 v[164:167], a[144:147], v[92:95], v[164:167]\n"
"ds_read_b128 v[84:87], v6 offset:12288\n"
"v_mfma_f32_16x16x32_bf16 v[180:183], v[92:95], a[152:155], v[180:183]\n"
"ds_read_b128 v[88:91], v6 offset:30720\n"
"v_mfma_f32_16x16x32_bf16 v[172:175], a[148:151], v[92:95], v[172:175]\n"
"v_mfma_f32_16x16x32_bf16 v[188:191], v[92:95], a[156:159], v[188:191]\n"
"v_mfma_f32_16x16x32_bf16 v[168:171], a[144:147], v[96:99], v[168:171]\n"
"v_mfma_f32_16x16x32_bf16 v[184:187], v[96:99], a[152:155], v[184:187]\n"
"v_mfma_f32_16x16x32_bf16 v[176:179], a[148:151], v[96:99], v[176:179]\n"
"v_mfma_f32_16x16x32_bf16 v[192:195], v[96:99], a[156:159], v[192:195]\n"
"s_waitcnt lgkmcnt(4)\n"
"v_mfma_f32_16x16x32_bf16 v[164:167], a[160:163], v[100:103], v[164:167]\n"
"ds_read_b128 v[92:95], v6 offset:13312\n"
"v_mfma_f32_16x16x32_bf16 v[180:183], v[100:103], a[168:171], v[180:183]\n"
"ds_read_b128 v[96:99], v6 offset:31744\n"
"v_mfma_f32_16x16x32_bf16 v[172:175], a[164:167], v[100:103], v[172:175]\n"
"v_mfma_f32_16x16x32_bf16 v[188:191], v[100:103], a[172:175], v[188:191]\n"
"v_mfma_f32_16x16x32_bf16 v[168:171], a[160:163], v[104:107], v[168:171]\n"
"v_mfma_f32_16x16x32_bf16 v[184:187], v[104:107], a[168:171], v[184:187]\n"
"v_mfma_f32_16x16x32_bf16 v[176:179], a[164:167], v[104:107], v[176:179]\n"
"v_mfma_f32_16x16x32_bf16 v[192:195], v[104:107], a[172:175], v[192:195]\n"
"s_waitcnt lgkmcnt(4)\n"
"v_mfma_f32_16x16x32_bf16 v[164:167], a[176:179], v[108:111], v[164:167]\n"
"ds_read_b128 v[100:103], v6 offset:14336\n"
"v_mfma_f32_16x16x32_bf16 v[180:183], v[108:111], a[184:187], v[180:183]\n"
"ds_read_b128 v[104:107], v6 offset:32768\n"
"v_mfma_f32_16x16x32_bf16 v[172:175], a[180:183], v[108:111], v[172:175]\n"
"v_mfma_f32_16x16x32_bf16 v[188:191], v[108:111], a[188:191], v[188:191]\n"
"v_mfma_f32_16x16x32_bf16 v[168:171], a[176:179], v[112:115], v[168:171]\n"
"v_mfma_f32_16x16x32_bf16 v[184:187], v[112:115], a[184:187], v[184:187]\n"
"v_mfma_f32_16x16x32_bf16 v[176:179], a[180:183], v[112:115], v[176:179]\n"
"v_mfma_f32_16x16x32_bf16 v[192:195], v[112:115], a[188:191], v[192:195]\n"
"s_waitcnt lgkmcnt(4)\n"
"v_mfma_f32_16x16x32_bf16 v[164:167], a[192:195], v[84:87], v[164:167]\n"
"ds_read_b128 v[108:111], v6 offset:15360\n"
"v_mfma_f32_16x16x32_bf16 v[180:183], v[84:87], a[200:203], v[180:183]\n"
"ds_read_b128 v[112:115], v6 offset:33792\n"
"v_mfma_f32_16x16x32_bf16 v[172:175], a[196:199], v[84:87], v[172:175]\n"
"v_mfma_f32_16x16x32_bf16 v[188:191], v[84:87], a[204:207], v[188:191]\n"
"v_mfma_f32_16x16x32_bf16 v[168:171], a[192:195], v[88:91], v[168:171]\n"
"v_mfma_f32_16x16x32_bf16 v[184:187], v[88:91], a[200:203], v[184:187]\n"
"v_mfma_f32_16x16x32_bf16 v[176:179], a[196:199], v[88:91], v[176:179]\n"
"v_mfma_f32_16x16x32_bf16 v[192:195], v[88:91], a[204:207], v[192:195]\n"
"s_waitcnt lgkmcnt(4)\n"
"s_waitcnt vmcnt(0)\n"
"s_add_u32 s59, s21, -1\n"
"s_and_b32 s41, s59, 1\n"
"s_lshl_b32 s41, s41, 2\n"
"s_add_u32 s41, s41, 90112\n"
"v_mov_b32 v8, s41\n"
"v_mov_b32 v10, 1\n"
"s_mov_b64 exec, 1\n"
"ds_add_rtn_u32 v9, v8, v10\n"
"s_mov_b64 exec, -1\n"
"v_mfma_f32_16x16x32_bf16 v[164:167], a[208:211], v[92:95], v[164:167]\n"
"ds_read_b128 v[84:87], v6 offset:16384\n"
"v_mfma_f32_16x16x32_bf16 v[180:183], v[92:95], a[216:219], v[180:183]\n"
"ds_read_b128 v[88:91], v6 offset:34816\n"
"v_mfma_f32_16x16x32_bf16 v[172:175], a[212:215], v[92:95], v[172:175]\n"
"ds_write_b128 v7, v[48:51] offset:0\n"
"v_mfma_f32_16x16x32_bf16 v[188:191], v[92:95], a[220:223], v[188:191]\n"
"v_mfma_f32_16x16x32_bf16 v[168:171], a[208:211], v[96:99], v[168:171]\n"
"v_mfma_f32_16x16x32_bf16 v[184:187], v[96:99], a[216:219], v[184:187]\n"
"ds_write_b128 v7, v[52:55] offset:1024\n"
"v_mfma_f32_16x16x32_bf16 v[176:179], a[212:215], v[96:99], v[176:179]\n"
"v_mfma_f32_16x16x32_bf16 v[192:195], v[96:99], a[220:223], v[192:195]\n"
"s_waitcnt lgkmcnt(7)\n"
"v_mfma_f32_16x16x32_bf16 v[164:167], a[224:227], v[100:103], v[164:167]\n"
"ds_read_b128 v[92:95], v6 offset:17408\n"
"v_mfma_f32_16x16x32_bf16 v[180:183], v[100:103], a[232:235], v[180:183]\n"
"ds_read_b128 v[96:99], v6 offset:35840\n"
"v_mfma_f32_16x16x32_bf16 v[172:175], a[228:231], v[100:103], v[172:175]\n"
"ds_write_b128 v7, v[56:59] offset:2048\n"
"v_mfma_f32_16x16x32_bf16 v[188:191], v[100:103], a[236:239], v[188:191]\n"
"v_mfma_f32_16x16x32_bf16 v[168:171], a[224:227], v[104:107], v[168:171]\n"
"v_mfma_f32_16x16x32_bf16 v[184:187], v[104:107], a[232:235], v[184:187]\n"
"ds_write_b128 v7, v[60:63] offset:3072\n"
"v_mfma_f32_16x16x32_bf16 v[176:179], a[228:231], v[104:107], v[176:179]\n"
"v_mfma_f32_16x16x32_bf16 v[192:195], v[104:107], a[236:239], v[192:195]\n"
"s_waitcnt lgkmcnt(9)\n"
"v_mfma_f32_16x16x32_bf16 v[164:167], a[240:243], v[108:111], v[164:167]\n"
"v_mfma_f32_16x16x32_bf16 v[180:183], v[108:111], a[248:251], v[180:183]\n"
"v_mfma_f32_16x16x32_bf16 v[172:175], a[244:247], v[108:111], v[172:175]\n"
"ds_write_b128 v7, v[64:67] offset:4096\n"
"v_mfma_f32_16x16x32_bf16 v[188:191], v[108:111], a[252:255], v[188:191]\n"
"v_mfma_f32_16x16x32_bf16 v[168:171], a[240:243], v[112:115], v[168:171]\n"
"v_mfma_f32_16x16x32_bf16 v[184:187], v[112:115], a[248:251], v[184:187]\n"
"ds_write_b128 v7, v[68:71] offset:5120\n"
"v_mfma_f32_16x16x32_bf16 v[176:179], a[244:247], v[112:115], v[176:179]\n"
"v_mfma_f32_16x16x32_bf16 v[192:195], v[112:115], a[252:255], v[192:195]\n"
"s_waitcnt lgkmcnt(8)\n"
"s_waitcnt lgkmcnt(10)\n"
"v_readfirstlane_b32 s41, v9\n"
"s_and_b32 s41, s41, 3\n"
"s_cmp_lg_u32 s41, 3\n"
"s_cbranch_scc1 Lar_66\n"
"s_lshl_b32 s43, s19, 7\n"
"s_xor_b32 s43, s43, s22\n"
"s_xor_b32 s43, s43, 1212416\n"
"s_add_u32 s42, s59, 1\n"
"v_mov_b32 v4, s42\n"
"buffer_store_dword v4, v3, s[24:27], s43 offen sc1\n"
"Lpf_67:\n"
"Lar_66:\n"
"v_mfma_f32_16x16x32_bf16 v[164:167], v[16:19], v[84:87], v[164:167]\n"
"v_mfma_f32_16x16x32_bf16 v[180:183], v[84:87], v[24:27], v[180:183]\n"
"v_mfma_f32_16x16x32_bf16 v[172:175], v[20:23], v[84:87], v[172:175]\n"
"ds_write_b128 v7, v[72:75] offset:6144\n"
"v_mfma_f32_16x16x32_bf16 v[188:191], v[84:87], v[28:31], v[188:191]\n"
"v_mfma_f32_16x16x32_bf16 v[168:171], v[16:19], v[88:91], v[168:171]\n"
"v_mfma_f32_16x16x32_bf16 v[184:187], v[88:91], v[24:27], v[184:187]\n"
"ds_write_b128 v7, v[76:79] offset:7168\n"
"v_mfma_f32_16x16x32_bf16 v[176:179], v[20:23], v[88:91], v[176:179]\n"
"v_mfma_f32_16x16x32_bf16 v[192:195], v[88:91], v[28:31], v[192:195]\n"
"s_waitcnt lgkmcnt(6)\n"
"v_mfma_f32_16x16x32_bf16 v[164:167], v[32:35], v[92:95], v[164:167]\n"
"v_mfma_f32_16x16x32_bf16 v[180:183], v[92:95], v[40:43], v[180:183]\n"
"v_mfma_f32_16x16x32_bf16 v[172:175], v[36:39], v[92:95], v[172:175]\n"
"ds_write_b128 v7, v[80:83] offset:8192\n"
"v_mfma_f32_16x16x32_bf16 v[188:191], v[92:95], v[44:47], v[188:191]\n"
"v_mfma_f32_16x16x32_bf16 v[168:171], v[32:35], v[96:99], v[168:171]\n"
"v_mfma_f32_16x16x32_bf16 v[184:187], v[96:99], v[40:43], v[184:187]\n"
"v_mfma_f32_16x16x32_bf16 v[176:179], v[36:39], v[96:99], v[176:179]\n"
"v_mfma_f32_16x16x32_bf16 v[192:195], v[96:99], v[44:47], v[192:195]\n"
"v_xor_b32 v4, v6, v2\n"
"v_sub_u32 v7, v7, v1\n"
"v_add_u32 v6, v7, v2\n"
"v_add_u32 v7, v4, v1\n"
"s_waitcnt lgkmcnt(0)\n"
"s_barrier\n"
"s_cmp_lt_u32 s21, 63\n"
"s_cbranch_scc1 LX1_loop\n"
"v_cvt_pk_bf16_f32 v148, v164, v165\n"
"v_cvt_pk_bf16_f32 v149, v166, v167\n"
"v_cvt_pk_bf16_f32 v150, v172, v173\n"
"v_cvt_pk_bf16_f32 v151, v174, v175\n"
"buffer_store_dwordx4 v[148:151], v2, s[24:27], s56 offen sc1\n"
"v_cvt_pk_bf16_f32 v152, v168, v169\n"
"v_cvt_pk_bf16_f32 v153, v170, v171\n"
"v_cvt_pk_bf16_f32 v154, v176, v177\n"
"v_cvt_pk_bf16_f32 v155, v178, v179\n"
"s_add_u32 s56, s56, 4096\n"
"buffer_store_dwordx4 v[152:155], v2, s[24:27], s56 offen sc1\n"
"v_cvt_pk_bf16_f32 v156, v180, v181\n"
"v_cvt_pk_bf16_f32 v157, v182, v183\n"
"v_cvt_pk_bf16_f32 v158, v184, v185\n"
"v_cvt_pk_bf16_f32 v159, v186, v187\n"
"buffer_store_dwordx4 v[156:159], v2, s[24:27], s57 offen offset:0 sc1\n"
"v_cvt_pk_bf16_f32 v160, v188, v189\n"
"v_cvt_pk_bf16_f32 v161, v190, v191\n"
"v_cvt_pk_bf16_f32 v162, v192, v193\n"
"v_cvt_pk_bf16_f32 v163, v194, v195\n"
"buffer_store_dwordx4 v[160:163], v2, s[24:27], s57 offen offset:1024 sc1\n"
"s_waitcnt vmcnt(0)\n"
"s_add_u32 s59, s21, 0\n"
"s_and_b32 s41, s59, 1\n"
"s_lshl_b32 s41, s41, 2\n"
"s_add_u32 s41, s41, 90112\n"
"v_mov_b32 v8, s41\n"
"v_mov_b32 v10, 1\n"
"s_mov_b64 exec, 1\n"
"ds_add_rtn_u32 v9, v8, v10\n"
"s_mov_b64 exec, -1\n"
"s_waitcnt lgkmcnt(0)\n"
"v_readfirstlane_b32 s41, v9\n"
"s_and_b32 s41, s41, 3\n"
"s_cmp_lg_u32 s41, 3\n"
"s_cbranch_scc1 Lar_68\n"
"s_lshl_b32 s43, s19, 7\n"
"s_xor_b32 s43, s43, s22\n"
"s_xor_b32 s43, s43, 1212416\n"
"s_add_u32 s42, s59, 1\n"
"v_mov_b32 v4, s42\n"
"buffer_store_dword v4, v3, s[24:27], s43 offen sc1\n"
"Lpf_69:\n"
"Lar_68:\n"
"LX1_end:\n"
"s_endpgm\n"
"LX2:\n"
"v_mov_b32 v3, 2146435072\n"
"v_cmp_eq_u32 vcc, 0, v0\n"
"s_nop 1\n"
"v_cndmask_b32 v3, v3, 0, vcc\n"
"v_lshlrev_b32 v2, 4, v0\n"
"s_mul_i32 s36, s20, 9216\n"
"v_add_u32 v1, s36, v2\n"
"s_mov_b32 s55, -1\n"
"v_min_u32 v5, 15, v0\n"
"v_lshlrev_b32 v5, 7, v5\n"
"s_and_b32 s59, s20, 1\n"
"s_lshr_b32 s60, s20, 1\n"
"s_mov_b32 s56, 0x3e0293ee\n"
"s_mov_b32 s57, -1\n"
"s_mov_b32 s58, -1\n"
"s_lshl_b32 s36, s19, 7\n"
"v_mov_b32 v12, s36\n"
"v_lshrrev_b32 v4, 4, v0\n"
"v_lshlrev_b32 v8, 7, v4\n"
"v_and_b32 v10, 1, v0\n"
"v_lshl_add_u32 v8, v10, 4, v8\n"
"v_bfe_u32 v10, v0, 2, 2\n"
"v_lshl_add_u32 v8, v10, 5, v8\n"
"v_lshlrev_b32 v9, 3, v0\n"
"s_lshl_b32 s36, s60, 10\n"
"s_add_u32 s36, s36, 2048\n"
"v_add_u32 v9, s36, v9\n"
"s_lshl_b32 s36, s20, 10\n"
"s_add_u32 s36, s36, 81920\n"
"v_add_u32 v13, s36, v2\n"
"s_lshl_b32 s36, s59, 12\n"
"s_add_u32 s36, s36, 81920\n"
"v_add_u32 v14, s36, v2\n"
"v_and_b32 v10, 15, v0\n"
"s_lshl_b32 s36, s59, 4\n"
"v_add_u32 v10, s36, v10\n"
"v_lshlrev_b32 v4, 2, v4\n"
"v_mov_b32 v11, 0xf149f2ca\n"
"v_add_u32 v244, 0, v4\n"
"v_cmp_le_u32 vcc, v244, v10\n"
"s_nop 1\n"
"v_cndmask_b32 v188, v11, 0, vcc\n"
"v_add_u32 v244, 1, v4\n"
"v_cmp_le_u32 vcc, v244, v10\n"
"s_nop 1\n"
"v_cndmask_b32 v189, v11, 0, vcc\n"
"v_add_u32 v244, 2, v4\n"
"v_cmp_le_u32 vcc, v244, v10\n"
"s_nop 1\n"
"v_cndmask_b32 v190, v11, 0, vcc\n"
"v_add_u32 v244, 3, v4\n"
"v_cmp_le_u32 vcc, v244, v10\n"
"s_nop 1\n"
"v_cndmask_b32 v191, v11, 0, vcc\n"
"v_add_u32 v244, 16, v4\n"
"v_cmp_le_u32 vcc, v244, v10\n"
"s_nop 1\n"
"v_cndmask_b32 v192, v11, 0, vcc\n"
"v_add_u32 v244, 17, v4\n"
"v_cmp_le_u32 vcc, v244, v10\n"
"s_nop 1\n"
"v_cndmask_b32 v193, v11, 0, vcc\n"
"v_add_u32 v244, 18, v4\n"
"v_cmp_le_u32 vcc, v244, v10\n"
"s_nop 1\n"
"v_cndmask_b32 v194, v11, 0, vcc\n"
"v_add_u32 v244, 19, v4\n"
"v_cmp_le_u32 vcc, v244, v10\n"
"s_nop 1\n"
"v_cndmask_b32 v195, v11, 0, vcc\n"
"v_mov_b32 v196, 0\n"
"v_mov_b32 v197, 0\n"
"v_mov_b32 v198, 0\n"
"v_mov_b32 v199, 0\n"
"v_mov_b32 v218, 0x3f803f80\n"
"v_mov_b32 v219, 0x3f803f80\n"
"v_mov_b32 v220, 0x3f803f80\n"
"v_mov_b32 v221, 0x3f803f80\n"
"s_sleep 32\n"
"s_sleep 32\n"
"s_sleep 32\n"
"s_sleep 32\n"
"s_sleep 32\n"
"s_sleep 32\n"
"s_sleep 32\n"
"v_lshrrev_b32 v248, 4, v0\n"
"v_mul_u32_u24 v244, 8192, v248\n"
"v_and_b32 v248, 15, v0\n"
"v_lshl_add_u32 v244, v248, 4, v244\n"
"s_mul_i32 s36, s20, 65536\n"
"v_add_u32 v244, s36, v244\n"
"v_lshlrev_b32 v245, 4, v0\n"
"s_mul_i32 s36, s20, 2048\n"
"s_add_u32 s36, s36, 98304\n"
"v_add_u32 v245, s36, v245\n"
"v_bfe_u32 v248, v0, 4, 2\n"
"v_lshlrev_b32 v248, 6, v248\n"
"v_xor_b32 v245, v245, v248\n"
"v_xor_b32 v246, 128, v245\n"
"v_lshrrev_b32 v248, 4, v0\n"
"v_lshlrev_b32 v247, 11, v248\n"
"v_bfe_u32 v248, v0, 2, 2\n"
"v_lshl_add_u32 v247, v248, 8, v247\n"
"v_bfe_u32 v248, v0, 2, 2\n"
"v_xor_b32 v248, s20, v248\n"
"v_lshl_add_u32 v247, v248, 6, v247\n"
"v_and_b32 v248, 3, v0\n"
"v_lshl_add_u32 v247, v248, 4, v247\n"
"v_add_u32 v247, 98304, v247\n"
"s_load_dwordx2 s[28:29], s[16:17], 16\n"
"s_mov_b32 s30, 4718592\n"
"s_mov_b32 s31, 0x00020000\n"
"s_waitcnt lgkmcnt(0)\n"
"s_and_b32 s29, s29, 0xffff\n"
"s_lshl_b32 s61, s19, 8\n"
"buffer_load_dwordx4 a[0:3], v244, s[28:31], s61 offen nt\n"
"s_add_u32 s61, s61, 32768\n"
"buffer_load_dwordx4 a[4:7], v244, s[28:31], s61 offen nt\n"
"s_add_u32 s61, s61, 229376\n"
"buffer_load_dwordx4 a[8:11], v244, s[28:31], s61 offen nt\n"
"s_add_u32 s61, s61, 32768\n"
"buffer_load_dwordx4 a[12:15], v244, s[28:31], s61 offen nt\n"
"s_add_u32 s61, s61, 229376\n"
"buffer_load_dwordx4 a[16:19], v244, s[28:31], s61 offen nt\n"
"s_add_u32 s61, s61, 32768\n"
"buffer_load_dwordx4 a[20:23], v244, s[28:31], s61 offen nt\n"
"s_add_u32 s61, s61, 229376\n"
"buffer_load_dwordx4 a[24:27], v244, s[28:31], s61 offen nt\n"
"s_add_u32 s61, s61, 32768\n"
"buffer_load_dwordx4 a[28:31], v244, s[28:31], s61 offen nt\n"
"s_add_u32 s61, s61, 229376\n"
"buffer_load_dwordx4 a[32:35], v244, s[28:31], s61 offen nt\n"
"s_add_u32 s61, s61, 32768\n"
"buffer_load_dwordx4 a[36:39], v244, s[28:31], s61 offen nt\n"
"s_add_u32 s61, s61, 229376\n"
"buffer_load_dwordx4 a[40:43], v244, s[28:31], s61 offen nt\n"
"s_add_u32 s61, s61, 32768\n"
"buffer_load_dwordx4 a[44:47], v244, s[28:31], s61 offen nt\n"
"s_add_u32 s61, s61, 229376\n"
"buffer_load_dwordx4 a[48:51], v244, s[28:31], s61 offen nt\n"
"s_add_u32 s61, s61, 32768\n"
"buffer_load_dwordx4 a[52:55], v244, s[28:31], s61 offen nt\n"
"s_add_u32 s61, s61, 229376\n"
"buffer_load_dwordx4 a[56:59], v244, s[28:31], s61 offen nt\n"
"s_add_u32 s61, s61, 32768\n"
"buffer_load_dwordx4 a[60:63], v244, s[28:31], s61 offen nt\n"
"s_add_u32 s61, s61, 229376\n"
"s_waitcnt vmcnt(14)\n"
"ds_write_b128 v245, a[0:3] offset:0\n"
"ds_write_b128 v245, a[4:7] offset:1024\n"
"s_waitcnt lgkmcnt(0)\n"
"s_barrier\n"
"ds_read_b64_tr_b16 a[0:1], v247 offset:0\n"
"ds_read_b64_tr_b16 a[2:3], v247 offset:1024\n"
"ds_read_b64_tr_b16 a[4:5], v247 offset:8\n"
"ds_read_b64_tr_b16 a[6:7], v247 offset:1032\n"
"buffer_load_dwordx4 a[64:67], v244, s[28:31], s61 offen nt\n"
"s_add_u32 s61, s61, 32768\n"
"buffer_load_dwordx4 a[68:71], v244, s[28:31], s61 offen nt\n"
"s_add_u32 s61, s61, 229376\n"
"s_waitcnt vmcnt(14)\n"
"ds_write_b128 v245, a[8:11] offset:8192\n"
"ds_write_b128 v245, a[12:15] offset:9216\n"
"s_waitcnt lgkmcnt(0)\n"
"s_barrier\n"
"ds_read_b64_tr_b16 a[8:9], v247 offset:8192\n"
"ds_read_b64_tr_b16 a[10:11], v247 offset:9216\n"
"ds_read_b64_tr_b16 a[12:13], v247 offset:8200\n"
"ds_read_b64_tr_b16 a[14:15], v247 offset:9224\n"
"buffer_load_dwordx4 a[72:75], v244, s[28:31], s61 offen nt\n"
"s_add_u32 s61, s61, 32768\n"
"buffer_load_dwordx4 a[76:79], v244, s[28:31], s61 offen nt\n"
"s_add_u32 s61, s61, 229376\n"
"s_waitcnt vmcnt(14)\n"
"ds_write_b128 v245, a[16:19] offset:0\n"
"ds_write_b128 v245, a[20:23] offset:1024\n"
"s_waitcnt lgkmcnt(0)\n"
"s_barrier\n"
"ds_read_b64_tr_b16 a[16:17], v247 offset:0\n"
"ds_read_b64_tr_b16 a[18:19], v247 offset:1024\n"
"ds_read_b64_tr_b16 a[20:21], v247 offset:8\n"
"ds_read_b64_tr_b16 a[22:23], v247 offset:1032\n"
"buffer_load_dwordx4 a[80:83], v244, s[28:31], s61 offen nt\n"
"s_add_u32 s61, s61, 32768\n"
"buffer_load_dwordx4 a[84:87], v244, s[28:31], s61 offen nt\n"
"s_add_u32 s61, s61, 229376\n"
"s_waitcnt vmcnt(14)\n"
"ds_write_b128 v245, a[24:27] offset:8192\n"
"ds_write_b128 v245, a[28:31] offset:9216\n"
"s_waitcnt lgkmcnt(0)\n"
"s_barrier\n"
"ds_read_b64_tr_b16 a[24:25], v247 offset:8192\n"
"ds_read_b64_tr_b16 a[26:27], v247 offset:9216\n"
"ds_read_b64_tr_b16 a[28:29], v247 offset:8200\n"
"ds_read_b64_tr_b16 a[30:31], v247 offset:9224\n"
"buffer_load_dwordx4 a[88:91], v244, s[28:31], s61 offen nt\n"
"s_add_u32 s61, s61, 32768\n"
"buffer_load_dwordx4 a[92:95], v244, s[28:31], s61 offen nt\n"
"s_add_u32 s61, s61, 229376\n"
"s_waitcnt vmcnt(14)\n"
"ds_write_b128 v245, a[32:35] offset:0\n"
"ds_write_b128 v245, a[36:39] offset:1024\n"
"s_waitcnt lgkmcnt(0)\n"
"s_barrier\n"
"ds_read_b64_tr_b16 a[32:33], v247 offset:0\n"
"ds_read_b64_tr_b16 a[34:35], v247 offset:1024\n"
"ds_read_b64_tr_b16 a[36:37], v247 offset:8\n"
"ds_read_b64_tr_b16 a[38:39], v247 offset:1032\n"
"buffer_load_dwordx4 a[96:99], v244, s[28:31], s61 offen nt\n"
"s_add_u32 s61, s61, 32768\n"
"buffer_load_dwordx4 a[100:103], v244, s[28:31], s61 offen nt\n"
"s_add_u32 s61, s61, 229376\n"
"s_waitcnt vmcnt(14)\n"
"ds_write_b128 v245, a[40:43] offset:8192\n"
"ds_write_b128 v245, a[44:47] offset:9216\n"
"s_waitcnt lgkmcnt(0)\n"
"s_barrier\n"
"ds_read_b64_tr_b16 a[40:41], v247 offset:8192\n"
"ds_read_b64_tr_b16 a[42:43], v247 offset:9216\n"
"ds_read_b64_tr_b16 a[44:45], v247 offset:8200\n"
"ds_read_b64_tr_b16 a[46:47], v247 offset:9224\n"
"buffer_load_dwordx4 a[104:107], v244, s[28:31], s61 offen nt\n"
"s_add_u32 s61, s61, 32768\n"
"buffer_load_dwordx4 a[108:111], v244, s[28:31], s61 offen nt\n"
"s_add_u32 s61, s61, 229376\n"
"s_waitcnt vmcnt(14)\n"
"ds_write_b128 v245, a[48:51] offset:0\n"
"ds_write_b128 v245, a[52:55] offset:1024\n"
"s_waitcnt lgkmcnt(0)\n"
"s_barrier\n"
"ds_read_b64_tr_b16 a[48:49], v247 offset:0\n"
"ds_read_b64_tr_b16 a[50:51], v247 offset:1024\n"
"ds_read_b64_tr_b16 a[52:53], v247 offset:8\n"
"ds_read_b64_tr_b16 a[54:55], v247 offset:1032\n"
"buffer_load_dwordx4 a[112:115], v244, s[28:31], s61 offen nt\n"
"s_add_u32 s61, s61, 32768\n"
"buffer_load_dwordx4 a[116:119], v244, s[28:31], s61 offen nt\n"
"s_add_u32 s61, s61, 229376\n"
"s_waitcnt vmcnt(14)\n"
"ds_write_b128 v245, a[56:59] offset:8192\n"
"ds_write_b128 v245, a[60:63] offset:9216\n"
"s_waitcnt lgkmcnt(0)\n"
"s_barrier\n"
"ds_read_b64_tr_b16 a[56:57], v247 offset:8192\n"
"ds_read_b64_tr_b16 a[58:59], v247 offset:9216\n"
"ds_read_b64_tr_b16 a[60:61], v247 offset:8200\n"
"ds_read_b64_tr_b16 a[62:63], v247 offset:9224\n"
"buffer_load_dwordx4 a[120:123], v244, s[28:31], s61 offen nt\n"
"s_add_u32 s61, s61, 32768\n"
"buffer_load_dwordx4 a[124:127], v244, s[28:31], s61 offen nt\n"
"s_add_u32 s61, s61, 229376\n"
"s_waitcnt vmcnt(14)\n"
"ds_write_b128 v245, a[64:67] offset:0\n"
"ds_write_b128 v245, a[68:71] offset:1024\n"
"s_waitcnt lgkmcnt(0)\n"
"s_barrier\n"
"ds_read_b64_tr_b16 a[64:65], v247 offset:0\n"
"ds_read_b64_tr_b16 a[66:67], v247 offset:1024\n"
"ds_read_b64_tr_b16 a[68:69], v247 offset:8\n"
"ds_read_b64_tr_b16 a[70:71], v247 offset:1032\n"
"buffer_load_dwordx4 a[128:131], v244, s[28:31], s61 offen nt\n"
"s_add_u32 s61, s61, 32768\n"
"buffer_load_dwordx4 a[132:135], v244, s[28:31], s61 offen nt\n"
"s_add_u32 s61, s61, 229376\n"
"s_waitcnt vmcnt(14)\n"
"ds_write_b128 v245, a[72:75] offset:8192\n"
"ds_write_b128 v245, a[76:79] offset:9216\n"
"s_waitcnt lgkmcnt(0)\n"
"s_barrier\n"
"ds_read_b64_tr_b16 a[72:73], v247 offset:8192\n"
"ds_read_b64_tr_b16 a[74:75], v247 offset:9216\n"
"ds_read_b64_tr_b16 a[76:77], v247 offset:8200\n"
"ds_read_b64_tr_b16 a[78:79], v247 offset:9224\n"
"buffer_load_dwordx4 a[136:139], v244, s[28:31], s61 offen nt\n"
"s_add_u32 s61, s61, 32768\n"
"buffer_load_dwordx4 a[140:143], v244, s[28:31], s61 offen nt\n"
"s_add_u32 s61, s61, 229376\n"
"s_waitcnt vmcnt(14)\n"
"ds_write_b128 v245, a[80:83] offset:0\n"
"ds_write_b128 v245, a[84:87] offset:1024\n"
"s_waitcnt lgkmcnt(0)\n"
"s_barrier\n"
"ds_read_b64_tr_b16 a[80:81], v247 offset:0\n"
"ds_read_b64_tr_b16 a[82:83], v247 offset:1024\n"
"ds_read_b64_tr_b16 a[84:85], v247 offset:8\n"
"ds_read_b64_tr_b16 a[86:87], v247 offset:1032\n"
"s_waitcnt vmcnt(12)\n"
"ds_write_b128 v245, a[88:91] offset:8192\n"
"ds_write_b128 v245, a[92:95] offset:9216\n"
"s_waitcnt lgkmcnt(0)\n"
"s_barrier\n"
"ds_read_b64_tr_b16 a[88:89], v247 offset:8192\n"
"ds_read_b64_tr_b16 a[90:91], v247 offset:9216\n"
"ds_read_b64_tr_b16 a[92:93], v247 offset:8200\n"
"ds_read_b64_tr_b16 a[94:95], v247 offset:9224\n"
"s_waitcnt vmcnt(10)\n"
"ds_write_b128 v245, a[96:99] offset:0\n"
"ds_write_b128 v245, a[100:103] offset:1024\n"
"s_waitcnt lgkmcnt(0)\n"
"s_barrier\n"
"ds_read_b64_tr_b16 a[96:97], v247 offset:0\n"
"ds_read_b64_tr_b16 a[98:99], v247 offset:1024\n"
"ds_read_b64_tr_b16 a[100:101], v247 offset:8\n"
"ds_read_b64_tr_b16 a[102:103], v247 offset:1032\n"
"s_waitcnt vmcnt(8)\n"
"ds_write_b128 v245, a[104:107] offset:8192\n"
"ds_write_b128 v245, a[108:111] offset:9216\n"
"s_waitcnt lgkmcnt(0)\n"
"s_barrier\n"
"ds_read_b64_tr_b16 a[104:105], v247 offset:8192\n"
"ds_read_b64_tr_b16 a[106:107], v247 offset:9216\n"
"ds_read_b64_tr_b16 a[108:109], v247 offset:8200\n"
"ds_read_b64_tr_b16 a[110:111], v247 offset:9224\n"
"s_waitcnt vmcnt(6)\n"
"ds_write_b128 v245, a[112:115] offset:0\n"
"ds_write_b128 v245, a[116:119] offset:1024\n"
"s_waitcnt lgkmcnt(0)\n"
"s_barrier\n"
"ds_read_b64_tr_b16 a[112:113], v247 offset:0\n"
"ds_read_b64_tr_b16 a[114:115], v247 offset:1024\n"
"ds_read_b64_tr_b16 a[116:117], v247 offset:8\n"
"ds_read_b64_tr_b16 a[118:119], v247 offset:1032\n"
"s_waitcnt vmcnt(4)\n"
"ds_write_b128 v245, a[120:123] offset:8192\n"
"ds_write_b128 v245, a[124:127] offset:9216\n"
"s_waitcnt lgkmcnt(0)\n"
"s_barrier\n"
"ds_read_b64_tr_b16 a[120:121], v247 offset:8192\n"
"ds_read_b64_tr_b16 a[122:123], v247 offset:9216\n"
"ds_read_b64_tr_b16 a[124:125], v247 offset:8200\n"
"ds_read_b64_tr_b16 a[126:127], v247 offset:9224\n"
"s_waitcnt vmcnt(2)\n"
"ds_write_b128 v245, a[128:131] offset:0\n"
"ds_write_b128 v245, a[132:135] offset:1024\n"
"s_waitcnt lgkmcnt(0)\n"
"s_barrier\n"
"ds_read_b64_tr_b16 a[128:129], v247 offset:0\n"
"ds_read_b64_tr_b16 a[130:131], v247 offset:1024\n"
"ds_read_b64_tr_b16 a[132:133], v247 offset:8\n"
"ds_read_b64_tr_b16 a[134:135], v247 offset:1032\n"
"s_waitcnt vmcnt(0)\n"
"ds_write_b128 v245, a[136:139] offset:8192\n"
"ds_write_b128 v245, a[140:143] offset:9216\n"
"s_waitcnt lgkmcnt(0)\n"
"s_barrier\n"
"ds_read_b64_tr_b16 a[136:137], v247 offset:8192\n"
"ds_read_b64_tr_b16 a[138:139], v247 offset:9216\n"
"ds_read_b64_tr_b16 a[140:141], v247 offset:8200\n"
"ds_read_b64_tr_b16 a[142:143], v247 offset:9224\n"
"s_waitcnt lgkmcnt(0)\n"
"s_barrier\n"
"s_mov_b32 s21, 0\n"
"v_mov_b32 v6, v2\n"
"v_add_u32 v7, 36864, v1\n"
"s_lshr_b32 s40, s21, 5\n"
"s_cmp_gt_i32 s55, s40\n"
"s_cbranch_scc1 Lln_71\n"
"s_lshr_b32 s41, s21, 1\n"
"s_and_b32 s41, s41, 15\n"
"Lln_70:\n"
"s_xor_b32 s43, s22, 1245184\n"
"buffer_load_dword v4, v5, s[24:27], s43 offen sc1\n"
"s_waitcnt vmcnt(0)\n"
"v_cmp_ge_i32 vcc, s40, v4\n"
"v_readlane_b32 s42, v4, s41\n"
"s_cbranch_vccnz Lln_72\n"
"s_add_u32 s55, s40, 1\n"
"Lln_72:\n"
"s_cmp_gt_i32 s42, s40\n"
"s_cbranch_scc1 Lln_71\n"
"s_sleep 2\n"
"s_memrealtime s[48:49]\n"
"s_waitcnt lgkmcnt(0)\n"
"s_sub_u32 s48, s48, s23\n"
"s_cmp_gt_i32 s48, 0\n"
"s_cbranch_scc1 LX2_end\n"
"s_branch Lln_70\n"
"Lln_71:\n"
"s_mul_i32 s54, s21, 36864\n"
"s_add_u32 s54, s54, 165675008\n"
"buffer_load_dwordx4 v[16:19], v1, s[24:27], s54 offen offset:0 sc1\n"
"buffer_load_dwordx4 v[20:23], v1, s[24:27], s54 offen offset:1024 sc1\n"
"buffer_load_dwordx4 v[24:27], v1, s[24:27], s54 offen offset:2048 sc1\n"
"buffer_load_dwordx4 v[28:31], v1, s[24:27], s54 offen offset:3072 sc1\n"
"s_add_u32 s54, s54, 4096\n"
"buffer_load_dwordx4 v[32:35], v1, s[24:27], s54 offen offset:0 sc1\n"
"buffer_load_dwordx4 v[36:39], v1, s[24:27], s54 offen offset:1024 sc1\n"
"buffer_load_dwordx4 v[40:43], v1, s[24:27], s54 offen offset:2048 sc1\n"
"buffer_load_dwordx4 v[44:47], v1, s[24:27], s54 offen offset:3072 sc1\n"
"s_add_u32 s54, s54, 4096\n"
"buffer_load_dwordx4 v[48:51], v1, s[24:27], s54 offen offset:0 sc1\n"
"s_waitcnt vmcnt(0)\n"
"ds_write_b128 v1, v[16:19] offset:0\n"
"ds_write_b128 v1, v[20:23] offset:1024\n"
"ds_write_b128 v1, v[24:27] offset:2048\n"
"ds_write_b128 v1, v[28:31] offset:3072\n"
"ds_write_b128 v1, v[32:35] offset:4096\n"
"ds_write_b128 v1, v[36:39] offset:5120\n"
"ds_write_b128 v1, v[40:43] offset:6144\n"
"ds_write_b128 v1, v[44:47] offset:7168\n"
"ds_write_b128 v1, v[48:51] offset:8192\n"
"s_waitcnt lgkmcnt(0)\n"
"s_barrier\n"
"s_lshl_b32 s36, s59, 18\n"
"s_lshr_b32 s37, s19, 1\n"
"s_lshl_b32 s37, s37, 14\n"
"s_add_u32 s36, s36, s37\n"
"s_and_b32 s37, s19, 1\n"
"s_lshl_b32 s37, s37, 12\n"
"s_add_u32 s36, s36, s37\n"
"s_lshl_b32 s37, s60, 11\n"
"s_add_u32 s36, s36, s37\n"
"s_add_u32 s67, s36, 69206016\n"
"v_mov_b32 v242, 114688\n"
"v_mov_b32 v243, 0\n"
"v_mov_b32 v186, 1\n"
"ds_write2_b32 v242, v243, v243 offset0:0 offset1:1\n"
"s_waitcnt lgkmcnt(0)\n"
"s_barrier\n"
"v_mov_b32 v185, 114704\n"
"v_mov_b32 v187, 0\n"
"v_mov_b32 v15, -1\n"
"ds_write_b32 v185, v187\n"
"s_mov_b32 s70, 0\n"
"s_waitcnt lgkmcnt(0)\n"
"s_barrier\n"
"s_xor_b32 s71, s22, 1228800\n"
"s_cmp_eq_u32 s20, 3\n"
"s_cselect_b32 s71, s71, 2146435072\n"
"v_mov_b32 v216, 114720\n"
"v_mov_b32 v217, 0\n"
"v_mov_b32 v184, -1\n"
"ds_write_b32 v216, v217\n"
"s_mov_b32 s72, 0\n"
"s_waitcnt lgkmcnt(0)\n"
"s_barrier\n"
"s_xor_b32 s73, s22, 1212416\n"
"s_cmp_eq_u32 s20, 2\n"
"s_cselect_b32 s73, s73, 2146435072\n"
"s_mov_b32 s53, 1\n"
"s_lshr_b32 s40, s53, 5\n"
"s_cmp_gt_i32 s55, s40\n"
"s_cbranch_scc1 Lln_74\n"
"s_lshr_b32 s41, s53, 1\n"
"s_and_b32 s41, s41, 15\n"
"Lln_73:\n"
"s_xor_b32 s43, s22, 1245184\n"
"buffer_load_dword v4, v5, s[24:27], s43 offen sc1\n"
"s_waitcnt vmcnt(0)\n"
"v_cmp_ge_i32 vcc, s40, v4\n"
"v_readlane_b32 s42, v4, s41\n"
"s_cbranch_vccnz Lln_75\n"
"s_add_u32 s55, s40, 1\n"
"Lln_75:\n"
"s_cmp_gt_i32 s42, s40\n"
"s_cbranch_scc1 Lln_74\n"
"s_sleep 2\n"
"s_memrealtime s[48:49]\n"
"s_waitcnt lgkmcnt(0)\n"
"s_sub_u32 s48, s48, s23\n"
"s_cmp_gt_i32 s48, 0\n"
"s_cbranch_scc1 LX2_end\n"
"s_branch Lln_73\n"
"Lln_74:\n"
"s_mul_i32 s54, s53, 36864\n"
"s_add_u32 s54, s54, 165675008\n"
"buffer_load_dwordx4 v[16:19], v1, s[24:27], s54 offen offset:0 sc1\n"
"buffer_load_dwordx4 v[20:23], v1, s[24:27], s54 offen offset:1024 sc1\n"
"buffer_load_dwordx4 v[24:27], v1, s[24:27], s54 offen offset:2048 sc1\n"
"buffer_load_dwordx4 v[28:31], v1, s[24:27], s54 offen offset:3072 sc1\n"
"s_add_u32 s54, s54, 4096\n"
"buffer_load_dwordx4 v[32:35], v1, s[24:27], s54 offen offset:0 sc1\n"
"buffer_load_dwordx4 v[36:39], v1, s[24:27], s54 offen offset:1024 sc1\n"
"buffer_load_dwordx4 v[40:43], v1, s[24:27], s54 offen offset:2048 sc1\n"
"buffer_load_dwordx4 v[44:47], v1, s[24:27], s54 offen offset:3072 sc1\n"
"s_add_u32 s54, s54, 4096\n"
"buffer_load_dwordx4 v[48:51], v1, s[24:27], s54 offen offset:0 sc1\n"
"LX2_loop:\n"
"ds_read_b128 v[52:55], v6 offset:0\n"
"ds_read_b128 v[56:59], v6 offset:18432\n"
"ds_read_b128 v[60:63], v6 offset:1024\n"
"ds_read_b128 v[64:67], v6 offset:19456\n"
"ds_read_b128 v[68:71], v6 offset:2048\n"
"ds_read_b128 v[72:75], v6 offset:20480\n"
"Lbk_76:\n"
"s_cmp_gt_i32 s72, s21\n"
"s_cbranch_scc1 Lbk_77\n"
"s_cmp_lg_u32 s20, 2\n"
"s_cbranch_scc1 Lbk_78\n"
"buffer_load_dword v184, v12, s[24:27], s73 offen sc1\n"
"Lbk_76w:\n"
"s_waitcnt vmcnt(0)\n"
"s_cmp_lg_u32 s20, 2\n"
"s_cbranch_scc1 Lav_79\n"
"v_cmp_ge_i32 vcc, s21, v184\n"
"s_cbranch_vccnz Lav_79\n"
"s_add_u32 s41, s21, 1\n"
"v_cmp_ge_i32 vcc, s41, v184\n"
"s_cbranch_vccnz Lav_80\n"
"s_add_u32 s41, s41, 1\n"
"Lav_80:\n"
"s_max_i32 s72, s72, s41\n"
"v_mov_b32 v217, s72\n"
"ds_write_b32 v216, v217\n"
"Lav_79:\n"
"s_cmp_gt_i32 s72, s21\n"
"s_cbranch_scc1 Lbk_77\n"
"s_sleep 2\n"
"s_memrealtime s[48:49]\n"
"s_waitcnt lgkmcnt(0)\n"
"s_sub_u32 s48, s48, s23\n"
"s_cmp_gt_i32 s48, 0\n"
"s_cbranch_scc1 LX2_end\n"
"s_branch Lbk_76\n"
"Lbk_78:\n"
"ds_read_b32 v217, v216\n"
"s_waitcnt lgkmcnt(0)\n"
"v_readfirstlane_b32 s72, v217\n"
"s_cmp_gt_i32 s72, s21\n"
"s_cbranch_scc1 Lbk_77\n"
"s_sleep 1\n"
"s_memrealtime s[48:49]\n"
"s_waitcnt lgkmcnt(0)\n"
"s_sub_u32 s48, s48, s23\n"
"s_cmp_gt_i32 s48, 0\n"
"s_cbranch_scc1 LX2_end\n"
"s_branch Lbk_78\n"
"Lbk_77:\n"
"s_lshl_b32 s36, s21, 20\n"
"s_lshl_b32 s37, s19, 15\n"
"s_add_u32 s36, s36, s37\n"
"s_add_u32 s63, s36, 2097152\n"
"s_lshr_b32 s66, s36, 3\n"
"s_add_u32 s66, s66, 148897792\n"
"s_lshr_b32 s43, s63, 8\n"
"s_xor_b32 s43, s43, s22\n"
"s_and_b32 s43, s43, 8192\n"
"s_or_b32 s63, s63, s43\n"
"s_add_u32 s64, s63, 4096\n"
"s_lshl_b32 s65, s60, 12\n"
"s_add_u32 s65, s65, s63\n"
"s_add_u32 s65, s65, 16384\n"
"s_cmp_gt_i32 s70, s21\n"
"s_cselect_b32 s62, 1, 0\n"
"s_cselect_b32 s69, s66, 2146435072\n"
"buffer_load_dword v15, v12, s[24:27], s71 offen sc1\n"
"buffer_load_dword v184, v12, s[24:27], s73 offen sc1\n"
"s_waitcnt lgkmcnt(4)\n"
"v_mfma_f32_16x16x32_bf16 v[84:87], a[0:3], v[52:55], 0\n"
"ds_read_b128 v[76:79], v6 offset:3072\n"
"v_mfma_f32_16x16x32_bf16 v[92:95], a[4:7], v[52:55], 0\n"
"ds_read_b128 v[80:83], v6 offset:21504\n"
"v_mfma_f32_16x16x32_bf16 v[88:91], a[0:3], v[56:59], 0\n"
"buffer_load_dwordx4 v[116:119], v2, s[24:27], s63 offen offset:0 sc1\n"
"v_mfma_f32_16x16x32_bf16 v[96:99], a[4:7], v[56:59], 0\n"
"s_waitcnt lgkmcnt(4)\n"
"v_mfma_f32_16x16x32_bf16 v[84:87], a[8:11], v[60:63], v[84:87]\n"
"ds_read_b128 v[52:55], v6 offset:4096\n"
"v_mfma_f32_16x16x32_bf16 v[92:95], a[12:15], v[60:63], v[92:95]\n"
"ds_read_b128 v[56:59], v6 offset:22528\n"
"v_mfma_f32_16x16x32_bf16 v[88:91], a[8:11], v[64:67], v[88:91]\n"
"buffer_load_dwordx4 v[120:123], v2, s[24:27], s63 offen offset:1024 sc1\n"
"v_mfma_f32_16x16x32_bf16 v[96:99], a[12:15], v[64:67], v[96:99]\n"
"s_waitcnt lgkmcnt(4)\n"
"v_mfma_f32_16x16x32_bf16 v[84:87], a[16:19], v[68:71], v[84:87]\n"
"ds_read_b128 v[60:63], v6 offset:5120\n"
"v_mfma_f32_16x16x32_bf16 v[92:95], a[20:23], v[68:71], v[92:95]\n"
"ds_read_b128 v[64:67], v6 offset:23552\n"
"v_mfma_f32_16x16x32_bf16 v[88:91], a[16:19], v[72:75], v[88:91]\n"
"buffer_load_dwordx4 v[124:127], v2, s[24:27], s63 offen offset:2048 sc1\n"
"v_mfma_f32_16x16x32_bf16 v[96:99], a[20:23], v[72:75], v[96:99]\n"
"s_waitcnt lgkmcnt(4)\n"
"v_mfma_f32_16x16x32_bf16 v[84:87], a[24:27], v[76:79], v[84:87]\n"
"ds_read_b128 v[68:71], v6 offset:6144\n"
"v_mfma_f32_16x16x32_bf16 v[92:95], a[28:31], v[76:79], v[92:95]\n"
"ds_read_b128 v[72:75], v6 offset:24576\n"
"v_mfma_f32_16x16x32_bf16 v[88:91], a[24:27], v[80:83], v[88:91]\n"
"buffer_load_dwordx4 v[128:131], v2, s[24:27], s63 offen offset:3072 sc1\n"
"v_mfma_f32_16x16x32_bf16 v[96:99], a[28:31], v[80:83], v[96:99]\n"
"s_waitcnt lgkmcnt(4)\n"
"v_mfma_f32_16x16x32_bf16 v[84:87], a[32:35], v[52:55], v[84:87]\n"
"ds_read_b128 v[76:79], v6 offset:7168\n"
"v_mfma_f32_16x16x32_bf16 v[92:95], a[36:39], v[52:55], v[92:95]\n"
"ds_read_b128 v[80:83], v6 offset:25600\n"
"v_mfma_f32_16x16x32_bf16 v[88:91], a[32:35], v[56:59], v[88:91]\n"
"buffer_load_dwordx4 v[132:135], v2, s[24:27], s64 offen offset:0 sc1\n"
"v_mfma_f32_16x16x32_bf16 v[96:99], a[36:39], v[56:59], v[96:99]\n"
"s_waitcnt lgkmcnt(4)\n"
"v_mfma_f32_16x16x32_bf16 v[84:87], a[40:43], v[60:63], v[84:87]\n"
"ds_read_b128 v[52:55], v6 offset:8192\n"
"v_mfma_f32_16x16x32_bf16 v[92:95], a[44:47], v[60:63], v[92:95]\n"
"ds_read_b128 v[56:59], v6 offset:26624\n"
"v_mfma_f32_16x16x32_bf16 v[88:91], a[40:43], v[64:67], v[88:91]\n"
"buffer_load_dwordx4 v[136:139], v2, s[24:27], s64 offen offset:1024 sc1\n"
"v_mfma_f32_16x16x32_bf16 v[96:99], a[44:47], v[64:67], v[96:99]\n"
"s_waitcnt lgkmcnt(4)\n"
"s_waitcnt vmcnt(8)\n"
"s_add_u32 s68, s21, -1\n"
"s_and_b32 s41, s68, 1\n"
"s_lshl_b32 s41, s41, 2\n"
"s_add_u32 s41, s41, 114688\n"
"v_mov_b32 v242, s41\n"
"v_mov_b32 v186, 1\n"
"s_mov_b64 exec, 1\n"
"ds_add_rtn_u32 v243, v242, v186\n"
"s_mov_b64 exec, -1\n"
"v_mfma_f32_16x16x32_bf16 v[84:87], a[48:51], v[68:71], v[84:87]\n"
"ds_read_b128 v[60:63], v6 offset:9216\n"
"v_mfma_f32_16x16x32_bf16 v[92:95], a[52:55], v[68:71], v[92:95]\n"
"ds_read_b128 v[64:67], v6 offset:27648\n"
"v_mfma_f32_16x16x32_bf16 v[88:91], a[48:51], v[72:75], v[88:91]\n"
"buffer_load_dwordx4 v[140:143], v2, s[24:27], s64 offen offset:2048 sc1\n"
"v_mfma_f32_16x16x32_bf16 v[96:99], a[52:55], v[72:75], v[96:99]\n"
"s_waitcnt lgkmcnt(5)\n"
"v_mfma_f32_16x16x32_bf16 v[84:87], a[56:59], v[76:79], v[84:87]\n"
"ds_read_b128 v[68:71], v6 offset:10240\n"
"v_mfma_f32_16x16x32_bf16 v[92:95], a[60:63], v[76:79], v[92:95]\n"
"ds_read_b128 v[72:75], v6 offset:28672\n"
"v_mfma_f32_16x16x32_bf16 v[88:91], a[56:59], v[80:83], v[88:91]\n"
"buffer_load_dwordx4 v[144:147], v2, s[24:27], s64 offen offset:3072 sc1\n"
"v_mfma_f32_16x16x32_bf16 v[96:99], a[60:63], v[80:83], v[96:99]\n"
"s_waitcnt lgkmcnt(5)\n"
"s_waitcnt lgkmcnt(4)\n"
"v_readfirstlane_b32 s41, v243\n"
"s_and_b32 s41, s41, 3\n"
"s_cmp_lg_u32 s41, 3\n"
"s_cbranch_scc1 Lar_81\n"
"s_lshl_b32 s43, s19, 7\n"
"s_xor_b32 s43, s43, s22\n"
"s_xor_b32 s43, s43, 1269760\n"
"s_add_u32 s42, s68, 1\n"
"v_mov_b32 v4, s42\n"
"buffer_store_dword v4, v3, s[24:27], s43 offen sc1\n"
"Lpf_82:\n"
"Lar_81:\n"
"v_mfma_f32_16x16x32_bf16 v[84:87], a[64:67], v[52:55], v[84:87]\n"
"ds_read_b128 v[76:79], v6 offset:11264\n"
"v_mfma_f32_16x16x32_bf16 v[92:95], a[68:71], v[52:55], v[92:95]\n"
"ds_read_b128 v[80:83], v6 offset:29696\n"
"ds_write_b128 v7, v[16:19] offset:0\n"
"v_mfma_f32_16x16x32_bf16 v[88:91], a[64:67], v[56:59], v[88:91]\n"
"buffer_load_dwordx4 v[148:151], v8, s[24:27], s69 offen offset:0 sc1\n"
"v_mfma_f32_16x16x32_bf16 v[96:99], a[68:71], v[56:59], v[96:99]\n"
"s_waitcnt lgkmcnt(5)\n"
"v_mfma_f32_16x16x32_bf16 v[84:87], a[72:75], v[60:63], v[84:87]\n"
"ds_read_b128 v[52:55], v6 offset:12288\n"
"v_mfma_f32_16x16x32_bf16 v[92:95], a[76:79], v[60:63], v[92:95]\n"
"ds_read_b128 v[56:59], v6 offset:30720\n"
"ds_write_b128 v7, v[20:23] offset:1024\n"
"v_mfma_f32_16x16x32_bf16 v[88:91], a[72:75], v[64:67], v[88:91]\n"
"buffer_load_dwordx4 v[152:155], v8, s[24:27], s69 offen offset:512 sc1\n"
"v_mfma_f32_16x16x32_bf16 v[96:99], a[76:79], v[64:67], v[96:99]\n"
"s_waitcnt lgkmcnt(6)\n"
"v_mfma_f32_16x16x32_bf16 v[84:87], a[80:83], v[68:71], v[84:87]\n"
"ds_read_b128 v[60:63], v6 offset:13312\n"
"v_mfma_f32_16x16x32_bf16 v[92:95], a[84:87], v[68:71], v[92:95]\n"
"ds_read_b128 v[64:67], v6 offset:31744\n"
"ds_write_b128 v7, v[24:27] offset:2048\n"
"v_mfma_f32_16x16x32_bf16 v[88:91], a[80:83], v[72:75], v[88:91]\n"
"buffer_load_dwordx4 v[156:159], v8, s[24:27], s69 offen offset:1024 sc1\n"
"v_mfma_f32_16x16x32_bf16 v[96:99], a[84:87], v[72:75], v[96:99]\n"
"s_waitcnt lgkmcnt(7)\n"
"v_mfma_f32_16x16x32_bf16 v[84:87], a[88:91], v[76:79], v[84:87]\n"
"ds_read_b128 v[68:71], v6 offset:14336\n"
"v_mfma_f32_16x16x32_bf16 v[92:95], a[92:95], v[76:79], v[92:95]\n"
"ds_read_b128 v[72:75], v6 offset:32768\n"
"ds_write_b128 v7, v[28:31] offset:3072\n"
"v_mfma_f32_16x16x32_bf16 v[88:91], a[88:91], v[80:83], v[88:91]\n"
"buffer_load_dwordx4 v[160:163], v8, s[24:27], s69 offen offset:1536 sc1\n"
"v_mfma_f32_16x16x32_bf16 v[96:99], a[92:95], v[80:83], v[96:99]\n"
"s_waitcnt lgkmcnt(7)\n"
"v_mfma_f32_16x16x32_bf16 v[84:87], a[96:99], v[52:55], v[84:87]\n"
"ds_read_b128 v[76:79], v6 offset:15360\n"
"v_mfma_f32_16x16x32_bf16 v[92:95], a[100:103], v[52:55], v[92:95]\n"
"ds_read_b128 v[80:83], v6 offset:33792\n"
"ds_write_b128 v7, v[32:35] offset:4096\n"
"v_mfma_f32_16x16x32_bf16 v[88:91], a[96:99], v[56:59], v[88:91]\n"
"buffer_load_dwordx2 v[180:181], v9, s[24:27], s69 offen offset:0 sc1\n"
"v_mfma_f32_16x16x32_bf16 v[96:99], a[100:103], v[56:59], v[96:99]\n"
"s_waitcnt lgkmcnt(7)\n"
"v_mfma_f32_16x16x32_bf16 v[84:87], a[104:107], v[60:63], v[84:87]\n"
"ds_read_b128 v[52:55], v6 offset:16384\n"
"v_mfma_f32_16x16x32_bf16 v[92:95], a[108:111], v[60:63], v[92:95]\n"
"ds_read_b128 v[56:59], v6 offset:34816\n"
"ds_write_b128 v7, v[36:39] offset:5120\n"
"v_mfma_f32_16x16x32_bf16 v[88:91], a[104:107], v[64:67], v[88:91]\n"
"buffer_load_dwordx2 v[182:183], v9, s[24:27], s69 offen offset:512 sc1\n"
"v_mfma_f32_16x16x32_bf16 v[96:99], a[108:111], v[64:67], v[96:99]\n"
"s_waitcnt lgkmcnt(7)\n"
"v_mfma_f32_16x16x32_bf16 v[84:87], a[112:115], v[68:71], v[84:87]\n"
"ds_read_b128 v[60:63], v6 offset:17408\n"
"v_mfma_f32_16x16x32_bf16 v[92:95], a[116:119], v[68:71], v[92:95]\n"
"ds_read_b128 v[64:67], v6 offset:35840\n"
"ds_write_b128 v7, v[40:43] offset:6144\n"
"v_mfma_f32_16x16x32_bf16 v[88:91], a[112:115], v[72:75], v[88:91]\n"
"buffer_load_dwordx4 v[164:167], v2, s[24:27], s65 offen offset:0 sc1\n"
"v_mfma_f32_16x16x32_bf16 v[96:99], a[116:119], v[72:75], v[96:99]\n"
"s_waitcnt lgkmcnt(7)\n"
"v_mfma_f32_16x16x32_bf16 v[84:87], a[120:123], v[76:79], v[84:87]\n"
"v_mfma_f32_16x16x32_bf16 v[92:95], a[124:127], v[76:79], v[92:95]\n"
"ds_write_b128 v7, v[44:47] offset:7168\n"
"v_mfma_f32_16x16x32_bf16 v[88:91], a[120:123], v[80:83], v[88:91]\n"
"buffer_load_dwordx4 v[168:171], v2, s[24:27], s65 offen offset:1024 sc1\n"
"v_mfma_f32_16x16x32_bf16 v[96:99], a[124:127], v[80:83], v[96:99]\n"
"s_waitcnt lgkmcnt(5)\n"
"v_mfma_f32_16x16x32_bf16 v[84:87], a[128:131], v[52:55], v[84:87]\n"
"v_mfma_f32_16x16x32_bf16 v[92:95], a[132:135], v[52:55], v[92:95]\n"
"ds_write_b128 v7, v[48:51] offset:8192\n"
"v_mfma_f32_16x16x32_bf16 v[88:91], a[128:131], v[56:59], v[88:91]\n"
"buffer_load_dwordx4 v[172:175], v2, s[24:27], s65 offen offset:2048 sc1\n"
"v_mfma_f32_16x16x32_bf16 v[96:99], a[132:135], v[56:59], v[96:99]\n"
"s_waitcnt lgkmcnt(3)\n"
"v_mfma_f32_16x16x32_bf16 v[84:87], a[136:139], v[60:63], v[84:87]\n"
"v_mfma_f32_16x16x32_bf16 v[92:95], a[140:143], v[60:63], v[92:95]\n"
"v_mfma_f32_16x16x32_bf16 v[88:91], a[136:139], v[64:67], v[88:91]\n"
"buffer_load_dwordx4 v[176:179], v2, s[24:27], s65 offen offset:3072 sc1\n"
"v_mfma_f32_16x16x32_bf16 v[96:99], a[140:143], v[64:67], v[96:99]\n"
"s_waitcnt vmcnt(18)\n"
"s_add_u32 s53, s21, 1\n"
"s_cmp_lg_u32 s20, 3\n"
"s_cbranch_scc1 Lav_83\n"
"v_cmp_ge_i32 vcc, s21, v15\n"
"s_cbranch_vccnz Lav_83\n"
"s_add_u32 s41, s21, 1\n"
"v_cmp_ge_i32 vcc, s41, v15\n"
"s_cbranch_vccnz Lav_84\n"
"s_add_u32 s41, s41, 1\n"
"Lav_84:\n"
"s_max_i32 s70, s70, s41\n"
"v_mov_b32 v187, s70\n"
"ds_write_b32 v185, v187\n"
"Lav_83:\n"
"s_cmp_lg_u32 s20, 2\n"
"s_cbranch_scc1 Lav_85\n"
"v_cmp_ge_i32 vcc, s53, v184\n"
"s_cbranch_vccnz Lav_85\n"
"s_add_u32 s41, s53, 1\n"
"v_cmp_ge_i32 vcc, s41, v184\n"
"s_cbranch_vccnz Lav_86\n"
"s_add_u32 s41, s41, 1\n"
"Lav_86:\n"
"s_max_i32 s72, s72, s41\n"
"v_mov_b32 v217, s72\n"
"ds_write_b32 v216, v217\n"
"Lav_85:\n"
"v_cvt_pk_bf16_f32 v244, v84, v85\n"
"v_cvt_pk_bf16_f32 v245, v86, v87\n"
"v_cvt_pk_bf16_f32 v246, v92, v93\n"
"v_cvt_pk_bf16_f32 v247, v94, v95\n"
"ds_write_b128 v13, v[244:247] offset:0\n"
"v_cvt_pk_bf16_f32 v248, v88, v89\n"
"v_cvt_pk_bf16_f32 v249, v90, v91\n"
"v_cvt_pk_bf16_f32 v250, v96, v97\n"
"v_cvt_pk_bf16_f32 v251, v98, v99\n"
"ds_write_b128 v13, v[248:251] offset:4096\n"
"v_xor_b32 v4, v6, v2\n"
"v_sub_u32 v7, v7, v1\n"
"v_add_u32 v6, v7, v2\n"
"v_add_u32 v7, v4, v1\n"
"s_waitcnt lgkmcnt(0)\n"
"s_barrier\n"
"ds_read_b128 v[100:103], v14 offset:0\n"
"ds_read_b128 v[104:107], v14 offset:1024\n"
"ds_read_b128 v[108:111], v14 offset:2048\n"
"ds_read_b128 v[112:115], v14 offset:3072\n"
"v_xor_b32 v13, 8192, v13\n"
"v_xor_b32 v14, 8192, v14\n"
"s_waitcnt vmcnt(10)\n"
"s_cmp_eq_u32 s62, 1\n"
"s_cbranch_scc1 Lhv_87\n"
"ds_read_b32 v187, v185\n"
"s_waitcnt lgkmcnt(0)\n"
"v_readfirstlane_b32 s70, v187\n"
"s_cmp_gt_i32 s70, s21\n"
"s_cbranch_scc0 Lhv_87\n"
"s_mov_b32 s62, 1\n"
"buffer_load_dwordx4 v[148:151], v8, s[24:27], s66 offen offset:0 sc1\n"
"buffer_load_dwordx4 v[152:155], v8, s[24:27], s66 offen offset:512 sc1\n"
"buffer_load_dwordx4 v[156:159], v8, s[24:27], s66 offen offset:1024 sc1\n"
"buffer_load_dwordx4 v[160:163], v8, s[24:27], s66 offen offset:1536 sc1\n"
"buffer_load_dwordx2 v[180:181], v9, s[24:27], s66 offen offset:0 sc1\n"
"buffer_load_dwordx2 v[182:183], v9, s[24:27], s66 offen offset:512 sc1\n"
"Lhv_87:\n"
"s_waitcnt lgkmcnt(3)\n"
"v_mfma_f32_16x16x32_bf16 v[200:203], v[116:119], v[100:103], v[188:191]\n"
"v_mfma_f32_16x16x32_bf16 v[204:207], v[132:135], v[100:103], v[192:195]\n"
"s_waitcnt lgkmcnt(2)\n"
"v_mfma_f32_16x16x32_bf16 v[200:203], v[120:123], v[104:107], v[200:203]\n"
"v_mfma_f32_16x16x32_bf16 v[204:207], v[136:139], v[104:107], v[204:207]\n"
"s_waitcnt lgkmcnt(1)\n"
"v_mfma_f32_16x16x32_bf16 v[200:203], v[124:127], v[108:111], v[200:203]\n"
"v_mfma_f32_16x16x32_bf16 v[204:207], v[140:143], v[108:111], v[204:207]\n"
"s_waitcnt lgkmcnt(0)\n"
"v_mfma_f32_16x16x32_bf16 v[200:203], v[128:131], v[112:115], v[200:203]\n"
"v_mfma_f32_16x16x32_bf16 v[204:207], v[144:147], v[112:115], v[204:207]\n"
"s_cmp_eq_u32 s62, 1\n"
"s_cbranch_scc1 Lhv_88\n"
"Lbk_89:\n"
"s_cmp_gt_i32 s70, s21\n"
"s_cbranch_scc1 Lbk_90\n"
"s_cmp_lg_u32 s20, 3\n"
"s_cbranch_scc1 Lbk_91\n"
"buffer_load_dword v15, v12, s[24:27], s71 offen sc1\n"
"Lbk_89w:\n"
"s_waitcnt vmcnt(0)\n"
"s_cmp_lg_u32 s20, 3\n"
"s_cbranch_scc1 Lav_92\n"
"v_cmp_ge_i32 vcc, s21, v15\n"
"s_cbranch_vccnz Lav_92\n"
"s_add_u32 s41, s21, 1\n"
"v_cmp_ge_i32 vcc, s41, v15\n"
"s_cbranch_vccnz Lav_93\n"
"s_add_u32 s41, s41, 1\n"
"Lav_93:\n"
"s_max_i32 s70, s70, s41\n"
"v_mov_b32 v187, s70\n"
"ds_write_b32 v185, v187\n"
"Lav_92:\n"
"s_cmp_gt_i32 s70, s21\n"
"s_cbranch_scc1 Lbk_90\n"
"s_sleep 1\n"
"s_memrealtime s[48:49]\n"
"s_waitcnt lgkmcnt(0)\n"
"s_sub_u32 s48, s48, s23\n"
"s_cmp_gt_i32 s48, 0\n"
"s_cbranch_scc1 LX2_end\n"
"s_branch Lbk_89\n"
"Lbk_91:\n"
"ds_read_b32 v187, v185\n"
"s_waitcnt lgkmcnt(0)\n"
"v_readfirstlane_b32 s70, v187\n"
"s_cmp_gt_i32 s70, s21\n"
"s_cbranch_scc1 Lbk_90\n"
"s_sleep 1\n"
"s_memrealtime s[48:49]\n"
"s_waitcnt lgkmcnt(0)\n"
"s_sub_u32 s48, s48, s23\n"
"s_cmp_gt_i32 s48, 0\n"
"s_cbranch_scc1 LX2_end\n"
"s_branch Lbk_91\n"
"Lbk_90:\n"
"buffer_load_dwordx4 v[148:151], v8, s[24:27], s66 offen offset:0 sc1\n"
"buffer_load_dwordx4 v[152:155], v8, s[24:27], s66 offen offset:512 sc1\n"
"buffer_load_dwordx4 v[156:159], v8, s[24:27], s66 offen offset:1024 sc1\n"
"buffer_load_dwordx4 v[160:163], v8, s[24:27], s66 offen offset:1536 sc1\n"
"buffer_load_dwordx2 v[180:181], v9, s[24:27], s66 offen offset:0 sc1\n"
"buffer_load_dwordx2 v[182:183], v9, s[24:27], s66 offen offset:512 sc1\n"
"Lhv_88:\n"
"s_add_u32 s53, s21, 2\n"
"s_min_u32 s53, s53, 63\n"
"s_lshr_b32 s40, s53, 5\n"
"s_cmp_gt_i32 s55, s40\n"
"s_cbranch_scc1 Lln_95\n"
"s_lshr_b32 s41, s53, 1\n"
"s_and_b32 s41, s41, 15\n"
"Lln_94:\n"
"s_xor_b32 s43, s22, 1245184\n"
"buffer_load_dword v4, v5, s[24:27], s43 offen sc1\n"
"s_waitcnt vmcnt(0)\n"
"v_cmp_ge_i32 vcc, s40, v4\n"
"v_readlane_b32 s42, v4, s41\n"
"s_cbranch_vccnz Lln_96\n"
"s_add_u32 s55, s40, 1\n"
"Lln_96:\n"
"s_cmp_gt_i32 s42, s40\n"
"s_cbranch_scc1 Lln_95\n"
"s_sleep 2\n"
"s_memrealtime s[48:49]\n"
"s_waitcnt lgkmcnt(0)\n"
"s_sub_u32 s48, s48, s23\n"
"s_cmp_gt_i32 s48, 0\n"
"s_cbranch_scc1 LX2_end\n"
"s_branch Lln_94\n"
"Lln_95:\n"
"s_mul_i32 s54, s53, 36864\n"
"s_add_u32 s54, s54, 165675008\n"
"s_waitcnt vmcnt(0)\n"
"v_mfma_f32_16x16x32_bf16 v[208:211], v[148:151], v[100:103], 0\n"
"buffer_load_dwordx4 v[16:19], v1, s[24:27], s54 offen offset:0 sc1\n"
"v_mfma_f32_16x16x32_bf16 v[208:211], v[152:155], v[104:107], v[208:211]\n"
"buffer_load_dwordx4 v[20:23], v1, s[24:27], s54 offen offset:1024 sc1\n"
"v_mfma_f32_16x16x32_bf16 v[208:211], v[156:159], v[108:111], v[208:211]\n"
"buffer_load_dwordx4 v[24:27], v1, s[24:27], s54 offen offset:2048 sc1\n"
"v_mfma_f32_16x16x32_bf16 v[208:211], v[160:163], v[112:115], v[208:211]\n"
"buffer_load_dwordx4 v[28:31], v1, s[24:27], s54 offen offset:3072 sc1\n"
"s_add_u32 s54, s54, 4096\n"
"v_max3_f32 v10, v200, v201, v202\n"
"v_max3_f32 v10, v10, v203, v204\n"
"v_max3_f32 v10, v10, v205, v206\n"
"s_nop 2\n"
"v_max3_f32 v10, v10, v207, v208\n"
"v_max_f32 v10, v10, v209\n"
"v_mov_b32 v11, v10\n"
"s_nop 1\n"
"v_permlane32_swap_b32 v10, v11\n"
"v_max_f32 v10, v10, v11\n"
"v_mov_b32 v11, v10\n"
"s_nop 1\n"
"v_permlane16_swap_b32 v10, v11\n"
"v_max_f32 v10, v10, v11\n"
"v_mul_f32 v10, s56, v10\n"
"v_fma_f32 v200, v200, s56, -v10\n"
"v_fma_f32 v201, v201, s56, -v10\n"
"v_fma_f32 v202, v202, s56, -v10\n"
"v_fma_f32 v203, v203, s56, -v10\n"
"buffer_load_dwordx4 v[32:35], v1, s[24:27], s54 offen offset:0 sc1\n"
"v_exp_f32 v200, v200\n"
"v_exp_f32 v201, v201\n"
"v_exp_f32 v202, v202\n"
"v_exp_f32 v203, v203\n"
"v_fma_f32 v204, v204, s56, -v10\n"
"v_fma_f32 v205, v205, s56, -v10\n"
"v_fma_f32 v206, v206, s56, -v10\n"
"v_fma_f32 v207, v207, s56, -v10\n"
"buffer_load_dwordx4 v[36:39], v1, s[24:27], s54 offen offset:1024 sc1\n"
"v_exp_f32 v204, v204\n"
"v_exp_f32 v205, v205\n"
"v_exp_f32 v206, v206\n"
"v_exp_f32 v207, v207\n"
"v_fma_f32 v208, v208, s56, -v10\n"
"v_fma_f32 v209, v209, s56, -v10\n"
"buffer_load_dwordx4 v[40:43], v1, s[24:27], s54 offen offset:2048 sc1\n"
"v_exp_f32 v208, v208\n"
"v_exp_f32 v209, v209\n"
"v_cvt_pk_bf16_f32 v212, v200, v201\n"
"v_cvt_pk_bf16_f32 v213, v202, v203\n"
"v_cvt_pk_bf16_f32 v214, v204, v205\n"
"v_cvt_pk_bf16_f32 v215, v206, v207\n"
"v_cvt_pk_bf16_f32 v196, v208, v209\n"
"v_cvt_pk_bf16_f32 v199, v208, v209\n"
"v_mfma_f32_16x16x32_bf16 v[222:225], v[218:221], v[212:215], 0\n"
"buffer_load_dwordx4 v[44:47], v1, s[24:27], s54 offen offset:3072 sc1\n"
"s_add_u32 s54, s54, 4096\n"
"buffer_load_dwordx4 v[48:51], v1, s[24:27], s54 offen offset:0 sc1\n"
"v_mfma_f32_16x16x32_bf16 v[226:229], v[164:167], v[212:215], 0\n"
"v_mfma_f32_16x16x32_bf16 v[230:233], v[168:171], v[212:215], 0\n"
"v_mfma_f32_16x16x32_bf16 v[234:237], v[172:175], v[212:215], 0\n"
"v_mfma_f32_16x16x32_bf16 v[238:241], v[176:179], v[212:215], 0\n"
"v_mfma_f32_16x16x16_bf16 v[222:225], v[218:219], v[196:197], v[222:225]\n"
"v_mfma_f32_16x16x16_bf16 v[226:229], v[180:181], v[196:197], v[226:229]\n"
"v_mfma_f32_16x16x16_bf16 v[230:233], v[180:181], v[198:199], v[230:233]\n"
"v_mfma_f32_16x16x16_bf16 v[234:237], v[182:183], v[196:197], v[234:237]\n"
"v_mfma_f32_16x16x16_bf16 v[238:241], v[182:183], v[198:199], v[238:241]\n"
"s_lshl_b32 s36, s21, 19\n"
"s_add_u32 s36, s36, s67\n"
"s_lshr_b32 s43, s36, 8\n"
"s_xor_b32 s43, s43, s22\n"
"s_not_b32 s43, s43\n"
"s_and_b32 s43, s43, 8192\n"
"s_or_b32 s36, s36, s43\n"
"v_rcp_f32 v10, v222\n"
"s_nop 0\n"
"v_mul_f32 v226, v226, v10\n"
"v_mul_f32 v227, v227, v10\n"
"v_mul_f32 v228, v228, v10\n"
"v_mul_f32 v229, v229, v10\n"
"v_mul_f32 v230, v230, v10\n"
"v_mul_f32 v231, v231, v10\n"
"v_mul_f32 v232, v232, v10\n"
"v_mul_f32 v233, v233, v10\n"
"v_cvt_pk_bf16_f32 v244, v226, v227\n"
"v_cvt_pk_bf16_f32 v245, v228, v229\n"
"v_cvt_pk_bf16_f32 v246, v230, v231\n"
"v_cvt_pk_bf16_f32 v247, v232, v233\n"
"buffer_store_dwordx4 v[244:247], v2, s[24:27], s36 offen offset:0 sc1\n"
"v_mul_f32 v234, v234, v10\n"
"v_mul_f32 v235, v235, v10\n"
"v_mul_f32 v236, v236, v10\n"
"v_mul_f32 v237, v237, v10\n"
"v_mul_f32 v238, v238, v10\n"
"v_mul_f32 v239, v239, v10\n"
"v_mul_f32 v240, v240, v10\n"
"v_mul_f32 v241, v241, v10\n"
"v_cvt_pk_bf16_f32 v248, v234, v235\n"
"v_cvt_pk_bf16_f32 v249, v236, v237\n"
"v_cvt_pk_bf16_f32 v250, v238, v239\n"
"v_cvt_pk_bf16_f32 v251, v240, v241\n"
"buffer_store_dwordx4 v[248:251], v2, s[24:27], s36 offen offset:1024 sc1\n"
"s_add_u32 s21, s21, 1\n"
"s_cmp_lt_u32 s21, 64\n"
"s_cbranch_scc1 LX2_loop\n"
"s_waitcnt vmcnt(0)\n"
"s_add_u32 s68, s21, -1\n"
"s_and_b32 s41, s68, 1\n"
"s_lshl_b32 s41, s41, 2\n"
"s_add_u32 s41, s41, 114688\n"
"v_mov_b32 v242, s41\n"
"v_mov_b32 v186, 1\n"
"s_mov_b64 exec, 1\n"
"ds_add_rtn_u32 v243, v242, v186\n"
"s_mov_b64 exec, -1\n"
"s_waitcnt lgkmcnt(0)\n"
"v_readfirstlane_b32 s41, v243\n"
"s_and_b32 s41, s41, 3\n"
"s_cmp_lg_u32 s41, 3\n"
"s_cbranch_scc1 Lar_97\n"
"s_lshl_b32 s43, s19, 7\n"
"s_xor_b32 s43, s43, s22\n"
"s_xor_b32 s43, s43, 1269760\n"
"s_add_u32 s42, s68, 1\n"
"v_mov_b32 v4, s42\n"
"buffer_store_dword v4, v3, s[24:27], s43 offen sc1\n"
"Lpf_98:\n"
"Lar_97:\n"
"LX2_end:\n"
"s_endpgm\n"
"LP2:\n"
"v_mov_b32 v2, 2146435072\n"
"v_cmp_eq_u32 vcc, 0, v0\n"
"s_nop 1\n"
"v_cndmask_b32 v2, v2, 0, vcc\n"
"v_min_u32 v5, 31, v0\n"
"v_lshlrev_b32 v5, 7, v5\n"
"s_and_b32 s53, s19, 1\n"
"s_lshr_b32 s54, s19, 1\n"
"s_mov_b32 s55, 0xffff0000\n"
"v_lshlrev_b32 v1, 4, v0\n"
"s_lshl_b32 s36, s20, 11\n"
"v_add_u32 v6, s36, v1\n"
"v_mov_b32 v9, v1\n"
"v_and_b32 v3, 15, v0\n"
"v_mul_u32_u24 v7, 1152, v3\n"
"v_lshrrev_b32 v4, 4, v0\n"
"v_lshl_add_u32 v7, v4, 4, v7\n"
"s_mul_i32 s36, s53, 18432\n"
"s_lshl_b32 s37, s54, 6\n"
"s_add_u32 s36, s36, s37\n"
"v_add_u32 v7, s36, v7\n"
"v_lshlrev_b32 v8, 3, v3\n"
"v_mov_b32 v3, 2146435072\n"
"v_cmp_gt_u32 vcc, 16, v0\n"
"s_nop 1\n"
"v_cndmask_b32 v8, v3, v8, vcc\n"
"s_sleep 32\n"
"s_sleep 32\n"
"s_sleep 32\n"
"s_sleep 32\n"
"s_sleep 32\n"
"s_sleep 32\n"
"s_lshl_b32 s36, s20, 12\n"
"s_add_u32 s36, s36, 16384\n"
"v_lshlrev_b32 v61, 4, v0\n"
"v_add_u32 v61, s36, v61\n"
"v_lshrrev_b32 v60, 5, v0\n"
"v_lshlrev_b32 v62, 10, v60\n"
"v_bfe_u32 v60, v0, 3, 1\n"
"v_lshl_add_u32 v62, v60, 9, v62\n"
"v_bfe_u32 v60, v0, 4, 1\n"
"v_lshl_add_u32 v62, v60, 8, v62\n"
"v_bfe_u32 v60, v0, 2, 1\n"
"v_lshl_add_u32 v62, v60, 6, v62\n"
"v_and_b32 v60, 3, v0\n"
"v_lshl_add_u32 v62, v60, 4, v62\n"
"v_add_u32 v62, s36, v62\n"
"v_lshrrev_b32 v60, 4, v0\n"
"v_lshlrev_b32 v63, 9, v60\n"
"v_bfe_u32 v60, v0, 2, 2\n"
"v_lshl_add_u32 v63, v60, 6, v63\n"
"v_and_b32 v60, 3, v0\n"
"v_lshl_add_u32 v63, v60, 4, v63\n"
"v_add_u32 v63, s36, v63\n"
"v_lshrrev_b32 v65, 2, v0\n"
"v_mul_u32_u24 v64, 1152, v65\n"
"v_and_b32 v65, 3, v0\n"
"v_lshl_add_u32 v64, v65, 4, v64\n"
"s_load_dwordx2 s[28:29], s[16:17], 40\n"
"s_mov_b32 s30, 4718592\n"
"s_mov_b32 s31, 0x00020000\n"
"s_waitcnt lgkmcnt(0)\n"
"s_and_b32 s29, s29, 0xffff\n"
"s_load_dwordx2 s[32:33], s[16:17], 0\n"
"s_mov_b32 s34, 2359296\n"
"s_mov_b32 s35, 0x00020000\n"
"s_waitcnt lgkmcnt(0)\n"
"s_and_b32 s33, s33, 0xffff\n"
"s_mul_i32 s60, s20, 1179648\n"
"s_lshl_b32 s36, s54, 6\n"
"s_add_u32 s60, s60, s36\n"
"buffer_load_dwordx4 a[0:3], v64, s[28:31], s60 offen nt\n"
"s_add_u32 s60, s60, 18432\n"
"buffer_load_dwordx4 a[4:7], v64, s[28:31], s60 offen nt\n"
"s_add_u32 s60, s60, 18432\n"
"buffer_load_dwordx4 a[8:11], v64, s[28:31], s60 offen nt\n"
"s_add_u32 s60, s60, 18432\n"
"buffer_load_dwordx4 a[12:15], v64, s[28:31], s60 offen nt\n"
"s_add_u32 s60, s60, 18432\n"
"buffer_load_dwordx4 a[16:19], v64, s[28:31], s60 offen nt\n"
"s_add_u32 s60, s60, 18432\n"
"buffer_load_dwordx4 a[20:23], v64, s[28:31], s60 offen nt\n"
"s_add_u32 s60, s60, 18432\n"
"buffer_load_dwordx4 a[24:27], v64, s[28:31], s60 offen nt\n"
"s_add_u32 s60, s60, 18432\n"
"buffer_load_dwordx4 a[28:31], v64, s[28:31], s60 offen nt\n"
"s_add_u32 s60, s60, 18432\n"
"buffer_load_dwordx4 a[32:35], v64, s[28:31], s60 offen nt\n"
"s_add_u32 s60, s60, 18432\n"
"buffer_load_dwordx4 a[36:39], v64, s[28:31], s60 offen nt\n"
"s_add_u32 s60, s60, 18432\n"
"buffer_load_dwordx4 a[40:43], v64, s[28:31], s60 offen nt\n"
"s_add_u32 s60, s60, 18432\n"
"buffer_load_dwordx4 a[44:47], v64, s[28:31], s60 offen nt\n"
"s_add_u32 s60, s60, 18432\n"
"buffer_load_dwordx4 a[48:51], v64, s[28:31], s60 offen nt\n"
"s_add_u32 s60, s60, 18432\n"
"buffer_load_dwordx4 a[52:55], v64, s[28:31], s60 offen nt\n"
"s_add_u32 s60, s60, 18432\n"
"buffer_load_dwordx4 a[56:59], v64, s[28:31], s60 offen nt\n"
"s_add_u32 s60, s60, 18432\n"
"buffer_load_dwordx4 a[60:63], v64, s[28:31], s60 offen nt\n"
"s_add_u32 s60, s60, 18432\n"
"buffer_load_dwordx4 a[64:67], v64, s[28:31], s60 offen nt\n"
"s_add_u32 s60, s60, 18432\n"
"buffer_load_dwordx4 a[68:71], v64, s[28:31], s60 offen nt\n"
"s_add_u32 s60, s60, 18432\n"
"buffer_load_dwordx4 a[72:75], v64, s[28:31], s60 offen nt\n"
"s_add_u32 s60, s60, 18432\n"
"buffer_load_dwordx4 a[76:79], v64, s[28:31], s60 offen nt\n"
"s_add_u32 s60, s60, 18432\n"
"buffer_load_dwordx4 a[80:83], v64, s[28:31], s60 offen nt\n"
"s_add_u32 s60, s60, 18432\n"
"buffer_load_dwordx4 a[84:87], v64, s[28:31], s60 offen nt\n"
"s_add_u32 s60, s60, 18432\n"
"buffer_load_dwordx4 a[88:91], v64, s[28:31], s60 offen nt\n"
"s_add_u32 s60, s60, 18432\n"
"buffer_load_dwordx4 a[92:95], v64, s[28:31], s60 offen nt\n"
"s_add_u32 s60, s60, 18432\n"
"s_waitcnt vmcnt(22)\n"
"ds_write_b128 v61, a[0:3] offset:0\n"
"ds_write_b128 v61, a[4:7] offset:1024\n"
"ds_read_b64_tr_b16 a[0:1], v63 offset:0\n"
"ds_read_b64_tr_b16 a[2:3], v63 offset:256\n"
"ds_read_b64_tr_b16 a[4:5], v63 offset:8\n"
"ds_read_b64_tr_b16 a[6:7], v63 offset:264\n"
"buffer_load_dwordx4 a[96:99], v64, s[28:31], s60 offen nt\n"
"s_add_u32 s60, s60, 18432\n"
"buffer_load_dwordx4 a[100:103], v64, s[28:31], s60 offen nt\n"
"s_add_u32 s60, s60, 18432\n"
"s_waitcnt vmcnt(22)\n"
"ds_write_b128 v61, a[8:11] offset:0\n"
"ds_write_b128 v61, a[12:15] offset:1024\n"
"ds_read_b64_tr_b16 a[8:9], v63 offset:0\n"
"ds_read_b64_tr_b16 a[10:11], v63 offset:256\n"
"ds_read_b64_tr_b16 a[12:13], v63 offset:8\n"
"ds_read_b64_tr_b16 a[14:15], v63 offset:264\n"
"buffer_load_dwordx4 a[104:107], v64, s[28:31], s60 offen nt\n"
"s_add_u32 s60, s60, 18432\n"
"buffer_load_dwordx4 a[108:111], v64, s[28:31], s60 offen nt\n"
"s_add_u32 s60, s60, 18432\n"
"s_waitcnt vmcnt(22)\n"
"ds_write_b128 v61, a[16:19] offset:0\n"
"ds_write_b128 v61, a[20:23] offset:1024\n"
"ds_read_b64_tr_b16 a[16:17], v63 offset:0\n"
"ds_read_b64_tr_b16 a[18:19], v63 offset:256\n"
"ds_read_b64_tr_b16 a[20:21], v63 offset:8\n"
"ds_read_b64_tr_b16 a[22:23], v63 offset:264\n"
"buffer_load_dwordx4 a[112:115], v64, s[28:31], s60 offen nt\n"
"s_add_u32 s60, s60, 18432\n"
"buffer_load_dwordx4 a[116:119], v64, s[28:31], s60 offen nt\n"
"s_add_u32 s60, s60, 18432\n"
"s_waitcnt vmcnt(22)\n"
"ds_write_b128 v61, a[24:27] offset:0\n"
"ds_write_b128 v61, a[28:31] offset:1024\n"
"ds_read_b64_tr_b16 a[24:25], v63 offset:0\n"
"ds_read_b64_tr_b16 a[26:27], v63 offset:256\n"
"ds_read_b64_tr_b16 a[28:29], v63 offset:8\n"
"ds_read_b64_tr_b16 a[30:31], v63 offset:264\n"
"buffer_load_dwordx4 a[120:123], v64, s[28:31], s60 offen nt\n"
"s_add_u32 s60, s60, 18432\n"
"buffer_load_dwordx4 a[124:127], v64, s[28:31], s60 offen nt\n"
"s_add_u32 s60, s60, 18432\n"
"s_waitcnt vmcnt(22)\n"
"ds_write_b128 v61, a[32:35] offset:0\n"
"ds_write_b128 v61, a[36:39] offset:1024\n"
"ds_read_b64_tr_b16 a[32:33], v63 offset:0\n"
"ds_read_b64_tr_b16 a[34:35], v63 offset:256\n"
"ds_read_b64_tr_b16 a[36:37], v63 offset:8\n"
"ds_read_b64_tr_b16 a[38:39], v63 offset:264\n"
"buffer_load_dwordx4 a[128:131], v64, s[28:31], s60 offen nt\n"
"s_add_u32 s60, s60, 18432\n"
"buffer_load_dwordx4 a[132:135], v64, s[28:31], s60 offen nt\n"
"s_add_u32 s60, s60, 18432\n"
"s_waitcnt vmcnt(22)\n"
"ds_write_b128 v61, a[40:43] offset:0\n"
"ds_write_b128 v61, a[44:47] offset:1024\n"
"ds_read_b64_tr_b16 a[40:41], v63 offset:0\n"
"ds_read_b64_tr_b16 a[42:43], v63 offset:256\n"
"ds_read_b64_tr_b16 a[44:45], v63 offset:8\n"
"ds_read_b64_tr_b16 a[46:47], v63 offset:264\n"
"buffer_load_dwordx4 a[136:139], v64, s[28:31], s60 offen nt\n"
"s_add_u32 s60, s60, 18432\n"
"buffer_load_dwordx4 a[140:143], v64, s[28:31], s60 offen nt\n"
"s_add_u32 s60, s60, 18432\n"
"s_waitcnt vmcnt(22)\n"
"ds_write_b128 v61, a[48:51] offset:0\n"
"ds_write_b128 v61, a[52:55] offset:1024\n"
"ds_read_b64_tr_b16 a[48:49], v63 offset:0\n"
"ds_read_b64_tr_b16 a[50:51], v63 offset:256\n"
"ds_read_b64_tr_b16 a[52:53], v63 offset:8\n"
"ds_read_b64_tr_b16 a[54:55], v63 offset:264\n"
"buffer_load_dwordx4 a[144:147], v64, s[28:31], s60 offen nt\n"
"s_add_u32 s60, s60, 18432\n"
"buffer_load_dwordx4 a[148:151], v64, s[28:31], s60 offen nt\n"
"s_add_u32 s60, s60, 18432\n"
"s_waitcnt vmcnt(22)\n"
"ds_write_b128 v61, a[56:59] offset:0\n"
"ds_write_b128 v61, a[60:63] offset:1024\n"
"ds_read_b64_tr_b16 a[56:57], v63 offset:0\n"
"ds_read_b64_tr_b16 a[58:59], v63 offset:256\n"
"ds_read_b64_tr_b16 a[60:61], v63 offset:8\n"
"ds_read_b64_tr_b16 a[62:63], v63 offset:264\n"
"buffer_load_dwordx4 a[152:155], v64, s[28:31], s60 offen nt\n"
"s_add_u32 s60, s60, 18432\n"
"buffer_load_dwordx4 a[156:159], v64, s[28:31], s60 offen nt\n"
"s_add_u32 s60, s60, 18432\n"
"s_waitcnt vmcnt(22)\n"
"ds_write_b128 v61, a[64:67] offset:0\n"
"ds_write_b128 v61, a[68:71] offset:1024\n"
"ds_read_b64_tr_b16 a[64:65], v63 offset:0\n"
"ds_read_b64_tr_b16 a[66:67], v63 offset:256\n"
"ds_read_b64_tr_b16 a[68:69], v63 offset:8\n"
"ds_read_b64_tr_b16 a[70:71], v63 offset:264\n"
"buffer_load_dwordx4 a[160:163], v64, s[28:31], s60 offen nt\n"
"s_add_u32 s60, s60, 18432\n"
"buffer_load_dwordx4 a[164:167], v64, s[28:31], s60 offen nt\n"
"s_add_u32 s60, s60, 18432\n"
"s_waitcnt vmcnt(22)\n"
"ds_write_b128 v61, a[72:75] offset:0\n"
"ds_write_b128 v61, a[76:79] offset:1024\n"
"ds_read_b64_tr_b16 a[72:73], v63 offset:0\n"
"ds_read_b64_tr_b16 a[74:75], v63 offset:256\n"
"ds_read_b64_tr_b16 a[76:77], v63 offset:8\n"
"ds_read_b64_tr_b16 a[78:79], v63 offset:264\n"
"buffer_load_dwordx4 a[168:171], v64, s[28:31], s60 offen nt\n"
"s_add_u32 s60, s60, 18432\n"
"buffer_load_dwordx4 a[172:175], v64, s[28:31], s60 offen nt\n"
"s_add_u32 s60, s60, 18432\n"
"s_waitcnt vmcnt(22)\n"
"ds_write_b128 v61, a[80:83] offset:0\n"
"ds_write_b128 v61, a[84:87] offset:1024\n"
"ds_read_b64_tr_b16 a[80:81], v63 offset:0\n"
"ds_read_b64_tr_b16 a[82:83], v63 offset:256\n"
"ds_read_b64_tr_b16 a[84:85], v63 offset:8\n"
"ds_read_b64_tr_b16 a[86:87], v63 offset:264\n"
"buffer_load_dwordx4 a[176:179], v64, s[28:31], s60 offen nt\n"
"s_add_u32 s60, s60, 18432\n"
"buffer_load_dwordx4 a[180:183], v64, s[28:31], s60 offen nt\n"
"s_add_u32 s60, s60, 18432\n"
"s_waitcnt vmcnt(22)\n"
"ds_write_b128 v61, a[88:91] offset:0\n"
"ds_write_b128 v61, a[92:95] offset:1024\n"
"ds_read_b64_tr_b16 a[88:89], v63 offset:0\n"
"ds_read_b64_tr_b16 a[90:91], v63 offset:256\n"
"ds_read_b64_tr_b16 a[92:93], v63 offset:8\n"
"ds_read_b64_tr_b16 a[94:95], v63 offset:264\n"
"buffer_load_dwordx4 a[184:187], v64, s[28:31], s60 offen nt\n"
"s_add_u32 s60, s60, 18432\n"
"buffer_load_dwordx4 a[188:191], v64, s[28:31], s60 offen nt\n"
"s_add_u32 s60, s60, 18432\n"
"s_waitcnt vmcnt(22)\n"
"ds_write_b128 v61, a[96:99] offset:0\n"
"ds_write_b128 v61, a[100:103] offset:1024\n"
"ds_read_b64_tr_b16 a[96:97], v63 offset:0\n"
"ds_read_b64_tr_b16 a[98:99], v63 offset:256\n"
"ds_read_b64_tr_b16 a[100:101], v63 offset:8\n"
"ds_read_b64_tr_b16 a[102:103], v63 offset:264\n"
"buffer_load_dwordx4 a[192:195], v64, s[28:31], s60 offen nt\n"
"s_add_u32 s60, s60, 18432\n"
"buffer_load_dwordx4 a[196:199], v64, s[28:31], s60 offen nt\n"
"s_add_u32 s60, s60, 18432\n"
"s_waitcnt vmcnt(22)\n"
"ds_write_b128 v61, a[104:107] offset:0\n"
"ds_write_b128 v61, a[108:111] offset:1024\n"
"ds_read_b64_tr_b16 a[104:105], v63 offset:0\n"
"ds_read_b64_tr_b16 a[106:107], v63 offset:256\n"
"ds_read_b64_tr_b16 a[108:109], v63 offset:8\n"
"ds_read_b64_tr_b16 a[110:111], v63 offset:264\n"
"buffer_load_dwordx4 a[200:203], v64, s[28:31], s60 offen nt\n"
"s_add_u32 s60, s60, 18432\n"
"buffer_load_dwordx4 a[204:207], v64, s[28:31], s60 offen nt\n"
"s_add_u32 s60, s60, 18432\n"
"s_waitcnt vmcnt(22)\n"
"ds_write_b128 v61, a[112:115] offset:0\n"
"ds_write_b128 v61, a[116:119] offset:1024\n"
"ds_read_b64_tr_b16 a[112:113], v63 offset:0\n"
"ds_read_b64_tr_b16 a[114:115], v63 offset:256\n"
"ds_read_b64_tr_b16 a[116:117], v63 offset:8\n"
"ds_read_b64_tr_b16 a[118:119], v63 offset:264\n"
"buffer_load_dwordx4 a[208:211], v64, s[28:31], s60 offen nt\n"
"s_add_u32 s60, s60, 18432\n"
"buffer_load_dwordx4 a[212:215], v64, s[28:31], s60 offen nt\n"
"s_add_u32 s60, s60, 18432\n"
"s_waitcnt vmcnt(22)\n"
"ds_write_b128 v61, a[120:123] offset:0\n"
"ds_write_b128 v61, a[124:127] offset:1024\n"
"ds_read_b64_tr_b16 a[120:121], v63 offset:0\n"
"ds_read_b64_tr_b16 a[122:123], v63 offset:256\n"
"ds_read_b64_tr_b16 a[124:125], v63 offset:8\n"
"ds_read_b64_tr_b16 a[126:127], v63 offset:264\n"
"buffer_load_dwordx4 a[216:219], v64, s[28:31], s60 offen nt\n"
"s_add_u32 s60, s60, 18432\n"
"buffer_load_dwordx4 a[220:223], v64, s[28:31], s60 offen nt\n"
"s_add_u32 s60, s60, 18432\n"
"s_waitcnt vmcnt(22)\n"
"ds_write_b128 v61, a[128:131] offset:0\n"
"ds_write_b128 v61, a[132:135] offset:1024\n"
"ds_read_b64_tr_b16 a[128:129], v63 offset:0\n"
"ds_read_b64_tr_b16 a[130:131], v63 offset:256\n"
"ds_read_b64_tr_b16 a[132:133], v63 offset:8\n"
"ds_read_b64_tr_b16 a[134:135], v63 offset:264\n"
"buffer_load_dwordx4 a[224:227], v64, s[28:31], s60 offen nt\n"
"s_add_u32 s60, s60, 18432\n"
"buffer_load_dwordx4 a[228:231], v64, s[28:31], s60 offen nt\n"
"s_add_u32 s60, s60, 18432\n"
"s_waitcnt vmcnt(22)\n"
"ds_write_b128 v61, a[136:139] offset:0\n"
"ds_write_b128 v61, a[140:143] offset:1024\n"
"ds_read_b64_tr_b16 a[136:137], v63 offset:0\n"
"ds_read_b64_tr_b16 a[138:139], v63 offset:256\n"
"ds_read_b64_tr_b16 a[140:141], v63 offset:8\n"
"ds_read_b64_tr_b16 a[142:143], v63 offset:264\n"
"buffer_load_dwordx4 a[232:235], v64, s[28:31], s60 offen nt\n"
"s_add_u32 s60, s60, 18432\n"
"buffer_load_dwordx4 a[236:239], v64, s[28:31], s60 offen nt\n"
"s_add_u32 s60, s60, 18432\n"
"s_waitcnt vmcnt(22)\n"
"ds_write_b128 v61, a[144:147] offset:0\n"
"ds_write_b128 v61, a[148:151] offset:1024\n"
"ds_read_b64_tr_b16 a[144:145], v63 offset:0\n"
"ds_read_b64_tr_b16 a[146:147], v63 offset:256\n"
"ds_read_b64_tr_b16 a[148:149], v63 offset:8\n"
"ds_read_b64_tr_b16 a[150:151], v63 offset:264\n"
"buffer_load_dwordx4 a[240:243], v64, s[28:31], s60 offen nt\n"
"s_add_u32 s60, s60, 18432\n"
"buffer_load_dwordx4 a[244:247], v64, s[28:31], s60 offen nt\n"
"s_add_u32 s60, s60, 18432\n"
"s_waitcnt vmcnt(22)\n"
"ds_write_b128 v61, a[152:155] offset:0\n"
"ds_write_b128 v61, a[156:159] offset:1024\n"
"ds_read_b64_tr_b16 a[152:153], v63 offset:0\n"
"ds_read_b64_tr_b16 a[154:155], v63 offset:256\n"
"ds_read_b64_tr_b16 a[156:157], v63 offset:8\n"
"ds_read_b64_tr_b16 a[158:159], v63 offset:264\n"
"buffer_load_dwordx4 a[248:251], v64, s[28:31], s60 offen nt\n"
"s_add_u32 s60, s60, 18432\n"
"buffer_load_dwordx4 a[252:255], v64, s[28:31], s60 offen nt\n"
"s_add_u32 s60, s60, 18432\n"
"s_waitcnt vmcnt(22)\n"
"ds_write_b128 v61, a[160:163] offset:0\n"
"ds_write_b128 v61, a[164:167] offset:1024\n"
"ds_read_b64_tr_b16 a[160:161], v63 offset:0\n"
"ds_read_b64_tr_b16 a[162:163], v63 offset:256\n"
"ds_read_b64_tr_b16 a[164:165], v63 offset:8\n"
"ds_read_b64_tr_b16 a[166:167], v63 offset:264\n"
"s_waitcnt vmcnt(20)\n"
"ds_write_b128 v61, a[168:171] offset:0\n"
"ds_write_b128 v61, a[172:175] offset:1024\n"
"ds_read_b64_tr_b16 a[168:169], v63 offset:0\n"
"ds_read_b64_tr_b16 a[170:171], v63 offset:256\n"
"ds_read_b64_tr_b16 a[172:173], v63 offset:8\n"
"ds_read_b64_tr_b16 a[174:175], v63 offset:264\n"
"s_waitcnt vmcnt(18)\n"
"ds_write_b128 v61, a[176:179] offset:0\n"
"ds_write_b128 v61, a[180:183] offset:1024\n"
"ds_read_b64_tr_b16 a[176:177], v63 offset:0\n"
"ds_read_b64_tr_b16 a[178:179], v63 offset:256\n"
"ds_read_b64_tr_b16 a[180:181], v63 offset:8\n"
"ds_read_b64_tr_b16 a[182:183], v63 offset:264\n"
"s_waitcnt vmcnt(16)\n"
"ds_write_b128 v61, a[184:187] offset:0\n"
"ds_write_b128 v61, a[188:191] offset:1024\n"
"ds_read_b64_tr_b16 a[184:185], v63 offset:0\n"
"ds_read_b64_tr_b16 a[186:187], v63 offset:256\n"
"ds_read_b64_tr_b16 a[188:189], v63 offset:8\n"
"ds_read_b64_tr_b16 a[190:191], v63 offset:264\n"
"s_waitcnt vmcnt(14)\n"
"ds_write_b128 v61, a[192:195] offset:0\n"
"ds_write_b128 v61, a[196:199] offset:1024\n"
"ds_read_b64_tr_b16 a[192:193], v63 offset:0\n"
"ds_read_b64_tr_b16 a[194:195], v63 offset:256\n"
"ds_read_b64_tr_b16 a[196:197], v63 offset:8\n"
"ds_read_b64_tr_b16 a[198:199], v63 offset:264\n"
"s_waitcnt vmcnt(12)\n"
"ds_write_b128 v61, a[200:203] offset:0\n"
"ds_write_b128 v61, a[204:207] offset:1024\n"
"ds_read_b64_tr_b16 a[200:201], v63 offset:0\n"
"ds_read_b64_tr_b16 a[202:203], v63 offset:256\n"
"ds_read_b64_tr_b16 a[204:205], v63 offset:8\n"
"ds_read_b64_tr_b16 a[206:207], v63 offset:264\n"
"s_waitcnt vmcnt(10)\n"
"ds_write_b128 v61, a[208:211] offset:0\n"
"ds_write_b128 v61, a[212:215] offset:1024\n"
"ds_read_b64_tr_b16 a[208:209], v63 offset:0\n"
"ds_read_b64_tr_b16 a[210:211], v63 offset:256\n"
"ds_read_b64_tr_b16 a[212:213], v63 offset:8\n"
"ds_read_b64_tr_b16 a[214:215], v63 offset:264\n"
"s_waitcnt vmcnt(8)\n"
"ds_write_b128 v61, a[216:219] offset:0\n"
"ds_write_b128 v61, a[220:223] offset:1024\n"
"ds_read_b64_tr_b16 a[216:217], v63 offset:0\n"
"ds_read_b64_tr_b16 a[218:219], v63 offset:256\n"
"ds_read_b64_tr_b16 a[220:221], v63 offset:8\n"
"ds_read_b64_tr_b16 a[222:223], v63 offset:264\n"
"s_waitcnt vmcnt(6)\n"
"ds_write_b128 v61, a[224:227] offset:0\n"
"ds_write_b128 v61, a[228:231] offset:1024\n"
"ds_read_b64_tr_b16 a[224:225], v63 offset:0\n"
"ds_read_b64_tr_b16 a[226:227], v63 offset:256\n"
"ds_read_b64_tr_b16 a[228:229], v63 offset:8\n"
"ds_read_b64_tr_b16 a[230:231], v63 offset:264\n"
"s_waitcnt vmcnt(4)\n"
"ds_write_b128 v61, a[232:235] offset:0\n"
"ds_write_b128 v61, a[236:239] offset:1024\n"
"ds_read_b64_tr_b16 a[232:233], v63 offset:0\n"
"ds_read_b64_tr_b16 a[234:235], v63 offset:256\n"
"ds_read_b64_tr_b16 a[236:237], v63 offset:8\n"
"ds_read_b64_tr_b16 a[238:239], v63 offset:264\n"
"s_waitcnt vmcnt(2)\n"
"ds_write_b128 v61, a[240:243] offset:0\n"
"ds_write_b128 v61, a[244:247] offset:1024\n"
"ds_read_b64_tr_b16 a[240:241], v63 offset:0\n"
"ds_read_b64_tr_b16 a[242:243], v63 offset:256\n"
"ds_read_b64_tr_b16 a[244:245], v63 offset:8\n"
"ds_read_b64_tr_b16 a[246:247], v63 offset:264\n"
"s_waitcnt vmcnt(0)\n"
"ds_write_b128 v61, a[248:251] offset:0\n"
"ds_write_b128 v61, a[252:255] offset:1024\n"
"ds_read_b64_tr_b16 a[248:249], v63 offset:0\n"
"ds_read_b64_tr_b16 a[250:251], v63 offset:256\n"
"ds_read_b64_tr_b16 a[252:253], v63 offset:8\n"
"ds_read_b64_tr_b16 a[254:255], v63 offset:264\n"
"s_waitcnt lgkmcnt(0)\n"
"s_lshr_b32 s36, s19, 3\n"
"s_lshl_b32 s36, s36, 14\n"
"s_and_b32 s72, s19, 7\n"
"s_lshl_b32 s72, s72, 10\n"
"s_add_u32 s72, s72, s36\n"
"s_mul_i32 s73, s53, 18\n"
"s_add_u32 s73, s73, s54\n"
"s_lshl_b32 s73, s73, 7\n"
"s_add_u32 s73, s73, 81920\n"
"s_lshl_b32 s68, s53, 2\n"
"s_add_u32 s68, s68, s20\n"
"s_mul_i32 s68, s68, 65536\n"
"v_mov_b32 v11, 32768\n"
"v_mov_b32 v4, 0\n"
"v_mov_b32 v10, -1\n"
"ds_write_b32 v11, v4\n"
"s_mov_b32 s70, 0\n"
"s_waitcnt lgkmcnt(0)\n"
"s_barrier\n"
"s_xor_b32 s71, s22, 1269760\n"
"s_cmp_eq_u32 s20, 3\n"
"s_cselect_b32 s71, s71, 2146435072\n"
"s_mov_b32 s21, 0\n"
"s_mov_b32 s64, 0\n"
"LP2_block:\n"
"s_cmp_eq_u32 s20, 3\n"
"s_cbranch_scc1 Lbk_99w\n"
"Lbk_99:\n"
"s_cmp_gt_i32 s70, s21\n"
"s_cbranch_scc1 Lbk_100\n"
"s_cmp_lg_u32 s20, 3\n"
"s_cbranch_scc1 Lbk_101\n"
"buffer_load_dword v10, v5, s[24:27], s71 offen sc1\n"
"Lbk_99w:\n"
"s_waitcnt vmcnt(0)\n"
"s_cmp_lg_u32 s20, 3\n"
"s_cbranch_scc1 Lav_102\n"
"v_cmp_ge_i32 vcc, s21, v10\n"
"s_cbranch_vccnz Lav_102\n"
"s_add_u32 s41, s21, 1\n"
"v_cmp_ge_i32 vcc, s41, v10\n"
"s_cbranch_vccnz Lav_103\n"
"s_add_u32 s41, s41, 1\n"
"Lav_103:\n"
"s_max_i32 s70, s70, s41\n"
"v_mov_b32 v4, s70\n"
"ds_write_b32 v11, v4\n"
"Lav_102:\n"
"s_cmp_gt_i32 s70, s21\n"
"s_cbranch_scc1 Lbk_100\n"
"s_sleep 1\n"
"s_memrealtime s[48:49]\n"
"s_waitcnt lgkmcnt(0)\n"
"s_sub_u32 s48, s48, s23\n"
"s_cmp_gt_i32 s48, 0\n"
"s_cbranch_scc1 LP2_end\n"
"s_branch Lbk_99\n"
"Lbk_101:\n"
"ds_read_b32 v4, v11\n"
"s_waitcnt lgkmcnt(0)\n"
"v_readfirstlane_b32 s70, v4\n"
"s_cmp_gt_i32 s70, s21\n"
"s_cbranch_scc1 Lbk_100\n"
"s_sleep 1\n"
"s_memrealtime s[48:49]\n"
"s_waitcnt lgkmcnt(0)\n"
"s_sub_u32 s48, s48, s23\n"
"s_cmp_gt_i32 s48, 0\n"
"s_cbranch_scc1 LP2_end\n"
"s_branch Lbk_101\n"
"Lbk_100:\n"
"s_lshl_b32 s56, s21, 19\n"
"s_add_u32 s56, s56, 69206016\n"
"s_add_u32 s56, s56, s68\n"
"s_lshr_b32 s43, s56, 8\n"
"s_xor_b32 s43, s43, s22\n"
"s_not_b32 s43, s43\n"
"s_and_b32 s43, s43, 8192\n"
"s_or_b32 s56, s56, s43\n"
"s_lshl_b32 s65, s21, 17\n"
"s_add_u32 s65, s65, 157286400\n"
"s_mul_i32 s67, s21, 36864\n"
"s_lshr_b32 s43, s65, 8\n"
"s_xor_b32 s43, s43, s22\n"
"s_not_b32 s43, s43\n"
"s_and_b32 s43, s43, 8192\n"
"s_or_b32 s65, s65, s43\n"
"s_add_u32 s66, s65, s73\n"
"s_add_u32 s65, s65, s72\n"
"buffer_load_dwordx4 v[96:99], v1, s[24:27], s56 offen offset:0 sc1\n"
"s_nop 5\n"
"buffer_load_dwordx4 v[100:103], v1, s[24:27], s56 offen offset:1024 sc1\n"
"s_nop 5\n"
"buffer_load_dwordx4 v[104:107], v1, s[24:27], s56 offen offset:2048 sc1\n"
"s_nop 5\n"
"buffer_load_dwordx4 v[108:111], v1, s[24:27], s56 offen offset:3072 sc1\n"
"s_add_u32 s56, s56, 4096\n"
"s_nop 5\n"
"buffer_load_dwordx4 v[112:115], v1, s[24:27], s56 offen offset:0 sc1\n"
"s_nop 5\n"
"buffer_load_dwordx4 v[116:119], v1, s[24:27], s56 offen offset:1024 sc1\n"
"s_nop 5\n"
"s_cmp_eq_u32 s64, 0\n"
"s_cbranch_scc1 Lpe_104\n"
"s_mov_b32 s64, 0\n"
"s_waitcnt vmcnt(6)\n"
"s_lshl_b32 s43, s19, 7\n"
"s_xor_b32 s43, s43, s22\n"
"s_xor_b32 s43, s43, 1286144\n"
"s_add_u32 s42, s21, 0\n"
"v_mov_b32 v3, s42\n"
"buffer_store_dword v3, v2, s[24:27], s43 offen sc1\n"
"Lpf_105:\n"
"Lpe_104:\n"
"buffer_load_dwordx4 v[120:123], v1, s[24:27], s56 offen offset:2048 sc1\n"
"s_nop 5\n"
"buffer_load_dwordx4 v[124:127], v1, s[24:27], s56 offen offset:3072 sc1\n"
"s_add_u32 s56, s56, 12288\n"
"s_nop 5\n"
"buffer_load_dwordx4 v[128:131], v1, s[24:27], s56 offen offset:0 sc1\n"
"s_nop 5\n"
"buffer_load_dwordx4 v[132:135], v1, s[24:27], s56 offen offset:1024 sc1\n"
"s_nop 5\n"
"buffer_load_dwordx4 v[136:139], v1, s[24:27], s56 offen offset:2048 sc1\n"
"s_nop 5\n"
"buffer_load_dwordx4 v[140:143], v1, s[24:27], s56 offen offset:3072 sc1\n"
"s_add_u32 s56, s56, 4096\n"
"s_nop 5\n"
"buffer_load_dwordx4 v[144:147], v1, s[24:27], s56 offen offset:0 sc1\n"
"s_nop 5\n"
"buffer_load_dwordx4 v[148:151], v1, s[24:27], s56 offen offset:1024 sc1\n"
"s_nop 5\n"
"buffer_load_dwordx4 v[152:155], v1, s[24:27], s56 offen offset:2048 sc1\n"
"s_nop 5\n"
"buffer_load_dwordx4 v[156:159], v1, s[24:27], s56 offen offset:3072 sc1\n"
"s_add_u32 s56, s56, 12288\n"
"s_nop 5\n"
"s_add_u32 s63, s21, 1\n"
"s_mov_b32 s59, s65\n"
"s_mov_b32 s58, s66\n"
"s_mov_b32 s61, s67\n"
"buffer_load_dword v10, v5, s[24:27], s71 offen sc1\n"
"s_waitcnt vmcnt(16)\n"
"v_mfma_f32_16x16x32_bf16 v[48:51], a[0:3], v[96:99], 0\n"
"buffer_load_dwordx4 v[160:163], v1, s[24:27], s56 offen offset:0 sc1\n"
"v_mfma_f32_16x16x32_bf16 v[52:55], a[4:7], v[96:99], 0\n"
"s_waitcnt vmcnt(16)\n"
"v_mfma_f32_16x16x32_bf16 v[48:51], a[8:11], v[100:103], v[48:51]\n"
"buffer_load_dwordx4 v[164:167], v1, s[24:27], s56 offen offset:1024 sc1\n"
"v_mfma_f32_16x16x32_bf16 v[52:55], a[12:15], v[100:103], v[52:55]\n"
"s_waitcnt vmcnt(16)\n"
"v_mfma_f32_16x16x32_bf16 v[48:51], a[16:19], v[104:107], v[48:51]\n"
"buffer_load_dwordx4 v[168:171], v1, s[24:27], s56 offen offset:2048 sc1\n"
"v_mfma_f32_16x16x32_bf16 v[52:55], a[20:23], v[104:107], v[52:55]\n"
"s_waitcnt vmcnt(16)\n"
"v_mfma_f32_16x16x32_bf16 v[48:51], a[24:27], v[108:111], v[48:51]\n"
"buffer_load_dwordx4 v[172:175], v1, s[24:27], s56 offen offset:3072 sc1\n"
"s_add_u32 s56, s56, 4096\n"
"v_mfma_f32_16x16x32_bf16 v[52:55], a[28:31], v[108:111], v[52:55]\n"
"s_waitcnt vmcnt(16)\n"
"v_mfma_f32_16x16x32_bf16 v[48:51], a[32:35], v[112:115], v[48:51]\n"
"buffer_load_dwordx4 v[176:179], v1, s[24:27], s56 offen offset:0 sc1\n"
"v_mfma_f32_16x16x32_bf16 v[52:55], a[36:39], v[112:115], v[52:55]\n"
"s_waitcnt vmcnt(16)\n"
"v_mfma_f32_16x16x32_bf16 v[48:51], a[40:43], v[116:119], v[48:51]\n"
"buffer_load_dwordx4 v[180:183], v1, s[24:27], s56 offen offset:1024 sc1\n"
"v_mfma_f32_16x16x32_bf16 v[52:55], a[44:47], v[116:119], v[52:55]\n"
"s_waitcnt vmcnt(16)\n"
"v_mfma_f32_16x16x32_bf16 v[48:51], a[48:51], v[120:123], v[48:51]\n"
"buffer_load_dwordx4 v[184:187], v1, s[24:27], s56 offen offset:2048 sc1\n"
"v_mfma_f32_16x16x32_bf16 v[52:55], a[52:55], v[120:123], v[52:55]\n"
"s_waitcnt vmcnt(16)\n"
"v_mfma_f32_16x16x32_bf16 v[48:51], a[56:59], v[124:127], v[48:51]\n"
"buffer_load_dwordx4 v[188:191], v1, s[24:27], s56 offen offset:3072 sc1\n"
"s_add_u32 s56, s56, 12288\n"
"v_mfma_f32_16x16x32_bf16 v[52:55], a[60:63], v[124:127], v[52:55]\n"
"s_waitcnt vmcnt(16)\n"
"v_mfma_f32_16x16x32_bf16 v[48:51], a[64:67], v[128:131], v[48:51]\n"
"buffer_load_dwordx4 v[192:195], v1, s[24:27], s56 offen offset:0 sc1\n"
"v_mfma_f32_16x16x32_bf16 v[52:55], a[68:71], v[128:131], v[52:55]\n"
"s_waitcnt vmcnt(16)\n"
"v_mfma_f32_16x16x32_bf16 v[48:51], a[72:75], v[132:135], v[48:51]\n"
"buffer_load_dwordx4 v[196:199], v1, s[24:27], s56 offen offset:1024 sc1\n"
"v_mfma_f32_16x16x32_bf16 v[52:55], a[76:79], v[132:135], v[52:55]\n"
"s_waitcnt vmcnt(16)\n"
"v_mfma_f32_16x16x32_bf16 v[48:51], a[80:83], v[136:139], v[48:51]\n"
"buffer_load_dwordx4 v[200:203], v1, s[24:27], s56 offen offset:2048 sc1\n"
"v_mfma_f32_16x16x32_bf16 v[52:55], a[84:87], v[136:139], v[52:55]\n"
"s_waitcnt vmcnt(16)\n"
"v_mfma_f32_16x16x32_bf16 v[48:51], a[88:91], v[140:143], v[48:51]\n"
"buffer_load_dwordx4 v[204:207], v1, s[24:27], s56 offen offset:3072 sc1\n"
"s_add_u32 s56, s56, 4096\n"
"v_mfma_f32_16x16x32_bf16 v[52:55], a[92:95], v[140:143], v[52:55]\n"
"s_waitcnt vmcnt(16)\n"
"v_mfma_f32_16x16x32_bf16 v[48:51], a[96:99], v[144:147], v[48:51]\n"
"buffer_load_dwordx4 v[208:211], v1, s[24:27], s56 offen offset:0 sc1\n"
"v_mfma_f32_16x16x32_bf16 v[52:55], a[100:103], v[144:147], v[52:55]\n"
"s_waitcnt vmcnt(16)\n"
"v_mfma_f32_16x16x32_bf16 v[48:51], a[104:107], v[148:151], v[48:51]\n"
"buffer_load_dwordx4 v[212:215], v1, s[24:27], s56 offen offset:1024 sc1\n"
"v_mfma_f32_16x16x32_bf16 v[52:55], a[108:111], v[148:151], v[52:55]\n"
"s_waitcnt vmcnt(16)\n"
"v_mfma_f32_16x16x32_bf16 v[48:51], a[112:115], v[152:155], v[48:51]\n"
"buffer_load_dwordx4 v[216:219], v1, s[24:27], s56 offen offset:2048 sc1\n"
"v_mfma_f32_16x16x32_bf16 v[52:55], a[116:119], v[152:155], v[52:55]\n"
"s_waitcnt vmcnt(16)\n"
"v_mfma_f32_16x16x32_bf16 v[48:51], a[120:123], v[156:159], v[48:51]\n"
"buffer_load_dwordx4 v[220:223], v1, s[24:27], s56 offen offset:3072 sc1\n"
"s_add_u32 s56, s56, 12288\n"
"v_mfma_f32_16x16x32_bf16 v[52:55], a[124:127], v[156:159], v[52:55]\n"
"s_branch LP2_mid\n"
"LP2_loop:\n"
"s_add_u32 s63, s21, 1\n"
"s_mov_b32 s59, s65\n"
"s_mov_b32 s58, s66\n"
"s_mov_b32 s61, s67\n"
"s_waitcnt vmcnt(32)\n"
"v_mfma_f32_16x16x32_bf16 v[48:51], a[0:3], v[96:99], 0\n"
"v_mfma_f32_16x16x32_bf16 v[52:55], a[4:7], v[96:99], 0\n"
"s_waitcnt vmcnt(31)\n"
"v_mfma_f32_16x16x32_bf16 v[48:51], a[8:11], v[100:103], v[48:51]\n"
"v_mfma_f32_16x16x32_bf16 v[52:55], a[12:15], v[100:103], v[52:55]\n"
"s_waitcnt vmcnt(30)\n"
"v_mfma_f32_16x16x32_bf16 v[48:51], a[16:19], v[104:107], v[48:51]\n"
"v_mfma_f32_16x16x32_bf16 v[52:55], a[20:23], v[104:107], v[52:55]\n"
"s_waitcnt vmcnt(29)\n"
"v_mfma_f32_16x16x32_bf16 v[48:51], a[24:27], v[108:111], v[48:51]\n"
"v_mfma_f32_16x16x32_bf16 v[52:55], a[28:31], v[108:111], v[52:55]\n"
"s_waitcnt vmcnt(28)\n"
"v_mfma_f32_16x16x32_bf16 v[48:51], a[32:35], v[112:115], v[48:51]\n"
"v_mfma_f32_16x16x32_bf16 v[52:55], a[36:39], v[112:115], v[52:55]\n"
"s_waitcnt vmcnt(27)\n"
"v_mfma_f32_16x16x32_bf16 v[48:51], a[40:43], v[116:119], v[48:51]\n"
"v_mfma_f32_16x16x32_bf16 v[52:55], a[44:47], v[116:119], v[52:55]\n"
"s_waitcnt vmcnt(26)\n"
"v_mfma_f32_16x16x32_bf16 v[48:51], a[48:51], v[120:123], v[48:51]\n"
"v_mfma_f32_16x16x32_bf16 v[52:55], a[52:55], v[120:123], v[52:55]\n"
"s_waitcnt vmcnt(25)\n"
"v_mfma_f32_16x16x32_bf16 v[48:51], a[56:59], v[124:127], v[48:51]\n"
"v_mfma_f32_16x16x32_bf16 v[52:55], a[60:63], v[124:127], v[52:55]\n"
"s_waitcnt vmcnt(24)\n"
"v_mfma_f32_16x16x32_bf16 v[48:51], a[64:67], v[128:131], v[48:51]\n"
"v_mfma_f32_16x16x32_bf16 v[52:55], a[68:71], v[128:131], v[52:55]\n"
"s_waitcnt vmcnt(23)\n"
"v_mfma_f32_16x16x32_bf16 v[48:51], a[72:75], v[132:135], v[48:51]\n"
"v_mfma_f32_16x16x32_bf16 v[52:55], a[76:79], v[132:135], v[52:55]\n"
"s_waitcnt vmcnt(22)\n"
"v_mfma_f32_16x16x32_bf16 v[48:51], a[80:83], v[136:139], v[48:51]\n"
"v_mfma_f32_16x16x32_bf16 v[52:55], a[84:87], v[136:139], v[52:55]\n"
"s_waitcnt vmcnt(21)\n"
"v_mfma_f32_16x16x32_bf16 v[48:51], a[88:91], v[140:143], v[48:51]\n"
"v_mfma_f32_16x16x32_bf16 v[52:55], a[92:95], v[140:143], v[52:55]\n"
"s_waitcnt vmcnt(20)\n"
"v_mfma_f32_16x16x32_bf16 v[48:51], a[96:99], v[144:147], v[48:51]\n"
"v_mfma_f32_16x16x32_bf16 v[52:55], a[100:103], v[144:147], v[52:55]\n"
"s_waitcnt vmcnt(19)\n"
"v_mfma_f32_16x16x32_bf16 v[48:51], a[104:107], v[148:151], v[48:51]\n"
"v_mfma_f32_16x16x32_bf16 v[52:55], a[108:111], v[148:151], v[52:55]\n"
"s_waitcnt vmcnt(18)\n"
"v_mfma_f32_16x16x32_bf16 v[48:51], a[112:115], v[152:155], v[48:51]\n"
"v_mfma_f32_16x16x32_bf16 v[52:55], a[116:119], v[152:155], v[52:55]\n"
"s_waitcnt vmcnt(17)\n"
"v_mfma_f32_16x16x32_bf16 v[48:51], a[120:123], v[156:159], v[48:51]\n"
"v_mfma_f32_16x16x32_bf16 v[52:55], a[124:127], v[156:159], v[52:55]\n"
"LP2_mid:\n"
"s_waitcnt vmcnt(16)\n"
"s_cmp_eq_u32 s20, 0\n"
"s_cselect_b32 s36, s67, 2146435072\n"
"buffer_load_dwordx4 v[56:59], v7, s[32:35], s36 offen\n"
"s_cmp_lg_u32 s20, 3\n"
"s_cbranch_scc1 Lav_108\n"
"v_cmp_ge_i32 vcc, s63, v10\n"
"s_cbranch_vccnz Lav_108\n"
"s_add_u32 s41, s63, 1\n"
"v_cmp_ge_i32 vcc, s41, v10\n"
"s_cbranch_vccnz Lav_109\n"
"s_add_u32 s41, s41, 1\n"
"Lav_109:\n"
"s_max_i32 s70, s70, s41\n"
"v_mov_b32 v4, s70\n"
"ds_write_b32 v11, v4\n"
"Lav_108:\n"
"ds_read_b32 v4, v11\n"
"s_waitcnt lgkmcnt(0)\n"
"v_readfirstlane_b32 s70, v4\n"
"s_add_u32 s36, s21, 1\n"
"s_cmp_gt_i32 s70, s36\n"
"s_cselect_b32 s62, 1, 0\n"
"s_cbranch_scc0 Lno_106\n"
"s_lshl_b32 s56, s63, 19\n"
"s_add_u32 s56, s56, 69206016\n"
"s_add_u32 s56, s56, s68\n"
"s_lshr_b32 s43, s56, 8\n"
"s_xor_b32 s43, s43, s22\n"
"s_not_b32 s43, s43\n"
"s_and_b32 s43, s43, 8192\n"
"s_or_b32 s56, s56, s43\n"
"s_lshl_b32 s65, s63, 17\n"
"s_add_u32 s65, s65, 157286400\n"
"s_mul_i32 s67, s63, 36864\n"
"s_lshr_b32 s43, s65, 8\n"
"s_xor_b32 s43, s43, s22\n"
"s_not_b32 s43, s43\n"
"s_and_b32 s43, s43, 8192\n"
"s_or_b32 s65, s65, s43\n"
"s_add_u32 s66, s65, s73\n"
"s_add_u32 s65, s65, s72\n"
"s_waitcnt vmcnt(16)\n"
"v_mfma_f32_16x16x32_bf16 v[48:51], a[128:131], v[160:163], v[48:51]\n"
"buffer_load_dwordx4 v[96:99], v1, s[24:27], s56 offen offset:0 sc1\n"
"v_mfma_f32_16x16x32_bf16 v[52:55], a[132:135], v[160:163], v[52:55]\n"
"s_waitcnt vmcnt(16)\n"
"v_mfma_f32_16x16x32_bf16 v[48:51], a[136:139], v[164:167], v[48:51]\n"
"buffer_load_dwordx4 v[100:103], v1, s[24:27], s56 offen offset:1024 sc1\n"
"v_mfma_f32_16x16x32_bf16 v[52:55], a[140:143], v[164:167], v[52:55]\n"
"s_waitcnt vmcnt(16)\n"
"v_mfma_f32_16x16x32_bf16 v[48:51], a[144:147], v[168:171], v[48:51]\n"
"buffer_load_dwordx4 v[104:107], v1, s[24:27], s56 offen offset:2048 sc1\n"
"v_mfma_f32_16x16x32_bf16 v[52:55], a[148:151], v[168:171], v[52:55]\n"
"s_waitcnt vmcnt(16)\n"
"v_mfma_f32_16x16x32_bf16 v[48:51], a[152:155], v[172:175], v[48:51]\n"
"buffer_load_dwordx4 v[108:111], v1, s[24:27], s56 offen offset:3072 sc1\n"
"s_add_u32 s56, s56, 4096\n"
"v_mfma_f32_16x16x32_bf16 v[52:55], a[156:159], v[172:175], v[52:55]\n"
"s_waitcnt vmcnt(16)\n"
"v_mfma_f32_16x16x32_bf16 v[48:51], a[160:163], v[176:179], v[48:51]\n"
"buffer_load_dwordx4 v[112:115], v1, s[24:27], s56 offen offset:0 sc1\n"
"v_mfma_f32_16x16x32_bf16 v[52:55], a[164:167], v[176:179], v[52:55]\n"
"s_waitcnt vmcnt(16)\n"
"v_mfma_f32_16x16x32_bf16 v[48:51], a[168:171], v[180:183], v[48:51]\n"
"buffer_load_dwordx4 v[116:119], v1, s[24:27], s56 offen offset:1024 sc1\n"
"v_mfma_f32_16x16x32_bf16 v[52:55], a[172:175], v[180:183], v[52:55]\n"
"s_waitcnt vmcnt(16)\n"
"v_mfma_f32_16x16x32_bf16 v[48:51], a[176:179], v[184:187], v[48:51]\n"
"buffer_load_dwordx4 v[120:123], v1, s[24:27], s56 offen offset:2048 sc1\n"
"v_mfma_f32_16x16x32_bf16 v[52:55], a[180:183], v[184:187], v[52:55]\n"
"s_waitcnt vmcnt(16)\n"
"v_mfma_f32_16x16x32_bf16 v[48:51], a[184:187], v[188:191], v[48:51]\n"
"buffer_load_dwordx4 v[124:127], v1, s[24:27], s56 offen offset:3072 sc1\n"
"s_add_u32 s56, s56, 12288\n"
"v_mfma_f32_16x16x32_bf16 v[52:55], a[188:191], v[188:191], v[52:55]\n"
"s_waitcnt vmcnt(16)\n"
"v_mfma_f32_16x16x32_bf16 v[48:51], a[192:195], v[192:195], v[48:51]\n"
"buffer_load_dwordx4 v[128:131], v1, s[24:27], s56 offen offset:0 sc1\n"
"v_mfma_f32_16x16x32_bf16 v[52:55], a[196:199], v[192:195], v[52:55]\n"
"s_waitcnt vmcnt(16)\n"
"v_mfma_f32_16x16x32_bf16 v[48:51], a[200:203], v[196:199], v[48:51]\n"
"buffer_load_dwordx4 v[132:135], v1, s[24:27], s56 offen offset:1024 sc1\n"
"v_mfma_f32_16x16x32_bf16 v[52:55], a[204:207], v[196:199], v[52:55]\n"
"s_waitcnt vmcnt(16)\n"
"v_mfma_f32_16x16x32_bf16 v[48:51], a[208:211], v[200:203], v[48:51]\n"
"buffer_load_dwordx4 v[136:139], v1, s[24:27], s56 offen offset:2048 sc1\n"
"v_mfma_f32_16x16x32_bf16 v[52:55], a[212:215], v[200:203], v[52:55]\n"
"s_waitcnt vmcnt(16)\n"
"v_mfma_f32_16x16x32_bf16 v[48:51], a[216:219], v[204:207], v[48:51]\n"
"buffer_load_dwordx4 v[140:143], v1, s[24:27], s56 offen offset:3072 sc1\n"
"s_add_u32 s56, s56, 4096\n"
"v_mfma_f32_16x16x32_bf16 v[52:55], a[220:223], v[204:207], v[52:55]\n"
"s_waitcnt vmcnt(16)\n"
"v_mfma_f32_16x16x32_bf16 v[48:51], a[224:227], v[208:211], v[48:51]\n"
"buffer_load_dwordx4 v[144:147], v1, s[24:27], s56 offen offset:0 sc1\n"
"v_mfma_f32_16x16x32_bf16 v[52:55], a[228:231], v[208:211], v[52:55]\n"
"s_waitcnt vmcnt(16)\n"
"v_mfma_f32_16x16x32_bf16 v[48:51], a[232:235], v[212:215], v[48:51]\n"
"buffer_load_dwordx4 v[148:151], v1, s[24:27], s56 offen offset:1024 sc1\n"
"v_mfma_f32_16x16x32_bf16 v[52:55], a[236:239], v[212:215], v[52:55]\n"
"s_waitcnt vmcnt(16)\n"
"v_mfma_f32_16x16x32_bf16 v[48:51], a[240:243], v[216:219], v[48:51]\n"
"buffer_load_dwordx4 v[152:155], v1, s[24:27], s56 offen offset:2048 sc1\n"
"v_mfma_f32_16x16x32_bf16 v[52:55], a[244:247], v[216:219], v[52:55]\n"
"s_waitcnt vmcnt(16)\n"
"v_mfma_f32_16x16x32_bf16 v[48:51], a[248:251], v[220:223], v[48:51]\n"
"buffer_load_dwordx4 v[156:159], v1, s[24:27], s56 offen offset:3072 sc1\n"
"s_add_u32 s56, s56, 12288\n"
"v_mfma_f32_16x16x32_bf16 v[52:55], a[252:255], v[220:223], v[52:55]\n"
"s_branch Lj_107\n"
"Lno_106:\n"
"s_waitcnt vmcnt(16)\n"
"v_mfma_f32_16x16x32_bf16 v[48:51], a[128:131], v[160:163], v[48:51]\n"
"v_mfma_f32_16x16x32_bf16 v[52:55], a[132:135], v[160:163], v[52:55]\n"
"s_waitcnt vmcnt(15)\n"
"v_mfma_f32_16x16x32_bf16 v[48:51], a[136:139], v[164:167], v[48:51]\n"
"v_mfma_f32_16x16x32_bf16 v[52:55], a[140:143], v[164:167], v[52:55]\n"
"s_waitcnt vmcnt(14)\n"
"v_mfma_f32_16x16x32_bf16 v[48:51], a[144:147], v[168:171], v[48:51]\n"
"v_mfma_f32_16x16x32_bf16 v[52:55], a[148:151], v[168:171], v[52:55]\n"
"s_waitcnt vmcnt(13)\n"
"v_mfma_f32_16x16x32_bf16 v[48:51], a[152:155], v[172:175], v[48:51]\n"
"v_mfma_f32_16x16x32_bf16 v[52:55], a[156:159], v[172:175], v[52:55]\n"
"s_waitcnt vmcnt(12)\n"
"v_mfma_f32_16x16x32_bf16 v[48:51], a[160:163], v[176:179], v[48:51]\n"
"v_mfma_f32_16x16x32_bf16 v[52:55], a[164:167], v[176:179], v[52:55]\n"
"s_waitcnt vmcnt(11)\n"
"v_mfma_f32_16x16x32_bf16 v[48:51], a[168:171], v[180:183], v[48:51]\n"
"v_mfma_f32_16x16x32_bf16 v[52:55], a[172:175], v[180:183], v[52:55]\n"
"s_waitcnt vmcnt(10)\n"
"v_mfma_f32_16x16x32_bf16 v[48:51], a[176:179], v[184:187], v[48:51]\n"
"buffer_load_dword v10, v5, s[24:27], s71 offen sc1\n"
"v_mfma_f32_16x16x32_bf16 v[52:55], a[180:183], v[184:187], v[52:55]\n"
"s_waitcnt vmcnt(10)\n"
"v_mfma_f32_16x16x32_bf16 v[48:51], a[184:187], v[188:191], v[48:51]\n"
"v_mfma_f32_16x16x32_bf16 v[52:55], a[188:191], v[188:191], v[52:55]\n"
"s_waitcnt vmcnt(9)\n"
"v_mfma_f32_16x16x32_bf16 v[48:51], a[192:195], v[192:195], v[48:51]\n"
"v_mfma_f32_16x16x32_bf16 v[52:55], a[196:199], v[192:195], v[52:55]\n"
"s_waitcnt vmcnt(8)\n"
"v_mfma_f32_16x16x32_bf16 v[48:51], a[200:203], v[196:199], v[48:51]\n"
"v_mfma_f32_16x16x32_bf16 v[52:55], a[204:207], v[196:199], v[52:55]\n"
"s_waitcnt vmcnt(7)\n"
"v_mfma_f32_16x16x32_bf16 v[48:51], a[208:211], v[200:203], v[48:51]\n"
"v_mfma_f32_16x16x32_bf16 v[52:55], a[212:215], v[200:203], v[52:55]\n"
"s_waitcnt vmcnt(6)\n"
"v_mfma_f32_16x16x32_bf16 v[48:51], a[216:219], v[204:207], v[48:51]\n"
"v_mfma_f32_16x16x32_bf16 v[52:55], a[220:223], v[204:207], v[52:55]\n"
"s_waitcnt vmcnt(5)\n"
"v_mfma_f32_16x16x32_bf16 v[48:51], a[224:227], v[208:211], v[48:51]\n"
"v_mfma_f32_16x16x32_bf16 v[52:55], a[228:231], v[208:211], v[52:55]\n"
"s_waitcnt vmcnt(4)\n"
"v_mfma_f32_16x16x32_bf16 v[48:51], a[232:235], v[212:215], v[48:51]\n"
"v_mfma_f32_16x16x32_bf16 v[52:55], a[236:239], v[212:215], v[52:55]\n"
"s_waitcnt vmcnt(3)\n"
"v_mfma_f32_16x16x32_bf16 v[48:51], a[240:243], v[216:219], v[48:51]\n"
"v_mfma_f32_16x16x32_bf16 v[52:55], a[244:247], v[216:219], v[52:55]\n"
"s_waitcnt vmcnt(2)\n"
"v_mfma_f32_16x16x32_bf16 v[48:51], a[248:251], v[220:223], v[48:51]\n"
"v_mfma_f32_16x16x32_bf16 v[52:55], a[252:255], v[220:223], v[52:55]\n"
"Lj_107:\n"
"s_nop 6\n"
"ds_write_b128 v6, v[48:51] offset:0\n"
"ds_write_b128 v6, v[52:55] offset:1024\n"
"s_waitcnt lgkmcnt(0)\n"
"s_barrier\n"
"s_cmp_lg_u32 s20, 0\n"
"s_cbranch_scc1 Lw0_110\n"
"s_cmp_eq_u32 s62, 1\n"
"s_cbranch_scc1 Leh_112\n"
"s_waitcnt vmcnt(1)\n"
"ds_read_b128 v[60:63], v9 offset:0\n"
"ds_read_b128 v[64:67], v9 offset:1024\n"
"ds_read_b128 v[68:71], v9 offset:2048\n"
"ds_read_b128 v[72:75], v9 offset:3072\n"
"ds_read_b128 v[76:79], v9 offset:4096\n"
"ds_read_b128 v[80:83], v9 offset:5120\n"
"ds_read_b128 v[84:87], v9 offset:6144\n"
"ds_read_b128 v[88:91], v9 offset:7168\n"
"s_waitcnt lgkmcnt(4)\n"
"v_pk_add_f32 v[60:61], v[60:61], v[68:69]\n"
"v_pk_add_f32 v[62:63], v[62:63], v[70:71]\n"
"v_pk_add_f32 v[64:65], v[64:65], v[72:73]\n"
"v_pk_add_f32 v[66:67], v[66:67], v[74:75]\n"
"s_waitcnt lgkmcnt(2)\n"
"v_pk_add_f32 v[60:61], v[60:61], v[76:77]\n"
"v_pk_add_f32 v[62:63], v[62:63], v[78:79]\n"
"v_pk_add_f32 v[64:65], v[64:65], v[80:81]\n"
"v_pk_add_f32 v[66:67], v[66:67], v[82:83]\n"
"s_waitcnt lgkmcnt(0)\n"
"v_pk_add_f32 v[60:61], v[60:61], v[84:85]\n"
"v_pk_add_f32 v[62:63], v[62:63], v[86:87]\n"
"v_pk_add_f32 v[64:65], v[64:65], v[88:89]\n"
"v_pk_add_f32 v[66:67], v[66:67], v[90:91]\n"
"v_cvt_pk_bf16_f32 v3, v60, v61\n"
"v_lshlrev_b32 v76, 16, v56\n"
"v_and_b32 v77, s55, v56\n"
"v_lshlrev_b32 v60, 16, v3\n"
"v_and_b32 v61, s55, v3\n"
"v_pk_add_f32 v[60:61], v[60:61], v[76:77]\n"
"v_cvt_pk_bf16_f32 v68, v60, v61\n"
"v_cvt_pk_bf16_f32 v3, v62, v63\n"
"v_lshlrev_b32 v76, 16, v57\n"
"v_and_b32 v77, s55, v57\n"
"v_lshlrev_b32 v62, 16, v3\n"
"v_and_b32 v63, s55, v3\n"
"v_pk_add_f32 v[62:63], v[62:63], v[76:77]\n"
"v_cvt_pk_bf16_f32 v69, v62, v63\n"
"v_cvt_pk_bf16_f32 v3, v64, v65\n"
"v_lshlrev_b32 v76, 16, v58\n"
"v_and_b32 v77, s55, v58\n"
"v_lshlrev_b32 v64, 16, v3\n"
"v_and_b32 v65, s55, v3\n"
"v_pk_add_f32 v[64:65], v[64:65], v[76:77]\n"
"v_cvt_pk_bf16_f32 v70, v64, v65\n"
"v_cvt_pk_bf16_f32 v3, v66, v67\n"
"v_lshlrev_b32 v76, 16, v59\n"
"v_and_b32 v77, s55, v59\n"
"v_lshlrev_b32 v66, 16, v3\n"
"v_and_b32 v67, s55, v3\n"
"v_pk_add_f32 v[66:67], v[66:67], v[76:77]\n"
"v_cvt_pk_bf16_f32 v71, v66, v67\n"
"buffer_store_dwordx4 v[68:71], v1, s[24:27], s59 offen sc1\n"
"v_lshlrev_b32 v76, 16, v68\n"
"v_and_b32 v77, s55, v68\n"
"v_pk_mul_f32 v[74:75], v[76:77], v[76:77]\n"
"v_mov_b64 v[72:73], v[76:77]\n"
"v_lshlrev_b32 v76, 16, v69\n"
"v_and_b32 v77, s55, v69\n"
"v_pk_fma_f32 v[74:75], v[76:77], v[76:77], v[74:75]\n"
"v_pk_add_f32 v[72:73], v[72:73], v[76:77]\n"
"v_lshlrev_b32 v76, 16, v70\n"
"v_and_b32 v77, s55, v70\n"
"v_pk_fma_f32 v[74:75], v[76:77], v[76:77], v[74:75]\n"
"v_pk_add_f32 v[72:73], v[72:73], v[76:77]\n"
"v_lshlrev_b32 v76, 16, v71\n"
"v_and_b32 v77, s55, v71\n"
"v_pk_fma_f32 v[74:75], v[76:77], v[76:77], v[74:75]\n"
"v_pk_add_f32 v[72:73], v[72:73], v[76:77]\n"
"v_add_f32 v72, v72, v73\n"
"v_add_f32 v73, v74, v75\n"
"v_mov_b64 v[76:77], v[72:73]\n"
"s_nop 1\n"
"v_permlane32_swap_b32 v72, v76\n"
"v_permlane32_swap_b32 v73, v77\n"
"v_pk_add_f32 v[72:73], v[72:73], v[76:77]\n"
"v_mov_b64 v[76:77], v[72:73]\n"
"s_nop 1\n"
"v_permlane16_swap_b32 v72, v76\n"
"v_permlane16_swap_b32 v73, v77\n"
"v_pk_add_f32 v[72:73], v[72:73], v[76:77]\n"
"buffer_store_dwordx2 v[72:73], v8, s[24:27], s58 offen sc1\n"
"ds_read_b32 v4, v11\n"
"s_waitcnt lgkmcnt(0)\n"
"v_readfirstlane_b32 s70, v4\n"
"s_cmp_gt_i32 s70, s63\n"
"s_cselect_b32 s64, 1, 0\n"
"s_cbranch_scc1 Lw0_111\n"
"s_waitcnt vmcnt(0)\n"
"s_lshl_b32 s43, s19, 7\n"
"s_xor_b32 s43, s43, s22\n"
"s_xor_b32 s43, s43, 1286144\n"
"s_add_u32 s42, s21, 1\n"
"v_mov_b32 v3, s42\n"
"buffer_store_dword v3, v2, s[24:27], s43 offen sc1\n"
"Lpf_113:\n"
"s_branch Lw0_111\n"
"Leh_112:\n"
"s_waitcnt vmcnt(16)\n"
"buffer_load_dword v10, v5, s[24:27], s71 offen sc1\n"
"ds_read_b128 v[60:63], v9 offset:0\n"
"ds_read_b128 v[64:67], v9 offset:1024\n"
"ds_read_b128 v[68:71], v9 offset:2048\n"
"ds_read_b128 v[72:75], v9 offset:3072\n"
"ds_read_b128 v[76:79], v9 offset:4096\n"
"ds_read_b128 v[80:83], v9 offset:5120\n"
"ds_read_b128 v[84:87], v9 offset:6144\n"
"ds_read_b128 v[88:91], v9 offset:7168\n"
"s_waitcnt lgkmcnt(4)\n"
"v_pk_add_f32 v[60:61], v[60:61], v[68:69]\n"
"v_pk_add_f32 v[62:63], v[62:63], v[70:71]\n"
"v_pk_add_f32 v[64:65], v[64:65], v[72:73]\n"
"v_pk_add_f32 v[66:67], v[66:67], v[74:75]\n"
"s_waitcnt lgkmcnt(2)\n"
"v_pk_add_f32 v[60:61], v[60:61], v[76:77]\n"
"v_pk_add_f32 v[62:63], v[62:63], v[78:79]\n"
"v_pk_add_f32 v[64:65], v[64:65], v[80:81]\n"
"v_pk_add_f32 v[66:67], v[66:67], v[82:83]\n"
"s_waitcnt lgkmcnt(0)\n"
"v_pk_add_f32 v[60:61], v[60:61], v[84:85]\n"
"v_pk_add_f32 v[62:63], v[62:63], v[86:87]\n"
"v_pk_add_f32 v[64:65], v[64:65], v[88:89]\n"
"v_pk_add_f32 v[66:67], v[66:67], v[90:91]\n"
"v_cvt_pk_bf16_f32 v3, v60, v61\n"
"v_lshlrev_b32 v76, 16, v56\n"
"v_and_b32 v77, s55, v56\n"
"v_lshlrev_b32 v60, 16, v3\n"
"v_and_b32 v61, s55, v3\n"
"v_pk_add_f32 v[60:61], v[60:61], v[76:77]\n"
"v_cvt_pk_bf16_f32 v68, v60, v61\n"
"v_cvt_pk_bf16_f32 v3, v62, v63\n"
"v_lshlrev_b32 v76, 16, v57\n"
"v_and_b32 v77, s55, v57\n"
"v_lshlrev_b32 v62, 16, v3\n"
"v_and_b32 v63, s55, v3\n"
"v_pk_add_f32 v[62:63], v[62:63], v[76:77]\n"
"v_cvt_pk_bf16_f32 v69, v62, v63\n"
"v_cvt_pk_bf16_f32 v3, v64, v65\n"
"v_lshlrev_b32 v76, 16, v58\n"
"v_and_b32 v77, s55, v58\n"
"v_lshlrev_b32 v64, 16, v3\n"
"v_and_b32 v65, s55, v3\n"
"v_pk_add_f32 v[64:65], v[64:65], v[76:77]\n"
"v_cvt_pk_bf16_f32 v70, v64, v65\n"
"v_cvt_pk_bf16_f32 v3, v66, v67\n"
"v_lshlrev_b32 v76, 16, v59\n"
"v_and_b32 v77, s55, v59\n"
"v_lshlrev_b32 v66, 16, v3\n"
"v_and_b32 v67, s55, v3\n"
"v_pk_add_f32 v[66:67], v[66:67], v[76:77]\n"
"v_cvt_pk_bf16_f32 v71, v66, v67\n"
"buffer_store_dwordx4 v[68:71], v1, s[24:27], s59 offen sc1\n"
"v_lshlrev_b32 v76, 16, v68\n"
"v_and_b32 v77, s55, v68\n"
"v_pk_mul_f32 v[74:75], v[76:77], v[76:77]\n"
"v_mov_b64 v[72:73], v[76:77]\n"
"v_lshlrev_b32 v76, 16, v69\n"
"v_and_b32 v77, s55, v69\n"
"v_pk_fma_f32 v[74:75], v[76:77], v[76:77], v[74:75]\n"
"v_pk_add_f32 v[72:73], v[72:73], v[76:77]\n"
"v_lshlrev_b32 v76, 16, v70\n"
"v_and_b32 v77, s55, v70\n"
"v_pk_fma_f32 v[74:75], v[76:77], v[76:77], v[74:75]\n"
"v_pk_add_f32 v[72:73], v[72:73], v[76:77]\n"
"v_lshlrev_b32 v76, 16, v71\n"
"v_and_b32 v77, s55, v71\n"
"v_pk_fma_f32 v[74:75], v[76:77], v[76:77], v[74:75]\n"
"v_pk_add_f32 v[72:73], v[72:73], v[76:77]\n"
"v_add_f32 v72, v72, v73\n"
"v_add_f32 v73, v74, v75\n"
"v_mov_b64 v[76:77], v[72:73]\n"
"s_nop 1\n"
"v_permlane32_swap_b32 v72, v76\n"
"v_permlane32_swap_b32 v73, v77\n"
"v_pk_add_f32 v[72:73], v[72:73], v[76:77]\n"
"v_mov_b64 v[76:77], v[72:73]\n"
"s_nop 1\n"
"v_permlane16_swap_b32 v72, v76\n"
"v_permlane16_swap_b32 v73, v77\n"
"v_pk_add_f32 v[72:73], v[72:73], v[76:77]\n"
"buffer_store_dwordx2 v[72:73], v8, s[24:27], s58 offen sc1\n"
"buffer_load_dwordx4 v[160:163], v1, s[24:27], s56 offen offset:0 sc1\n"
"buffer_load_dwordx4 v[164:167], v1, s[24:27], s56 offen offset:1024 sc1\n"
"buffer_load_dwordx4 v[168:171], v1, s[24:27], s56 offen offset:2048 sc1\n"
"buffer_load_dwordx4 v[172:175], v1, s[24:27], s56 offen offset:3072 sc1\n"
"s_add_u32 s56, s56, 4096\n"
"s_waitcnt vmcnt(4)\n"
"s_lshl_b32 s43, s19, 7\n"
"s_xor_b32 s43, s43, s22\n"
"s_xor_b32 s43, s43, 1286144\n"
"s_add_u32 s42, s21, 1\n"
"v_mov_b32 v3, s42\n"
"buffer_store_dword v3, v2, s[24:27], s43 offen sc1\n"
"Lpf_114:\n"
"buffer_load_dwordx4 v[176:179], v1, s[24:27], s56 offen offset:0 sc1\n"
"buffer_load_dwordx4 v[180:183], v1, s[24:27], s56 offen offset:1024 sc1\n"
"buffer_load_dwordx4 v[184:187], v1, s[24:27], s56 offen offset:2048 sc1\n"
"buffer_load_dwordx4 v[188:191], v1, s[24:27], s56 offen offset:3072 sc1\n"
"s_add_u32 s56, s56, 12288\n"
"buffer_load_dwordx4 v[192:195], v1, s[24:27], s56 offen offset:0 sc1\n"
"buffer_load_dwordx4 v[196:199], v1, s[24:27], s56 offen offset:1024 sc1\n"
"buffer_load_dwordx4 v[200:203], v1, s[24:27], s56 offen offset:2048 sc1\n"
"buffer_load_dwordx4 v[204:207], v1, s[24:27], s56 offen offset:3072 sc1\n"
"s_add_u32 s56, s56, 4096\n"
"buffer_load_dwordx4 v[208:211], v1, s[24:27], s56 offen offset:0 sc1\n"
"buffer_load_dwordx4 v[212:215], v1, s[24:27], s56 offen offset:1024 sc1\n"
"buffer_load_dwordx4 v[216:219], v1, s[24:27], s56 offen offset:2048 sc1\n"
"buffer_load_dwordx4 v[220:223], v1, s[24:27], s56 offen offset:3072 sc1\n"
"s_add_u32 s56, s56, 12288\n"
"Lw0_111:\n"
"s_branch Lw0_111x\n"
"Lw0_110:\n"
"s_cmp_eq_u32 s62, 0\n"
"s_cbranch_scc1 Lw0_111x\n"
"buffer_load_dword v10, v5, s[24:27], s71 offen sc1\n"
"buffer_load_dwordx4 v[160:163], v1, s[24:27], s56 offen offset:0 sc1\n"
"s_nop 9\n"
"buffer_load_dwordx4 v[164:167], v1, s[24:27], s56 offen offset:1024 sc1\n"
"s_nop 9\n"
"buffer_load_dwordx4 v[168:171], v1, s[24:27], s56 offen offset:2048 sc1\n"
"s_nop 9\n"
"buffer_load_dwordx4 v[172:175], v1, s[24:27], s56 offen offset:3072 sc1\n"
"s_add_u32 s56, s56, 4096\n"
"s_nop 9\n"
"buffer_load_dwordx4 v[176:179], v1, s[24:27], s56 offen offset:0 sc1\n"
"s_nop 9\n"
"buffer_load_dwordx4 v[180:183], v1, s[24:27], s56 offen offset:1024 sc1\n"
"s_nop 9\n"
"buffer_load_dwordx4 v[184:187], v1, s[24:27], s56 offen offset:2048 sc1\n"
"s_nop 9\n"
"buffer_load_dwordx4 v[188:191], v1, s[24:27], s56 offen offset:3072 sc1\n"
"s_add_u32 s56, s56, 12288\n"
"s_nop 9\n"
"buffer_load_dwordx4 v[192:195], v1, s[24:27], s56 offen offset:0 sc1\n"
"s_nop 9\n"
"buffer_load_dwordx4 v[196:199], v1, s[24:27], s56 offen offset:1024 sc1\n"
"s_nop 9\n"
"buffer_load_dwordx4 v[200:203], v1, s[24:27], s56 offen offset:2048 sc1\n"
"s_nop 9\n"
"buffer_load_dwordx4 v[204:207], v1, s[24:27], s56 offen offset:3072 sc1\n"
"s_add_u32 s56, s56, 4096\n"
"s_nop 9\n"
"buffer_load_dwordx4 v[208:211], v1, s[24:27], s56 offen offset:0 sc1\n"
"s_nop 9\n"
"buffer_load_dwordx4 v[212:215], v1, s[24:27], s56 offen offset:1024 sc1\n"
"s_nop 9\n"
"buffer_load_dwordx4 v[216:219], v1, s[24:27], s56 offen offset:2048 sc1\n"
"s_nop 9\n"
"buffer_load_dwordx4 v[220:223], v1, s[24:27], s56 offen offset:3072 sc1\n"
"s_add_u32 s56, s56, 12288\n"
"s_nop 9\n"
"Lw0_111x:\n"
"v_xor_b32 v6, 8192, v6\n"
"v_xor_b32 v9, 8192, v9\n"
"s_add_u32 s21, s21, 1\n"
"s_cmp_eq_u32 s21, 64\n"
"s_cbranch_scc1 LP2_fin\n"
"s_cmp_eq_u32 s62, 0\n"
"s_cbranch_scc1 LP2_block\n"
"s_branch LP2_loop\n"
"LP2_fin:\n"
"LP2_end:\n"
"s_endpgm\n"
"LP3:\n"
"v_mov_b32 v2, 2146435072\n"
"v_cmp_eq_u32 vcc, 0, v0\n"
"s_nop 1\n"
"v_cndmask_b32 v2, v2, 0, vcc\n"
"v_min_u32 v4, 35, v0\n"
"v_lshlrev_b32 v4, 7, v4\n"
"v_lshlrev_b32 v1, 4, v0\n"
"s_and_b32 s36, s20, 1\n"
"s_mul_i32 s36, s36, 18432\n"
"s_lshr_b32 s37, s20, 1\n"
"s_lshl_b32 s37, s37, 10\n"
"s_add_u32 s36, s36, s37\n"
"v_add_u32 v5, s36, v1\n"
"v_mov_b32 v8, v5\n"
"s_lshl_b32 s36, s20, 10\n"
"v_add_u32 v5, s36, v1\n"
"s_mov_b32 s57, 0xbfb8aa3b\n"
"v_and_b32 v3, 15, v0\n"
"v_lshlrev_b32 v6, 3, v3\n"
"v_lshlrev_b32 v7, 4, v3\n"
"v_lshlrev_b32 v14, 4, v3\n"
"v_bfe_u32 v3, v0, 4, 1\n"
"v_mul_u32_u24 v11, 2304, v3\n"
"v_add_u32 v6, v6, v11\n"
"v_mul_u32_u24 v11, 18432, v3\n"
"v_add_u32 v7, v7, v11\n"
"v_mul_u32_u24 v11, 327680, v3\n"
"v_add_u32 v14, v14, v11\n"
"v_lshrrev_b32 v3, 5, v0\n"
"v_lshl_add_u32 v7, v3, 8, v7\n"
"v_add_u32 v7, 36864, v7\n"
"v_lshl_add_u32 v14, v3, 9, v14\n"
"v_mul_u32_u24 v3, 1152, v3\n"
"v_add_u32 v6, v6, v3\n"
"v_add_u32 v6, 98304, v6\n"
"s_mul_i32 s36, s20, 1152\n"
"v_add_u32 v11, s36, v1\n"
"v_add_u32 v13, 106496, v11\n"
"v_add_u32 v12, 1024, v11\n"
"v_mov_b32 v3, 2146435072\n"
"v_cmp_gt_u32 vcc, 8, v0\n"
"s_nop 1\n"
"v_cndmask_b32 v12, v3, v12, vcc\n"
"s_sleep 32\n"
"s_sleep 32\n"
"s_sleep 32\n"
"s_sleep 32\n"
"s_sleep 32\n"
"s_sleep 32\n"
"v_lshrrev_b32 v234, 4, v0\n"
"v_mul_u32_u24 v230, 9216, v234\n"
"v_and_b32 v234, 15, v0\n"
"v_lshl_add_u32 v230, v234, 4, v230\n"
"s_mul_i32 s36, s20, 73728\n"
"v_add_u32 v230, s36, v230\n"
"v_lshlrev_b32 v231, 4, v0\n"
"s_mul_i32 s36, s20, 2048\n"
"s_add_u32 s36, s36, 73728\n"
"v_add_u32 v231, s36, v231\n"
"v_bfe_u32 v234, v0, 4, 2\n"
"v_lshlrev_b32 v234, 6, v234\n"
"v_xor_b32 v231, v231, v234\n"
"v_xor_b32 v232, 128, v231\n"
"v_lshrrev_b32 v234, 5, v0\n"
"v_lshlrev_b32 v233, 11, v234\n"
"v_bfe_u32 v234, v0, 2, 2\n"
"v_lshl_add_u32 v233, v234, 8, v233\n"
"v_xor_b32 v234, s20, v234\n"
"v_lshl_add_u32 v233, v234, 6, v233\n"
"v_and_b32 v234, 1, v0\n"
"v_lshl_add_u32 v233, v234, 5, v233\n"
"v_bfe_u32 v234, v0, 4, 1\n"
"v_lshl_add_u32 v233, v234, 4, v233\n"
"v_bfe_u32 v234, v0, 1, 1\n"
"v_lshl_add_u32 v233, v234, 3, v233\n"
"v_add_u32 v233, 73728, v233\n"
"s_load_dwordx2 s[28:29], s[16:17], 48\n"
"s_mov_b32 s30, 5308416\n"
"s_mov_b32 s31, 0x00020000\n"
"s_waitcnt lgkmcnt(0)\n"
"s_and_b32 s29, s29, 0xffff\n"
"s_load_dwordx2 s[32:33], s[16:17], 56\n"
"s_mov_b32 s34, 5308416\n"
"s_mov_b32 s35, 0x00020000\n"
"s_waitcnt lgkmcnt(0)\n"
"s_and_b32 s33, s33, 0xffff\n"
"s_lshl_b32 s60, s19, 8\n"
"s_lshl_b32 s61, s19, 8\n"
"buffer_load_dwordx4 a[0:3], v230, s[28:31], s60 offen nt\n"
"s_add_u32 s60, s60, 36864\n"
"buffer_load_dwordx4 a[4:7], v230, s[28:31], s60 offen nt\n"
"s_add_u32 s60, s60, 258048\n"
"buffer_load_dwordx4 a[8:11], v230, s[28:31], s60 offen nt\n"
"s_add_u32 s60, s60, 36864\n"
"buffer_load_dwordx4 a[12:15], v230, s[28:31], s60 offen nt\n"
"s_add_u32 s60, s60, 258048\n"
"buffer_load_dwordx4 a[16:19], v230, s[28:31], s60 offen nt\n"
"s_add_u32 s60, s60, 36864\n"
"buffer_load_dwordx4 a[20:23], v230, s[28:31], s60 offen nt\n"
"s_add_u32 s60, s60, 258048\n"
"buffer_load_dwordx4 a[24:27], v230, s[28:31], s60 offen nt\n"
"s_add_u32 s60, s60, 36864\n"
"buffer_load_dwordx4 a[28:31], v230, s[28:31], s60 offen nt\n"
"s_add_u32 s60, s60, 258048\n"
"buffer_load_dwordx4 a[32:35], v230, s[28:31], s60 offen nt\n"
"s_add_u32 s60, s60, 36864\n"
"buffer_load_dwordx4 a[36:39], v230, s[28:31], s60 offen nt\n"
"s_add_u32 s60, s60, 258048\n"
"buffer_load_dwordx4 a[40:43], v230, s[28:31], s60 offen nt\n"
"s_add_u32 s60, s60, 36864\n"
"buffer_load_dwordx4 a[44:47], v230, s[28:31], s60 offen nt\n"
"s_add_u32 s60, s60, 258048\n"
"buffer_load_dwordx4 a[48:51], v230, s[28:31], s60 offen nt\n"
"s_add_u32 s60, s60, 36864\n"
"buffer_load_dwordx4 a[52:55], v230, s[28:31], s60 offen nt\n"
"s_add_u32 s60, s60, 258048\n"
"buffer_load_dwordx4 a[56:59], v230, s[28:31], s60 offen nt\n"
"s_add_u32 s60, s60, 36864\n"
"buffer_load_dwordx4 a[60:63], v230, s[28:31], s60 offen nt\n"
"s_add_u32 s60, s60, 258048\n"
"s_waitcnt vmcnt(14)\n"
"ds_write_b128 v231, a[0:3] offset:0\n"
"ds_write_b128 v231, a[4:7] offset:1024\n"
"s_waitcnt lgkmcnt(0)\n"
"s_barrier\n"
"ds_read_b64_tr_b16 a[0:1], v233 offset:0\n"
"ds_read_b64_tr_b16 a[2:3], v233 offset:1024\n"
"ds_read_b64_tr_b16 a[4:5], v233 offset:4096\n"
"ds_read_b64_tr_b16 a[6:7], v233 offset:5120\n"
"buffer_load_dwordx4 a[64:67], v230, s[28:31], s60 offen nt\n"
"s_add_u32 s60, s60, 36864\n"
"buffer_load_dwordx4 a[68:71], v230, s[28:31], s60 offen nt\n"
"s_add_u32 s60, s60, 258048\n"
"s_waitcnt vmcnt(14)\n"
"ds_write_b128 v231, a[8:11] offset:8192\n"
"ds_write_b128 v231, a[12:15] offset:9216\n"
"s_waitcnt lgkmcnt(0)\n"
"s_barrier\n"
"ds_read_b64_tr_b16 a[8:9], v233 offset:8192\n"
"ds_read_b64_tr_b16 a[10:11], v233 offset:9216\n"
"ds_read_b64_tr_b16 a[12:13], v233 offset:12288\n"
"ds_read_b64_tr_b16 a[14:15], v233 offset:13312\n"
"buffer_load_dwordx4 a[72:75], v230, s[28:31], s60 offen nt\n"
"s_add_u32 s60, s60, 36864\n"
"buffer_load_dwordx4 a[76:79], v230, s[28:31], s60 offen nt\n"
"s_add_u32 s60, s60, 258048\n"
"s_waitcnt vmcnt(14)\n"
"ds_write_b128 v231, a[16:19] offset:0\n"
"ds_write_b128 v231, a[20:23] offset:1024\n"
"s_waitcnt lgkmcnt(0)\n"
"s_barrier\n"
"ds_read_b64_tr_b16 a[16:17], v233 offset:0\n"
"ds_read_b64_tr_b16 a[18:19], v233 offset:1024\n"
"ds_read_b64_tr_b16 a[20:21], v233 offset:4096\n"
"ds_read_b64_tr_b16 a[22:23], v233 offset:5120\n"
"buffer_load_dwordx4 a[80:83], v230, s[28:31], s60 offen nt\n"
"s_add_u32 s60, s60, 36864\n"
"buffer_load_dwordx4 a[84:87], v230, s[28:31], s60 offen nt\n"
"s_add_u32 s60, s60, 258048\n"
"s_waitcnt vmcnt(14)\n"
"ds_write_b128 v231, a[24:27] offset:8192\n"
"ds_write_b128 v231, a[28:31] offset:9216\n"
"s_waitcnt lgkmcnt(0)\n"
"s_barrier\n"
"ds_read_b64_tr_b16 a[24:25], v233 offset:8192\n"
"ds_read_b64_tr_b16 a[26:27], v233 offset:9216\n"
"ds_read_b64_tr_b16 a[28:29], v233 offset:12288\n"
"ds_read_b64_tr_b16 a[30:31], v233 offset:13312\n"
"buffer_load_dwordx4 a[88:91], v230, s[28:31], s60 offen nt\n"
"s_add_u32 s60, s60, 36864\n"
"buffer_load_dwordx4 a[92:95], v230, s[28:31], s60 offen nt\n"
"s_add_u32 s60, s60, 258048\n"
"s_waitcnt vmcnt(14)\n"
"ds_write_b128 v231, a[32:35] offset:0\n"
"ds_write_b128 v231, a[36:39] offset:1024\n"
"s_waitcnt lgkmcnt(0)\n"
"s_barrier\n"
"ds_read_b64_tr_b16 a[32:33], v233 offset:0\n"
"ds_read_b64_tr_b16 a[34:35], v233 offset:1024\n"
"ds_read_b64_tr_b16 a[36:37], v233 offset:4096\n"
"ds_read_b64_tr_b16 a[38:39], v233 offset:5120\n"
"buffer_load_dwordx4 a[96:99], v230, s[28:31], s60 offen nt\n"
"s_add_u32 s60, s60, 36864\n"
"buffer_load_dwordx4 a[100:103], v230, s[28:31], s60 offen nt\n"
"s_add_u32 s60, s60, 258048\n"
"s_waitcnt vmcnt(14)\n"
"ds_write_b128 v231, a[40:43] offset:8192\n"
"ds_write_b128 v231, a[44:47] offset:9216\n"
"s_waitcnt lgkmcnt(0)\n"
"s_barrier\n"
"ds_read_b64_tr_b16 a[40:41], v233 offset:8192\n"
"ds_read_b64_tr_b16 a[42:43], v233 offset:9216\n"
"ds_read_b64_tr_b16 a[44:45], v233 offset:12288\n"
"ds_read_b64_tr_b16 a[46:47], v233 offset:13312\n"
"buffer_load_dwordx4 a[104:107], v230, s[28:31], s60 offen nt\n"
"s_add_u32 s60, s60, 36864\n"
"buffer_load_dwordx4 a[108:111], v230, s[28:31], s60 offen nt\n"
"s_add_u32 s60, s60, 258048\n"
"s_waitcnt vmcnt(14)\n"
"ds_write_b128 v231, a[48:51] offset:0\n"
"ds_write_b128 v231, a[52:55] offset:1024\n"
"s_waitcnt lgkmcnt(0)\n"
"s_barrier\n"
"ds_read_b64_tr_b16 a[48:49], v233 offset:0\n"
"ds_read_b64_tr_b16 a[50:51], v233 offset:1024\n"
"ds_read_b64_tr_b16 a[52:53], v233 offset:4096\n"
"ds_read_b64_tr_b16 a[54:55], v233 offset:5120\n"
"buffer_load_dwordx4 a[112:115], v230, s[28:31], s60 offen nt\n"
"s_add_u32 s60, s60, 36864\n"
"buffer_load_dwordx4 a[116:119], v230, s[28:31], s60 offen nt\n"
"s_add_u32 s60, s60, 258048\n"
"s_waitcnt vmcnt(14)\n"
"ds_write_b128 v231, a[56:59] offset:8192\n"
"ds_write_b128 v231, a[60:63] offset:9216\n"
"s_waitcnt lgkmcnt(0)\n"
"s_barrier\n"
"ds_read_b64_tr_b16 a[56:57], v233 offset:8192\n"
"ds_read_b64_tr_b16 a[58:59], v233 offset:9216\n"
"ds_read_b64_tr_b16 a[60:61], v233 offset:12288\n"
"ds_read_b64_tr_b16 a[62:63], v233 offset:13312\n"
"buffer_load_dwordx4 a[120:123], v230, s[28:31], s60 offen nt\n"
"s_add_u32 s60, s60, 36864\n"
"buffer_load_dwordx4 a[124:127], v230, s[28:31], s60 offen nt\n"
"s_add_u32 s60, s60, 258048\n"
"s_waitcnt vmcnt(14)\n"
"ds_write_b128 v231, a[64:67] offset:0\n"
"ds_write_b128 v231, a[68:71] offset:1024\n"
"s_waitcnt lgkmcnt(0)\n"
"s_barrier\n"
"ds_read_b64_tr_b16 a[64:65], v233 offset:0\n"
"ds_read_b64_tr_b16 a[66:67], v233 offset:1024\n"
"ds_read_b64_tr_b16 a[68:69], v233 offset:4096\n"
"ds_read_b64_tr_b16 a[70:71], v233 offset:5120\n"
"buffer_load_dwordx4 a[128:131], v230, s[28:31], s60 offen nt\n"
"s_add_u32 s60, s60, 36864\n"
"buffer_load_dwordx4 a[132:135], v230, s[28:31], s60 offen nt\n"
"s_add_u32 s60, s60, 258048\n"
"s_waitcnt vmcnt(14)\n"
"ds_write_b128 v231, a[72:75] offset:8192\n"
"ds_write_b128 v231, a[76:79] offset:9216\n"
"s_waitcnt lgkmcnt(0)\n"
"s_barrier\n"
"ds_read_b64_tr_b16 a[72:73], v233 offset:8192\n"
"ds_read_b64_tr_b16 a[74:75], v233 offset:9216\n"
"ds_read_b64_tr_b16 a[76:77], v233 offset:12288\n"
"ds_read_b64_tr_b16 a[78:79], v233 offset:13312\n"
"buffer_load_dwordx4 a[136:139], v230, s[28:31], s60 offen nt\n"
"s_add_u32 s60, s60, 36864\n"
"buffer_load_dwordx4 a[140:143], v230, s[28:31], s60 offen nt\n"
"s_add_u32 s60, s60, 258048\n"
"s_waitcnt vmcnt(14)\n"
"ds_write_b128 v231, a[80:83] offset:0\n"
"ds_write_b128 v231, a[84:87] offset:1024\n"
"s_waitcnt lgkmcnt(0)\n"
"s_barrier\n"
"ds_read_b64_tr_b16 a[80:81], v233 offset:0\n"
"ds_read_b64_tr_b16 a[82:83], v233 offset:1024\n"
"ds_read_b64_tr_b16 a[84:85], v233 offset:4096\n"
"ds_read_b64_tr_b16 a[86:87], v233 offset:5120\n"
"buffer_load_dwordx4 a[144:147], v230, s[32:35], s61 offen nt\n"
"s_add_u32 s61, s61, 36864\n"
"buffer_load_dwordx4 a[148:151], v230, s[32:35], s61 offen nt\n"
"s_add_u32 s61, s61, 258048\n"
"s_waitcnt vmcnt(14)\n"
"ds_write_b128 v231, a[88:91] offset:8192\n"
"ds_write_b128 v231, a[92:95] offset:9216\n"
"s_waitcnt lgkmcnt(0)\n"
"s_barrier\n"
"ds_read_b64_tr_b16 a[88:89], v233 offset:8192\n"
"ds_read_b64_tr_b16 a[90:91], v233 offset:9216\n"
"ds_read_b64_tr_b16 a[92:93], v233 offset:12288\n"
"ds_read_b64_tr_b16 a[94:95], v233 offset:13312\n"
"buffer_load_dwordx4 a[152:155], v230, s[32:35], s61 offen nt\n"
"s_add_u32 s61, s61, 36864\n"
"buffer_load_dwordx4 a[156:159], v230, s[32:35], s61 offen nt\n"
"s_add_u32 s61, s61, 258048\n"
"s_waitcnt vmcnt(14)\n"
"ds_write_b128 v231, a[96:99] offset:0\n"
"ds_write_b128 v231, a[100:103] offset:1024\n"
"s_waitcnt lgkmcnt(0)\n"
"s_barrier\n"
"ds_read_b64_tr_b16 a[96:97], v233 offset:0\n"
"ds_read_b64_tr_b16 a[98:99], v233 offset:1024\n"
"ds_read_b64_tr_b16 a[100:101], v233 offset:4096\n"
"ds_read_b64_tr_b16 a[102:103], v233 offset:5120\n"
"buffer_load_dwordx4 a[160:163], v230, s[32:35], s61 offen nt\n"
"s_add_u32 s61, s61, 36864\n"
"buffer_load_dwordx4 a[164:167], v230, s[32:35], s61 offen nt\n"
"s_add_u32 s61, s61, 258048\n"
"s_waitcnt vmcnt(14)\n"
"ds_write_b128 v231, a[104:107] offset:8192\n"
"ds_write_b128 v231, a[108:111] offset:9216\n"
"s_waitcnt lgkmcnt(0)\n"
"s_barrier\n"
"ds_read_b64_tr_b16 a[104:105], v233 offset:8192\n"
"ds_read_b64_tr_b16 a[106:107], v233 offset:9216\n"
"ds_read_b64_tr_b16 a[108:109], v233 offset:12288\n"
"ds_read_b64_tr_b16 a[110:111], v233 offset:13312\n"
"buffer_load_dwordx4 a[168:171], v230, s[32:35], s61 offen nt\n"
"s_add_u32 s61, s61, 36864\n"
"buffer_load_dwordx4 a[172:175], v230, s[32:35], s61 offen nt\n"
"s_add_u32 s61, s61, 258048\n"
"s_waitcnt vmcnt(14)\n"
"ds_write_b128 v231, a[112:115] offset:0\n"
"ds_write_b128 v231, a[116:119] offset:1024\n"
"s_waitcnt lgkmcnt(0)\n"
"s_barrier\n"
"ds_read_b64_tr_b16 a[112:113], v233 offset:0\n"
"ds_read_b64_tr_b16 a[114:115], v233 offset:1024\n"
"ds_read_b64_tr_b16 a[116:117], v233 offset:4096\n"
"ds_read_b64_tr_b16 a[118:119], v233 offset:5120\n"
"buffer_load_dwordx4 a[176:179], v230, s[32:35], s61 offen nt\n"
"s_add_u32 s61, s61, 36864\n"
"buffer_load_dwordx4 a[180:183], v230, s[32:35], s61 offen nt\n"
"s_add_u32 s61, s61, 258048\n"
"s_waitcnt vmcnt(14)\n"
"ds_write_b128 v231, a[120:123] offset:8192\n"
"ds_write_b128 v231, a[124:127] offset:9216\n"
"s_waitcnt lgkmcnt(0)\n"
"s_barrier\n"
"ds_read_b64_tr_b16 a[120:121], v233 offset:8192\n"
"ds_read_b64_tr_b16 a[122:123], v233 offset:9216\n"
"ds_read_b64_tr_b16 a[124:125], v233 offset:12288\n"
"ds_read_b64_tr_b16 a[126:127], v233 offset:13312\n"
"buffer_load_dwordx4 a[184:187], v230, s[32:35], s61 offen nt\n"
"s_add_u32 s61, s61, 36864\n"
"buffer_load_dwordx4 a[188:191], v230, s[32:35], s61 offen nt\n"
"s_add_u32 s61, s61, 258048\n"
"s_waitcnt vmcnt(14)\n"
"ds_write_b128 v231, a[128:131] offset:0\n"
"ds_write_b128 v231, a[132:135] offset:1024\n"
"s_waitcnt lgkmcnt(0)\n"
"s_barrier\n"
"ds_read_b64_tr_b16 a[128:129], v233 offset:0\n"
"ds_read_b64_tr_b16 a[130:131], v233 offset:1024\n"
"ds_read_b64_tr_b16 a[132:133], v233 offset:4096\n"
"ds_read_b64_tr_b16 a[134:135], v233 offset:5120\n"
"buffer_load_dwordx4 a[192:195], v230, s[32:35], s61 offen nt\n"
"s_add_u32 s61, s61, 36864\n"
"buffer_load_dwordx4 a[196:199], v230, s[32:35], s61 offen nt\n"
"s_add_u32 s61, s61, 258048\n"
"s_waitcnt vmcnt(14)\n"
"ds_write_b128 v231, a[136:139] offset:8192\n"
"ds_write_b128 v231, a[140:143] offset:9216\n"
"s_waitcnt lgkmcnt(0)\n"
"s_barrier\n"
"ds_read_b64_tr_b16 a[136:137], v233 offset:8192\n"
"ds_read_b64_tr_b16 a[138:139], v233 offset:9216\n"
"ds_read_b64_tr_b16 a[140:141], v233 offset:12288\n"
"ds_read_b64_tr_b16 a[142:143], v233 offset:13312\n"
"buffer_load_dwordx4 a[200:203], v230, s[32:35], s61 offen nt\n"
"s_add_u32 s61, s61, 36864\n"
"buffer_load_dwordx4 a[204:207], v230, s[32:35], s61 offen nt\n"
"s_add_u32 s61, s61, 258048\n"
"s_waitcnt vmcnt(14)\n"
"ds_write_b128 v231, a[144:147] offset:0\n"
"ds_write_b128 v231, a[148:151] offset:1024\n"
"s_waitcnt lgkmcnt(0)\n"
"s_barrier\n"
"ds_read_b64_tr_b16 a[144:145], v233 offset:0\n"
"ds_read_b64_tr_b16 a[146:147], v233 offset:1024\n"
"ds_read_b64_tr_b16 a[148:149], v233 offset:4096\n"
"ds_read_b64_tr_b16 a[150:151], v233 offset:5120\n"
"buffer_load_dwordx4 a[208:211], v230, s[32:35], s61 offen nt\n"
"s_add_u32 s61, s61, 36864\n"
"buffer_load_dwordx4 a[212:215], v230, s[32:35], s61 offen nt\n"
"s_add_u32 s61, s61, 258048\n"
"s_waitcnt vmcnt(14)\n"
"ds_write_b128 v231, a[152:155] offset:8192\n"
"ds_write_b128 v231, a[156:159] offset:9216\n"
"s_waitcnt lgkmcnt(0)\n"
"s_barrier\n"
"ds_read_b64_tr_b16 a[152:153], v233 offset:8192\n"
"ds_read_b64_tr_b16 a[154:155], v233 offset:9216\n"
"ds_read_b64_tr_b16 a[156:157], v233 offset:12288\n"
"ds_read_b64_tr_b16 a[158:159], v233 offset:13312\n"
"buffer_load_dwordx4 a[216:219], v230, s[32:35], s61 offen nt\n"
"s_add_u32 s61, s61, 36864\n"
"buffer_load_dwordx4 a[220:223], v230, s[32:35], s61 offen nt\n"
"s_add_u32 s61, s61, 258048\n"
"s_waitcnt vmcnt(14)\n"
"ds_write_b128 v231, a[160:163] offset:0\n"
"ds_write_b128 v231, a[164:167] offset:1024\n"
"s_waitcnt lgkmcnt(0)\n"
"s_barrier\n"
"ds_read_b64_tr_b16 a[160:161], v233 offset:0\n"
"ds_read_b64_tr_b16 a[162:163], v233 offset:1024\n"
"ds_read_b64_tr_b16 a[164:165], v233 offset:4096\n"
"ds_read_b64_tr_b16 a[166:167], v233 offset:5120\n"
"buffer_load_dwordx4 a[224:227], v230, s[32:35], s61 offen nt\n"
"s_add_u32 s61, s61, 36864\n"
"buffer_load_dwordx4 a[228:231], v230, s[32:35], s61 offen nt\n"
"s_add_u32 s61, s61, 258048\n"
"s_waitcnt vmcnt(14)\n"
"ds_write_b128 v231, a[168:171] offset:8192\n"
"ds_write_b128 v231, a[172:175] offset:9216\n"
"s_waitcnt lgkmcnt(0)\n"
"s_barrier\n"
"ds_read_b64_tr_b16 a[168:169], v233 offset:8192\n"
"ds_read_b64_tr_b16 a[170:171], v233 offset:9216\n"
"ds_read_b64_tr_b16 a[172:173], v233 offset:12288\n"
"ds_read_b64_tr_b16 a[174:175], v233 offset:13312\n"
"buffer_load_dwordx4 a[232:235], v230, s[32:35], s61 offen nt\n"
"s_add_u32 s61, s61, 36864\n"
"buffer_load_dwordx4 a[236:239], v230, s[32:35], s61 offen nt\n"
"s_add_u32 s61, s61, 258048\n"
"s_waitcnt vmcnt(14)\n"
"ds_write_b128 v231, a[176:179] offset:0\n"
"ds_write_b128 v231, a[180:183] offset:1024\n"
"s_waitcnt lgkmcnt(0)\n"
"s_barrier\n"
"ds_read_b64_tr_b16 a[176:177], v233 offset:0\n"
"ds_read_b64_tr_b16 a[178:179], v233 offset:1024\n"
"ds_read_b64_tr_b16 a[180:181], v233 offset:4096\n"
"ds_read_b64_tr_b16 a[182:183], v233 offset:5120\n"
"buffer_load_dwordx4 a[240:243], v230, s[32:35], s61 offen nt\n"
"s_add_u32 s61, s61, 36864\n"
"buffer_load_dwordx4 a[244:247], v230, s[32:35], s61 offen nt\n"
"s_add_u32 s61, s61, 258048\n"
"s_waitcnt vmcnt(14)\n"
"ds_write_b128 v231, a[184:187] offset:8192\n"
"ds_write_b128 v231, a[188:191] offset:9216\n"
"s_waitcnt lgkmcnt(0)\n"
"s_barrier\n"
"ds_read_b64_tr_b16 a[184:185], v233 offset:8192\n"
"ds_read_b64_tr_b16 a[186:187], v233 offset:9216\n"
"ds_read_b64_tr_b16 a[188:189], v233 offset:12288\n"
"ds_read_b64_tr_b16 a[190:191], v233 offset:13312\n"
"buffer_load_dwordx4 a[248:251], v230, s[32:35], s61 offen nt\n"
"s_add_u32 s61, s61, 36864\n"
"buffer_load_dwordx4 a[252:255], v230, s[32:35], s61 offen nt\n"
"s_add_u32 s61, s61, 258048\n"
"s_waitcnt vmcnt(14)\n"
"ds_write_b128 v231, a[192:195] offset:0\n"
"ds_write_b128 v231, a[196:199] offset:1024\n"
"s_waitcnt lgkmcnt(0)\n"
"s_barrier\n"
"ds_read_b64_tr_b16 a[192:193], v233 offset:0\n"
"ds_read_b64_tr_b16 a[194:195], v233 offset:1024\n"
"ds_read_b64_tr_b16 a[196:197], v233 offset:4096\n"
"ds_read_b64_tr_b16 a[198:199], v233 offset:5120\n"
"buffer_load_dwordx4 v[16:19], v230, s[32:35], s61 offen nt\n"
"s_add_u32 s61, s61, 36864\n"
"buffer_load_dwordx4 v[20:23], v230, s[32:35], s61 offen nt\n"
"s_add_u32 s61, s61, 258048\n"
"s_waitcnt vmcnt(14)\n"
"ds_write_b128 v231, a[200:203] offset:8192\n"
"ds_write_b128 v231, a[204:207] offset:9216\n"
"s_waitcnt lgkmcnt(0)\n"
"s_barrier\n"
"ds_read_b64_tr_b16 a[200:201], v233 offset:8192\n"
"ds_read_b64_tr_b16 a[202:203], v233 offset:9216\n"
"ds_read_b64_tr_b16 a[204:205], v233 offset:12288\n"
"ds_read_b64_tr_b16 a[206:207], v233 offset:13312\n"
"buffer_load_dwordx4 v[24:27], v230, s[32:35], s61 offen nt\n"
"s_add_u32 s61, s61, 36864\n"
"buffer_load_dwordx4 v[28:31], v230, s[32:35], s61 offen nt\n"
"s_add_u32 s61, s61, 258048\n"
"s_waitcnt vmcnt(14)\n"
"ds_write_b128 v231, a[208:211] offset:0\n"
"ds_write_b128 v231, a[212:215] offset:1024\n"
"s_waitcnt lgkmcnt(0)\n"
"s_barrier\n"
"ds_read_b64_tr_b16 a[208:209], v233 offset:0\n"
"ds_read_b64_tr_b16 a[210:211], v233 offset:1024\n"
"ds_read_b64_tr_b16 a[212:213], v233 offset:4096\n"
"ds_read_b64_tr_b16 a[214:215], v233 offset:5120\n"
"buffer_load_dwordx4 v[32:35], v230, s[32:35], s61 offen nt\n"
"s_add_u32 s61, s61, 36864\n"
"buffer_load_dwordx4 v[36:39], v230, s[32:35], s61 offen nt\n"
"s_add_u32 s61, s61, 258048\n"
"s_waitcnt vmcnt(14)\n"
"ds_write_b128 v231, a[216:219] offset:8192\n"
"ds_write_b128 v231, a[220:223] offset:9216\n"
"s_waitcnt lgkmcnt(0)\n"
"s_barrier\n"
"ds_read_b64_tr_b16 a[216:217], v233 offset:8192\n"
"ds_read_b64_tr_b16 a[218:219], v233 offset:9216\n"
"ds_read_b64_tr_b16 a[220:221], v233 offset:12288\n"
"ds_read_b64_tr_b16 a[222:223], v233 offset:13312\n"
"buffer_load_dwordx4 v[40:43], v230, s[32:35], s61 offen nt\n"
"s_add_u32 s61, s61, 36864\n"
"buffer_load_dwordx4 v[44:47], v230, s[32:35], s61 offen nt\n"
"s_add_u32 s61, s61, 258048\n"
"s_waitcnt vmcnt(14)\n"
"ds_write_b128 v231, a[224:227] offset:0\n"
"ds_write_b128 v231, a[228:231] offset:1024\n"
"s_waitcnt lgkmcnt(0)\n"
"s_barrier\n"
"ds_read_b64_tr_b16 a[224:225], v233 offset:0\n"
"ds_read_b64_tr_b16 a[226:227], v233 offset:1024\n"
"ds_read_b64_tr_b16 a[228:229], v233 offset:4096\n"
"ds_read_b64_tr_b16 a[230:231], v233 offset:5120\n"
"s_waitcnt vmcnt(12)\n"
"ds_write_b128 v231, a[232:235] offset:8192\n"
"ds_write_b128 v231, a[236:239] offset:9216\n"
"s_waitcnt lgkmcnt(0)\n"
"s_barrier\n"
"ds_read_b64_tr_b16 a[232:233], v233 offset:8192\n"
"ds_read_b64_tr_b16 a[234:235], v233 offset:9216\n"
"ds_read_b64_tr_b16 a[236:237], v233 offset:12288\n"
"ds_read_b64_tr_b16 a[238:239], v233 offset:13312\n"
"s_waitcnt vmcnt(10)\n"
"ds_write_b128 v231, a[240:243] offset:0\n"
"ds_write_b128 v231, a[244:247] offset:1024\n"
"s_waitcnt lgkmcnt(0)\n"
"s_barrier\n"
"ds_read_b64_tr_b16 a[240:241], v233 offset:0\n"
"ds_read_b64_tr_b16 a[242:243], v233 offset:1024\n"
"ds_read_b64_tr_b16 a[244:245], v233 offset:4096\n"
"ds_read_b64_tr_b16 a[246:247], v233 offset:5120\n"
"s_waitcnt vmcnt(8)\n"
"ds_write_b128 v231, a[248:251] offset:8192\n"
"ds_write_b128 v231, a[252:255] offset:9216\n"
"s_waitcnt lgkmcnt(0)\n"
"s_barrier\n"
"ds_read_b64_tr_b16 a[248:249], v233 offset:8192\n"
"ds_read_b64_tr_b16 a[250:251], v233 offset:9216\n"
"ds_read_b64_tr_b16 a[252:253], v233 offset:12288\n"
"ds_read_b64_tr_b16 a[254:255], v233 offset:13312\n"
"s_waitcnt vmcnt(6)\n"
"ds_write_b128 v231, v[16:19] offset:0\n"
"ds_write_b128 v231, v[20:23] offset:1024\n"
"s_waitcnt lgkmcnt(0)\n"
"s_barrier\n"
"ds_read_b64_tr_b16 v[16:17], v233 offset:0\n"
"ds_read_b64_tr_b16 v[18:19], v233 offset:1024\n"
"ds_read_b64_tr_b16 v[20:21], v233 offset:4096\n"
"ds_read_b64_tr_b16 v[22:23], v233 offset:5120\n"
"s_waitcnt vmcnt(4)\n"
"ds_write_b128 v231, v[24:27] offset:8192\n"
"ds_write_b128 v231, v[28:31] offset:9216\n"
"s_waitcnt lgkmcnt(0)\n"
"s_barrier\n"
"ds_read_b64_tr_b16 v[24:25], v233 offset:8192\n"
"ds_read_b64_tr_b16 v[26:27], v233 offset:9216\n"
"ds_read_b64_tr_b16 v[28:29], v233 offset:12288\n"
"ds_read_b64_tr_b16 v[30:31], v233 offset:13312\n"
"s_waitcnt vmcnt(2)\n"
"ds_write_b128 v231, v[32:35] offset:0\n"
"ds_write_b128 v231, v[36:39] offset:1024\n"
"s_waitcnt lgkmcnt(0)\n"
"s_barrier\n"
"ds_read_b64_tr_b16 v[32:33], v233 offset:0\n"
"ds_read_b64_tr_b16 v[34:35], v233 offset:1024\n"
"ds_read_b64_tr_b16 v[36:37], v233 offset:4096\n"
"ds_read_b64_tr_b16 v[38:39], v233 offset:5120\n"
"s_waitcnt vmcnt(0)\n"
"ds_write_b128 v231, v[40:43] offset:8192\n"
"ds_write_b128 v231, v[44:47] offset:9216\n"
"s_waitcnt lgkmcnt(0)\n"
"s_barrier\n"
"ds_read_b64_tr_b16 v[40:41], v233 offset:8192\n"
"ds_read_b64_tr_b16 v[42:43], v233 offset:9216\n"
"ds_read_b64_tr_b16 v[44:45], v233 offset:12288\n"
"ds_read_b64_tr_b16 v[46:47], v233 offset:13312\n"
"s_waitcnt lgkmcnt(0)\n"
"s_barrier\n"
"v_mov_b32 v144, 0x3f803f80\n"
"v_mov_b32 v145, 0x3f803f80\n"
"v_mov_b32 v146, 0x3f803f80\n"
"v_mov_b32 v147, 0x3f803f80\n"
"s_nop 1\n"
"v_mfma_f32_32x32x16_bf16 v[48:63], a[0:3], v[144:147], 0\n"
"v_mfma_f32_32x32x16_bf16 v[64:79], a[144:147], v[144:147], 0\n"
"v_mfma_f32_32x32x16_bf16 v[48:63], a[4:7], v[144:147], v[48:63]\n"
"v_mfma_f32_32x32x16_bf16 v[64:79], a[148:151], v[144:147], v[64:79]\n"
"v_mfma_f32_32x32x16_bf16 v[48:63], a[8:11], v[144:147], v[48:63]\n"
"v_mfma_f32_32x32x16_bf16 v[64:79], a[152:155], v[144:147], v[64:79]\n"
"v_mfma_f32_32x32x16_bf16 v[48:63], a[12:15], v[144:147], v[48:63]\n"
"v_mfma_f32_32x32x16_bf16 v[64:79], a[156:159], v[144:147], v[64:79]\n"
"v_mfma_f32_32x32x16_bf16 v[48:63], a[16:19], v[144:147], v[48:63]\n"
"v_mfma_f32_32x32x16_bf16 v[64:79], a[160:163], v[144:147], v[64:79]\n"
"v_mfma_f32_32x32x16_bf16 v[48:63], a[20:23], v[144:147], v[48:63]\n"
"v_mfma_f32_32x32x16_bf16 v[64:79], a[164:167], v[144:147], v[64:79]\n"
"v_mfma_f32_32x32x16_bf16 v[48:63], a[24:27], v[144:147], v[48:63]\n"
"v_mfma_f32_32x32x16_bf16 v[64:79], a[168:171], v[144:147], v[64:79]\n"
"v_mfma_f32_32x32x16_bf16 v[48:63], a[28:31], v[144:147], v[48:63]\n"
"v_mfma_f32_32x32x16_bf16 v[64:79], a[172:175], v[144:147], v[64:79]\n"
"v_mfma_f32_32x32x16_bf16 v[48:63], a[32:35], v[144:147], v[48:63]\n"
"v_mfma_f32_32x32x16_bf16 v[64:79], a[176:179], v[144:147], v[64:79]\n"
"v_mfma_f32_32x32x16_bf16 v[48:63], a[36:39], v[144:147], v[48:63]\n"
"v_mfma_f32_32x32x16_bf16 v[64:79], a[180:183], v[144:147], v[64:79]\n"
"v_mfma_f32_32x32x16_bf16 v[48:63], a[40:43], v[144:147], v[48:63]\n"
"v_mfma_f32_32x32x16_bf16 v[64:79], a[184:187], v[144:147], v[64:79]\n"
"v_mfma_f32_32x32x16_bf16 v[48:63], a[44:47], v[144:147], v[48:63]\n"
"v_mfma_f32_32x32x16_bf16 v[64:79], a[188:191], v[144:147], v[64:79]\n"
"v_mfma_f32_32x32x16_bf16 v[48:63], a[48:51], v[144:147], v[48:63]\n"
"v_mfma_f32_32x32x16_bf16 v[64:79], a[192:195], v[144:147], v[64:79]\n"
"v_mfma_f32_32x32x16_bf16 v[48:63], a[52:55], v[144:147], v[48:63]\n"
"v_mfma_f32_32x32x16_bf16 v[64:79], a[196:199], v[144:147], v[64:79]\n"
"v_mfma_f32_32x32x16_bf16 v[48:63], a[56:59], v[144:147], v[48:63]\n"
"v_mfma_f32_32x32x16_bf16 v[64:79], a[200:203], v[144:147], v[64:79]\n"
"v_mfma_f32_32x32x16_bf16 v[48:63], a[60:63], v[144:147], v[48:63]\n"
"v_mfma_f32_32x32x16_bf16 v[64:79], a[204:207], v[144:147], v[64:79]\n"
"v_mfma_f32_32x32x16_bf16 v[48:63], a[64:67], v[144:147], v[48:63]\n"
"v_mfma_f32_32x32x16_bf16 v[64:79], a[208:211], v[144:147], v[64:79]\n"
"v_mfma_f32_32x32x16_bf16 v[48:63], a[68:71], v[144:147], v[48:63]\n"
"v_mfma_f32_32x32x16_bf16 v[64:79], a[212:215], v[144:147], v[64:79]\n"
"v_mfma_f32_32x32x16_bf16 v[48:63], a[72:75], v[144:147], v[48:63]\n"
"v_mfma_f32_32x32x16_bf16 v[64:79], a[216:219], v[144:147], v[64:79]\n"
"v_mfma_f32_32x32x16_bf16 v[48:63], a[76:79], v[144:147], v[48:63]\n"
"v_mfma_f32_32x32x16_bf16 v[64:79], a[220:223], v[144:147], v[64:79]\n"
"v_mfma_f32_32x32x16_bf16 v[48:63], a[80:83], v[144:147], v[48:63]\n"
"v_mfma_f32_32x32x16_bf16 v[64:79], a[224:227], v[144:147], v[64:79]\n"
"v_mfma_f32_32x32x16_bf16 v[48:63], a[84:87], v[144:147], v[48:63]\n"
"v_mfma_f32_32x32x16_bf16 v[64:79], a[228:231], v[144:147], v[64:79]\n"
"v_mfma_f32_32x32x16_bf16 v[48:63], a[88:91], v[144:147], v[48:63]\n"
"v_mfma_f32_32x32x16_bf16 v[64:79], a[232:235], v[144:147], v[64:79]\n"
"v_mfma_f32_32x32x16_bf16 v[48:63], a[92:95], v[144:147], v[48:63]\n"
"v_mfma_f32_32x32x16_bf16 v[64:79], a[236:239], v[144:147], v[64:79]\n"
"v_mfma_f32_32x32x16_bf16 v[48:63], a[96:99], v[144:147], v[48:63]\n"
"v_mfma_f32_32x32x16_bf16 v[64:79], a[240:243], v[144:147], v[64:79]\n"
"v_mfma_f32_32x32x16_bf16 v[48:63], a[100:103], v[144:147], v[48:63]\n"
"v_mfma_f32_32x32x16_bf16 v[64:79], a[244:247], v[144:147], v[64:79]\n"
"v_mfma_f32_32x32x16_bf16 v[48:63], a[104:107], v[144:147], v[48:63]\n"
"v_mfma_f32_32x32x16_bf16 v[64:79], a[248:251], v[144:147], v[64:79]\n"
"v_mfma_f32_32x32x16_bf16 v[48:63], a[108:111], v[144:147], v[48:63]\n"
"v_mfma_f32_32x32x16_bf16 v[64:79], a[252:255], v[144:147], v[64:79]\n"
"v_mfma_f32_32x32x16_bf16 v[48:63], a[112:115], v[144:147], v[48:63]\n"
"v_mfma_f32_32x32x16_bf16 v[64:79], v[16:19], v[144:147], v[64:79]\n"
"v_mfma_f32_32x32x16_bf16 v[48:63], a[116:119], v[144:147], v[48:63]\n"
"v_mfma_f32_32x32x16_bf16 v[64:79], v[20:23], v[144:147], v[64:79]\n"
"v_mfma_f32_32x32x16_bf16 v[48:63], a[120:123], v[144:147], v[48:63]\n"
"v_mfma_f32_32x32x16_bf16 v[64:79], v[24:27], v[144:147], v[64:79]\n"
"v_mfma_f32_32x32x16_bf16 v[48:63], a[124:127], v[144:147], v[48:63]\n"
"v_mfma_f32_32x32x16_bf16 v[64:79], v[28:31], v[144:147], v[64:79]\n"
"v_mfma_f32_32x32x16_bf16 v[48:63], a[128:131], v[144:147], v[48:63]\n"
"v_mfma_f32_32x32x16_bf16 v[64:79], v[32:35], v[144:147], v[64:79]\n"
"v_mfma_f32_32x32x16_bf16 v[48:63], a[132:135], v[144:147], v[48:63]\n"
"v_mfma_f32_32x32x16_bf16 v[64:79], v[36:39], v[144:147], v[64:79]\n"
"v_mfma_f32_32x32x16_bf16 v[48:63], a[136:139], v[144:147], v[48:63]\n"
"v_mfma_f32_32x32x16_bf16 v[64:79], v[40:43], v[144:147], v[64:79]\n"
"v_mfma_f32_32x32x16_bf16 v[48:63], a[140:143], v[144:147], v[48:63]\n"
"v_mfma_f32_32x32x16_bf16 v[64:79], v[44:47], v[144:147], v[64:79]\n"
"v_mov_b32 v248, 98240\n"
"v_mov_b32 v249, 0\n"
"v_mov_b32 v250, 1\n"
"ds_write2_b32 v248, v249, v249 offset0:0 offset1:1\n"
"s_waitcnt lgkmcnt(0)\n"
"s_barrier\n"
"v_mov_b32 v15, 98256\n"
"v_mov_b32 v251, 0\n"
"v_mov_b32 v10, -1\n"
"ds_write_b32 v15, v251\n"
"s_mov_b32 s70, 0\n"
"s_waitcnt lgkmcnt(0)\n"
"s_barrier\n"
"s_xor_b32 s71, s22, 1286144\n"
"s_cmp_eq_u32 s20, 3\n"
"s_cselect_b32 s71, s71, 2146435072\n"
"s_mov_b32 s21, -1\n"
"s_mov_b32 s68, 0\n"
"s_lshl_b32 s36, s19, 2\n"
"s_add_u32 s36, s36, s20\n"
"s_mul_i32 s37, s36, 57\n"
"s_lshr_b32 s37, s37, 11\n"
"s_lshl_b32 s37, s37, 2\n"
"s_add_u32 s36, s36, s37\n"
"s_lshr_b32 s37, s36, 3\n"
"s_lshl_b32 s37, s37, 14\n"
"s_and_b32 s36, s36, 7\n"
"s_lshl_b32 s36, s36, 10\n"
"s_add_u32 s66, s36, s37\n"
"s_branch LP3_next1\n"
"LP3_00:\n"
"ds_read_b128 v[144:147], v7 offset:0\n"
"ds_read_b128 v[148:151], v7 offset:512\n"
"ds_read_b128 v[152:155], v7 offset:1024\n"
"ds_read_b128 v[156:159], v7 offset:1536\n"
"s_waitcnt lgkmcnt(3)\n"
"v_mfma_f32_32x32x16_bf16 v[80:95], a[0:3], v[144:147], 0\n"
"ds_read_b128 v[160:163], v7 offset:2048\n"
"s_add_u32 s59, s21, 1\n"
"s_lshl_b32 s62, s59, 17\n"
"s_add_u32 s62, s62, 157286400\n"
"s_lshr_b32 s43, s62, 8\n"
"v_mfma_f32_32x32x16_bf16 v[96:111], a[144:147], v[144:147], 0\n"
"s_xor_b32 s43, s43, s22\n"
"s_not_b32 s43, s43\n"
"s_and_b32 s43, s43, 8192\n"
"s_or_b32 s62, s62, s43\n"
"s_waitcnt lgkmcnt(3)\n"
"v_mfma_f32_32x32x16_bf16 v[80:95], a[4:7], v[148:151], v[80:95]\n"
"ds_read_b128 v[164:167], v7 offset:2560\n"
"s_add_u32 s63, s62, 81920\n"
"s_mul_i32 s42, s21, 86\n"
"s_lshr_b32 s42, s42, 8\n"
"s_mul_i32 s56, s21, 655360\n"
"v_mfma_f32_32x32x16_bf16 v[96:111], a[148:151], v[148:151], v[96:111]\n"
"s_mul_i32 s42, s42, 131072\n"
"s_add_u32 s56, s56, s42\n"
"s_add_u32 s56, s56, 102760448\n"
"s_add_u32 s56, s56, s66\n"
"s_waitcnt lgkmcnt(3)\n"
"v_mfma_f32_32x32x16_bf16 v[80:95], a[8:11], v[152:155], v[80:95]\n"
"ds_read_b128 v[144:147], v7 offset:3072\n"
"s_lshr_b32 s43, s56, 8\n"
"s_xor_b32 s43, s43, s22\n"
"s_not_b32 s43, s43\n"
"s_and_b32 s43, s43, 8192\n"
"v_mfma_f32_32x32x16_bf16 v[96:111], a[152:155], v[152:155], v[96:111]\n"
"s_or_b32 s56, s56, s43\n"
"s_waitcnt lgkmcnt(3)\n"
"s_waitcnt vmcnt(2)\n"
"s_cmp_lg_u32 s68, 0\n"
"s_cbranch_scc1 Lpe_115\n"
"s_waitcnt vmcnt(0)\n"
"Lpe_115:\n"
"v_mfma_f32_32x32x16_bf16 v[80:95], a[12:15], v[156:159], v[80:95]\n"
"ds_read_b128 v[148:151], v7 offset:3584\n"
"v_mfma_f32_32x32x16_bf16 v[96:111], a[156:159], v[156:159], v[96:111]\n"
"ds_write_b128 v9, v[184:187] offset:8192\n"
"s_waitcnt lgkmcnt(4)\n"
"v_mfma_f32_32x32x16_bf16 v[80:95], a[16:19], v[160:163], v[80:95]\n"
"ds_read_b128 v[152:155], v7 offset:4096\n"
"buffer_load_dword v10, v4, s[24:27], s71 offen sc1\n"
"v_mfma_f32_32x32x16_bf16 v[96:111], a[160:163], v[160:163], v[96:111]\n"
"ds_write_b128 v9, v[188:191] offset:10240\n"
"s_waitcnt lgkmcnt(5)\n"
"v_mfma_f32_32x32x16_bf16 v[80:95], a[20:23], v[164:167], v[80:95]\n"
"ds_read_b128 v[156:159], v7 offset:4608\n"
"v_mfma_f32_32x32x16_bf16 v[96:111], a[164:167], v[164:167], v[96:111]\n"
"ds_write_b128 v9, v[192:195] offset:12288\n"
"s_waitcnt lgkmcnt(6)\n"
"s_cmp_eq_u32 s68, 0\n"
"s_cbranch_scc1 Lpe_116\n"
"s_waitcnt vmcnt(1)\n"
"s_add_u32 s65, s21, -1\n"
"s_and_b32 s41, s65, 1\n"
"s_lshl_b32 s41, s41, 2\n"
"s_add_u32 s41, s41, 98240\n"
"v_mov_b32 v248, s41\n"
"v_mov_b32 v250, 1\n"
"s_mov_b64 exec, 1\n"
"ds_add_rtn_u32 v249, v248, v250\n"
"s_mov_b64 exec, -1\n"
"Lpe_116:\n"
"v_mfma_f32_32x32x16_bf16 v[80:95], a[24:27], v[144:147], v[80:95]\n"
"ds_read_b128 v[160:163], v7 offset:5120\n"
"v_mfma_f32_32x32x16_bf16 v[96:111], a[168:171], v[144:147], v[96:111]\n"
"ds_write_b128 v9, v[196:199] offset:14336\n"
"s_waitcnt lgkmcnt(7)\n"
"v_mfma_f32_32x32x16_bf16 v[80:95], a[28:31], v[148:151], v[80:95]\n"
"ds_read_b128 v[164:167], v7 offset:5632\n"
"v_mfma_f32_32x32x16_bf16 v[96:111], a[172:175], v[148:151], v[96:111]\n"
"ds_write_b128 v9, v[200:203] offset:16384\n"
"s_waitcnt lgkmcnt(7)\n"
"s_cmp_eq_u32 s68, 0\n"
"s_cbranch_scc1 Lpe_117\n"
"s_mov_b32 s68, 0\n"
"s_waitcnt lgkmcnt(3)\n"
"v_readfirstlane_b32 s41, v249\n"
"s_and_b32 s41, s41, 3\n"
"s_cmp_lg_u32 s41, 3\n"
"s_cbranch_scc1 Lar_118\n"
"s_lshl_b32 s43, s19, 7\n"
"s_xor_b32 s43, s43, s22\n"
"s_xor_b32 s43, s43, 1302528\n"
"s_add_u32 s42, s65, 1\n"
"v_mov_b32 v3, s42\n"
"buffer_store_dword v3, v2, s[24:27], s43 offen sc1\n"
"Lpf_119:\n"
"Lar_118:\n"
"Lpe_117:\n"
"v_mfma_f32_32x32x16_bf16 v[80:95], a[32:35], v[152:155], v[80:95]\n"
"ds_read_b128 v[144:147], v7 offset:6144\n"
"v_mfma_f32_32x32x16_bf16 v[96:111], a[176:179], v[152:155], v[96:111]\n"
"s_waitcnt lgkmcnt(6)\n"
"v_mfma_f32_32x32x16_bf16 v[80:95], a[36:39], v[156:159], v[80:95]\n"
"ds_read_b128 v[148:151], v7 offset:6656\n"
"v_mfma_f32_32x32x16_bf16 v[96:111], a[180:183], v[156:159], v[96:111]\n"
"s_waitcnt lgkmcnt(5)\n"
"v_mfma_f32_32x32x16_bf16 v[80:95], a[40:43], v[160:163], v[80:95]\n"
"ds_read_b128 v[152:155], v7 offset:7168\n"
"v_mfma_f32_32x32x16_bf16 v[96:111], a[184:187], v[160:163], v[96:111]\n"
"s_waitcnt lgkmcnt(4)\n"
"s_waitcnt lgkmcnt(4)\n"
"s_barrier\n"
"v_mfma_f32_32x32x16_bf16 v[80:95], a[44:47], v[164:167], v[80:95]\n"
"ds_read_b128 v[156:159], v7 offset:7680\n"
"v_mfma_f32_32x32x16_bf16 v[96:111], a[188:191], v[164:167], v[96:111]\n"
"s_waitcnt lgkmcnt(3)\n"
"v_mfma_f32_32x32x16_bf16 v[80:95], a[48:51], v[144:147], v[80:95]\n"
"ds_read_b128 v[160:163], v7 offset:8192\n"
"v_mfma_f32_32x32x16_bf16 v[96:111], a[192:195], v[144:147], v[96:111]\n"
"s_waitcnt lgkmcnt(3)\n"
"v_mfma_f32_32x32x16_bf16 v[80:95], a[52:55], v[148:151], v[80:95]\n"
"ds_read_b128 v[164:167], v7 offset:8704\n"
"v_mfma_f32_32x32x16_bf16 v[96:111], a[196:199], v[148:151], v[96:111]\n"
"s_waitcnt lgkmcnt(3)\n"
"s_cmp_lg_u32 s20, 3\n"
"s_cbranch_scc1 Lpb_120\n"
"s_waitcnt vmcnt(0)\n"
"s_cmp_lg_u32 s20, 3\n"
"s_cbranch_scc1 Lav_121\n"
"v_cmp_ge_i32 vcc, s59, v10\n"
"s_cbranch_vccnz Lav_121\n"
"s_add_u32 s41, s59, 1\n"
"v_cmp_ge_i32 vcc, s41, v10\n"
"s_cbranch_vccnz Lav_122\n"
"s_add_u32 s41, s41, 1\n"
"Lav_122:\n"
"s_max_i32 s70, s70, s41\n"
"v_mov_b32 v251, s70\n"
"ds_write_b32 v15, v251\n"
"Lav_121:\n"
"Lpb_120:\n"
"v_mfma_f32_32x32x16_bf16 v[80:95], a[56:59], v[152:155], v[80:95]\n"
"ds_read_b128 v[144:147], v7 offset:9216\n"
"v_mfma_f32_32x32x16_bf16 v[96:111], a[200:203], v[152:155], v[96:111]\n"
"s_waitcnt lgkmcnt(3)\n"
"v_mfma_f32_32x32x16_bf16 v[80:95], a[60:63], v[156:159], v[80:95]\n"
"ds_read_b128 v[148:151], v7 offset:9728\n"
"ds_read_b32 v251, v15\n"
"v_mfma_f32_32x32x16_bf16 v[96:111], a[204:207], v[156:159], v[96:111]\n"
"s_waitcnt lgkmcnt(0)\n"
"v_readfirstlane_b32 s70, v251\n"
"s_add_u32 s36, s21, 2\n"
"s_cmp_gt_i32 s70, s36\n"
"s_cbranch_scc0 LP3_n00\n"
"s_waitcnt lgkmcnt(4)\n"
"v_mfma_f32_32x32x16_bf16 v[80:95], a[64:67], v[160:163], v[80:95]\n"
"ds_read_b128 v[152:155], v7 offset:10240\n"
"v_mfma_f32_32x32x16_bf16 v[96:111], a[208:211], v[160:163], v[96:111]\n"
"buffer_load_dwordx4 v[168:171], v5, s[24:27], s62 offen sc1\n"
"s_add_u32 s62, s62, 4096\n"
"s_waitcnt lgkmcnt(4)\n"
"v_mfma_f32_32x32x16_bf16 v[80:95], a[68:71], v[164:167], v[80:95]\n"
"ds_read_b128 v[156:159], v7 offset:10752\n"
"v_mfma_f32_32x32x16_bf16 v[96:111], a[212:215], v[164:167], v[96:111]\n"
"buffer_load_dwordx4 v[172:175], v5, s[24:27], s62 offen sc1\n"
"s_add_u32 s62, s62, 12288\n"
"s_waitcnt lgkmcnt(4)\n"
"v_mfma_f32_32x32x16_bf16 v[80:95], a[72:75], v[144:147], v[80:95]\n"
"ds_read_b128 v[160:163], v7 offset:11264\n"
"v_mfma_f32_32x32x16_bf16 v[96:111], a[216:219], v[144:147], v[96:111]\n"
"buffer_load_dwordx4 v[176:179], v5, s[24:27], s62 offen sc1\n"
"s_add_u32 s62, s62, 4096\n"
"s_waitcnt lgkmcnt(4)\n"
"v_mfma_f32_32x32x16_bf16 v[80:95], a[76:79], v[148:151], v[80:95]\n"
"ds_read_b128 v[164:167], v7 offset:11776\n"
"v_mfma_f32_32x32x16_bf16 v[96:111], a[220:223], v[148:151], v[96:111]\n"
"buffer_load_dwordx4 v[180:183], v5, s[24:27], s62 offen sc1\n"
"s_add_u32 s62, s62, 12288\n"
"s_waitcnt lgkmcnt(3)\n"
"v_mfma_f32_32x32x16_bf16 v[80:95], a[80:83], v[152:155], v[80:95]\n"
"ds_read_b128 v[144:147], v7 offset:12288\n"
"v_mfma_f32_32x32x16_bf16 v[96:111], a[224:227], v[152:155], v[96:111]\n"
"buffer_load_dwordx4 v[204:207], v11, s[24:27], s63 offen sc1\n"
"s_waitcnt lgkmcnt(3)\n"
"v_mfma_f32_32x32x16_bf16 v[80:95], a[84:87], v[156:159], v[80:95]\n"
"ds_read_b128 v[148:151], v7 offset:12800\n"
"v_mfma_f32_32x32x16_bf16 v[96:111], a[228:231], v[156:159], v[96:111]\n"
"buffer_load_dwordx4 v[208:211], v12, s[24:27], s63 offen sc1\n"
"s_waitcnt lgkmcnt(3)\n"
"v_mfma_f32_32x32x16_bf16 v[80:95], a[88:91], v[160:163], v[80:95]\n"
"ds_read_b128 v[152:155], v7 offset:13312\n"
"v_mfma_f32_32x32x16_bf16 v[96:111], a[232:235], v[160:163], v[96:111]\n"
"buffer_load_dwordx4 v[184:187], v5, s[24:27], s62 offen sc1\n"
"s_add_u32 s62, s62, 4096\n"
"s_waitcnt lgkmcnt(3)\n"
"v_mfma_f32_32x32x16_bf16 v[80:95], a[92:95], v[164:167], v[80:95]\n"
"ds_read_b128 v[156:159], v7 offset:13824\n"
"v_mfma_f32_32x32x16_bf16 v[96:111], a[236:239], v[164:167], v[96:111]\n"
"buffer_load_dwordx4 v[188:191], v5, s[24:27], s62 offen sc1\n"
"s_add_u32 s62, s62, 12288\n"
"s_waitcnt lgkmcnt(3)\n"
"v_mfma_f32_32x32x16_bf16 v[80:95], a[96:99], v[144:147], v[80:95]\n"
"ds_read_b128 v[160:163], v7 offset:14336\n"
"ds_read_b64 v[212:213], v6 offset:0\n"
"ds_read_b64 v[214:215], v6 offset:128\n"
"ds_read_b64 v[216:217], v6 offset:256\n"
"v_mfma_f32_32x32x16_bf16 v[96:111], a[240:243], v[144:147], v[96:111]\n"
"buffer_load_dwordx4 v[192:195], v5, s[24:27], s62 offen sc1\n"
"s_add_u32 s62, s62, 4096\n"
"s_waitcnt lgkmcnt(6)\n"
"v_mfma_f32_32x32x16_bf16 v[80:95], a[100:103], v[148:151], v[80:95]\n"
"ds_read_b128 v[164:167], v7 offset:14848\n"
"ds_read_b64 v[218:219], v6 offset:384\n"
"ds_read_b64 v[220:221], v6 offset:512\n"
"ds_read_b64 v[222:223], v6 offset:640\n"
"v_mfma_f32_32x32x16_bf16 v[96:111], a[244:247], v[148:151], v[96:111]\n"
"buffer_load_dwordx4 v[196:199], v5, s[24:27], s62 offen sc1\n"
"s_add_u32 s62, s62, 12288\n"
"s_waitcnt lgkmcnt(9)\n"
"v_mfma_f32_32x32x16_bf16 v[80:95], a[104:107], v[152:155], v[80:95]\n"
"ds_read_b128 v[144:147], v7 offset:15360\n"
"ds_read_b64 v[224:225], v6 offset:768\n"
"ds_read_b64 v[226:227], v6 offset:896\n"
"ds_read_b64 v[228:229], v6 offset:1024\n"
"v_mfma_f32_32x32x16_bf16 v[96:111], a[248:251], v[152:155], v[96:111]\n"
"buffer_load_dwordx4 v[200:203], v5, s[24:27], s62 offen sc1\n"
"s_add_u32 s62, s62, 4096\n"
"s_waitcnt lgkmcnt(12)\n"
"v_mfma_f32_32x32x16_bf16 v[80:95], a[108:111], v[156:159], v[80:95]\n"
"ds_read_b128 v[148:151], v7 offset:15872\n"
"v_mfma_f32_32x32x16_bf16 v[96:111], a[252:255], v[156:159], v[96:111]\n"
"s_waitcnt lgkmcnt(12)\n"
"s_waitcnt lgkmcnt(2)\n"
"v_mfma_f32_32x32x16_bf16 v[80:95], a[112:115], v[160:163], v[80:95]\n"
"ds_read_b128 v[152:155], v7 offset:16384\n"
"v_add_f32 v212, v212, v214\n"
"v_add_f32 v213, v213, v215\n"
"v_add_f32 v212, v212, v216\n"
"v_add_f32 v213, v213, v217\n"
"v_mfma_f32_32x32x16_bf16 v[96:111], v[16:19], v[160:163], v[96:111]\n"
"v_add_f32 v212, v212, v218\n"
"v_add_f32 v213, v213, v219\n"
"v_add_f32 v212, v212, v220\n"
"v_add_f32 v213, v213, v221\n"
"s_waitcnt lgkmcnt(9)\n"
"v_mfma_f32_32x32x16_bf16 v[80:95], a[116:119], v[164:167], v[80:95]\n"
"ds_read_b128 v[156:159], v7 offset:16896\n"
"v_add_f32 v212, v212, v222\n"
"v_add_f32 v213, v213, v223\n"
"v_add_f32 v212, v212, v224\n"
"v_add_f32 v213, v213, v225\n"
"v_mfma_f32_32x32x16_bf16 v[96:111], v[20:23], v[164:167], v[96:111]\n"
"v_add_f32 v212, v212, v226\n"
"v_add_f32 v213, v213, v227\n"
"v_add_f32 v212, v212, v228\n"
"v_add_f32 v213, v213, v229\n"
"s_waitcnt lgkmcnt(6)\n"
"s_waitcnt vmcnt(5)\n"
"v_mfma_f32_32x32x16_bf16 v[80:95], a[120:123], v[144:147], v[80:95]\n"
"ds_read_b128 v[160:163], v7 offset:17408\n"
"v_mov_b32 v214, v212\n"
"v_mov_b32 v215, v213\n"
"s_nop 0\n"
"v_permlane32_swap_b32 v212, v214\n"
"v_mfma_f32_32x32x16_bf16 v[96:111], v[24:27], v[144:147], v[96:111]\n"
"ds_write_b128 v8, v[168:171] offset:0\n"
"v_permlane32_swap_b32 v213, v215\n"
"v_add_f32 v212, v212, v214\n"
"v_add_f32 v213, v213, v215\n"
"s_waitcnt lgkmcnt(4)\n"
"v_mfma_f32_32x32x16_bf16 v[80:95], a[124:127], v[148:151], v[80:95]\n"
"ds_read_b128 v[164:167], v7 offset:17920\n"
"v_mul_f32 v242, 0x3ae38e39, v212\n"
"v_mul_f32 v213, 0x3ae38e39, v213\n"
"v_fma_f32 v213, -v242, v242, v213\n"
"v_max_f32 v213, 0, v213\n"
"v_mfma_f32_32x32x16_bf16 v[96:111], v[28:31], v[148:151], v[96:111]\n"
"ds_write_b128 v8, v[172:175] offset:2048\n"
"v_add_f32 v244, 0x3727c5ac, v213\n"
"v_rsq_f32 v243, v244\n"
"s_nop 0\n"
"s_waitcnt lgkmcnt(5)\n"
"v_mfma_f32_32x32x16_bf16 v[80:95], a[128:131], v[152:155], v[80:95]\n"
"v_mul_f32 v243, s57, v243\n"
"v_mfma_f32_32x32x16_bf16 v[96:111], v[32:35], v[152:155], v[96:111]\n"
"ds_write_b128 v8, v[176:179] offset:4096\n"
"s_waitcnt lgkmcnt(5)\n"
"v_mfma_f32_32x32x16_bf16 v[80:95], a[132:135], v[156:159], v[80:95]\n"
"v_mfma_f32_32x32x16_bf16 v[96:111], v[36:39], v[156:159], v[96:111]\n"
"ds_write_b128 v8, v[180:183] offset:6144\n"
"s_waitcnt lgkmcnt(5)\n"
"v_mfma_f32_32x32x16_bf16 v[80:95], a[136:139], v[160:163], v[80:95]\n"
"v_mfma_f32_32x32x16_bf16 v[96:111], v[40:43], v[160:163], v[96:111]\n"
"ds_write_b128 v13, v[204:207]\n"
"s_waitcnt lgkmcnt(4)\n"
"v_mfma_f32_32x32x16_bf16 v[80:95], a[140:143], v[164:167], v[80:95]\n"
"v_mfma_f32_32x32x16_bf16 v[96:111], v[44:47], v[164:167], v[96:111]\n"
"s_mov_b64 exec, 0xff\n"
"ds_write_b128 v13, v[208:211] offset:1024\n"
"s_mov_b64 exec, -1\n"
"v_mov_b32 v245, v242\n"
"v_mov_b32 v246, v243\n"
"v_mov_b32 v247, v244\n"
"v_mov_b32 v9, v8\n"
"v_xor_b32 v6, 8192, v6\n"
"v_xor_b32 v13, 8192, v13\n"
"v_add_u32 v7, 36864, v7\n"
"v_add_u32 v8, -36864, v8\n"
"s_add_u32 s21, s21, 1\n"
"s_waitcnt lgkmcnt(0)\n"
"s_barrier\n"
"s_branch LP3_11\n"
"LP3_n00:\n"
"s_waitcnt lgkmcnt(4)\n"
"v_mfma_f32_32x32x16_bf16 v[80:95], a[64:67], v[160:163], v[80:95]\n"
"ds_read_b128 v[152:155], v7 offset:10240\n"
"v_mfma_f32_32x32x16_bf16 v[96:111], a[208:211], v[160:163], v[96:111]\n"
"s_waitcnt lgkmcnt(4)\n"
"v_mfma_f32_32x32x16_bf16 v[80:95], a[68:71], v[164:167], v[80:95]\n"
"ds_read_b128 v[156:159], v7 offset:10752\n"
"v_mfma_f32_32x32x16_bf16 v[96:111], a[212:215], v[164:167], v[96:111]\n"
"s_waitcnt lgkmcnt(4)\n"
"v_mfma_f32_32x32x16_bf16 v[80:95], a[72:75], v[144:147], v[80:95]\n"
"ds_read_b128 v[160:163], v7 offset:11264\n"
"v_mfma_f32_32x32x16_bf16 v[96:111], a[216:219], v[144:147], v[96:111]\n"
"s_waitcnt lgkmcnt(4)\n"
"v_mfma_f32_32x32x16_bf16 v[80:95], a[76:79], v[148:151], v[80:95]\n"
"ds_read_b128 v[164:167], v7 offset:11776\n"
"v_mfma_f32_32x32x16_bf16 v[96:111], a[220:223], v[148:151], v[96:111]\n"
"s_waitcnt lgkmcnt(3)\n"
"v_mfma_f32_32x32x16_bf16 v[80:95], a[80:83], v[152:155], v[80:95]\n"
"ds_read_b128 v[144:147], v7 offset:12288\n"
"v_mfma_f32_32x32x16_bf16 v[96:111], a[224:227], v[152:155], v[96:111]\n"
"buffer_load_dword v10, v4, s[24:27], s71 offen sc1\n"
"s_waitcnt lgkmcnt(3)\n"
"v_mfma_f32_32x32x16_bf16 v[80:95], a[84:87], v[156:159], v[80:95]\n"
"ds_read_b128 v[148:151], v7 offset:12800\n"
"v_mfma_f32_32x32x16_bf16 v[96:111], a[228:231], v[156:159], v[96:111]\n"
"s_waitcnt lgkmcnt(3)\n"
"v_mfma_f32_32x32x16_bf16 v[80:95], a[88:91], v[160:163], v[80:95]\n"
"ds_read_b128 v[152:155], v7 offset:13312\n"
"v_mfma_f32_32x32x16_bf16 v[96:111], a[232:235], v[160:163], v[96:111]\n"
"s_waitcnt lgkmcnt(3)\n"
"v_mfma_f32_32x32x16_bf16 v[80:95], a[92:95], v[164:167], v[80:95]\n"
"ds_read_b128 v[156:159], v7 offset:13824\n"
"v_mfma_f32_32x32x16_bf16 v[96:111], a[236:239], v[164:167], v[96:111]\n"
"s_waitcnt lgkmcnt(3)\n"
"v_mfma_f32_32x32x16_bf16 v[80:95], a[96:99], v[144:147], v[80:95]\n"
"ds_read_b128 v[160:163], v7 offset:14336\n"
"ds_read_b64 v[212:213], v6 offset:0\n"
"ds_read_b64 v[214:215], v6 offset:128\n"
"ds_read_b64 v[216:217], v6 offset:256\n"
"v_mfma_f32_32x32x16_bf16 v[96:111], a[240:243], v[144:147], v[96:111]\n"
"s_waitcnt lgkmcnt(6)\n"
"v_mfma_f32_32x32x16_bf16 v[80:95], a[100:103], v[148:151], v[80:95]\n"
"ds_read_b128 v[164:167], v7 offset:14848\n"
"ds_read_b64 v[218:219], v6 offset:384\n"
"ds_read_b64 v[220:221], v6 offset:512\n"
"ds_read_b64 v[222:223], v6 offset:640\n"
"v_mfma_f32_32x32x16_bf16 v[96:111], a[244:247], v[148:151], v[96:111]\n"
"s_waitcnt lgkmcnt(9)\n"
"v_mfma_f32_32x32x16_bf16 v[80:95], a[104:107], v[152:155], v[80:95]\n"
"ds_read_b128 v[144:147], v7 offset:15360\n"
"ds_read_b64 v[224:225], v6 offset:768\n"
"ds_read_b64 v[226:227], v6 offset:896\n"
"ds_read_b64 v[228:229], v6 offset:1024\n"
"v_mfma_f32_32x32x16_bf16 v[96:111], a[248:251], v[152:155], v[96:111]\n"
"s_waitcnt lgkmcnt(12)\n"
"v_mfma_f32_32x32x16_bf16 v[80:95], a[108:111], v[156:159], v[80:95]\n"
"ds_read_b128 v[148:151], v7 offset:15872\n"
"v_mfma_f32_32x32x16_bf16 v[96:111], a[252:255], v[156:159], v[96:111]\n"
"s_waitcnt lgkmcnt(12)\n"
"s_waitcnt lgkmcnt(2)\n"
"s_cmp_lg_u32 s20, 3\n"
"s_cbranch_scc1 Lpb_123\n"
"s_waitcnt vmcnt(0)\n"
"s_cmp_lg_u32 s20, 3\n"
"s_cbranch_scc1 Lav_124\n"
"v_cmp_ge_i32 vcc, s59, v10\n"
"s_cbranch_vccnz Lav_124\n"
"s_add_u32 s41, s59, 1\n"
"v_cmp_ge_i32 vcc, s41, v10\n"
"s_cbranch_vccnz Lav_125\n"
"s_add_u32 s41, s41, 1\n"
"Lav_125:\n"
"s_max_i32 s70, s70, s41\n"
"v_mov_b32 v251, s70\n"
"ds_write_b32 v15, v251\n"
"Lav_124:\n"
"Lpb_123:\n"
"v_mfma_f32_32x32x16_bf16 v[80:95], a[112:115], v[160:163], v[80:95]\n"
"ds_read_b128 v[152:155], v7 offset:16384\n"
"v_add_f32 v212, v212, v214\n"
"v_add_f32 v213, v213, v215\n"
"v_add_f32 v212, v212, v216\n"
"v_add_f32 v213, v213, v217\n"
"v_mfma_f32_32x32x16_bf16 v[96:111], v[16:19], v[160:163], v[96:111]\n"
"v_add_f32 v212, v212, v218\n"
"v_add_f32 v213, v213, v219\n"
"v_add_f32 v212, v212, v220\n"
"v_add_f32 v213, v213, v221\n"
"s_waitcnt lgkmcnt(9)\n"
"v_mfma_f32_32x32x16_bf16 v[80:95], a[116:119], v[164:167], v[80:95]\n"
"ds_read_b128 v[156:159], v7 offset:16896\n"
"ds_read_b32 v251, v15\n"
"v_add_f32 v212, v212, v222\n"
"v_add_f32 v213, v213, v223\n"
"v_add_f32 v212, v212, v224\n"
"v_mfma_f32_32x32x16_bf16 v[96:111], v[20:23], v[164:167], v[96:111]\n"
"v_add_f32 v213, v213, v225\n"
"v_add_f32 v212, v212, v226\n"
"v_add_f32 v213, v213, v227\n"
"v_add_f32 v212, v212, v228\n"
"s_waitcnt lgkmcnt(0)\n"
"v_readfirstlane_b32 s70, v251\n"
"s_cmp_gt_i32 s70, s59\n"
"s_cbranch_scc0 LP3_x00\n"
"s_waitcnt lgkmcnt(7)\n"
"v_mfma_f32_32x32x16_bf16 v[80:95], a[120:123], v[144:147], v[80:95]\n"
"ds_read_b128 v[160:163], v7 offset:17408\n"
"buffer_load_dwordx4 v[168:171], v5, s[24:27], s62 offen sc1\n"
"s_add_u32 s62, s62, 4096\n"
"v_add_f32 v213, v213, v229\n"
"v_mov_b32 v214, v212\n"
"v_mfma_f32_32x32x16_bf16 v[96:111], v[24:27], v[144:147], v[96:111]\n"
"buffer_load_dwordx4 v[172:175], v5, s[24:27], s62 offen sc1\n"
"s_add_u32 s62, s62, 12288\n"
"v_mov_b32 v215, v213\n"
"s_nop 0\n"
"s_waitcnt lgkmcnt(4)\n"
"v_mfma_f32_32x32x16_bf16 v[80:95], a[124:127], v[148:151], v[80:95]\n"
"ds_read_b128 v[164:167], v7 offset:17920\n"
"buffer_load_dwordx4 v[176:179], v5, s[24:27], s62 offen sc1\n"
"s_add_u32 s62, s62, 4096\n"
"v_permlane32_swap_b32 v212, v214\n"
"v_mfma_f32_32x32x16_bf16 v[96:111], v[28:31], v[148:151], v[96:111]\n"
"buffer_load_dwordx4 v[180:183], v5, s[24:27], s62 offen sc1\n"
"s_add_u32 s62, s62, 12288\n"
"v_permlane32_swap_b32 v213, v215\n"
"v_add_f32 v212, v212, v214\n"
"s_waitcnt lgkmcnt(4)\n"
"v_mfma_f32_32x32x16_bf16 v[80:95], a[128:131], v[152:155], v[80:95]\n"
"buffer_load_dwordx4 v[204:207], v11, s[24:27], s63 offen sc1\n"
"v_add_f32 v213, v213, v215\n"
"v_mul_f32 v242, 0x3ae38e39, v212\n"
"v_mul_f32 v213, 0x3ae38e39, v213\n"
"v_fma_f32 v213, -v242, v242, v213\n"
"v_mfma_f32_32x32x16_bf16 v[96:111], v[32:35], v[152:155], v[96:111]\n"
"buffer_load_dwordx4 v[208:211], v12, s[24:27], s63 offen sc1\n"
"v_max_f32 v213, 0, v213\n"
"v_add_f32 v244, 0x3727c5ac, v213\n"
"v_rsq_f32 v243, v244\n"
"s_waitcnt lgkmcnt(3)\n"
"v_mfma_f32_32x32x16_bf16 v[80:95], a[132:135], v[156:159], v[80:95]\n"
"buffer_load_dwordx4 v[184:187], v5, s[24:27], s62 offen sc1\n"
"s_add_u32 s62, s62, 4096\n"
"s_nop 0\n"
"v_mul_f32 v243, s57, v243\n"
"v_mfma_f32_32x32x16_bf16 v[96:111], v[36:39], v[156:159], v[96:111]\n"
"buffer_load_dwordx4 v[188:191], v5, s[24:27], s62 offen sc1\n"
"s_add_u32 s62, s62, 12288\n"
"s_waitcnt lgkmcnt(1)\n"
"v_mfma_f32_32x32x16_bf16 v[80:95], a[136:139], v[160:163], v[80:95]\n"
"buffer_load_dwordx4 v[192:195], v5, s[24:27], s62 offen sc1\n"
"s_add_u32 s62, s62, 4096\n"
"v_mfma_f32_32x32x16_bf16 v[96:111], v[40:43], v[160:163], v[96:111]\n"
"buffer_load_dwordx4 v[196:199], v5, s[24:27], s62 offen sc1\n"
"s_add_u32 s62, s62, 12288\n"
"s_waitcnt lgkmcnt(0)\n"
"v_mfma_f32_32x32x16_bf16 v[80:95], a[140:143], v[164:167], v[80:95]\n"
"buffer_load_dwordx4 v[200:203], v5, s[24:27], s62 offen sc1\n"
"s_add_u32 s62, s62, 4096\n"
"v_mfma_f32_32x32x16_bf16 v[96:111], v[44:47], v[164:167], v[96:111]\n"
"s_nop 8\n"
"s_branch LP3_late0\n"
"LP3_x00:\n"
"s_waitcnt lgkmcnt(7)\n"
"v_mfma_f32_32x32x16_bf16 v[80:95], a[120:123], v[144:147], v[80:95]\n"
"ds_read_b128 v[160:163], v7 offset:17408\n"
"v_add_f32 v213, v213, v229\n"
"v_mov_b32 v214, v212\n"
"v_mov_b32 v215, v213\n"
"s_nop 0\n"
"v_mfma_f32_32x32x16_bf16 v[96:111], v[24:27], v[144:147], v[96:111]\n"
"v_permlane32_swap_b32 v212, v214\n"
"v_permlane32_swap_b32 v213, v215\n"
"v_add_f32 v212, v212, v214\n"
"s_waitcnt lgkmcnt(4)\n"
"v_mfma_f32_32x32x16_bf16 v[80:95], a[124:127], v[148:151], v[80:95]\n"
"ds_read_b128 v[164:167], v7 offset:17920\n"
"v_add_f32 v213, v213, v215\n"
"v_mul_f32 v242, 0x3ae38e39, v212\n"
"v_mul_f32 v213, 0x3ae38e39, v213\n"
"v_fma_f32 v213, -v242, v242, v213\n"
"v_mfma_f32_32x32x16_bf16 v[96:111], v[28:31], v[148:151], v[96:111]\n"
"v_max_f32 v213, 0, v213\n"
"v_add_f32 v244, 0x3727c5ac, v213\n"
"v_rsq_f32 v243, v244\n"
"s_nop 0\n"
"s_waitcnt lgkmcnt(4)\n"
"v_mfma_f32_32x32x16_bf16 v[80:95], a[128:131], v[152:155], v[80:95]\n"
"v_mul_f32 v243, s57, v243\n"
"v_mfma_f32_32x32x16_bf16 v[96:111], v[32:35], v[152:155], v[96:111]\n"
"s_waitcnt lgkmcnt(3)\n"
"v_mfma_f32_32x32x16_bf16 v[80:95], a[132:135], v[156:159], v[80:95]\n"
"v_mfma_f32_32x32x16_bf16 v[96:111], v[36:39], v[156:159], v[96:111]\n"
"s_waitcnt lgkmcnt(1)\n"
"v_mfma_f32_32x32x16_bf16 v[80:95], a[136:139], v[160:163], v[80:95]\n"
"v_mfma_f32_32x32x16_bf16 v[96:111], v[40:43], v[160:163], v[96:111]\n"
"buffer_load_dword v10, v4, s[24:27], s71 offen sc1\n"
"s_waitcnt lgkmcnt(0)\n"
"v_mfma_f32_32x32x16_bf16 v[80:95], a[140:143], v[164:167], v[80:95]\n"
"v_mfma_f32_32x32x16_bf16 v[96:111], v[44:47], v[164:167], v[96:111]\n"
"s_nop 9\n"
"s_branch LP3_fin0\n"
"LP3_01:\n"
"ds_read_b128 v[144:147], v7 offset:0\n"
"ds_read_b128 v[148:151], v7 offset:512\n"
"ds_read_b128 v[152:155], v7 offset:1024\n"
"ds_read_b128 v[156:159], v7 offset:1536\n"
"s_mov_b32 s67, s56\n"
"s_waitcnt lgkmcnt(3)\n"
"v_mfma_f32_32x32x16_bf16 v[80:95], a[0:3], v[144:147], 0\n"
"ds_read_b128 v[160:163], v7 offset:2048\n"
"s_add_u32 s59, s21, 1\n"
"s_lshl_b32 s62, s59, 17\n"
"s_add_u32 s62, s62, 157286400\n"
"s_lshr_b32 s43, s62, 8\n"
"v_mfma_f32_32x32x16_bf16 v[96:111], a[144:147], v[144:147], 0\n"
"s_xor_b32 s43, s43, s22\n"
"s_not_b32 s43, s43\n"
"s_and_b32 s43, s43, 8192\n"
"s_or_b32 s62, s62, s43\n"
"s_waitcnt lgkmcnt(3)\n"
"v_mfma_f32_32x32x16_bf16 v[80:95], a[4:7], v[148:151], v[80:95]\n"
"ds_read_b128 v[164:167], v7 offset:2560\n"
"s_add_u32 s63, s62, 81920\n"
"s_mul_i32 s42, s21, 86\n"
"s_lshr_b32 s42, s42, 8\n"
"s_mul_i32 s56, s21, 655360\n"
"v_mfma_f32_32x32x16_bf16 v[96:111], a[148:151], v[148:151], v[96:111]\n"
"s_mul_i32 s42, s42, 131072\n"
"s_add_u32 s56, s56, s42\n"
"s_add_u32 s56, s56, 102760448\n"
"s_add_u32 s56, s56, s66\n"
"s_waitcnt lgkmcnt(3)\n"
"v_mfma_f32_32x32x16_bf16 v[80:95], a[8:11], v[152:155], v[80:95]\n"
"ds_read_b128 v[144:147], v7 offset:3072\n"
"s_lshr_b32 s43, s56, 8\n"
"s_xor_b32 s43, s43, s22\n"
"s_not_b32 s43, s43\n"
"s_and_b32 s43, s43, 8192\n"
"v_mfma_f32_32x32x16_bf16 v[96:111], a[152:155], v[152:155], v[96:111]\n"
"s_or_b32 s56, s56, s43\n"
"v_fma_f32 v112, -v245, v48, v112\n"
"v_fma_f32 v113, -v245, v49, v113\n"
"v_fma_f32 v128, -v245, v64, v128\n"
"s_waitcnt lgkmcnt(3)\n"
"s_waitcnt vmcnt(0)\n"
"v_mfma_f32_32x32x16_bf16 v[80:95], a[12:15], v[156:159], v[80:95]\n"
"ds_read_b128 v[148:151], v7 offset:3584\n"
"v_fma_f32 v129, -v245, v65, v129\n"
"v_mul_f32 v238, v246, v112\n"
"v_mul_f32 v239, v246, v113\n"
"v_mfma_f32_32x32x16_bf16 v[96:111], a[156:159], v[156:159], v[96:111]\n"
"ds_write_b128 v9, v[184:187] offset:8192\n"
"v_exp_f32 v238, v238\n"
"v_exp_f32 v239, v239\n"
"s_waitcnt lgkmcnt(4)\n"
"v_mfma_f32_32x32x16_bf16 v[80:95], a[16:19], v[160:163], v[80:95]\n"
"ds_read_b128 v[152:155], v7 offset:4096\n"
"buffer_load_dword v10, v4, s[24:27], s71 offen sc1\n"
"v_mul_f32 v112, v112, v128\n"
"v_mul_f32 v113, v113, v129\n"
"v_fma_f32 v238, v238, v247, v247\n"
"v_mfma_f32_32x32x16_bf16 v[96:111], a[160:163], v[160:163], v[96:111]\n"
"ds_write_b128 v9, v[188:191] offset:10240\n"
"v_fma_f32 v239, v239, v247, v247\n"
"v_rcp_f32 v238, v238\n"
"s_waitcnt lgkmcnt(5)\n"
"v_mfma_f32_32x32x16_bf16 v[80:95], a[20:23], v[164:167], v[80:95]\n"
"ds_read_b128 v[156:159], v7 offset:4608\n"
"v_rcp_f32 v239, v239\n"
"v_mul_f32 v112, v112, v238\n"
"v_mul_f32 v113, v113, v239\n"
"v_mfma_f32_32x32x16_bf16 v[96:111], a[164:167], v[164:167], v[96:111]\n"
"ds_write_b128 v9, v[192:195] offset:12288\n"
"v_cvt_pk_bf16_f32 v230, v112, v113\n"
"v_fma_f32 v114, -v245, v50, v114\n"
"v_fma_f32 v115, -v245, v51, v115\n"
"s_waitcnt lgkmcnt(6)\n"
"v_mfma_f32_32x32x16_bf16 v[80:95], a[24:27], v[144:147], v[80:95]\n"
"ds_read_b128 v[160:163], v7 offset:5120\n"
"v_fma_f32 v130, -v245, v66, v130\n"
"v_fma_f32 v131, -v245, v67, v131\n"
"v_mul_f32 v238, v246, v114\n"
"v_mul_f32 v239, v246, v115\n"
"v_mfma_f32_32x32x16_bf16 v[96:111], a[168:171], v[144:147], v[96:111]\n"
"ds_write_b128 v9, v[196:199] offset:14336\n"
"v_exp_f32 v238, v238\n"
"v_exp_f32 v239, v239\n"
"s_waitcnt lgkmcnt(7)\n"
"v_mfma_f32_32x32x16_bf16 v[80:95], a[28:31], v[148:151], v[80:95]\n"
"ds_read_b128 v[164:167], v7 offset:5632\n"
"v_mul_f32 v114, v114, v130\n"
"v_mul_f32 v115, v115, v131\n"
"v_fma_f32 v238, v238, v247, v247\n"
"v_fma_f32 v239, v239, v247, v247\n"
"v_mfma_f32_32x32x16_bf16 v[96:111], a[172:175], v[148:151], v[96:111]\n"
"ds_write_b128 v9, v[200:203] offset:16384\n"
"v_rcp_f32 v238, v238\n"
"v_rcp_f32 v239, v239\n"
"s_waitcnt lgkmcnt(7)\n"
"v_mfma_f32_32x32x16_bf16 v[80:95], a[32:35], v[152:155], v[80:95]\n"
"ds_read_b128 v[144:147], v7 offset:6144\n"
"v_mul_f32 v114, v114, v238\n"
"v_mul_f32 v115, v115, v239\n"
"v_cvt_pk_bf16_f32 v231, v114, v115\n"
"v_fma_f32 v116, -v245, v52, v116\n"
"v_mfma_f32_32x32x16_bf16 v[96:111], a[176:179], v[152:155], v[96:111]\n"
"v_fma_f32 v117, -v245, v53, v117\n"
"v_fma_f32 v132, -v245, v68, v132\n"
"v_fma_f32 v133, -v245, v69, v133\n"
"v_mul_f32 v238, v246, v116\n"
"s_waitcnt lgkmcnt(6)\n"
"v_mfma_f32_32x32x16_bf16 v[80:95], a[36:39], v[156:159], v[80:95]\n"
"ds_read_b128 v[148:151], v7 offset:6656\n"
"v_mul_f32 v239, v246, v117\n"
"v_exp_f32 v238, v238\n"
"v_exp_f32 v239, v239\n"
"v_mfma_f32_32x32x16_bf16 v[96:111], a[180:183], v[156:159], v[96:111]\n"
"v_mul_f32 v116, v116, v132\n"
"v_mul_f32 v117, v117, v133\n"
"v_fma_f32 v238, v238, v247, v247\n"
"v_fma_f32 v239, v239, v247, v247\n"
"s_waitcnt lgkmcnt(5)\n"
"v_mfma_f32_32x32x16_bf16 v[80:95], a[40:43], v[160:163], v[80:95]\n"
"ds_read_b128 v[152:155], v7 offset:7168\n"
"v_rcp_f32 v238, v238\n"
"v_rcp_f32 v239, v239\n"
"v_mul_f32 v116, v116, v238\n"
"v_mfma_f32_32x32x16_bf16 v[96:111], a[184:187], v[160:163], v[96:111]\n"
"v_mul_f32 v117, v117, v239\n"
"v_cvt_pk_bf16_f32 v232, v116, v117\n"
"v_fma_f32 v118, -v245, v54, v118\n"
"v_fma_f32 v119, -v245, v55, v119\n"
"s_waitcnt lgkmcnt(4)\n"
"s_waitcnt lgkmcnt(4)\n"
"s_barrier\n"
"v_mfma_f32_32x32x16_bf16 v[80:95], a[44:47], v[164:167], v[80:95]\n"
"ds_read_b128 v[156:159], v7 offset:7680\n"
"v_fma_f32 v134, -v245, v70, v134\n"
"v_fma_f32 v135, -v245, v71, v135\n"
"v_mul_f32 v238, v246, v118\n"
"v_mul_f32 v239, v246, v119\n"
"v_mfma_f32_32x32x16_bf16 v[96:111], a[188:191], v[164:167], v[96:111]\n"
"v_exp_f32 v238, v238\n"
"v_exp_f32 v239, v239\n"
"v_mul_f32 v118, v118, v134\n"
"s_waitcnt lgkmcnt(3)\n"
"v_mfma_f32_32x32x16_bf16 v[80:95], a[48:51], v[144:147], v[80:95]\n"
"ds_read_b128 v[160:163], v7 offset:8192\n"
"v_mul_f32 v119, v119, v135\n"
"v_fma_f32 v238, v238, v247, v247\n"
"v_fma_f32 v239, v239, v247, v247\n"
"v_mfma_f32_32x32x16_bf16 v[96:111], a[192:195], v[144:147], v[96:111]\n"
"v_rcp_f32 v238, v238\n"
"v_rcp_f32 v239, v239\n"
"v_mul_f32 v118, v118, v238\n"
"s_waitcnt lgkmcnt(3)\n"
"v_mfma_f32_32x32x16_bf16 v[80:95], a[52:55], v[148:151], v[80:95]\n"
"ds_read_b128 v[164:167], v7 offset:8704\n"
"v_mul_f32 v119, v119, v239\n"
"v_cvt_pk_bf16_f32 v233, v118, v119\n"
"buffer_store_dwordx4 v[230:233], v14, s[24:27], s67 offen offset:0 sc1\n"
"v_fma_f32 v120, -v245, v56, v120\n"
"v_mfma_f32_32x32x16_bf16 v[96:111], a[196:199], v[148:151], v[96:111]\n"
"v_fma_f32 v121, -v245, v57, v121\n"
"v_fma_f32 v136, -v245, v72, v136\n"
"v_fma_f32 v137, -v245, v73, v137\n"
"v_mul_f32 v238, v246, v120\n"
"s_waitcnt lgkmcnt(3)\n"
"s_cmp_lg_u32 s20, 3\n"
"s_cbranch_scc1 Lpb_126\n"
"s_waitcnt vmcnt(1)\n"
"s_cmp_lg_u32 s20, 3\n"
"s_cbranch_scc1 Lav_127\n"
"v_cmp_ge_i32 vcc, s59, v10\n"
"s_cbranch_vccnz Lav_127\n"
"s_add_u32 s41, s59, 1\n"
"v_cmp_ge_i32 vcc, s41, v10\n"
"s_cbranch_vccnz Lav_128\n"
"s_add_u32 s41, s41, 1\n"
"Lav_128:\n"
"s_max_i32 s70, s70, s41\n"
"v_mov_b32 v251, s70\n"
"ds_write_b32 v15, v251\n"
"Lav_127:\n"
"Lpb_126:\n"
"v_mfma_f32_32x32x16_bf16 v[80:95], a[56:59], v[152:155], v[80:95]\n"
"ds_read_b128 v[144:147], v7 offset:9216\n"
"v_mul_f32 v239, v246, v121\n"
"v_exp_f32 v238, v238\n"
"v_exp_f32 v239, v239\n"
"v_mfma_f32_32x32x16_bf16 v[96:111], a[200:203], v[152:155], v[96:111]\n"
"v_mul_f32 v120, v120, v136\n"
"v_mul_f32 v121, v121, v137\n"
"v_fma_f32 v238, v238, v247, v247\n"
"v_fma_f32 v239, v239, v247, v247\n"
"s_waitcnt lgkmcnt(3)\n"
"v_mfma_f32_32x32x16_bf16 v[80:95], a[60:63], v[156:159], v[80:95]\n"
"ds_read_b128 v[148:151], v7 offset:9728\n"
"ds_read_b32 v251, v15\n"
"v_rcp_f32 v238, v238\n"
"v_rcp_f32 v239, v239\n"
"v_mfma_f32_32x32x16_bf16 v[96:111], a[204:207], v[156:159], v[96:111]\n"
"v_mul_f32 v120, v120, v238\n"
"v_mul_f32 v121, v121, v239\n"
"v_cvt_pk_bf16_f32 v234, v120, v121\n"
"v_fma_f32 v122, -v245, v58, v122\n"
"s_waitcnt lgkmcnt(0)\n"
"v_readfirstlane_b32 s70, v251\n"
"s_add_u32 s36, s21, 2\n"
"s_cmp_gt_i32 s70, s36\n"
"s_cbranch_scc0 LP3_n01\n"
"s_waitcnt lgkmcnt(4)\n"
"v_mfma_f32_32x32x16_bf16 v[80:95], a[64:67], v[160:163], v[80:95]\n"
"ds_read_b128 v[152:155], v7 offset:10240\n"
"v_fma_f32 v123, -v245, v59, v123\n"
"v_fma_f32 v138, -v245, v74, v138\n"
"v_fma_f32 v139, -v245, v75, v139\n"
"v_mul_f32 v238, v246, v122\n"
"v_mfma_f32_32x32x16_bf16 v[96:111], a[208:211], v[160:163], v[96:111]\n"
"buffer_load_dwordx4 v[168:171], v5, s[24:27], s62 offen sc1\n"
"s_add_u32 s62, s62, 4096\n"
"v_mul_f32 v239, v246, v123\n"
"v_exp_f32 v238, v238\n"
"s_waitcnt lgkmcnt(4)\n"
"v_mfma_f32_32x32x16_bf16 v[80:95], a[68:71], v[164:167], v[80:95]\n"
"ds_read_b128 v[156:159], v7 offset:10752\n"
"v_exp_f32 v239, v239\n"
"v_mul_f32 v122, v122, v138\n"
"v_mul_f32 v123, v123, v139\n"
"v_mfma_f32_32x32x16_bf16 v[96:111], a[212:215], v[164:167], v[96:111]\n"
"buffer_load_dwordx4 v[172:175], v5, s[24:27], s62 offen sc1\n"
"s_add_u32 s62, s62, 12288\n"
"v_fma_f32 v238, v238, v247, v247\n"
"v_fma_f32 v239, v239, v247, v247\n"
"s_waitcnt lgkmcnt(4)\n"
"v_mfma_f32_32x32x16_bf16 v[80:95], a[72:75], v[144:147], v[80:95]\n"
"ds_read_b128 v[160:163], v7 offset:11264\n"
"v_rcp_f32 v238, v238\n"
"v_rcp_f32 v239, v239\n"
"v_mul_f32 v122, v122, v238\n"
"v_mfma_f32_32x32x16_bf16 v[96:111], a[216:219], v[144:147], v[96:111]\n"
"buffer_load_dwordx4 v[176:179], v5, s[24:27], s62 offen sc1\n"
"s_add_u32 s62, s62, 4096\n"
"v_mul_f32 v123, v123, v239\n"
"v_cvt_pk_bf16_f32 v235, v122, v123\n"
"s_waitcnt lgkmcnt(4)\n"
"v_mfma_f32_32x32x16_bf16 v[80:95], a[76:79], v[148:151], v[80:95]\n"
"ds_read_b128 v[164:167], v7 offset:11776\n"
"v_fma_f32 v124, -v245, v60, v124\n"
"v_fma_f32 v125, -v245, v61, v125\n"
"v_fma_f32 v140, -v245, v76, v140\n"
"v_fma_f32 v141, -v245, v77, v141\n"
"v_mfma_f32_32x32x16_bf16 v[96:111], a[220:223], v[148:151], v[96:111]\n"
"buffer_load_dwordx4 v[180:183], v5, s[24:27], s62 offen sc1\n"
"s_add_u32 s62, s62, 12288\n"
"v_mul_f32 v238, v246, v124\n"
"v_mul_f32 v239, v246, v125\n"
"s_waitcnt lgkmcnt(3)\n"
"v_mfma_f32_32x32x16_bf16 v[80:95], a[80:83], v[152:155], v[80:95]\n"
"ds_read_b128 v[144:147], v7 offset:12288\n"
"v_exp_f32 v238, v238\n"
"v_exp_f32 v239, v239\n"
"v_mul_f32 v124, v124, v140\n"
"v_mfma_f32_32x32x16_bf16 v[96:111], a[224:227], v[152:155], v[96:111]\n"
"buffer_load_dwordx4 v[204:207], v11, s[24:27], s63 offen sc1\n"
"v_mul_f32 v125, v125, v141\n"
"v_fma_f32 v238, v238, v247, v247\n"
"v_fma_f32 v239, v239, v247, v247\n"
"s_waitcnt lgkmcnt(3)\n"
"v_mfma_f32_32x32x16_bf16 v[80:95], a[84:87], v[156:159], v[80:95]\n"
"ds_read_b128 v[148:151], v7 offset:12800\n"
"v_rcp_f32 v238, v238\n"
"v_rcp_f32 v239, v239\n"
"v_mul_f32 v124, v124, v238\n"
"v_mfma_f32_32x32x16_bf16 v[96:111], a[228:231], v[156:159], v[96:111]\n"
"buffer_load_dwordx4 v[208:211], v12, s[24:27], s63 offen sc1\n"
"v_mul_f32 v125, v125, v239\n"
"v_cvt_pk_bf16_f32 v236, v124, v125\n"
"v_fma_f32 v126, -v245, v62, v126\n"
"s_waitcnt lgkmcnt(3)\n"
"v_mfma_f32_32x32x16_bf16 v[80:95], a[88:91], v[160:163], v[80:95]\n"
"ds_read_b128 v[152:155], v7 offset:13312\n"
"v_fma_f32 v127, -v245, v63, v127\n"
"v_fma_f32 v142, -v245, v78, v142\n"
"v_fma_f32 v143, -v245, v79, v143\n"
"v_mul_f32 v238, v246, v126\n"
"v_mfma_f32_32x32x16_bf16 v[96:111], a[232:235], v[160:163], v[96:111]\n"
"buffer_load_dwordx4 v[184:187], v5, s[24:27], s62 offen sc1\n"
"s_add_u32 s62, s62, 4096\n"
"v_mul_f32 v239, v246, v127\n"
"v_exp_f32 v238, v238\n"
"s_waitcnt lgkmcnt(3)\n"
"v_mfma_f32_32x32x16_bf16 v[80:95], a[92:95], v[164:167], v[80:95]\n"
"ds_read_b128 v[156:159], v7 offset:13824\n"
"v_exp_f32 v239, v239\n"
"v_mul_f32 v126, v126, v142\n"
"v_mul_f32 v127, v127, v143\n"
"v_mfma_f32_32x32x16_bf16 v[96:111], a[236:239], v[164:167], v[96:111]\n"
"buffer_load_dwordx4 v[188:191], v5, s[24:27], s62 offen sc1\n"
"s_add_u32 s62, s62, 12288\n"
"v_fma_f32 v238, v238, v247, v247\n"
"v_fma_f32 v239, v239, v247, v247\n"
"s_waitcnt lgkmcnt(3)\n"
"v_mfma_f32_32x32x16_bf16 v[80:95], a[96:99], v[144:147], v[80:95]\n"
"ds_read_b128 v[160:163], v7 offset:14336\n"
"ds_read_b64 v[212:213], v6 offset:0\n"
"ds_read_b64 v[214:215], v6 offset:128\n"
"ds_read_b64 v[216:217], v6 offset:256\n"
"v_mfma_f32_32x32x16_bf16 v[96:111], a[240:243], v[144:147], v[96:111]\n"
"buffer_load_dwordx4 v[192:195], v5, s[24:27], s62 offen sc1\n"
"s_add_u32 s62, s62, 4096\n"
"v_rcp_f32 v238, v238\n"
"v_rcp_f32 v239, v239\n"
"s_waitcnt lgkmcnt(6)\n"
"v_mfma_f32_32x32x16_bf16 v[80:95], a[100:103], v[148:151], v[80:95]\n"
"ds_read_b128 v[164:167], v7 offset:14848\n"
"ds_read_b64 v[218:219], v6 offset:384\n"
"ds_read_b64 v[220:221], v6 offset:512\n"
"ds_read_b64 v[222:223], v6 offset:640\n"
"v_mul_f32 v126, v126, v238\n"
"v_mfma_f32_32x32x16_bf16 v[96:111], a[244:247], v[148:151], v[96:111]\n"
"buffer_load_dwordx4 v[196:199], v5, s[24:27], s62 offen sc1\n"
"s_add_u32 s62, s62, 12288\n"
"v_mul_f32 v127, v127, v239\n"
"v_cvt_pk_bf16_f32 v237, v126, v127\n"
"s_waitcnt lgkmcnt(9)\n"
"v_mfma_f32_32x32x16_bf16 v[80:95], a[104:107], v[152:155], v[80:95]\n"
"ds_read_b128 v[144:147], v7 offset:15360\n"
"ds_read_b64 v[224:225], v6 offset:768\n"
"ds_read_b64 v[226:227], v6 offset:896\n"
"ds_read_b64 v[228:229], v6 offset:1024\n"
"buffer_store_dwordx4 v[234:237], v14, s[24:27], s67 offen offset:256 sc1\n"
"v_mfma_f32_32x32x16_bf16 v[96:111], a[248:251], v[152:155], v[96:111]\n"
"buffer_load_dwordx4 v[200:203], v5, s[24:27], s62 offen sc1\n"
"s_add_u32 s62, s62, 4096\n"
"s_waitcnt lgkmcnt(12)\n"
"v_mfma_f32_32x32x16_bf16 v[80:95], a[108:111], v[156:159], v[80:95]\n"
"ds_read_b128 v[148:151], v7 offset:15872\n"
"v_mfma_f32_32x32x16_bf16 v[96:111], a[252:255], v[156:159], v[96:111]\n"
"s_waitcnt lgkmcnt(12)\n"
"s_waitcnt lgkmcnt(2)\n"
"v_mfma_f32_32x32x16_bf16 v[80:95], a[112:115], v[160:163], v[80:95]\n"
"ds_read_b128 v[152:155], v7 offset:16384\n"
"v_add_f32 v212, v212, v214\n"
"v_add_f32 v213, v213, v215\n"
"v_add_f32 v212, v212, v216\n"
"v_add_f32 v213, v213, v217\n"
"v_mfma_f32_32x32x16_bf16 v[96:111], v[16:19], v[160:163], v[96:111]\n"
"v_add_f32 v212, v212, v218\n"
"v_add_f32 v213, v213, v219\n"
"v_add_f32 v212, v212, v220\n"
"v_add_f32 v213, v213, v221\n"
"s_waitcnt lgkmcnt(9)\n"
"v_mfma_f32_32x32x16_bf16 v[80:95], a[116:119], v[164:167], v[80:95]\n"
"ds_read_b128 v[156:159], v7 offset:16896\n"
"v_add_f32 v212, v212, v222\n"
"v_add_f32 v213, v213, v223\n"
"v_add_f32 v212, v212, v224\n"
"v_add_f32 v213, v213, v225\n"
"v_mfma_f32_32x32x16_bf16 v[96:111], v[20:23], v[164:167], v[96:111]\n"
"v_add_f32 v212, v212, v226\n"
"v_add_f32 v213, v213, v227\n"
"v_add_f32 v212, v212, v228\n"
"v_add_f32 v213, v213, v229\n"
"s_waitcnt lgkmcnt(6)\n"
"s_waitcnt vmcnt(6)\n"
"v_mfma_f32_32x32x16_bf16 v[80:95], a[120:123], v[144:147], v[80:95]\n"
"ds_read_b128 v[160:163], v7 offset:17408\n"
"v_mov_b32 v214, v212\n"
"v_mov_b32 v215, v213\n"
"s_nop 0\n"
"v_permlane32_swap_b32 v212, v214\n"
"v_mfma_f32_32x32x16_bf16 v[96:111], v[24:27], v[144:147], v[96:111]\n"
"ds_write_b128 v8, v[168:171] offset:0\n"
"v_permlane32_swap_b32 v213, v215\n"
"v_add_f32 v212, v212, v214\n"
"v_add_f32 v213, v213, v215\n"
"s_waitcnt lgkmcnt(4)\n"
"v_mfma_f32_32x32x16_bf16 v[80:95], a[124:127], v[148:151], v[80:95]\n"
"ds_read_b128 v[164:167], v7 offset:17920\n"
"v_mul_f32 v242, 0x3ae38e39, v212\n"
"v_mul_f32 v213, 0x3ae38e39, v213\n"
"v_fma_f32 v213, -v242, v242, v213\n"
"v_max_f32 v213, 0, v213\n"
"v_mfma_f32_32x32x16_bf16 v[96:111], v[28:31], v[148:151], v[96:111]\n"
"ds_write_b128 v8, v[172:175] offset:2048\n"
"v_add_f32 v244, 0x3727c5ac, v213\n"
"v_rsq_f32 v243, v244\n"
"s_nop 0\n"
"s_waitcnt lgkmcnt(5)\n"
"s_waitcnt vmcnt(1)\n"
"s_add_u32 s65, s21, -1\n"
"s_and_b32 s41, s65, 1\n"
"s_lshl_b32 s41, s41, 2\n"
"s_add_u32 s41, s41, 98240\n"
"v_mov_b32 v248, s41\n"
"v_mov_b32 v250, 1\n"
"s_mov_b64 exec, 1\n"
"ds_add_rtn_u32 v249, v248, v250\n"
"s_mov_b64 exec, -1\n"
"v_mfma_f32_32x32x16_bf16 v[80:95], a[128:131], v[152:155], v[80:95]\n"
"v_mul_f32 v243, s57, v243\n"
"v_mfma_f32_32x32x16_bf16 v[96:111], v[32:35], v[152:155], v[96:111]\n"
"ds_write_b128 v8, v[176:179] offset:4096\n"
"s_waitcnt lgkmcnt(6)\n"
"v_mfma_f32_32x32x16_bf16 v[80:95], a[132:135], v[156:159], v[80:95]\n"
"v_mfma_f32_32x32x16_bf16 v[96:111], v[36:39], v[156:159], v[96:111]\n"
"ds_write_b128 v8, v[180:183] offset:6144\n"
"s_waitcnt lgkmcnt(6)\n"
"s_waitcnt lgkmcnt(2)\n"
"v_readfirstlane_b32 s41, v249\n"
"s_and_b32 s41, s41, 3\n"
"s_cmp_lg_u32 s41, 3\n"
"s_cbranch_scc1 Lar_129\n"
"s_lshl_b32 s43, s19, 7\n"
"s_xor_b32 s43, s43, s22\n"
"s_xor_b32 s43, s43, 1302528\n"
"s_add_u32 s42, s65, 1\n"
"v_mov_b32 v3, s42\n"
"buffer_store_dword v3, v2, s[24:27], s43 offen sc1\n"
"Lpf_130:\n"
"Lar_129:\n"
"v_mfma_f32_32x32x16_bf16 v[80:95], a[136:139], v[160:163], v[80:95]\n"
"v_mfma_f32_32x32x16_bf16 v[96:111], v[40:43], v[160:163], v[96:111]\n"
"ds_write_b128 v13, v[204:207]\n"
"s_waitcnt lgkmcnt(5)\n"
"v_mfma_f32_32x32x16_bf16 v[80:95], a[140:143], v[164:167], v[80:95]\n"
"v_mfma_f32_32x32x16_bf16 v[96:111], v[44:47], v[164:167], v[96:111]\n"
"s_mov_b64 exec, 0xff\n"
"ds_write_b128 v13, v[208:211] offset:1024\n"
"s_mov_b64 exec, -1\n"
"v_mov_b32 v245, v242\n"
"v_mov_b32 v246, v243\n"
"v_mov_b32 v247, v244\n"
"v_mov_b32 v9, v8\n"
"v_xor_b32 v6, 8192, v6\n"
"v_xor_b32 v13, 8192, v13\n"
"v_add_u32 v7, 36864, v7\n"
"v_add_u32 v8, -36864, v8\n"
"s_add_u32 s21, s21, 1\n"
"s_waitcnt lgkmcnt(0)\n"
"s_barrier\n"
"s_branch LP3_11\n"
"LP3_n01:\n"
"s_waitcnt lgkmcnt(4)\n"
"v_mfma_f32_32x32x16_bf16 v[80:95], a[64:67], v[160:163], v[80:95]\n"
"ds_read_b128 v[152:155], v7 offset:10240\n"
"v_fma_f32 v123, -v245, v59, v123\n"
"v_fma_f32 v138, -v245, v74, v138\n"
"v_fma_f32 v139, -v245, v75, v139\n"
"v_mul_f32 v238, v246, v122\n"
"v_mfma_f32_32x32x16_bf16 v[96:111], a[208:211], v[160:163], v[96:111]\n"
"v_mul_f32 v239, v246, v123\n"
"v_exp_f32 v238, v238\n"
"v_exp_f32 v239, v239\n"
"s_waitcnt lgkmcnt(4)\n"
"v_mfma_f32_32x32x16_bf16 v[80:95], a[68:71], v[164:167], v[80:95]\n"
"ds_read_b128 v[156:159], v7 offset:10752\n"
"v_mul_f32 v122, v122, v138\n"
"v_mul_f32 v123, v123, v139\n"
"v_fma_f32 v238, v238, v247, v247\n"
"v_fma_f32 v239, v239, v247, v247\n"
"v_mfma_f32_32x32x16_bf16 v[96:111], a[212:215], v[164:167], v[96:111]\n"
"v_rcp_f32 v238, v238\n"
"v_rcp_f32 v239, v239\n"
"v_mul_f32 v122, v122, v238\n"
"s_waitcnt lgkmcnt(4)\n"
"v_mfma_f32_32x32x16_bf16 v[80:95], a[72:75], v[144:147], v[80:95]\n"
"ds_read_b128 v[160:163], v7 offset:11264\n"
"v_mul_f32 v123, v123, v239\n"
"v_cvt_pk_bf16_f32 v235, v122, v123\n"
"v_fma_f32 v124, -v245, v60, v124\n"
"v_fma_f32 v125, -v245, v61, v125\n"
"v_mfma_f32_32x32x16_bf16 v[96:111], a[216:219], v[144:147], v[96:111]\n"
"v_fma_f32 v140, -v245, v76, v140\n"
"v_fma_f32 v141, -v245, v77, v141\n"
"v_mul_f32 v238, v246, v124\n"
"v_mul_f32 v239, v246, v125\n"
"s_waitcnt lgkmcnt(4)\n"
"v_mfma_f32_32x32x16_bf16 v[80:95], a[76:79], v[148:151], v[80:95]\n"
"ds_read_b128 v[164:167], v7 offset:11776\n"
"v_exp_f32 v238, v238\n"
"v_exp_f32 v239, v239\n"
"v_mul_f32 v124, v124, v140\n"
"v_mfma_f32_32x32x16_bf16 v[96:111], a[220:223], v[148:151], v[96:111]\n"
"v_mul_f32 v125, v125, v141\n"
"v_fma_f32 v238, v238, v247, v247\n"
"v_fma_f32 v239, v239, v247, v247\n"
"v_rcp_f32 v238, v238\n"
"s_waitcnt lgkmcnt(3)\n"
"v_mfma_f32_32x32x16_bf16 v[80:95], a[80:83], v[152:155], v[80:95]\n"
"ds_read_b128 v[144:147], v7 offset:12288\n"
"v_rcp_f32 v239, v239\n"
"v_mul_f32 v124, v124, v238\n"
"v_mul_f32 v125, v125, v239\n"
"v_mfma_f32_32x32x16_bf16 v[96:111], a[224:227], v[152:155], v[96:111]\n"
"buffer_load_dword v10, v4, s[24:27], s71 offen sc1\n"
"v_cvt_pk_bf16_f32 v236, v124, v125\n"
"v_fma_f32 v126, -v245, v62, v126\n"
"v_fma_f32 v127, -v245, v63, v127\n"
"s_waitcnt lgkmcnt(3)\n"
"v_mfma_f32_32x32x16_bf16 v[80:95], a[84:87], v[156:159], v[80:95]\n"
"ds_read_b128 v[148:151], v7 offset:12800\n"
"v_fma_f32 v142, -v245, v78, v142\n"
"v_fma_f32 v143, -v245, v79, v143\n"
"v_mul_f32 v238, v246, v126\n"
"v_mul_f32 v239, v246, v127\n"
"v_mfma_f32_32x32x16_bf16 v[96:111], a[228:231], v[156:159], v[96:111]\n"
"v_exp_f32 v238, v238\n"
"v_exp_f32 v239, v239\n"
"v_mul_f32 v126, v126, v142\n"
"s_waitcnt lgkmcnt(3)\n"
"v_mfma_f32_32x32x16_bf16 v[80:95], a[88:91], v[160:163], v[80:95]\n"
"ds_read_b128 v[152:155], v7 offset:13312\n"
"v_mul_f32 v127, v127, v143\n"
"v_fma_f32 v238, v238, v247, v247\n"
"v_fma_f32 v239, v239, v247, v247\n"
"v_mfma_f32_32x32x16_bf16 v[96:111], a[232:235], v[160:163], v[96:111]\n"
"v_rcp_f32 v238, v238\n"
"v_rcp_f32 v239, v239\n"
"v_mul_f32 v126, v126, v238\n"
"s_waitcnt lgkmcnt(3)\n"
"v_mfma_f32_32x32x16_bf16 v[80:95], a[92:95], v[164:167], v[80:95]\n"
"ds_read_b128 v[156:159], v7 offset:13824\n"
"v_mul_f32 v127, v127, v239\n"
"v_cvt_pk_bf16_f32 v237, v126, v127\n"
"buffer_store_dwordx4 v[234:237], v14, s[24:27], s67 offen offset:256 sc1\n"
"v_mfma_f32_32x32x16_bf16 v[96:111], a[236:239], v[164:167], v[96:111]\n"
"s_waitcnt lgkmcnt(3)\n"
"v_mfma_f32_32x32x16_bf16 v[80:95], a[96:99], v[144:147], v[80:95]\n"
"ds_read_b128 v[160:163], v7 offset:14336\n"
"ds_read_b64 v[212:213], v6 offset:0\n"
"ds_read_b64 v[214:215], v6 offset:128\n"
"ds_read_b64 v[216:217], v6 offset:256\n"
"v_mfma_f32_32x32x16_bf16 v[96:111], a[240:243], v[144:147], v[96:111]\n"
"s_waitcnt lgkmcnt(6)\n"
"v_mfma_f32_32x32x16_bf16 v[80:95], a[100:103], v[148:151], v[80:95]\n"
"ds_read_b128 v[164:167], v7 offset:14848\n"
"ds_read_b64 v[218:219], v6 offset:384\n"
"ds_read_b64 v[220:221], v6 offset:512\n"
"ds_read_b64 v[222:223], v6 offset:640\n"
"v_mfma_f32_32x32x16_bf16 v[96:111], a[244:247], v[148:151], v[96:111]\n"
"s_waitcnt lgkmcnt(9)\n"
"v_mfma_f32_32x32x16_bf16 v[80:95], a[104:107], v[152:155], v[80:95]\n"
"ds_read_b128 v[144:147], v7 offset:15360\n"
"ds_read_b64 v[224:225], v6 offset:768\n"
"ds_read_b64 v[226:227], v6 offset:896\n"
"ds_read_b64 v[228:229], v6 offset:1024\n"
"v_mfma_f32_32x32x16_bf16 v[96:111], a[248:251], v[152:155], v[96:111]\n"
"s_waitcnt lgkmcnt(12)\n"
"v_mfma_f32_32x32x16_bf16 v[80:95], a[108:111], v[156:159], v[80:95]\n"
"ds_read_b128 v[148:151], v7 offset:15872\n"
"v_mfma_f32_32x32x16_bf16 v[96:111], a[252:255], v[156:159], v[96:111]\n"
"s_waitcnt lgkmcnt(12)\n"
"s_waitcnt lgkmcnt(2)\n"
"s_cmp_lg_u32 s20, 3\n"
"s_cbranch_scc1 Lpb_131\n"
"s_waitcnt vmcnt(1)\n"
"s_cmp_lg_u32 s20, 3\n"
"s_cbranch_scc1 Lav_132\n"
"v_cmp_ge_i32 vcc, s59, v10\n"
"s_cbranch_vccnz Lav_132\n"
"s_add_u32 s41, s59, 1\n"
"v_cmp_ge_i32 vcc, s41, v10\n"
"s_cbranch_vccnz Lav_133\n"
"s_add_u32 s41, s41, 1\n"
"Lav_133:\n"
"s_max_i32 s70, s70, s41\n"
"v_mov_b32 v251, s70\n"
"ds_write_b32 v15, v251\n"
"Lav_132:\n"
"Lpb_131:\n"
"v_mfma_f32_32x32x16_bf16 v[80:95], a[112:115], v[160:163], v[80:95]\n"
"ds_read_b128 v[152:155], v7 offset:16384\n"
"v_add_f32 v212, v212, v214\n"
"v_add_f32 v213, v213, v215\n"
"v_add_f32 v212, v212, v216\n"
"v_add_f32 v213, v213, v217\n"
"v_mfma_f32_32x32x16_bf16 v[96:111], v[16:19], v[160:163], v[96:111]\n"
"v_add_f32 v212, v212, v218\n"
"v_add_f32 v213, v213, v219\n"
"v_add_f32 v212, v212, v220\n"
"v_add_f32 v213, v213, v221\n"
"s_waitcnt lgkmcnt(9)\n"
"v_mfma_f32_32x32x16_bf16 v[80:95], a[116:119], v[164:167], v[80:95]\n"
"ds_read_b128 v[156:159], v7 offset:16896\n"
"ds_read_b32 v251, v15\n"
"v_add_f32 v212, v212, v222\n"
"v_add_f32 v213, v213, v223\n"
"v_add_f32 v212, v212, v224\n"
"v_mfma_f32_32x32x16_bf16 v[96:111], v[20:23], v[164:167], v[96:111]\n"
"v_add_f32 v213, v213, v225\n"
"v_add_f32 v212, v212, v226\n"
"v_add_f32 v213, v213, v227\n"
"v_add_f32 v212, v212, v228\n"
"s_waitcnt lgkmcnt(0)\n"
"v_readfirstlane_b32 s70, v251\n"
"s_cmp_gt_i32 s70, s59\n"
"s_cbranch_scc0 LP3_x01\n"
"s_waitcnt lgkmcnt(7)\n"
"v_mfma_f32_32x32x16_bf16 v[80:95], a[120:123], v[144:147], v[80:95]\n"
"ds_read_b128 v[160:163], v7 offset:17408\n"
"buffer_load_dwordx4 v[168:171], v5, s[24:27], s62 offen sc1\n"
"s_add_u32 s62, s62, 4096\n"
"v_add_f32 v213, v213, v229\n"
"v_mov_b32 v214, v212\n"
"v_mfma_f32_32x32x16_bf16 v[96:111], v[24:27], v[144:147], v[96:111]\n"
"buffer_load_dwordx4 v[172:175], v5, s[24:27], s62 offen sc1\n"
"s_add_u32 s62, s62, 12288\n"
"v_mov_b32 v215, v213\n"
"s_nop 0\n"
"s_waitcnt lgkmcnt(4)\n"
"v_mfma_f32_32x32x16_bf16 v[80:95], a[124:127], v[148:151], v[80:95]\n"
"ds_read_b128 v[164:167], v7 offset:17920\n"
"buffer_load_dwordx4 v[176:179], v5, s[24:27], s62 offen sc1\n"
"s_add_u32 s62, s62, 4096\n"
"v_permlane32_swap_b32 v212, v214\n"
"v_mfma_f32_32x32x16_bf16 v[96:111], v[28:31], v[148:151], v[96:111]\n"
"buffer_load_dwordx4 v[180:183], v5, s[24:27], s62 offen sc1\n"
"s_add_u32 s62, s62, 12288\n"
"v_permlane32_swap_b32 v213, v215\n"
"v_add_f32 v212, v212, v214\n"
"s_waitcnt lgkmcnt(4)\n"
"s_waitcnt vmcnt(4)\n"
"s_add_u32 s65, s21, -1\n"
"s_and_b32 s41, s65, 1\n"
"s_lshl_b32 s41, s41, 2\n"
"s_add_u32 s41, s41, 98240\n"
"v_mov_b32 v248, s41\n"
"v_mov_b32 v250, 1\n"
"s_mov_b64 exec, 1\n"
"ds_add_rtn_u32 v249, v248, v250\n"
"s_mov_b64 exec, -1\n"
"v_mfma_f32_32x32x16_bf16 v[80:95], a[128:131], v[152:155], v[80:95]\n"
"buffer_load_dwordx4 v[204:207], v11, s[24:27], s63 offen sc1\n"
"v_add_f32 v213, v213, v215\n"
"v_mul_f32 v242, 0x3ae38e39, v212\n"
"v_mul_f32 v213, 0x3ae38e39, v213\n"
"v_fma_f32 v213, -v242, v242, v213\n"
"v_mfma_f32_32x32x16_bf16 v[96:111], v[32:35], v[152:155], v[96:111]\n"
"buffer_load_dwordx4 v[208:211], v12, s[24:27], s63 offen sc1\n"
"v_max_f32 v213, 0, v213\n"
"v_add_f32 v244, 0x3727c5ac, v213\n"
"v_rsq_f32 v243, v244\n"
"s_waitcnt lgkmcnt(4)\n"
"v_mfma_f32_32x32x16_bf16 v[80:95], a[132:135], v[156:159], v[80:95]\n"
"buffer_load_dwordx4 v[184:187], v5, s[24:27], s62 offen sc1\n"
"s_add_u32 s62, s62, 4096\n"
"s_nop 0\n"
"v_mul_f32 v243, s57, v243\n"
"v_mfma_f32_32x32x16_bf16 v[96:111], v[36:39], v[156:159], v[96:111]\n"
"buffer_load_dwordx4 v[188:191], v5, s[24:27], s62 offen sc1\n"
"s_add_u32 s62, s62, 12288\n"
"s_waitcnt lgkmcnt(2)\n"
"s_waitcnt lgkmcnt(0)\n"
"v_readfirstlane_b32 s41, v249\n"
"s_and_b32 s41, s41, 3\n"
"s_cmp_lg_u32 s41, 3\n"
"s_cbranch_scc1 Lar_134\n"
"s_lshl_b32 s43, s19, 7\n"
"s_xor_b32 s43, s43, s22\n"
"s_xor_b32 s43, s43, 1302528\n"
"s_add_u32 s42, s65, 1\n"
"v_mov_b32 v3, s42\n"
"buffer_store_dword v3, v2, s[24:27], s43 offen sc1\n"
"Lpf_135:\n"
"Lar_134:\n"
"v_mfma_f32_32x32x16_bf16 v[80:95], a[136:139], v[160:163], v[80:95]\n"
"buffer_load_dwordx4 v[192:195], v5, s[24:27], s62 offen sc1\n"
"s_add_u32 s62, s62, 4096\n"
"v_mfma_f32_32x32x16_bf16 v[96:111], v[40:43], v[160:163], v[96:111]\n"
"buffer_load_dwordx4 v[196:199], v5, s[24:27], s62 offen sc1\n"
"s_add_u32 s62, s62, 12288\n"
"s_waitcnt lgkmcnt(1)\n"
"v_mfma_f32_32x32x16_bf16 v[80:95], a[140:143], v[164:167], v[80:95]\n"
"buffer_load_dwordx4 v[200:203], v5, s[24:27], s62 offen sc1\n"
"s_add_u32 s62, s62, 4096\n"
"v_mfma_f32_32x32x16_bf16 v[96:111], v[44:47], v[164:167], v[96:111]\n"
"s_nop 8\n"
"s_branch LP3_late0\n"
"LP3_x01:\n"
"s_waitcnt lgkmcnt(7)\n"
"v_mfma_f32_32x32x16_bf16 v[80:95], a[120:123], v[144:147], v[80:95]\n"
"ds_read_b128 v[160:163], v7 offset:17408\n"
"v_add_f32 v213, v213, v229\n"
"v_mov_b32 v214, v212\n"
"v_mov_b32 v215, v213\n"
"s_nop 0\n"
"v_mfma_f32_32x32x16_bf16 v[96:111], v[24:27], v[144:147], v[96:111]\n"
"v_permlane32_swap_b32 v212, v214\n"
"v_permlane32_swap_b32 v213, v215\n"
"v_add_f32 v212, v212, v214\n"
"s_waitcnt lgkmcnt(4)\n"
"v_mfma_f32_32x32x16_bf16 v[80:95], a[124:127], v[148:151], v[80:95]\n"
"ds_read_b128 v[164:167], v7 offset:17920\n"
"v_add_f32 v213, v213, v215\n"
"v_mul_f32 v242, 0x3ae38e39, v212\n"
"v_mul_f32 v213, 0x3ae38e39, v213\n"
"v_fma_f32 v213, -v242, v242, v213\n"
"v_mfma_f32_32x32x16_bf16 v[96:111], v[28:31], v[148:151], v[96:111]\n"
"v_max_f32 v213, 0, v213\n"
"v_add_f32 v244, 0x3727c5ac, v213\n"
"v_rsq_f32 v243, v244\n"
"s_nop 0\n"
"s_waitcnt lgkmcnt(4)\n"
"s_waitcnt vmcnt(0)\n"
"s_add_u32 s65, s21, -1\n"
"s_and_b32 s41, s65, 1\n"
"s_lshl_b32 s41, s41, 2\n"
"s_add_u32 s41, s41, 98240\n"
"v_mov_b32 v248, s41\n"
"v_mov_b32 v250, 1\n"
"s_mov_b64 exec, 1\n"
"ds_add_rtn_u32 v249, v248, v250\n"
"s_mov_b64 exec, -1\n"
"v_mfma_f32_32x32x16_bf16 v[80:95], a[128:131], v[152:155], v[80:95]\n"
"v_mul_f32 v243, s57, v243\n"
"v_mfma_f32_32x32x16_bf16 v[96:111], v[32:35], v[152:155], v[96:111]\n"
"s_waitcnt lgkmcnt(4)\n"
"v_mfma_f32_32x32x16_bf16 v[80:95], a[132:135], v[156:159], v[80:95]\n"
"v_mfma_f32_32x32x16_bf16 v[96:111], v[36:39], v[156:159], v[96:111]\n"
"s_waitcnt lgkmcnt(2)\n"
"s_waitcnt lgkmcnt(0)\n"
"v_readfirstlane_b32 s41, v249\n"
"s_and_b32 s41, s41, 3\n"
"s_cmp_lg_u32 s41, 3\n"
"s_cbranch_scc1 Lar_136\n"
"s_lshl_b32 s43, s19, 7\n"
"s_xor_b32 s43, s43, s22\n"
"s_xor_b32 s43, s43, 1302528\n"
"s_add_u32 s42, s65, 1\n"
"v_mov_b32 v3, s42\n"
"buffer_store_dword v3, v2, s[24:27], s43 offen sc1\n"
"Lpf_137:\n"
"Lar_136:\n"
"v_mfma_f32_32x32x16_bf16 v[80:95], a[136:139], v[160:163], v[80:95]\n"
"v_mfma_f32_32x32x16_bf16 v[96:111], v[40:43], v[160:163], v[96:111]\n"
"buffer_load_dword v10, v4, s[24:27], s71 offen sc1\n"
"s_waitcnt lgkmcnt(1)\n"
"v_mfma_f32_32x32x16_bf16 v[80:95], a[140:143], v[164:167], v[80:95]\n"
"v_mfma_f32_32x32x16_bf16 v[96:111], v[44:47], v[164:167], v[96:111]\n"
"s_nop 9\n"
"s_branch LP3_fin0\n"
"LP3_10:\n"
"ds_read_b128 v[144:147], v7 offset:0\n"
"ds_read_b128 v[148:151], v7 offset:512\n"
"ds_read_b128 v[152:155], v7 offset:1024\n"
"ds_read_b128 v[156:159], v7 offset:1536\n"
"s_waitcnt lgkmcnt(3)\n"
"v_mfma_f32_32x32x16_bf16 v[112:127], a[0:3], v[144:147], 0\n"
"ds_read_b128 v[160:163], v7 offset:2048\n"
"s_add_u32 s59, s21, 1\n"
"s_lshl_b32 s62, s59, 17\n"
"s_add_u32 s62, s62, 157286400\n"
"s_lshr_b32 s43, s62, 8\n"
"v_mfma_f32_32x32x16_bf16 v[128:143], a[144:147], v[144:147], 0\n"
"s_xor_b32 s43, s43, s22\n"
"s_not_b32 s43, s43\n"
"s_and_b32 s43, s43, 8192\n"
"s_or_b32 s62, s62, s43\n"
"s_waitcnt lgkmcnt(3)\n"
"v_mfma_f32_32x32x16_bf16 v[112:127], a[4:7], v[148:151], v[112:127]\n"
"ds_read_b128 v[164:167], v7 offset:2560\n"
"s_add_u32 s63, s62, 81920\n"
"s_mul_i32 s42, s21, 86\n"
"s_lshr_b32 s42, s42, 8\n"
"s_mul_i32 s56, s21, 655360\n"
"v_mfma_f32_32x32x16_bf16 v[128:143], a[148:151], v[148:151], v[128:143]\n"
"s_mul_i32 s42, s42, 131072\n"
"s_add_u32 s56, s56, s42\n"
"s_add_u32 s56, s56, 102760448\n"
"s_add_u32 s56, s56, s66\n"
"s_waitcnt lgkmcnt(3)\n"
"v_mfma_f32_32x32x16_bf16 v[112:127], a[8:11], v[152:155], v[112:127]\n"
"ds_read_b128 v[144:147], v7 offset:3072\n"
"s_lshr_b32 s43, s56, 8\n"
"s_xor_b32 s43, s43, s22\n"
"s_not_b32 s43, s43\n"
"s_and_b32 s43, s43, 8192\n"
"v_mfma_f32_32x32x16_bf16 v[128:143], a[152:155], v[152:155], v[128:143]\n"
"s_or_b32 s56, s56, s43\n"
"s_waitcnt lgkmcnt(3)\n"
"s_waitcnt vmcnt(2)\n"
"s_cmp_lg_u32 s68, 0\n"
"s_cbranch_scc1 Lpe_138\n"
"s_waitcnt vmcnt(0)\n"
"Lpe_138:\n"
"v_mfma_f32_32x32x16_bf16 v[112:127], a[12:15], v[156:159], v[112:127]\n"
"ds_read_b128 v[148:151], v7 offset:3584\n"
"v_mfma_f32_32x32x16_bf16 v[128:143], a[156:159], v[156:159], v[128:143]\n"
"ds_write_b128 v9, v[184:187] offset:8192\n"
"s_waitcnt lgkmcnt(4)\n"
"v_mfma_f32_32x32x16_bf16 v[112:127], a[16:19], v[160:163], v[112:127]\n"
"ds_read_b128 v[152:155], v7 offset:4096\n"
"buffer_load_dword v10, v4, s[24:27], s71 offen sc1\n"
"v_mfma_f32_32x32x16_bf16 v[128:143], a[160:163], v[160:163], v[128:143]\n"
"ds_write_b128 v9, v[188:191] offset:10240\n"
"s_waitcnt lgkmcnt(5)\n"
"v_mfma_f32_32x32x16_bf16 v[112:127], a[20:23], v[164:167], v[112:127]\n"
"ds_read_b128 v[156:159], v7 offset:4608\n"
"v_mfma_f32_32x32x16_bf16 v[128:143], a[164:167], v[164:167], v[128:143]\n"
"ds_write_b128 v9, v[192:195] offset:12288\n"
"s_waitcnt lgkmcnt(6)\n"
"s_cmp_eq_u32 s68, 0\n"
"s_cbranch_scc1 Lpe_139\n"
"s_waitcnt vmcnt(1)\n"
"s_add_u32 s65, s21, -1\n"
"s_and_b32 s41, s65, 1\n"
"s_lshl_b32 s41, s41, 2\n"
"s_add_u32 s41, s41, 98240\n"
"v_mov_b32 v248, s41\n"
"v_mov_b32 v250, 1\n"
"s_mov_b64 exec, 1\n"
"ds_add_rtn_u32 v249, v248, v250\n"
"s_mov_b64 exec, -1\n"
"Lpe_139:\n"
"v_mfma_f32_32x32x16_bf16 v[112:127], a[24:27], v[144:147], v[112:127]\n"
"ds_read_b128 v[160:163], v7 offset:5120\n"
"v_mfma_f32_32x32x16_bf16 v[128:143], a[168:171], v[144:147], v[128:143]\n"
"ds_write_b128 v9, v[196:199] offset:14336\n"
"s_waitcnt lgkmcnt(7)\n"
"v_mfma_f32_32x32x16_bf16 v[112:127], a[28:31], v[148:151], v[112:127]\n"
"ds_read_b128 v[164:167], v7 offset:5632\n"
"v_mfma_f32_32x32x16_bf16 v[128:143], a[172:175], v[148:151], v[128:143]\n"
"ds_write_b128 v9, v[200:203] offset:16384\n"
"s_waitcnt lgkmcnt(7)\n"
"s_cmp_eq_u32 s68, 0\n"
"s_cbranch_scc1 Lpe_140\n"
"s_mov_b32 s68, 0\n"
"s_waitcnt lgkmcnt(3)\n"
"v_readfirstlane_b32 s41, v249\n"
"s_and_b32 s41, s41, 3\n"
"s_cmp_lg_u32 s41, 3\n"
"s_cbranch_scc1 Lar_141\n"
"s_lshl_b32 s43, s19, 7\n"
"s_xor_b32 s43, s43, s22\n"
"s_xor_b32 s43, s43, 1302528\n"
"s_add_u32 s42, s65, 1\n"
"v_mov_b32 v3, s42\n"
"buffer_store_dword v3, v2, s[24:27], s43 offen sc1\n"
"Lpf_142:\n"
"Lar_141:\n"
"Lpe_140:\n"
"v_mfma_f32_32x32x16_bf16 v[112:127], a[32:35], v[152:155], v[112:127]\n"
"ds_read_b128 v[144:147], v7 offset:6144\n"
"v_mfma_f32_32x32x16_bf16 v[128:143], a[176:179], v[152:155], v[128:143]\n"
"s_waitcnt lgkmcnt(6)\n"
"v_mfma_f32_32x32x16_bf16 v[112:127], a[36:39], v[156:159], v[112:127]\n"
"ds_read_b128 v[148:151], v7 offset:6656\n"
"v_mfma_f32_32x32x16_bf16 v[128:143], a[180:183], v[156:159], v[128:143]\n"
"s_waitcnt lgkmcnt(5)\n"
"v_mfma_f32_32x32x16_bf16 v[112:127], a[40:43], v[160:163], v[112:127]\n"
"ds_read_b128 v[152:155], v7 offset:7168\n"
"v_mfma_f32_32x32x16_bf16 v[128:143], a[184:187], v[160:163], v[128:143]\n"
"s_waitcnt lgkmcnt(4)\n"
"s_waitcnt lgkmcnt(4)\n"
"s_barrier\n"
"v_mfma_f32_32x32x16_bf16 v[112:127], a[44:47], v[164:167], v[112:127]\n"
"ds_read_b128 v[156:159], v7 offset:7680\n"
"v_mfma_f32_32x32x16_bf16 v[128:143], a[188:191], v[164:167], v[128:143]\n"
"s_waitcnt lgkmcnt(3)\n"
"v_mfma_f32_32x32x16_bf16 v[112:127], a[48:51], v[144:147], v[112:127]\n"
"ds_read_b128 v[160:163], v7 offset:8192\n"
"v_mfma_f32_32x32x16_bf16 v[128:143], a[192:195], v[144:147], v[128:143]\n"
"s_waitcnt lgkmcnt(3)\n"
"v_mfma_f32_32x32x16_bf16 v[112:127], a[52:55], v[148:151], v[112:127]\n"
"ds_read_b128 v[164:167], v7 offset:8704\n"
"v_mfma_f32_32x32x16_bf16 v[128:143], a[196:199], v[148:151], v[128:143]\n"
"s_waitcnt lgkmcnt(3)\n"
"s_cmp_lg_u32 s20, 3\n"
"s_cbranch_scc1 Lpb_143\n"
"s_waitcnt vmcnt(0)\n"
"s_cmp_lg_u32 s20, 3\n"
"s_cbranch_scc1 Lav_144\n"
"v_cmp_ge_i32 vcc, s59, v10\n"
"s_cbranch_vccnz Lav_144\n"
"s_add_u32 s41, s59, 1\n"
"v_cmp_ge_i32 vcc, s41, v10\n"
"s_cbranch_vccnz Lav_145\n"
"s_add_u32 s41, s41, 1\n"
"Lav_145:\n"
"s_max_i32 s70, s70, s41\n"
"v_mov_b32 v251, s70\n"
"ds_write_b32 v15, v251\n"
"Lav_144:\n"
"Lpb_143:\n"
"v_mfma_f32_32x32x16_bf16 v[112:127], a[56:59], v[152:155], v[112:127]\n"
"ds_read_b128 v[144:147], v7 offset:9216\n"
"v_mfma_f32_32x32x16_bf16 v[128:143], a[200:203], v[152:155], v[128:143]\n"
"s_waitcnt lgkmcnt(3)\n"
"v_mfma_f32_32x32x16_bf16 v[112:127], a[60:63], v[156:159], v[112:127]\n"
"ds_read_b128 v[148:151], v7 offset:9728\n"
"ds_read_b32 v251, v15\n"
"v_mfma_f32_32x32x16_bf16 v[128:143], a[204:207], v[156:159], v[128:143]\n"
"s_waitcnt lgkmcnt(0)\n"
"v_readfirstlane_b32 s70, v251\n"
"s_add_u32 s36, s21, 2\n"
"s_cmp_gt_i32 s70, s36\n"
"s_cbranch_scc0 LP3_n10\n"
"s_waitcnt lgkmcnt(4)\n"
"v_mfma_f32_32x32x16_bf16 v[112:127], a[64:67], v[160:163], v[112:127]\n"
"ds_read_b128 v[152:155], v7 offset:10240\n"
"v_mfma_f32_32x32x16_bf16 v[128:143], a[208:211], v[160:163], v[128:143]\n"
"buffer_load_dwordx4 v[168:171], v5, s[24:27], s62 offen sc1\n"
"s_add_u32 s62, s62, 4096\n"
"s_waitcnt lgkmcnt(4)\n"
"v_mfma_f32_32x32x16_bf16 v[112:127], a[68:71], v[164:167], v[112:127]\n"
"ds_read_b128 v[156:159], v7 offset:10752\n"
"v_mfma_f32_32x32x16_bf16 v[128:143], a[212:215], v[164:167], v[128:143]\n"
"buffer_load_dwordx4 v[172:175], v5, s[24:27], s62 offen sc1\n"
"s_add_u32 s62, s62, 12288\n"
"s_waitcnt lgkmcnt(4)\n"
"v_mfma_f32_32x32x16_bf16 v[112:127], a[72:75], v[144:147], v[112:127]\n"
"ds_read_b128 v[160:163], v7 offset:11264\n"
"v_mfma_f32_32x32x16_bf16 v[128:143], a[216:219], v[144:147], v[128:143]\n"
"buffer_load_dwordx4 v[176:179], v5, s[24:27], s62 offen sc1\n"
"s_add_u32 s62, s62, 4096\n"
"s_waitcnt lgkmcnt(4)\n"
"v_mfma_f32_32x32x16_bf16 v[112:127], a[76:79], v[148:151], v[112:127]\n"
"ds_read_b128 v[164:167], v7 offset:11776\n"
"v_mfma_f32_32x32x16_bf16 v[128:143], a[220:223], v[148:151], v[128:143]\n"
"buffer_load_dwordx4 v[180:183], v5, s[24:27], s62 offen sc1\n"
"s_add_u32 s62, s62, 12288\n"
"s_waitcnt lgkmcnt(3)\n"
"v_mfma_f32_32x32x16_bf16 v[112:127], a[80:83], v[152:155], v[112:127]\n"
"ds_read_b128 v[144:147], v7 offset:12288\n"
"v_mfma_f32_32x32x16_bf16 v[128:143], a[224:227], v[152:155], v[128:143]\n"
"buffer_load_dwordx4 v[204:207], v11, s[24:27], s63 offen sc1\n"
"s_waitcnt lgkmcnt(3)\n"
"v_mfma_f32_32x32x16_bf16 v[112:127], a[84:87], v[156:159], v[112:127]\n"
"ds_read_b128 v[148:151], v7 offset:12800\n"
"v_mfma_f32_32x32x16_bf16 v[128:143], a[228:231], v[156:159], v[128:143]\n"
"buffer_load_dwordx4 v[208:211], v12, s[24:27], s63 offen sc1\n"
"s_waitcnt lgkmcnt(3)\n"
"v_mfma_f32_32x32x16_bf16 v[112:127], a[88:91], v[160:163], v[112:127]\n"
"ds_read_b128 v[152:155], v7 offset:13312\n"
"v_mfma_f32_32x32x16_bf16 v[128:143], a[232:235], v[160:163], v[128:143]\n"
"buffer_load_dwordx4 v[184:187], v5, s[24:27], s62 offen sc1\n"
"s_add_u32 s62, s62, 4096\n"
"s_waitcnt lgkmcnt(3)\n"
"v_mfma_f32_32x32x16_bf16 v[112:127], a[92:95], v[164:167], v[112:127]\n"
"ds_read_b128 v[156:159], v7 offset:13824\n"
"v_mfma_f32_32x32x16_bf16 v[128:143], a[236:239], v[164:167], v[128:143]\n"
"buffer_load_dwordx4 v[188:191], v5, s[24:27], s62 offen sc1\n"
"s_add_u32 s62, s62, 12288\n"
"s_waitcnt lgkmcnt(3)\n"
"v_mfma_f32_32x32x16_bf16 v[112:127], a[96:99], v[144:147], v[112:127]\n"
"ds_read_b128 v[160:163], v7 offset:14336\n"
"ds_read_b64 v[212:213], v6 offset:0\n"
"ds_read_b64 v[214:215], v6 offset:128\n"
"ds_read_b64 v[216:217], v6 offset:256\n"
"v_mfma_f32_32x32x16_bf16 v[128:143], a[240:243], v[144:147], v[128:143]\n"
"buffer_load_dwordx4 v[192:195], v5, s[24:27], s62 offen sc1\n"
"s_add_u32 s62, s62, 4096\n"
"s_waitcnt lgkmcnt(6)\n"
"v_mfma_f32_32x32x16_bf16 v[112:127], a[100:103], v[148:151], v[112:127]\n"
"ds_read_b128 v[164:167], v7 offset:14848\n"
"ds_read_b64 v[218:219], v6 offset:384\n"
"ds_read_b64 v[220:221], v6 offset:512\n"
"ds_read_b64 v[222:223], v6 offset:640\n"
"v_mfma_f32_32x32x16_bf16 v[128:143], a[244:247], v[148:151], v[128:143]\n"
"buffer_load_dwordx4 v[196:199], v5, s[24:27], s62 offen sc1\n"
"s_add_u32 s62, s62, 12288\n"
"s_waitcnt lgkmcnt(9)\n"
"v_mfma_f32_32x32x16_bf16 v[112:127], a[104:107], v[152:155], v[112:127]\n"
"ds_read_b128 v[144:147], v7 offset:15360\n"
"ds_read_b64 v[224:225], v6 offset:768\n"
"ds_read_b64 v[226:227], v6 offset:896\n"
"ds_read_b64 v[228:229], v6 offset:1024\n"
"v_mfma_f32_32x32x16_bf16 v[128:143], a[248:251], v[152:155], v[128:143]\n"
"buffer_load_dwordx4 v[200:203], v5, s[24:27], s62 offen sc1\n"
"s_add_u32 s62, s62, 4096\n"
"s_waitcnt lgkmcnt(12)\n"
"v_mfma_f32_32x32x16_bf16 v[112:127], a[108:111], v[156:159], v[112:127]\n"
"ds_read_b128 v[148:151], v7 offset:15872\n"
"v_mfma_f32_32x32x16_bf16 v[128:143], a[252:255], v[156:159], v[128:143]\n"
"s_waitcnt lgkmcnt(12)\n"
"s_waitcnt lgkmcnt(2)\n"
"v_mfma_f32_32x32x16_bf16 v[112:127], a[112:115], v[160:163], v[112:127]\n"
"ds_read_b128 v[152:155], v7 offset:16384\n"
"v_add_f32 v212, v212, v214\n"
"v_add_f32 v213, v213, v215\n"
"v_add_f32 v212, v212, v216\n"
"v_add_f32 v213, v213, v217\n"
"v_mfma_f32_32x32x16_bf16 v[128:143], v[16:19], v[160:163], v[128:143]\n"
"v_add_f32 v212, v212, v218\n"
"v_add_f32 v213, v213, v219\n"
"v_add_f32 v212, v212, v220\n"
"v_add_f32 v213, v213, v221\n"
"s_waitcnt lgkmcnt(9)\n"
"v_mfma_f32_32x32x16_bf16 v[112:127], a[116:119], v[164:167], v[112:127]\n"
"ds_read_b128 v[156:159], v7 offset:16896\n"
"v_add_f32 v212, v212, v222\n"
"v_add_f32 v213, v213, v223\n"
"v_add_f32 v212, v212, v224\n"
"v_add_f32 v213, v213, v225\n"
"v_mfma_f32_32x32x16_bf16 v[128:143], v[20:23], v[164:167], v[128:143]\n"
"v_add_f32 v212, v212, v226\n"
"v_add_f32 v213, v213, v227\n"
"v_add_f32 v212, v212, v228\n"
"v_add_f32 v213, v213, v229\n"
"s_waitcnt lgkmcnt(6)\n"
"s_waitcnt vmcnt(5)\n"
"v_mfma_f32_32x32x16_bf16 v[112:127], a[120:123], v[144:147], v[112:127]\n"
"ds_read_b128 v[160:163], v7 offset:17408\n"
"v_mov_b32 v214, v212\n"
"v_mov_b32 v215, v213\n"
"s_nop 0\n"
"v_permlane32_swap_b32 v212, v214\n"
"v_mfma_f32_32x32x16_bf16 v[128:143], v[24:27], v[144:147], v[128:143]\n"
"ds_write_b128 v8, v[168:171] offset:0\n"
"v_permlane32_swap_b32 v213, v215\n"
"v_add_f32 v212, v212, v214\n"
"v_add_f32 v213, v213, v215\n"
"s_waitcnt lgkmcnt(4)\n"
"v_mfma_f32_32x32x16_bf16 v[112:127], a[124:127], v[148:151], v[112:127]\n"
"ds_read_b128 v[164:167], v7 offset:17920\n"
"v_mul_f32 v242, 0x3ae38e39, v212\n"
"v_mul_f32 v213, 0x3ae38e39, v213\n"
"v_fma_f32 v213, -v242, v242, v213\n"
"v_max_f32 v213, 0, v213\n"
"v_mfma_f32_32x32x16_bf16 v[128:143], v[28:31], v[148:151], v[128:143]\n"
"ds_write_b128 v8, v[172:175] offset:2048\n"
"v_add_f32 v244, 0x3727c5ac, v213\n"
"v_rsq_f32 v243, v244\n"
"s_nop 0\n"
"s_waitcnt lgkmcnt(5)\n"
"v_mfma_f32_32x32x16_bf16 v[112:127], a[128:131], v[152:155], v[112:127]\n"
"v_mul_f32 v243, s57, v243\n"
"v_mfma_f32_32x32x16_bf16 v[128:143], v[32:35], v[152:155], v[128:143]\n"
"ds_write_b128 v8, v[176:179] offset:4096\n"
"s_waitcnt lgkmcnt(5)\n"
"v_mfma_f32_32x32x16_bf16 v[112:127], a[132:135], v[156:159], v[112:127]\n"
"v_mfma_f32_32x32x16_bf16 v[128:143], v[36:39], v[156:159], v[128:143]\n"
"ds_write_b128 v8, v[180:183] offset:6144\n"
"s_waitcnt lgkmcnt(5)\n"
"v_mfma_f32_32x32x16_bf16 v[112:127], a[136:139], v[160:163], v[112:127]\n"
"v_mfma_f32_32x32x16_bf16 v[128:143], v[40:43], v[160:163], v[128:143]\n"
"ds_write_b128 v13, v[204:207]\n"
"s_waitcnt lgkmcnt(4)\n"
"v_mfma_f32_32x32x16_bf16 v[112:127], a[140:143], v[164:167], v[112:127]\n"
"v_mfma_f32_32x32x16_bf16 v[128:143], v[44:47], v[164:167], v[128:143]\n"
"s_mov_b64 exec, 0xff\n"
"ds_write_b128 v13, v[208:211] offset:1024\n"
"s_mov_b64 exec, -1\n"
"v_mov_b32 v245, v242\n"
"v_mov_b32 v246, v243\n"
"v_mov_b32 v247, v244\n"
"v_mov_b32 v9, v8\n"
"v_xor_b32 v6, 8192, v6\n"
"v_xor_b32 v13, 8192, v13\n"
"v_add_u32 v7, -36864, v7\n"
"v_add_u32 v8, 36864, v8\n"
"s_add_u32 s21, s21, 1\n"
"s_waitcnt lgkmcnt(0)\n"
"s_barrier\n"
"s_branch LP3_01\n"
"LP3_n10:\n"
"s_waitcnt lgkmcnt(4)\n"
"v_mfma_f32_32x32x16_bf16 v[112:127], a[64:67], v[160:163], v[112:127]\n"
"ds_read_b128 v[152:155], v7 offset:10240\n"
"v_mfma_f32_32x32x16_bf16 v[128:143], a[208:211], v[160:163], v[128:143]\n"
"s_waitcnt lgkmcnt(4)\n"
"v_mfma_f32_32x32x16_bf16 v[112:127], a[68:71], v[164:167], v[112:127]\n"
"ds_read_b128 v[156:159], v7 offset:10752\n"
"v_mfma_f32_32x32x16_bf16 v[128:143], a[212:215], v[164:167], v[128:143]\n"
"s_waitcnt lgkmcnt(4)\n"
"v_mfma_f32_32x32x16_bf16 v[112:127], a[72:75], v[144:147], v[112:127]\n"
"ds_read_b128 v[160:163], v7 offset:11264\n"
"v_mfma_f32_32x32x16_bf16 v[128:143], a[216:219], v[144:147], v[128:143]\n"
"s_waitcnt lgkmcnt(4)\n"
"v_mfma_f32_32x32x16_bf16 v[112:127], a[76:79], v[148:151], v[112:127]\n"
"ds_read_b128 v[164:167], v7 offset:11776\n"
"v_mfma_f32_32x32x16_bf16 v[128:143], a[220:223], v[148:151], v[128:143]\n"
"s_waitcnt lgkmcnt(3)\n"
"v_mfma_f32_32x32x16_bf16 v[112:127], a[80:83], v[152:155], v[112:127]\n"
"ds_read_b128 v[144:147], v7 offset:12288\n"
"v_mfma_f32_32x32x16_bf16 v[128:143], a[224:227], v[152:155], v[128:143]\n"
"buffer_load_dword v10, v4, s[24:27], s71 offen sc1\n"
"s_waitcnt lgkmcnt(3)\n"
"v_mfma_f32_32x32x16_bf16 v[112:127], a[84:87], v[156:159], v[112:127]\n"
"ds_read_b128 v[148:151], v7 offset:12800\n"
"v_mfma_f32_32x32x16_bf16 v[128:143], a[228:231], v[156:159], v[128:143]\n"
"s_waitcnt lgkmcnt(3)\n"
"v_mfma_f32_32x32x16_bf16 v[112:127], a[88:91], v[160:163], v[112:127]\n"
"ds_read_b128 v[152:155], v7 offset:13312\n"
"v_mfma_f32_32x32x16_bf16 v[128:143], a[232:235], v[160:163], v[128:143]\n"
"s_waitcnt lgkmcnt(3)\n"
"v_mfma_f32_32x32x16_bf16 v[112:127], a[92:95], v[164:167], v[112:127]\n"
"ds_read_b128 v[156:159], v7 offset:13824\n"
"v_mfma_f32_32x32x16_bf16 v[128:143], a[236:239], v[164:167], v[128:143]\n"
"s_waitcnt lgkmcnt(3)\n"
"v_mfma_f32_32x32x16_bf16 v[112:127], a[96:99], v[144:147], v[112:127]\n"
"ds_read_b128 v[160:163], v7 offset:14336\n"
"ds_read_b64 v[212:213], v6 offset:0\n"
"ds_read_b64 v[214:215], v6 offset:128\n"
"ds_read_b64 v[216:217], v6 offset:256\n"
"v_mfma_f32_32x32x16_bf16 v[128:143], a[240:243], v[144:147], v[128:143]\n"
"s_waitcnt lgkmcnt(6)\n"
"v_mfma_f32_32x32x16_bf16 v[112:127], a[100:103], v[148:151], v[112:127]\n"
"ds_read_b128 v[164:167], v7 offset:14848\n"
"ds_read_b64 v[218:219], v6 offset:384\n"
"ds_read_b64 v[220:221], v6 offset:512\n"
"ds_read_b64 v[222:223], v6 offset:640\n"
"v_mfma_f32_32x32x16_bf16 v[128:143], a[244:247], v[148:151], v[128:143]\n"
"s_waitcnt lgkmcnt(9)\n"
"v_mfma_f32_32x32x16_bf16 v[112:127], a[104:107], v[152:155], v[112:127]\n"
"ds_read_b128 v[144:147], v7 offset:15360\n"
"ds_read_b64 v[224:225], v6 offset:768\n"
"ds_read_b64 v[226:227], v6 offset:896\n"
"ds_read_b64 v[228:229], v6 offset:1024\n"
"v_mfma_f32_32x32x16_bf16 v[128:143], a[248:251], v[152:155], v[128:143]\n"
"s_waitcnt lgkmcnt(12)\n"
"v_mfma_f32_32x32x16_bf16 v[112:127], a[108:111], v[156:159], v[112:127]\n"
"ds_read_b128 v[148:151], v7 offset:15872\n"
"v_mfma_f32_32x32x16_bf16 v[128:143], a[252:255], v[156:159], v[128:143]\n"
"s_waitcnt lgkmcnt(12)\n"
"s_waitcnt lgkmcnt(2)\n"
"s_cmp_lg_u32 s20, 3\n"
"s_cbranch_scc1 Lpb_146\n"
"s_waitcnt vmcnt(0)\n"
"s_cmp_lg_u32 s20, 3\n"
"s_cbranch_scc1 Lav_147\n"
"v_cmp_ge_i32 vcc, s59, v10\n"
"s_cbranch_vccnz Lav_147\n"
"s_add_u32 s41, s59, 1\n"
"v_cmp_ge_i32 vcc, s41, v10\n"
"s_cbranch_vccnz Lav_148\n"
"s_add_u32 s41, s41, 1\n"
"Lav_148:\n"
"s_max_i32 s70, s70, s41\n"
"v_mov_b32 v251, s70\n"
"ds_write_b32 v15, v251\n"
"Lav_147:\n"
"Lpb_146:\n"
"v_mfma_f32_32x32x16_bf16 v[112:127], a[112:115], v[160:163], v[112:127]\n"
"ds_read_b128 v[152:155], v7 offset:16384\n"
"v_add_f32 v212, v212, v214\n"
"v_add_f32 v213, v213, v215\n"
"v_add_f32 v212, v212, v216\n"
"v_add_f32 v213, v213, v217\n"
"v_mfma_f32_32x32x16_bf16 v[128:143], v[16:19], v[160:163], v[128:143]\n"
"v_add_f32 v212, v212, v218\n"
"v_add_f32 v213, v213, v219\n"
"v_add_f32 v212, v212, v220\n"
"v_add_f32 v213, v213, v221\n"
"s_waitcnt lgkmcnt(9)\n"
"v_mfma_f32_32x32x16_bf16 v[112:127], a[116:119], v[164:167], v[112:127]\n"
"ds_read_b128 v[156:159], v7 offset:16896\n"
"ds_read_b32 v251, v15\n"
"v_add_f32 v212, v212, v222\n"
"v_add_f32 v213, v213, v223\n"
"v_add_f32 v212, v212, v224\n"
"v_mfma_f32_32x32x16_bf16 v[128:143], v[20:23], v[164:167], v[128:143]\n"
"v_add_f32 v213, v213, v225\n"
"v_add_f32 v212, v212, v226\n"
"v_add_f32 v213, v213, v227\n"
"v_add_f32 v212, v212, v228\n"
"s_waitcnt lgkmcnt(0)\n"
"v_readfirstlane_b32 s70, v251\n"
"s_cmp_gt_i32 s70, s59\n"
"s_cbranch_scc0 LP3_x10\n"
"s_waitcnt lgkmcnt(7)\n"
"v_mfma_f32_32x32x16_bf16 v[112:127], a[120:123], v[144:147], v[112:127]\n"
"ds_read_b128 v[160:163], v7 offset:17408\n"
"buffer_load_dwordx4 v[168:171], v5, s[24:27], s62 offen sc1\n"
"s_add_u32 s62, s62, 4096\n"
"v_add_f32 v213, v213, v229\n"
"v_mov_b32 v214, v212\n"
"v_mfma_f32_32x32x16_bf16 v[128:143], v[24:27], v[144:147], v[128:143]\n"
"buffer_load_dwordx4 v[172:175], v5, s[24:27], s62 offen sc1\n"
"s_add_u32 s62, s62, 12288\n"
"v_mov_b32 v215, v213\n"
"s_nop 0\n"
"s_waitcnt lgkmcnt(4)\n"
"v_mfma_f32_32x32x16_bf16 v[112:127], a[124:127], v[148:151], v[112:127]\n"
"ds_read_b128 v[164:167], v7 offset:17920\n"
"buffer_load_dwordx4 v[176:179], v5, s[24:27], s62 offen sc1\n"
"s_add_u32 s62, s62, 4096\n"
"v_permlane32_swap_b32 v212, v214\n"
"v_mfma_f32_32x32x16_bf16 v[128:143], v[28:31], v[148:151], v[128:143]\n"
"buffer_load_dwordx4 v[180:183], v5, s[24:27], s62 offen sc1\n"
"s_add_u32 s62, s62, 12288\n"
"v_permlane32_swap_b32 v213, v215\n"
"v_add_f32 v212, v212, v214\n"
"s_waitcnt lgkmcnt(4)\n"
"v_mfma_f32_32x32x16_bf16 v[112:127], a[128:131], v[152:155], v[112:127]\n"
"buffer_load_dwordx4 v[204:207], v11, s[24:27], s63 offen sc1\n"
"v_add_f32 v213, v213, v215\n"
"v_mul_f32 v242, 0x3ae38e39, v212\n"
"v_mul_f32 v213, 0x3ae38e39, v213\n"
"v_fma_f32 v213, -v242, v242, v213\n"
"v_mfma_f32_32x32x16_bf16 v[128:143], v[32:35], v[152:155], v[128:143]\n"
"buffer_load_dwordx4 v[208:211], v12, s[24:27], s63 offen sc1\n"
"v_max_f32 v213, 0, v213\n"
"v_add_f32 v244, 0x3727c5ac, v213\n"
"v_rsq_f32 v243, v244\n"
"s_waitcnt lgkmcnt(3)\n"
"v_mfma_f32_32x32x16_bf16 v[112:127], a[132:135], v[156:159], v[112:127]\n"
"buffer_load_dwordx4 v[184:187], v5, s[24:27], s62 offen sc1\n"
"s_add_u32 s62, s62, 4096\n"
"s_nop 0\n"
"v_mul_f32 v243, s57, v243\n"
"v_mfma_f32_32x32x16_bf16 v[128:143], v[36:39], v[156:159], v[128:143]\n"
"buffer_load_dwordx4 v[188:191], v5, s[24:27], s62 offen sc1\n"
"s_add_u32 s62, s62, 12288\n"
"s_waitcnt lgkmcnt(1)\n"
"v_mfma_f32_32x32x16_bf16 v[112:127], a[136:139], v[160:163], v[112:127]\n"
"buffer_load_dwordx4 v[192:195], v5, s[24:27], s62 offen sc1\n"
"s_add_u32 s62, s62, 4096\n"
"v_mfma_f32_32x32x16_bf16 v[128:143], v[40:43], v[160:163], v[128:143]\n"
"buffer_load_dwordx4 v[196:199], v5, s[24:27], s62 offen sc1\n"
"s_add_u32 s62, s62, 12288\n"
"s_waitcnt lgkmcnt(0)\n"
"v_mfma_f32_32x32x16_bf16 v[112:127], a[140:143], v[164:167], v[112:127]\n"
"buffer_load_dwordx4 v[200:203], v5, s[24:27], s62 offen sc1\n"
"s_add_u32 s62, s62, 4096\n"
"v_mfma_f32_32x32x16_bf16 v[128:143], v[44:47], v[164:167], v[128:143]\n"
"s_nop 8\n"
"s_branch LP3_late1\n"
"LP3_x10:\n"
"s_waitcnt lgkmcnt(7)\n"
"v_mfma_f32_32x32x16_bf16 v[112:127], a[120:123], v[144:147], v[112:127]\n"
"ds_read_b128 v[160:163], v7 offset:17408\n"
"v_add_f32 v213, v213, v229\n"
"v_mov_b32 v214, v212\n"
"v_mov_b32 v215, v213\n"
"s_nop 0\n"
"v_mfma_f32_32x32x16_bf16 v[128:143], v[24:27], v[144:147], v[128:143]\n"
"v_permlane32_swap_b32 v212, v214\n"
"v_permlane32_swap_b32 v213, v215\n"
"v_add_f32 v212, v212, v214\n"
"s_waitcnt lgkmcnt(4)\n"
"v_mfma_f32_32x32x16_bf16 v[112:127], a[124:127], v[148:151], v[112:127]\n"
"ds_read_b128 v[164:167], v7 offset:17920\n"
"v_add_f32 v213, v213, v215\n"
"v_mul_f32 v242, 0x3ae38e39, v212\n"
"v_mul_f32 v213, 0x3ae38e39, v213\n"
"v_fma_f32 v213, -v242, v242, v213\n"
"v_mfma_f32_32x32x16_bf16 v[128:143], v[28:31], v[148:151], v[128:143]\n"
"v_max_f32 v213, 0, v213\n"
"v_add_f32 v244, 0x3727c5ac, v213\n"
"v_rsq_f32 v243, v244\n"
"s_nop 0\n"
"s_waitcnt lgkmcnt(4)\n"
"v_mfma_f32_32x32x16_bf16 v[112:127], a[128:131], v[152:155], v[112:127]\n"
"v_mul_f32 v243, s57, v243\n"
"v_mfma_f32_32x32x16_bf16 v[128:143], v[32:35], v[152:155], v[128:143]\n"
"s_waitcnt lgkmcnt(3)\n"
"v_mfma_f32_32x32x16_bf16 v[112:127], a[132:135], v[156:159], v[112:127]\n"
"v_mfma_f32_32x32x16_bf16 v[128:143], v[36:39], v[156:159], v[128:143]\n"
"s_waitcnt lgkmcnt(1)\n"
"v_mfma_f32_32x32x16_bf16 v[112:127], a[136:139], v[160:163], v[112:127]\n"
"v_mfma_f32_32x32x16_bf16 v[128:143], v[40:43], v[160:163], v[128:143]\n"
"buffer_load_dword v10, v4, s[24:27], s71 offen sc1\n"
"s_waitcnt lgkmcnt(0)\n"
"v_mfma_f32_32x32x16_bf16 v[112:127], a[140:143], v[164:167], v[112:127]\n"
"v_mfma_f32_32x32x16_bf16 v[128:143], v[44:47], v[164:167], v[128:143]\n"
"s_nop 9\n"
"s_branch LP3_fin1\n"
"LP3_11:\n"
"ds_read_b128 v[144:147], v7 offset:0\n"
"ds_read_b128 v[148:151], v7 offset:512\n"
"ds_read_b128 v[152:155], v7 offset:1024\n"
"ds_read_b128 v[156:159], v7 offset:1536\n"
"s_mov_b32 s67, s56\n"
"s_waitcnt lgkmcnt(3)\n"
"v_mfma_f32_32x32x16_bf16 v[112:127], a[0:3], v[144:147], 0\n"
"ds_read_b128 v[160:163], v7 offset:2048\n"
"s_add_u32 s59, s21, 1\n"
"s_lshl_b32 s62, s59, 17\n"
"s_add_u32 s62, s62, 157286400\n"
"s_lshr_b32 s43, s62, 8\n"
"v_mfma_f32_32x32x16_bf16 v[128:143], a[144:147], v[144:147], 0\n"
"s_xor_b32 s43, s43, s22\n"
"s_not_b32 s43, s43\n"
"s_and_b32 s43, s43, 8192\n"
"s_or_b32 s62, s62, s43\n"
"s_waitcnt lgkmcnt(3)\n"
"v_mfma_f32_32x32x16_bf16 v[112:127], a[4:7], v[148:151], v[112:127]\n"
"ds_read_b128 v[164:167], v7 offset:2560\n"
"s_add_u32 s63, s62, 81920\n"
"s_mul_i32 s42, s21, 86\n"
"s_lshr_b32 s42, s42, 8\n"
"s_mul_i32 s56, s21, 655360\n"
"v_mfma_f32_32x32x16_bf16 v[128:143], a[148:151], v[148:151], v[128:143]\n"
"s_mul_i32 s42, s42, 131072\n"
"s_add_u32 s56, s56, s42\n"
"s_add_u32 s56, s56, 102760448\n"
"s_add_u32 s56, s56, s66\n"
"s_waitcnt lgkmcnt(3)\n"
"v_mfma_f32_32x32x16_bf16 v[112:127], a[8:11], v[152:155], v[112:127]\n"
"ds_read_b128 v[144:147], v7 offset:3072\n"
"s_lshr_b32 s43, s56, 8\n"
"s_xor_b32 s43, s43, s22\n"
"s_not_b32 s43, s43\n"
"s_and_b32 s43, s43, 8192\n"
"v_mfma_f32_32x32x16_bf16 v[128:143], a[152:155], v[152:155], v[128:143]\n"
"s_or_b32 s56, s56, s43\n"
"v_fma_f32 v80, -v245, v48, v80\n"
"v_fma_f32 v81, -v245, v49, v81\n"
"v_fma_f32 v96, -v245, v64, v96\n"
"s_waitcnt lgkmcnt(3)\n"
"s_waitcnt vmcnt(0)\n"
"v_mfma_f32_32x32x16_bf16 v[112:127], a[12:15], v[156:159], v[112:127]\n"
"ds_read_b128 v[148:151], v7 offset:3584\n"
"v_fma_f32 v97, -v245, v65, v97\n"
"v_mul_f32 v238, v246, v80\n"
"v_mul_f32 v239, v246, v81\n"
"v_mfma_f32_32x32x16_bf16 v[128:143], a[156:159], v[156:159], v[128:143]\n"
"ds_write_b128 v9, v[184:187] offset:8192\n"
"v_exp_f32 v238, v238\n"
"v_exp_f32 v239, v239\n"
"s_waitcnt lgkmcnt(4)\n"
"v_mfma_f32_32x32x16_bf16 v[112:127], a[16:19], v[160:163], v[112:127]\n"
"ds_read_b128 v[152:155], v7 offset:4096\n"
"buffer_load_dword v10, v4, s[24:27], s71 offen sc1\n"
"v_mul_f32 v80, v80, v96\n"
"v_mul_f32 v81, v81, v97\n"
"v_fma_f32 v238, v238, v247, v247\n"
"v_mfma_f32_32x32x16_bf16 v[128:143], a[160:163], v[160:163], v[128:143]\n"
"ds_write_b128 v9, v[188:191] offset:10240\n"
"v_fma_f32 v239, v239, v247, v247\n"
"v_rcp_f32 v238, v238\n"
"s_waitcnt lgkmcnt(5)\n"
"v_mfma_f32_32x32x16_bf16 v[112:127], a[20:23], v[164:167], v[112:127]\n"
"ds_read_b128 v[156:159], v7 offset:4608\n"
"v_rcp_f32 v239, v239\n"
"v_mul_f32 v80, v80, v238\n"
"v_mul_f32 v81, v81, v239\n"
"v_mfma_f32_32x32x16_bf16 v[128:143], a[164:167], v[164:167], v[128:143]\n"
"ds_write_b128 v9, v[192:195] offset:12288\n"
"v_cvt_pk_bf16_f32 v230, v80, v81\n"
"v_fma_f32 v82, -v245, v50, v82\n"
"v_fma_f32 v83, -v245, v51, v83\n"
"s_waitcnt lgkmcnt(6)\n"
"v_mfma_f32_32x32x16_bf16 v[112:127], a[24:27], v[144:147], v[112:127]\n"
"ds_read_b128 v[160:163], v7 offset:5120\n"
"v_fma_f32 v98, -v245, v66, v98\n"
"v_fma_f32 v99, -v245, v67, v99\n"
"v_mul_f32 v238, v246, v82\n"
"v_mul_f32 v239, v246, v83\n"
"v_mfma_f32_32x32x16_bf16 v[128:143], a[168:171], v[144:147], v[128:143]\n"
"ds_write_b128 v9, v[196:199] offset:14336\n"
"v_exp_f32 v238, v238\n"
"v_exp_f32 v239, v239\n"
"s_waitcnt lgkmcnt(7)\n"
"v_mfma_f32_32x32x16_bf16 v[112:127], a[28:31], v[148:151], v[112:127]\n"
"ds_read_b128 v[164:167], v7 offset:5632\n"
"v_mul_f32 v82, v82, v98\n"
"v_mul_f32 v83, v83, v99\n"
"v_fma_f32 v238, v238, v247, v247\n"
"v_fma_f32 v239, v239, v247, v247\n"
"v_mfma_f32_32x32x16_bf16 v[128:143], a[172:175], v[148:151], v[128:143]\n"
"ds_write_b128 v9, v[200:203] offset:16384\n"
"v_rcp_f32 v238, v238\n"
"v_rcp_f32 v239, v239\n"
"s_waitcnt lgkmcnt(7)\n"
"v_mfma_f32_32x32x16_bf16 v[112:127], a[32:35], v[152:155], v[112:127]\n"
"ds_read_b128 v[144:147], v7 offset:6144\n"
"v_mul_f32 v82, v82, v238\n"
"v_mul_f32 v83, v83, v239\n"
"v_cvt_pk_bf16_f32 v231, v82, v83\n"
"v_fma_f32 v84, -v245, v52, v84\n"
"v_mfma_f32_32x32x16_bf16 v[128:143], a[176:179], v[152:155], v[128:143]\n"
"v_fma_f32 v85, -v245, v53, v85\n"
"v_fma_f32 v100, -v245, v68, v100\n"
"v_fma_f32 v101, -v245, v69, v101\n"
"v_mul_f32 v238, v246, v84\n"
"s_waitcnt lgkmcnt(6)\n"
"v_mfma_f32_32x32x16_bf16 v[112:127], a[36:39], v[156:159], v[112:127]\n"
"ds_read_b128 v[148:151], v7 offset:6656\n"
"v_mul_f32 v239, v246, v85\n"
"v_exp_f32 v238, v238\n"
"v_exp_f32 v239, v239\n"
"v_mfma_f32_32x32x16_bf16 v[128:143], a[180:183], v[156:159], v[128:143]\n"
"v_mul_f32 v84, v84, v100\n"
"v_mul_f32 v85, v85, v101\n"
"v_fma_f32 v238, v238, v247, v247\n"
"v_fma_f32 v239, v239, v247, v247\n"
"s_waitcnt lgkmcnt(5)\n"
"v_mfma_f32_32x32x16_bf16 v[112:127], a[40:43], v[160:163], v[112:127]\n"
"ds_read_b128 v[152:155], v7 offset:7168\n"
"v_rcp_f32 v238, v238\n"
"v_rcp_f32 v239, v239\n"
"v_mul_f32 v84, v84, v238\n"
"v_mfma_f32_32x32x16_bf16 v[128:143], a[184:187], v[160:163], v[128:143]\n"
"v_mul_f32 v85, v85, v239\n"
"v_cvt_pk_bf16_f32 v232, v84, v85\n"
"v_fma_f32 v86, -v245, v54, v86\n"
"v_fma_f32 v87, -v245, v55, v87\n"
"s_waitcnt lgkmcnt(4)\n"
"s_waitcnt lgkmcnt(4)\n"
"s_barrier\n"
"v_mfma_f32_32x32x16_bf16 v[112:127], a[44:47], v[164:167], v[112:127]\n"
"ds_read_b128 v[156:159], v7 offset:7680\n"
"v_fma_f32 v102, -v245, v70, v102\n"
"v_fma_f32 v103, -v245, v71, v103\n"
"v_mul_f32 v238, v246, v86\n"
"v_mul_f32 v239, v246, v87\n"
"v_mfma_f32_32x32x16_bf16 v[128:143], a[188:191], v[164:167], v[128:143]\n"
"v_exp_f32 v238, v238\n"
"v_exp_f32 v239, v239\n"
"v_mul_f32 v86, v86, v102\n"
"s_waitcnt lgkmcnt(3)\n"
"v_mfma_f32_32x32x16_bf16 v[112:127], a[48:51], v[144:147], v[112:127]\n"
"ds_read_b128 v[160:163], v7 offset:8192\n"
"v_mul_f32 v87, v87, v103\n"
"v_fma_f32 v238, v238, v247, v247\n"
"v_fma_f32 v239, v239, v247, v247\n"
"v_mfma_f32_32x32x16_bf16 v[128:143], a[192:195], v[144:147], v[128:143]\n"
"v_rcp_f32 v238, v238\n"
"v_rcp_f32 v239, v239\n"
"v_mul_f32 v86, v86, v238\n"
"s_waitcnt lgkmcnt(3)\n"
"v_mfma_f32_32x32x16_bf16 v[112:127], a[52:55], v[148:151], v[112:127]\n"
"ds_read_b128 v[164:167], v7 offset:8704\n"
"v_mul_f32 v87, v87, v239\n"
"v_cvt_pk_bf16_f32 v233, v86, v87\n"
"buffer_store_dwordx4 v[230:233], v14, s[24:27], s67 offen offset:0 sc1\n"
"v_fma_f32 v88, -v245, v56, v88\n"
"v_mfma_f32_32x32x16_bf16 v[128:143], a[196:199], v[148:151], v[128:143]\n"
"v_fma_f32 v89, -v245, v57, v89\n"
"v_fma_f32 v104, -v245, v72, v104\n"
"v_fma_f32 v105, -v245, v73, v105\n"
"v_mul_f32 v238, v246, v88\n"
"s_waitcnt lgkmcnt(3)\n"
"s_cmp_lg_u32 s20, 3\n"
"s_cbranch_scc1 Lpb_149\n"
"s_waitcnt vmcnt(1)\n"
"s_cmp_lg_u32 s20, 3\n"
"s_cbranch_scc1 Lav_150\n"
"v_cmp_ge_i32 vcc, s59, v10\n"
"s_cbranch_vccnz Lav_150\n"
"s_add_u32 s41, s59, 1\n"
"v_cmp_ge_i32 vcc, s41, v10\n"
"s_cbranch_vccnz Lav_151\n"
"s_add_u32 s41, s41, 1\n"
"Lav_151:\n"
"s_max_i32 s70, s70, s41\n"
"v_mov_b32 v251, s70\n"
"ds_write_b32 v15, v251\n"
"Lav_150:\n"
"Lpb_149:\n"
"v_mfma_f32_32x32x16_bf16 v[112:127], a[56:59], v[152:155], v[112:127]\n"
"ds_read_b128 v[144:147], v7 offset:9216\n"
"v_mul_f32 v239, v246, v89\n"
"v_exp_f32 v238, v238\n"
"v_exp_f32 v239, v239\n"
"v_mfma_f32_32x32x16_bf16 v[128:143], a[200:203], v[152:155], v[128:143]\n"
"v_mul_f32 v88, v88, v104\n"
"v_mul_f32 v89, v89, v105\n"
"v_fma_f32 v238, v238, v247, v247\n"
"v_fma_f32 v239, v239, v247, v247\n"
"s_waitcnt lgkmcnt(3)\n"
"v_mfma_f32_32x32x16_bf16 v[112:127], a[60:63], v[156:159], v[112:127]\n"
"ds_read_b128 v[148:151], v7 offset:9728\n"
"ds_read_b32 v251, v15\n"
"v_rcp_f32 v238, v238\n"
"v_rcp_f32 v239, v239\n"
"v_mfma_f32_32x32x16_bf16 v[128:143], a[204:207], v[156:159], v[128:143]\n"
"v_mul_f32 v88, v88, v238\n"
"v_mul_f32 v89, v89, v239\n"
"v_cvt_pk_bf16_f32 v234, v88, v89\n"
"v_fma_f32 v90, -v245, v58, v90\n"
"s_waitcnt lgkmcnt(0)\n"
"v_readfirstlane_b32 s70, v251\n"
"s_add_u32 s36, s21, 2\n"
"s_cmp_gt_i32 s70, s36\n"
"s_cbranch_scc0 LP3_n11\n"
"s_waitcnt lgkmcnt(4)\n"
"v_mfma_f32_32x32x16_bf16 v[112:127], a[64:67], v[160:163], v[112:127]\n"
"ds_read_b128 v[152:155], v7 offset:10240\n"
"v_fma_f32 v91, -v245, v59, v91\n"
"v_fma_f32 v106, -v245, v74, v106\n"
"v_fma_f32 v107, -v245, v75, v107\n"
"v_mul_f32 v238, v246, v90\n"
"v_mfma_f32_32x32x16_bf16 v[128:143], a[208:211], v[160:163], v[128:143]\n"
"buffer_load_dwordx4 v[168:171], v5, s[24:27], s62 offen sc1\n"
"s_add_u32 s62, s62, 4096\n"
"v_mul_f32 v239, v246, v91\n"
"v_exp_f32 v238, v238\n"
"s_waitcnt lgkmcnt(4)\n"
"v_mfma_f32_32x32x16_bf16 v[112:127], a[68:71], v[164:167], v[112:127]\n"
"ds_read_b128 v[156:159], v7 offset:10752\n"
"v_exp_f32 v239, v239\n"
"v_mul_f32 v90, v90, v106\n"
"v_mul_f32 v91, v91, v107\n"
"v_mfma_f32_32x32x16_bf16 v[128:143], a[212:215], v[164:167], v[128:143]\n"
"buffer_load_dwordx4 v[172:175], v5, s[24:27], s62 offen sc1\n"
"s_add_u32 s62, s62, 12288\n"
"v_fma_f32 v238, v238, v247, v247\n"
"v_fma_f32 v239, v239, v247, v247\n"
"s_waitcnt lgkmcnt(4)\n"
"v_mfma_f32_32x32x16_bf16 v[112:127], a[72:75], v[144:147], v[112:127]\n"
"ds_read_b128 v[160:163], v7 offset:11264\n"
"v_rcp_f32 v238, v238\n"
"v_rcp_f32 v239, v239\n"
"v_mul_f32 v90, v90, v238\n"
"v_mfma_f32_32x32x16_bf16 v[128:143], a[216:219], v[144:147], v[128:143]\n"
"buffer_load_dwordx4 v[176:179], v5, s[24:27], s62 offen sc1\n"
"s_add_u32 s62, s62, 4096\n"
"v_mul_f32 v91, v91, v239\n"
"v_cvt_pk_bf16_f32 v235, v90, v91\n"
"s_waitcnt lgkmcnt(4)\n"
"v_mfma_f32_32x32x16_bf16 v[112:127], a[76:79], v[148:151], v[112:127]\n"
"ds_read_b128 v[164:167], v7 offset:11776\n"
"v_fma_f32 v92, -v245, v60, v92\n"
"v_fma_f32 v93, -v245, v61, v93\n"
"v_fma_f32 v108, -v245, v76, v108\n"
"v_fma_f32 v109, -v245, v77, v109\n"
"v_mfma_f32_32x32x16_bf16 v[128:143], a[220:223], v[148:151], v[128:143]\n"
"buffer_load_dwordx4 v[180:183], v5, s[24:27], s62 offen sc1\n"
"s_add_u32 s62, s62, 12288\n"
"v_mul_f32 v238, v246, v92\n"
"v_mul_f32 v239, v246, v93\n"
"s_waitcnt lgkmcnt(3)\n"
"v_mfma_f32_32x32x16_bf16 v[112:127], a[80:83], v[152:155], v[112:127]\n"
"ds_read_b128 v[144:147], v7 offset:12288\n"
"v_exp_f32 v238, v238\n"
"v_exp_f32 v239, v239\n"
"v_mul_f32 v92, v92, v108\n"
"v_mfma_f32_32x32x16_bf16 v[128:143], a[224:227], v[152:155], v[128:143]\n"
"buffer_load_dwordx4 v[204:207], v11, s[24:27], s63 offen sc1\n"
"v_mul_f32 v93, v93, v109\n"
"v_fma_f32 v238, v238, v247, v247\n"
"v_fma_f32 v239, v239, v247, v247\n"
"s_waitcnt lgkmcnt(3)\n"
"v_mfma_f32_32x32x16_bf16 v[112:127], a[84:87], v[156:159], v[112:127]\n"
"ds_read_b128 v[148:151], v7 offset:12800\n"
"v_rcp_f32 v238, v238\n"
"v_rcp_f32 v239, v239\n"
"v_mul_f32 v92, v92, v238\n"
"v_mfma_f32_32x32x16_bf16 v[128:143], a[228:231], v[156:159], v[128:143]\n"
"buffer_load_dwordx4 v[208:211], v12, s[24:27], s63 offen sc1\n"
"v_mul_f32 v93, v93, v239\n"
"v_cvt_pk_bf16_f32 v236, v92, v93\n"
"v_fma_f32 v94, -v245, v62, v94\n"
"s_waitcnt lgkmcnt(3)\n"
"v_mfma_f32_32x32x16_bf16 v[112:127], a[88:91], v[160:163], v[112:127]\n"
"ds_read_b128 v[152:155], v7 offset:13312\n"
"v_fma_f32 v95, -v245, v63, v95\n"
"v_fma_f32 v110, -v245, v78, v110\n"
"v_fma_f32 v111, -v245, v79, v111\n"
"v_mul_f32 v238, v246, v94\n"
"v_mfma_f32_32x32x16_bf16 v[128:143], a[232:235], v[160:163], v[128:143]\n"
"buffer_load_dwordx4 v[184:187], v5, s[24:27], s62 offen sc1\n"
"s_add_u32 s62, s62, 4096\n"
"v_mul_f32 v239, v246, v95\n"
"v_exp_f32 v238, v238\n"
"s_waitcnt lgkmcnt(3)\n"
"v_mfma_f32_32x32x16_bf16 v[112:127], a[92:95], v[164:167], v[112:127]\n"
"ds_read_b128 v[156:159], v7 offset:13824\n"
"v_exp_f32 v239, v239\n"
"v_mul_f32 v94, v94, v110\n"
"v_mul_f32 v95, v95, v111\n"
"v_mfma_f32_32x32x16_bf16 v[128:143], a[236:239], v[164:167], v[128:143]\n"
"buffer_load_dwordx4 v[188:191], v5, s[24:27], s62 offen sc1\n"
"s_add_u32 s62, s62, 12288\n"
"v_fma_f32 v238, v238, v247, v247\n"
"v_fma_f32 v239, v239, v247, v247\n"
"s_waitcnt lgkmcnt(3)\n"
"v_mfma_f32_32x32x16_bf16 v[112:127], a[96:99], v[144:147], v[112:127]\n"
"ds_read_b128 v[160:163], v7 offset:14336\n"
"ds_read_b64 v[212:213], v6 offset:0\n"
"ds_read_b64 v[214:215], v6 offset:128\n"
"ds_read_b64 v[216:217], v6 offset:256\n"
"v_mfma_f32_32x32x16_bf16 v[128:143], a[240:243], v[144:147], v[128:143]\n"
"buffer_load_dwordx4 v[192:195], v5, s[24:27], s62 offen sc1\n"
"s_add_u32 s62, s62, 4096\n"
"v_rcp_f32 v238, v238\n"
"v_rcp_f32 v239, v239\n"
"s_waitcnt lgkmcnt(6)\n"
"v_mfma_f32_32x32x16_bf16 v[112:127], a[100:103], v[148:151], v[112:127]\n"
"ds_read_b128 v[164:167], v7 offset:14848\n"
"ds_read_b64 v[218:219], v6 offset:384\n"
"ds_read_b64 v[220:221], v6 offset:512\n"
"ds_read_b64 v[222:223], v6 offset:640\n"
"v_mul_f32 v94, v94, v238\n"
"v_mfma_f32_32x32x16_bf16 v[128:143], a[244:247], v[148:151], v[128:143]\n"
"buffer_load_dwordx4 v[196:199], v5, s[24:27], s62 offen sc1\n"
"s_add_u32 s62, s62, 12288\n"
"v_mul_f32 v95, v95, v239\n"
"v_cvt_pk_bf16_f32 v237, v94, v95\n"
"s_waitcnt lgkmcnt(9)\n"
"v_mfma_f32_32x32x16_bf16 v[112:127], a[104:107], v[152:155], v[112:127]\n"
"ds_read_b128 v[144:147], v7 offset:15360\n"
"ds_read_b64 v[224:225], v6 offset:768\n"
"ds_read_b64 v[226:227], v6 offset:896\n"
"ds_read_b64 v[228:229], v6 offset:1024\n"
"buffer_store_dwordx4 v[234:237], v14, s[24:27], s67 offen offset:256 sc1\n"
"v_mfma_f32_32x32x16_bf16 v[128:143], a[248:251], v[152:155], v[128:143]\n"
"buffer_load_dwordx4 v[200:203], v5, s[24:27], s62 offen sc1\n"
"s_add_u32 s62, s62, 4096\n"
"s_waitcnt lgkmcnt(12)\n"
"v_mfma_f32_32x32x16_bf16 v[112:127], a[108:111], v[156:159], v[112:127]\n"
"ds_read_b128 v[148:151], v7 offset:15872\n"
"v_mfma_f32_32x32x16_bf16 v[128:143], a[252:255], v[156:159], v[128:143]\n"
"s_waitcnt lgkmcnt(12)\n"
"s_waitcnt lgkmcnt(2)\n"
"v_mfma_f32_32x32x16_bf16 v[112:127], a[112:115], v[160:163], v[112:127]\n"
"ds_read_b128 v[152:155], v7 offset:16384\n"
"v_add_f32 v212, v212, v214\n"
"v_add_f32 v213, v213, v215\n"
"v_add_f32 v212, v212, v216\n"
"v_add_f32 v213, v213, v217\n"
"v_mfma_f32_32x32x16_bf16 v[128:143], v[16:19], v[160:163], v[128:143]\n"
"v_add_f32 v212, v212, v218\n"
"v_add_f32 v213, v213, v219\n"
"v_add_f32 v212, v212, v220\n"
"v_add_f32 v213, v213, v221\n"
"s_waitcnt lgkmcnt(9)\n"
"v_mfma_f32_32x32x16_bf16 v[112:127], a[116:119], v[164:167], v[112:127]\n"
"ds_read_b128 v[156:159], v7 offset:16896\n"
"v_add_f32 v212, v212, v222\n"
"v_add_f32 v213, v213, v223\n"
"v_add_f32 v212, v212, v224\n"
"v_add_f32 v213, v213, v225\n"
"v_mfma_f32_32x32x16_bf16 v[128:143], v[20:23], v[164:167], v[128:143]\n"
"v_add_f32 v212, v212, v226\n"
"v_add_f32 v213, v213, v227\n"
"v_add_f32 v212, v212, v228\n"
"v_add_f32 v213, v213, v229\n"
"s_waitcnt lgkmcnt(6)\n"
"s_waitcnt vmcnt(6)\n"
"v_mfma_f32_32x32x16_bf16 v[112:127], a[120:123], v[144:147], v[112:127]\n"
"ds_read_b128 v[160:163], v7 offset:17408\n"
"v_mov_b32 v214, v212\n"
"v_mov_b32 v215, v213\n"
"s_nop 0\n"
"v_permlane32_swap_b32 v212, v214\n"
"v_mfma_f32_32x32x16_bf16 v[128:143], v[24:27], v[144:147], v[128:143]\n"
"ds_write_b128 v8, v[168:171] offset:0\n"
"v_permlane32_swap_b32 v213, v215\n"
"v_add_f32 v212, v212, v214\n"
"v_add_f32 v213, v213, v215\n"
"s_waitcnt lgkmcnt(4)\n"
"v_mfma_f32_32x32x16_bf16 v[112:127], a[124:127], v[148:151], v[112:127]\n"
"ds_read_b128 v[164:167], v7 offset:17920\n"
"v_mul_f32 v242, 0x3ae38e39, v212\n"
"v_mul_f32 v213, 0x3ae38e39, v213\n"
"v_fma_f32 v213, -v242, v242, v213\n"
"v_max_f32 v213, 0, v213\n"
"v_mfma_f32_32x32x16_bf16 v[128:143], v[28:31], v[148:151], v[128:143]\n"
"ds_write_b128 v8, v[172:175] offset:2048\n"
"v_add_f32 v244, 0x3727c5ac, v213\n"
"v_rsq_f32 v243, v244\n"
"s_nop 0\n"
"s_waitcnt lgkmcnt(5)\n"
"s_waitcnt vmcnt(1)\n"
"s_add_u32 s65, s21, -1\n"
"s_and_b32 s41, s65, 1\n"
"s_lshl_b32 s41, s41, 2\n"
"s_add_u32 s41, s41, 98240\n"
"v_mov_b32 v248, s41\n"
"v_mov_b32 v250, 1\n"
"s_mov_b64 exec, 1\n"
"ds_add_rtn_u32 v249, v248, v250\n"
"s_mov_b64 exec, -1\n"
"v_mfma_f32_32x32x16_bf16 v[112:127], a[128:131], v[152:155], v[112:127]\n"
"v_mul_f32 v243, s57, v243\n"
"v_mfma_f32_32x32x16_bf16 v[128:143], v[32:35], v[152:155], v[128:143]\n"
"ds_write_b128 v8, v[176:179] offset:4096\n"
"s_waitcnt lgkmcnt(6)\n"
"v_mfma_f32_32x32x16_bf16 v[112:127], a[132:135], v[156:159], v[112:127]\n"
"v_mfma_f32_32x32x16_bf16 v[128:143], v[36:39], v[156:159], v[128:143]\n"
"ds_write_b128 v8, v[180:183] offset:6144\n"
"s_waitcnt lgkmcnt(6)\n"
"s_waitcnt lgkmcnt(2)\n"
"v_readfirstlane_b32 s41, v249\n"
"s_and_b32 s41, s41, 3\n"
"s_cmp_lg_u32 s41, 3\n"
"s_cbranch_scc1 Lar_152\n"
"s_lshl_b32 s43, s19, 7\n"
"s_xor_b32 s43, s43, s22\n"
"s_xor_b32 s43, s43, 1302528\n"
"s_add_u32 s42, s65, 1\n"
"v_mov_b32 v3, s42\n"
"buffer_store_dword v3, v2, s[24:27], s43 offen sc1\n"
"Lpf_153:\n"
"Lar_152:\n"
"v_mfma_f32_32x32x16_bf16 v[112:127], a[136:139], v[160:163], v[112:127]\n"
"v_mfma_f32_32x32x16_bf16 v[128:143], v[40:43], v[160:163], v[128:143]\n"
"ds_write_b128 v13, v[204:207]\n"
"s_waitcnt lgkmcnt(5)\n"
"v_mfma_f32_32x32x16_bf16 v[112:127], a[140:143], v[164:167], v[112:127]\n"
"v_mfma_f32_32x32x16_bf16 v[128:143], v[44:47], v[164:167], v[128:143]\n"
"s_mov_b64 exec, 0xff\n"
"ds_write_b128 v13, v[208:211] offset:1024\n"
"s_mov_b64 exec, -1\n"
"v_mov_b32 v245, v242\n"
"v_mov_b32 v246, v243\n"
"v_mov_b32 v247, v244\n"
"v_mov_b32 v9, v8\n"
"v_xor_b32 v6, 8192, v6\n"
"v_xor_b32 v13, 8192, v13\n"
"v_add_u32 v7, -36864, v7\n"
"v_add_u32 v8, 36864, v8\n"
"s_add_u32 s21, s21, 1\n"
"s_waitcnt lgkmcnt(0)\n"
"s_barrier\n"
"s_branch LP3_01\n"
"LP3_n11:\n"
"s_waitcnt lgkmcnt(4)\n"
"v_mfma_f32_32x32x16_bf16 v[112:127], a[64:67], v[160:163], v[112:127]\n"
"ds_read_b128 v[152:155], v7 offset:10240\n"
"v_fma_f32 v91, -v245, v59, v91\n"
"v_fma_f32 v106, -v245, v74, v106\n"
"v_fma_f32 v107, -v245, v75, v107\n"
"v_mul_f32 v238, v246, v90\n"
"v_mfma_f32_32x32x16_bf16 v[128:143], a[208:211], v[160:163], v[128:143]\n"
"v_mul_f32 v239, v246, v91\n"
"v_exp_f32 v238, v238\n"
"v_exp_f32 v239, v239\n"
"s_waitcnt lgkmcnt(4)\n"
"v_mfma_f32_32x32x16_bf16 v[112:127], a[68:71], v[164:167], v[112:127]\n"
"ds_read_b128 v[156:159], v7 offset:10752\n"
"v_mul_f32 v90, v90, v106\n"
"v_mul_f32 v91, v91, v107\n"
"v_fma_f32 v238, v238, v247, v247\n"
"v_fma_f32 v239, v239, v247, v247\n"
"v_mfma_f32_32x32x16_bf16 v[128:143], a[212:215], v[164:167], v[128:143]\n"
"v_rcp_f32 v238, v238\n"
"v_rcp_f32 v239, v239\n"
"v_mul_f32 v90, v90, v238\n"
"s_waitcnt lgkmcnt(4)\n"
"v_mfma_f32_32x32x16_bf16 v[112:127], a[72:75], v[144:147], v[112:127]\n"
"ds_read_b128 v[160:163], v7 offset:11264\n"
"v_mul_f32 v91, v91, v239\n"
"v_cvt_pk_bf16_f32 v235, v90, v91\n"
"v_fma_f32 v92, -v245, v60, v92\n"
"v_fma_f32 v93, -v245, v61, v93\n"
"v_mfma_f32_32x32x16_bf16 v[128:143], a[216:219], v[144:147], v[128:143]\n"
"v_fma_f32 v108, -v245, v76, v108\n"
"v_fma_f32 v109, -v245, v77, v109\n"
"v_mul_f32 v238, v246, v92\n"
"v_mul_f32 v239, v246, v93\n"
"s_waitcnt lgkmcnt(4)\n"
"v_mfma_f32_32x32x16_bf16 v[112:127], a[76:79], v[148:151], v[112:127]\n"
"ds_read_b128 v[164:167], v7 offset:11776\n"
"v_exp_f32 v238, v238\n"
"v_exp_f32 v239, v239\n"
"v_mul_f32 v92, v92, v108\n"
"v_mfma_f32_32x32x16_bf16 v[128:143], a[220:223], v[148:151], v[128:143]\n"
"v_mul_f32 v93, v93, v109\n"
"v_fma_f32 v238, v238, v247, v247\n"
"v_fma_f32 v239, v239, v247, v247\n"
"v_rcp_f32 v238, v238\n"
"s_waitcnt lgkmcnt(3)\n"
"v_mfma_f32_32x32x16_bf16 v[112:127], a[80:83], v[152:155], v[112:127]\n"
"ds_read_b128 v[144:147], v7 offset:12288\n"
"v_rcp_f32 v239, v239\n"
"v_mul_f32 v92, v92, v238\n"
"v_mul_f32 v93, v93, v239\n"
"v_mfma_f32_32x32x16_bf16 v[128:143], a[224:227], v[152:155], v[128:143]\n"
"buffer_load_dword v10, v4, s[24:27], s71 offen sc1\n"
"v_cvt_pk_bf16_f32 v236, v92, v93\n"
"v_fma_f32 v94, -v245, v62, v94\n"
"v_fma_f32 v95, -v245, v63, v95\n"
"s_waitcnt lgkmcnt(3)\n"
"v_mfma_f32_32x32x16_bf16 v[112:127], a[84:87], v[156:159], v[112:127]\n"
"ds_read_b128 v[148:151], v7 offset:12800\n"
"v_fma_f32 v110, -v245, v78, v110\n"
"v_fma_f32 v111, -v245, v79, v111\n"
"v_mul_f32 v238, v246, v94\n"
"v_mul_f32 v239, v246, v95\n"
"v_mfma_f32_32x32x16_bf16 v[128:143], a[228:231], v[156:159], v[128:143]\n"
"v_exp_f32 v238, v238\n"
"v_exp_f32 v239, v239\n"
"v_mul_f32 v94, v94, v110\n"
"s_waitcnt lgkmcnt(3)\n"
"v_mfma_f32_32x32x16_bf16 v[112:127], a[88:91], v[160:163], v[112:127]\n"
"ds_read_b128 v[152:155], v7 offset:13312\n"
"v_mul_f32 v95, v95, v111\n"
"v_fma_f32 v238, v238, v247, v247\n"
"v_fma_f32 v239, v239, v247, v247\n"
"v_mfma_f32_32x32x16_bf16 v[128:143], a[232:235], v[160:163], v[128:143]\n"
"v_rcp_f32 v238, v238\n"
"v_rcp_f32 v239, v239\n"
"v_mul_f32 v94, v94, v238\n"
"s_waitcnt lgkmcnt(3)\n"
"v_mfma_f32_32x32x16_bf16 v[112:127], a[92:95], v[164:167], v[112:127]\n"
"ds_read_b128 v[156:159], v7 offset:13824\n"
"v_mul_f32 v95, v95, v239\n"
"v_cvt_pk_bf16_f32 v237, v94, v95\n"
"buffer_store_dwordx4 v[234:237], v14, s[24:27], s67 offen offset:256 sc1\n"
"v_mfma_f32_32x32x16_bf16 v[128:143], a[236:239], v[164:167], v[128:143]\n"
"s_waitcnt lgkmcnt(3)\n"
"v_mfma_f32_32x32x16_bf16 v[112:127], a[96:99], v[144:147], v[112:127]\n"
"ds_read_b128 v[160:163], v7 offset:14336\n"
"ds_read_b64 v[212:213], v6 offset:0\n"
"ds_read_b64 v[214:215], v6 offset:128\n"
"ds_read_b64 v[216:217], v6 offset:256\n"
"v_mfma_f32_32x32x16_bf16 v[128:143], a[240:243], v[144:147], v[128:143]\n"
"s_waitcnt lgkmcnt(6)\n"
"v_mfma_f32_32x32x16_bf16 v[112:127], a[100:103], v[148:151], v[112:127]\n"
"ds_read_b128 v[164:167], v7 offset:14848\n"
"ds_read_b64 v[218:219], v6 offset:384\n"
"ds_read_b64 v[220:221], v6 offset:512\n"
"ds_read_b64 v[222:223], v6 offset:640\n"
"v_mfma_f32_32x32x16_bf16 v[128:143], a[244:247], v[148:151], v[128:143]\n"
"s_waitcnt lgkmcnt(9)\n"
"v_mfma_f32_32x32x16_bf16 v[112:127], a[104:107], v[152:155], v[112:127]\n"
"ds_read_b128 v[144:147], v7 offset:15360\n"
"ds_read_b64 v[224:225], v6 offset:768\n"
"ds_read_b64 v[226:227], v6 offset:896\n"
"ds_read_b64 v[228:229], v6 offset:1024\n"
"v_mfma_f32_32x32x16_bf16 v[128:143], a[248:251], v[152:155], v[128:143]\n"
"s_waitcnt lgkmcnt(12)\n"
"v_mfma_f32_32x32x16_bf16 v[112:127], a[108:111], v[156:159], v[112:127]\n"
"ds_read_b128 v[148:151], v7 offset:15872\n"
"v_mfma_f32_32x32x16_bf16 v[128:143], a[252:255], v[156:159], v[128:143]\n"
"s_waitcnt lgkmcnt(12)\n"
"s_waitcnt lgkmcnt(2)\n"
"s_cmp_lg_u32 s20, 3\n"
"s_cbranch_scc1 Lpb_154\n"
"s_waitcnt vmcnt(1)\n"
"s_cmp_lg_u32 s20, 3\n"
"s_cbranch_scc1 Lav_155\n"
"v_cmp_ge_i32 vcc, s59, v10\n"
"s_cbranch_vccnz Lav_155\n"
"s_add_u32 s41, s59, 1\n"
"v_cmp_ge_i32 vcc, s41, v10\n"
"s_cbranch_vccnz Lav_156\n"
"s_add_u32 s41, s41, 1\n"
"Lav_156:\n"
"s_max_i32 s70, s70, s41\n"
"v_mov_b32 v251, s70\n"
"ds_write_b32 v15, v251\n"
"Lav_155:\n"
"Lpb_154:\n"
"v_mfma_f32_32x32x16_bf16 v[112:127], a[112:115], v[160:163], v[112:127]\n"
"ds_read_b128 v[152:155], v7 offset:16384\n"
"v_add_f32 v212, v212, v214\n"
"v_add_f32 v213, v213, v215\n"
"v_add_f32 v212, v212, v216\n"
"v_add_f32 v213, v213, v217\n"
"v_mfma_f32_32x32x16_bf16 v[128:143], v[16:19], v[160:163], v[128:143]\n"
"v_add_f32 v212, v212, v218\n"
"v_add_f32 v213, v213, v219\n"
"v_add_f32 v212, v212, v220\n"
"v_add_f32 v213, v213, v221\n"
"s_waitcnt lgkmcnt(9)\n"
"v_mfma_f32_32x32x16_bf16 v[112:127], a[116:119], v[164:167], v[112:127]\n"
"ds_read_b128 v[156:159], v7 offset:16896\n"
"ds_read_b32 v251, v15\n"
"v_add_f32 v212, v212, v222\n"
"v_add_f32 v213, v213, v223\n"
"v_add_f32 v212, v212, v224\n"
"v_mfma_f32_32x32x16_bf16 v[128:143], v[20:23], v[164:167], v[128:143]\n"
"v_add_f32 v213, v213, v225\n"
"v_add_f32 v212, v212, v226\n"
"v_add_f32 v213, v213, v227\n"
"v_add_f32 v212, v212, v228\n"
"s_waitcnt lgkmcnt(0)\n"
"v_readfirstlane_b32 s70, v251\n"
"s_cmp_gt_i32 s70, s59\n"
"s_cbranch_scc0 LP3_x11\n"
"s_waitcnt lgkmcnt(7)\n"
"v_mfma_f32_32x32x16_bf16 v[112:127], a[120:123], v[144:147], v[112:127]\n"
"ds_read_b128 v[160:163], v7 offset:17408\n"
"buffer_load_dwordx4 v[168:171], v5, s[24:27], s62 offen sc1\n"
"s_add_u32 s62, s62, 4096\n"
"v_add_f32 v213, v213, v229\n"
"v_mov_b32 v214, v212\n"
"v_mfma_f32_32x32x16_bf16 v[128:143], v[24:27], v[144:147], v[128:143]\n"
"buffer_load_dwordx4 v[172:175], v5, s[24:27], s62 offen sc1\n"
"s_add_u32 s62, s62, 12288\n"
"v_mov_b32 v215, v213\n"
"s_nop 0\n"
"s_waitcnt lgkmcnt(4)\n"
"v_mfma_f32_32x32x16_bf16 v[112:127], a[124:127], v[148:151], v[112:127]\n"
"ds_read_b128 v[164:167], v7 offset:17920\n"
"buffer_load_dwordx4 v[176:179], v5, s[24:27], s62 offen sc1\n"
"s_add_u32 s62, s62, 4096\n"
"v_permlane32_swap_b32 v212, v214\n"
"v_mfma_f32_32x32x16_bf16 v[128:143], v[28:31], v[148:151], v[128:143]\n"
"buffer_load_dwordx4 v[180:183], v5, s[24:27], s62 offen sc1\n"
"s_add_u32 s62, s62, 12288\n"
"v_permlane32_swap_b32 v213, v215\n"
"v_add_f32 v212, v212, v214\n"
"s_waitcnt lgkmcnt(4)\n"
"s_waitcnt vmcnt(4)\n"
"s_add_u32 s65, s21, -1\n"
"s_and_b32 s41, s65, 1\n"
"s_lshl_b32 s41, s41, 2\n"
"s_add_u32 s41, s41, 98240\n"
"v_mov_b32 v248, s41\n"
"v_mov_b32 v250, 1\n"
"s_mov_b64 exec, 1\n"
"ds_add_rtn_u32 v249, v248, v250\n"
"s_mov_b64 exec, -1\n"
"v_mfma_f32_32x32x16_bf16 v[112:127], a[128:131], v[152:155], v[112:127]\n"
"buffer_load_dwordx4 v[204:207], v11, s[24:27], s63 offen sc1\n"
"v_add_f32 v213, v213, v215\n"
"v_mul_f32 v242, 0x3ae38e39, v212\n"
"v_mul_f32 v213, 0x3ae38e39, v213\n"
"v_fma_f32 v213, -v242, v242, v213\n"
"v_mfma_f32_32x32x16_bf16 v[128:143], v[32:35], v[152:155], v[128:143]\n"
"buffer_load_dwordx4 v[208:211], v12, s[24:27], s63 offen sc1\n"
"v_max_f32 v213, 0, v213\n"
"v_add_f32 v244, 0x3727c5ac, v213\n"
"v_rsq_f32 v243, v244\n"
"s_waitcnt lgkmcnt(4)\n"
"v_mfma_f32_32x32x16_bf16 v[112:127], a[132:135], v[156:159], v[112:127]\n"
"buffer_load_dwordx4 v[184:187], v5, s[24:27], s62 offen sc1\n"
"s_add_u32 s62, s62, 4096\n"
"s_nop 0\n"
"v_mul_f32 v243, s57, v243\n"
"v_mfma_f32_32x32x16_bf16 v[128:143], v[36:39], v[156:159], v[128:143]\n"
"buffer_load_dwordx4 v[188:191], v5, s[24:27], s62 offen sc1\n"
"s_add_u32 s62, s62, 12288\n"
"s_waitcnt lgkmcnt(2)\n"
"s_waitcnt lgkmcnt(0)\n"
"v_readfirstlane_b32 s41, v249\n"
"s_and_b32 s41, s41, 3\n"
"s_cmp_lg_u32 s41, 3\n"
"s_cbranch_scc1 Lar_157\n"
"s_lshl_b32 s43, s19, 7\n"
"s_xor_b32 s43, s43, s22\n"
"s_xor_b32 s43, s43, 1302528\n"
"s_add_u32 s42, s65, 1\n"
"v_mov_b32 v3, s42\n"
"buffer_store_dword v3, v2, s[24:27], s43 offen sc1\n"
"Lpf_158:\n"
"Lar_157:\n"
"v_mfma_f32_32x32x16_bf16 v[112:127], a[136:139], v[160:163], v[112:127]\n"
"buffer_load_dwordx4 v[192:195], v5, s[24:27], s62 offen sc1\n"
"s_add_u32 s62, s62, 4096\n"
"v_mfma_f32_32x32x16_bf16 v[128:143], v[40:43], v[160:163], v[128:143]\n"
"buffer_load_dwordx4 v[196:199], v5, s[24:27], s62 offen sc1\n"
"s_add_u32 s62, s62, 12288\n"
"s_waitcnt lgkmcnt(1)\n"
"v_mfma_f32_32x32x16_bf16 v[112:127], a[140:143], v[164:167], v[112:127]\n"
"buffer_load_dwordx4 v[200:203], v5, s[24:27], s62 offen sc1\n"
"s_add_u32 s62, s62, 4096\n"
"v_mfma_f32_32x32x16_bf16 v[128:143], v[44:47], v[164:167], v[128:143]\n"
"s_nop 8\n"
"s_branch LP3_late1\n"
"LP3_x11:\n"
"s_waitcnt lgkmcnt(7)\n"
"v_mfma_f32_32x32x16_bf16 v[112:127], a[120:123], v[144:147], v[112:127]\n"
"ds_read_b128 v[160:163], v7 offset:17408\n"
"v_add_f32 v213, v213, v229\n"
"v_mov_b32 v214, v212\n"
"v_mov_b32 v215, v213\n"
"s_nop 0\n"
"v_mfma_f32_32x32x16_bf16 v[128:143], v[24:27], v[144:147], v[128:143]\n"
"v_permlane32_swap_b32 v212, v214\n"
"v_permlane32_swap_b32 v213, v215\n"
"v_add_f32 v212, v212, v214\n"
"s_waitcnt lgkmcnt(4)\n"
"v_mfma_f32_32x32x16_bf16 v[112:127], a[124:127], v[148:151], v[112:127]\n"
"ds_read_b128 v[164:167], v7 offset:17920\n"
"v_add_f32 v213, v213, v215\n"
"v_mul_f32 v242, 0x3ae38e39, v212\n"
"v_mul_f32 v213, 0x3ae38e39, v213\n"
"v_fma_f32 v213, -v242, v242, v213\n"
"v_mfma_f32_32x32x16_bf16 v[128:143], v[28:31], v[148:151], v[128:143]\n"
"v_max_f32 v213, 0, v213\n"
"v_add_f32 v244, 0x3727c5ac, v213\n"
"v_rsq_f32 v243, v244\n"
"s_nop 0\n"
"s_waitcnt lgkmcnt(4)\n"
"s_waitcnt vmcnt(0)\n"
"s_add_u32 s65, s21, -1\n"
"s_and_b32 s41, s65, 1\n"
"s_lshl_b32 s41, s41, 2\n"
"s_add_u32 s41, s41, 98240\n"
"v_mov_b32 v248, s41\n"
"v_mov_b32 v250, 1\n"
"s_mov_b64 exec, 1\n"
"ds_add_rtn_u32 v249, v248, v250\n"
"s_mov_b64 exec, -1\n"
"v_mfma_f32_32x32x16_bf16 v[112:127], a[128:131], v[152:155], v[112:127]\n"
"v_mul_f32 v243, s57, v243\n"
"v_mfma_f32_32x32x16_bf16 v[128:143], v[32:35], v[152:155], v[128:143]\n"
"s_waitcnt lgkmcnt(4)\n"
"v_mfma_f32_32x32x16_bf16 v[112:127], a[132:135], v[156:159], v[112:127]\n"
"v_mfma_f32_32x32x16_bf16 v[128:143], v[36:39], v[156:159], v[128:143]\n"
"s_waitcnt lgkmcnt(2)\n"
"s_waitcnt lgkmcnt(0)\n"
"v_readfirstlane_b32 s41, v249\n"
"s_and_b32 s41, s41, 3\n"
"s_cmp_lg_u32 s41, 3\n"
"s_cbranch_scc1 Lar_159\n"
"s_lshl_b32 s43, s19, 7\n"
"s_xor_b32 s43, s43, s22\n"
"s_xor_b32 s43, s43, 1302528\n"
"s_add_u32 s42, s65, 1\n"
"v_mov_b32 v3, s42\n"
"buffer_store_dword v3, v2, s[24:27], s43 offen sc1\n"
"Lpf_160:\n"
"Lar_159:\n"
"v_mfma_f32_32x32x16_bf16 v[112:127], a[136:139], v[160:163], v[112:127]\n"
"v_mfma_f32_32x32x16_bf16 v[128:143], v[40:43], v[160:163], v[128:143]\n"
"buffer_load_dword v10, v4, s[24:27], s71 offen sc1\n"
"s_waitcnt lgkmcnt(1)\n"
"v_mfma_f32_32x32x16_bf16 v[112:127], a[140:143], v[164:167], v[112:127]\n"
"v_mfma_f32_32x32x16_bf16 v[128:143], v[44:47], v[164:167], v[128:143]\n"
"s_nop 9\n"
"s_branch LP3_fin1\n"
"LP3_late0:\n"
"v_pk_fma_f32 v[80:81], v[242:243], v[48:49], v[80:81] op_sel:[0,0,0] op_sel_hi:[0,1,1] neg_lo:[1,0,0] neg_hi:[1,0,0]\n"
"v_pk_fma_f32 v[82:83], v[242:243], v[50:51], v[82:83] op_sel:[0,0,0] op_sel_hi:[0,1,1] neg_lo:[1,0,0] neg_hi:[1,0,0]\n"
"v_pk_fma_f32 v[96:97], v[242:243], v[64:65], v[96:97] op_sel:[0,0,0] op_sel_hi:[0,1,1] neg_lo:[1,0,0] neg_hi:[1,0,0]\n"
"v_pk_fma_f32 v[98:99], v[242:243], v[66:67], v[98:99] op_sel:[0,0,0] op_sel_hi:[0,1,1] neg_lo:[1,0,0] neg_hi:[1,0,0]\n"
"v_pk_mul_f32 v[238:239], v[242:243], v[80:81] op_sel:[1,0] op_sel_hi:[1,1]\n"
"v_pk_mul_f32 v[240:241], v[242:243], v[82:83] op_sel:[1,0] op_sel_hi:[1,1]\n"
"v_exp_f32 v238, v238\n"
"v_exp_f32 v240, v240\n"
"v_exp_f32 v239, v239\n"
"v_exp_f32 v241, v241\n"
"v_pk_mul_f32 v[80:81], v[80:81], v[96:97]\n"
"v_pk_mul_f32 v[82:83], v[82:83], v[98:99]\n"
"v_pk_fma_f32 v[238:239], v[244:245], v[238:239], v[244:245] op_sel:[0,0,0] op_sel_hi:[0,1,0]\n"
"v_pk_fma_f32 v[240:241], v[244:245], v[240:241], v[244:245] op_sel:[0,0,0] op_sel_hi:[0,1,0]\n"
"v_rcp_f32 v238, v238\n"
"v_rcp_f32 v240, v240\n"
"v_rcp_f32 v239, v239\n"
"v_rcp_f32 v241, v241\n"
"v_pk_mul_f32 v[80:81], v[80:81], v[238:239]\n"
"v_pk_mul_f32 v[82:83], v[82:83], v[240:241]\n"
"v_cvt_pk_bf16_f32 v230, v80, v81\n"
"v_cvt_pk_bf16_f32 v231, v82, v83\n"
"v_pk_fma_f32 v[84:85], v[242:243], v[52:53], v[84:85] op_sel:[0,0,0] op_sel_hi:[0,1,1] neg_lo:[1,0,0] neg_hi:[1,0,0]\n"
"v_pk_fma_f32 v[86:87], v[242:243], v[54:55], v[86:87] op_sel:[0,0,0] op_sel_hi:[0,1,1] neg_lo:[1,0,0] neg_hi:[1,0,0]\n"
"v_pk_fma_f32 v[100:101], v[242:243], v[68:69], v[100:101] op_sel:[0,0,0] op_sel_hi:[0,1,1] neg_lo:[1,0,0] neg_hi:[1,0,0]\n"
"v_pk_fma_f32 v[102:103], v[242:243], v[70:71], v[102:103] op_sel:[0,0,0] op_sel_hi:[0,1,1] neg_lo:[1,0,0] neg_hi:[1,0,0]\n"
"v_pk_mul_f32 v[238:239], v[242:243], v[84:85] op_sel:[1,0] op_sel_hi:[1,1]\n"
"v_pk_mul_f32 v[240:241], v[242:243], v[86:87] op_sel:[1,0] op_sel_hi:[1,1]\n"
"v_exp_f32 v238, v238\n"
"v_exp_f32 v240, v240\n"
"v_exp_f32 v239, v239\n"
"v_exp_f32 v241, v241\n"
"v_pk_mul_f32 v[84:85], v[84:85], v[100:101]\n"
"v_pk_mul_f32 v[86:87], v[86:87], v[102:103]\n"
"v_pk_fma_f32 v[238:239], v[244:245], v[238:239], v[244:245] op_sel:[0,0,0] op_sel_hi:[0,1,0]\n"
"v_pk_fma_f32 v[240:241], v[244:245], v[240:241], v[244:245] op_sel:[0,0,0] op_sel_hi:[0,1,0]\n"
"v_rcp_f32 v238, v238\n"
"v_rcp_f32 v240, v240\n"
"v_rcp_f32 v239, v239\n"
"v_rcp_f32 v241, v241\n"
"v_pk_mul_f32 v[84:85], v[84:85], v[238:239]\n"
"v_pk_mul_f32 v[86:87], v[86:87], v[240:241]\n"
"v_cvt_pk_bf16_f32 v232, v84, v85\n"
"v_cvt_pk_bf16_f32 v233, v86, v87\n"
"buffer_store_dwordx4 v[230:233], v14, s[24:27], s56 offen offset:0 sc1\n"
"v_pk_fma_f32 v[88:89], v[242:243], v[56:57], v[88:89] op_sel:[0,0,0] op_sel_hi:[0,1,1] neg_lo:[1,0,0] neg_hi:[1,0,0]\n"
"v_pk_fma_f32 v[90:91], v[242:243], v[58:59], v[90:91] op_sel:[0,0,0] op_sel_hi:[0,1,1] neg_lo:[1,0,0] neg_hi:[1,0,0]\n"
"v_pk_fma_f32 v[104:105], v[242:243], v[72:73], v[104:105] op_sel:[0,0,0] op_sel_hi:[0,1,1] neg_lo:[1,0,0] neg_hi:[1,0,0]\n"
"v_pk_fma_f32 v[106:107], v[242:243], v[74:75], v[106:107] op_sel:[0,0,0] op_sel_hi:[0,1,1] neg_lo:[1,0,0] neg_hi:[1,0,0]\n"
"s_waitcnt vmcnt(6)\n"
"v_pk_mul_f32 v[238:239], v[242:243], v[88:89] op_sel:[1,0] op_sel_hi:[1,1]\n"
"ds_write_b128 v8, v[168:171] offset:0\n"
"v_pk_mul_f32 v[240:241], v[242:243], v[90:91] op_sel:[1,0] op_sel_hi:[1,1]\n"
"v_exp_f32 v238, v238\n"
"v_exp_f32 v240, v240\n"
"v_exp_f32 v239, v239\n"
"v_exp_f32 v241, v241\n"
"v_pk_mul_f32 v[88:89], v[88:89], v[104:105]\n"
"ds_write_b128 v8, v[172:175] offset:2048\n"
"v_pk_mul_f32 v[90:91], v[90:91], v[106:107]\n"
"v_pk_fma_f32 v[238:239], v[244:245], v[238:239], v[244:245] op_sel:[0,0,0] op_sel_hi:[0,1,0]\n"
"v_pk_fma_f32 v[240:241], v[244:245], v[240:241], v[244:245] op_sel:[0,0,0] op_sel_hi:[0,1,0]\n"
"v_rcp_f32 v238, v238\n"
"v_rcp_f32 v240, v240\n"
"v_rcp_f32 v239, v239\n"
"ds_write_b128 v8, v[176:179] offset:4096\n"
"v_rcp_f32 v241, v241\n"
"v_pk_mul_f32 v[88:89], v[88:89], v[238:239]\n"
"v_pk_mul_f32 v[90:91], v[90:91], v[240:241]\n"
"v_cvt_pk_bf16_f32 v234, v88, v89\n"
"v_cvt_pk_bf16_f32 v235, v90, v91\n"
"v_pk_fma_f32 v[92:93], v[242:243], v[60:61], v[92:93] op_sel:[0,0,0] op_sel_hi:[0,1,1] neg_lo:[1,0,0] neg_hi:[1,0,0]\n"
"ds_write_b128 v8, v[180:183] offset:6144\n"
"v_pk_fma_f32 v[94:95], v[242:243], v[62:63], v[94:95] op_sel:[0,0,0] op_sel_hi:[0,1,1] neg_lo:[1,0,0] neg_hi:[1,0,0]\n"
"v_pk_fma_f32 v[108:109], v[242:243], v[76:77], v[108:109] op_sel:[0,0,0] op_sel_hi:[0,1,1] neg_lo:[1,0,0] neg_hi:[1,0,0]\n"
"v_pk_fma_f32 v[110:111], v[242:243], v[78:79], v[110:111] op_sel:[0,0,0] op_sel_hi:[0,1,1] neg_lo:[1,0,0] neg_hi:[1,0,0]\n"
"v_pk_mul_f32 v[238:239], v[242:243], v[92:93] op_sel:[1,0] op_sel_hi:[1,1]\n"
"v_pk_mul_f32 v[240:241], v[242:243], v[94:95] op_sel:[1,0] op_sel_hi:[1,1]\n"
"v_exp_f32 v238, v238\n"
"ds_write_b128 v13, v[204:207]\n"
"v_exp_f32 v240, v240\n"
"v_exp_f32 v239, v239\n"
"v_exp_f32 v241, v241\n"
"v_pk_mul_f32 v[92:93], v[92:93], v[108:109]\n"
"v_pk_mul_f32 v[94:95], v[94:95], v[110:111]\n"
"v_pk_fma_f32 v[238:239], v[244:245], v[238:239], v[244:245] op_sel:[0,0,0] op_sel_hi:[0,1,0]\n"
"s_mov_b64 exec, 0xff\n"
"ds_write_b128 v13, v[208:211] offset:1024\n"
"s_mov_b64 exec, -1\n"
"v_pk_fma_f32 v[240:241], v[244:245], v[240:241], v[244:245] op_sel:[0,0,0] op_sel_hi:[0,1,0]\n"
"v_rcp_f32 v238, v238\n"
"v_rcp_f32 v240, v240\n"
"v_rcp_f32 v239, v239\n"
"v_rcp_f32 v241, v241\n"
"v_pk_mul_f32 v[92:93], v[92:93], v[238:239]\n"
"v_pk_mul_f32 v[94:95], v[94:95], v[240:241]\n"
"v_cvt_pk_bf16_f32 v236, v92, v93\n"
"v_cvt_pk_bf16_f32 v237, v94, v95\n"
"buffer_store_dwordx4 v[234:237], v14, s[24:27], s56 offen offset:256 sc1\n"
"s_mov_b32 s68, 1\n"
"v_mov_b32 v9, v8\n"
"v_xor_b32 v6, 8192, v6\n"
"v_xor_b32 v13, 8192, v13\n"
"v_add_u32 v7, 36864, v7\n"
"v_add_u32 v8, -36864, v8\n"
"s_add_u32 s21, s21, 1\n"
"s_waitcnt lgkmcnt(0)\n"
"s_barrier\n"
"s_branch LP3_10\n"
"LP3_fin0:\n"
"v_pk_fma_f32 v[80:81], v[242:243], v[48:49], v[80:81] op_sel:[0,0,0] op_sel_hi:[0,1,1] neg_lo:[1,0,0] neg_hi:[1,0,0]\n"
"v_pk_fma_f32 v[82:83], v[242:243], v[50:51], v[82:83] op_sel:[0,0,0] op_sel_hi:[0,1,1] neg_lo:[1,0,0] neg_hi:[1,0,0]\n"
"v_pk_fma_f32 v[96:97], v[242:243], v[64:65], v[96:97] op_sel:[0,0,0] op_sel_hi:[0,1,1] neg_lo:[1,0,0] neg_hi:[1,0,0]\n"
"v_pk_fma_f32 v[98:99], v[242:243], v[66:67], v[98:99] op_sel:[0,0,0] op_sel_hi:[0,1,1] neg_lo:[1,0,0] neg_hi:[1,0,0]\n"
"v_pk_mul_f32 v[238:239], v[242:243], v[80:81] op_sel:[1,0] op_sel_hi:[1,1]\n"
"v_pk_mul_f32 v[240:241], v[242:243], v[82:83] op_sel:[1,0] op_sel_hi:[1,1]\n"
"v_exp_f32 v238, v238\n"
"v_exp_f32 v240, v240\n"
"v_exp_f32 v239, v239\n"
"v_exp_f32 v241, v241\n"
"v_pk_mul_f32 v[80:81], v[80:81], v[96:97]\n"
"v_pk_mul_f32 v[82:83], v[82:83], v[98:99]\n"
"v_pk_fma_f32 v[238:239], v[244:245], v[238:239], v[244:245] op_sel:[0,0,0] op_sel_hi:[0,1,0]\n"
"v_pk_fma_f32 v[240:241], v[244:245], v[240:241], v[244:245] op_sel:[0,0,0] op_sel_hi:[0,1,0]\n"
"v_rcp_f32 v238, v238\n"
"v_rcp_f32 v240, v240\n"
"v_rcp_f32 v239, v239\n"
"v_rcp_f32 v241, v241\n"
"v_pk_mul_f32 v[80:81], v[80:81], v[238:239]\n"
"v_pk_mul_f32 v[82:83], v[82:83], v[240:241]\n"
"v_cvt_pk_bf16_f32 v230, v80, v81\n"
"v_cvt_pk_bf16_f32 v231, v82, v83\n"
"v_pk_fma_f32 v[84:85], v[242:243], v[52:53], v[84:85] op_sel:[0,0,0] op_sel_hi:[0,1,1] neg_lo:[1,0,0] neg_hi:[1,0,0]\n"
"v_pk_fma_f32 v[86:87], v[242:243], v[54:55], v[86:87] op_sel:[0,0,0] op_sel_hi:[0,1,1] neg_lo:[1,0,0] neg_hi:[1,0,0]\n"
"v_pk_fma_f32 v[100:101], v[242:243], v[68:69], v[100:101] op_sel:[0,0,0] op_sel_hi:[0,1,1] neg_lo:[1,0,0] neg_hi:[1,0,0]\n"
"v_pk_fma_f32 v[102:103], v[242:243], v[70:71], v[102:103] op_sel:[0,0,0] op_sel_hi:[0,1,1] neg_lo:[1,0,0] neg_hi:[1,0,0]\n"
"v_pk_mul_f32 v[238:239], v[242:243], v[84:85] op_sel:[1,0] op_sel_hi:[1,1]\n"
"v_pk_mul_f32 v[240:241], v[242:243], v[86:87] op_sel:[1,0] op_sel_hi:[1,1]\n"
"v_exp_f32 v238, v238\n"
"v_exp_f32 v240, v240\n"
"v_exp_f32 v239, v239\n"
"v_exp_f32 v241, v241\n"
"v_pk_mul_f32 v[84:85], v[84:85], v[100:101]\n"
"v_pk_mul_f32 v[86:87], v[86:87], v[102:103]\n"
"v_pk_fma_f32 v[238:239], v[244:245], v[238:239], v[244:245] op_sel:[0,0,0] op_sel_hi:[0,1,0]\n"
"v_pk_fma_f32 v[240:241], v[244:245], v[240:241], v[244:245] op_sel:[0,0,0] op_sel_hi:[0,1,0]\n"
"v_rcp_f32 v238, v238\n"
"v_rcp_f32 v240, v240\n"
"v_rcp_f32 v239, v239\n"
"v_rcp_f32 v241, v241\n"
"v_pk_mul_f32 v[84:85], v[84:85], v[238:239]\n"
"v_pk_mul_f32 v[86:87], v[86:87], v[240:241]\n"
"v_cvt_pk_bf16_f32 v232, v84, v85\n"
"v_cvt_pk_bf16_f32 v233, v86, v87\n"
"buffer_store_dwordx4 v[230:233], v14, s[24:27], s56 offen offset:0 sc1\n"
"v_pk_fma_f32 v[88:89], v[242:243], v[56:57], v[88:89] op_sel:[0,0,0] op_sel_hi:[0,1,1] neg_lo:[1,0,0] neg_hi:[1,0,0]\n"
"v_pk_fma_f32 v[90:91], v[242:243], v[58:59], v[90:91] op_sel:[0,0,0] op_sel_hi:[0,1,1] neg_lo:[1,0,0] neg_hi:[1,0,0]\n"
"v_pk_fma_f32 v[104:105], v[242:243], v[72:73], v[104:105] op_sel:[0,0,0] op_sel_hi:[0,1,1] neg_lo:[1,0,0] neg_hi:[1,0,0]\n"
"v_pk_fma_f32 v[106:107], v[242:243], v[74:75], v[106:107] op_sel:[0,0,0] op_sel_hi:[0,1,1] neg_lo:[1,0,0] neg_hi:[1,0,0]\n"
"v_pk_mul_f32 v[238:239], v[242:243], v[88:89] op_sel:[1,0] op_sel_hi:[1,1]\n"
"v_pk_mul_f32 v[240:241], v[242:243], v[90:91] op_sel:[1,0] op_sel_hi:[1,1]\n"
"v_exp_f32 v238, v238\n"
"v_exp_f32 v240, v240\n"
"v_exp_f32 v239, v239\n"
"v_exp_f32 v241, v241\n"
"v_pk_mul_f32 v[88:89], v[88:89], v[104:105]\n"
"v_pk_mul_f32 v[90:91], v[90:91], v[106:107]\n"
"v_pk_fma_f32 v[238:239], v[244:245], v[238:239], v[244:245] op_sel:[0,0,0] op_sel_hi:[0,1,0]\n"
"v_pk_fma_f32 v[240:241], v[244:245], v[240:241], v[244:245] op_sel:[0,0,0] op_sel_hi:[0,1,0]\n"
"v_rcp_f32 v238, v238\n"
"v_rcp_f32 v240, v240\n"
"v_rcp_f32 v239, v239\n"
"v_rcp_f32 v241, v241\n"
"v_pk_mul_f32 v[88:89], v[88:89], v[238:239]\n"
"v_pk_mul_f32 v[90:91], v[90:91], v[240:241]\n"
"v_cvt_pk_bf16_f32 v234, v88, v89\n"
"v_cvt_pk_bf16_f32 v235, v90, v91\n"
"v_pk_fma_f32 v[92:93], v[242:243], v[60:61], v[92:93] op_sel:[0,0,0] op_sel_hi:[0,1,1] neg_lo:[1,0,0] neg_hi:[1,0,0]\n"
"v_pk_fma_f32 v[94:95], v[242:243], v[62:63], v[94:95] op_sel:[0,0,0] op_sel_hi:[0,1,1] neg_lo:[1,0,0] neg_hi:[1,0,0]\n"
"v_pk_fma_f32 v[108:109], v[242:243], v[76:77], v[108:109] op_sel:[0,0,0] op_sel_hi:[0,1,1] neg_lo:[1,0,0] neg_hi:[1,0,0]\n"
"v_pk_fma_f32 v[110:111], v[242:243], v[78:79], v[110:111] op_sel:[0,0,0] op_sel_hi:[0,1,1] neg_lo:[1,0,0] neg_hi:[1,0,0]\n"
"v_pk_mul_f32 v[238:239], v[242:243], v[92:93] op_sel:[1,0] op_sel_hi:[1,1]\n"
"v_pk_mul_f32 v[240:241], v[242:243], v[94:95] op_sel:[1,0] op_sel_hi:[1,1]\n"
"v_exp_f32 v238, v238\n"
"v_exp_f32 v240, v240\n"
"v_exp_f32 v239, v239\n"
"v_exp_f32 v241, v241\n"
"v_pk_mul_f32 v[92:93], v[92:93], v[108:109]\n"
"v_pk_mul_f32 v[94:95], v[94:95], v[110:111]\n"
"v_pk_fma_f32 v[238:239], v[244:245], v[238:239], v[244:245] op_sel:[0,0,0] op_sel_hi:[0,1,0]\n"
"v_pk_fma_f32 v[240:241], v[244:245], v[240:241], v[244:245] op_sel:[0,0,0] op_sel_hi:[0,1,0]\n"
"v_rcp_f32 v238, v238\n"
"v_rcp_f32 v240, v240\n"
"v_rcp_f32 v239, v239\n"
"v_rcp_f32 v241, v241\n"
"v_pk_mul_f32 v[92:93], v[92:93], v[238:239]\n"
"v_pk_mul_f32 v[94:95], v[94:95], v[240:241]\n"
"v_cvt_pk_bf16_f32 v236, v92, v93\n"
"v_cvt_pk_bf16_f32 v237, v94, v95\n"
"buffer_store_dwordx4 v[234:237], v14, s[24:27], s56 offen offset:256 sc1\n"
"s_waitcnt vmcnt(0)\n"
"s_add_u32 s65, s21, 0\n"
"s_and_b32 s41, s65, 1\n"
"s_lshl_b32 s41, s41, 2\n"
"s_add_u32 s41, s41, 98240\n"
"v_mov_b32 v248, s41\n"
"v_mov_b32 v250, 1\n"
"s_mov_b64 exec, 1\n"
"ds_add_rtn_u32 v249, v248, v250\n"
"s_mov_b64 exec, -1\n"
"s_waitcnt lgkmcnt(0)\n"
"v_readfirstlane_b32 s41, v249\n"
"s_and_b32 s41, s41, 3\n"
"s_cmp_lg_u32 s41, 3\n"
"s_cbranch_scc1 Lar_161\n"
"s_lshl_b32 s43, s19, 7\n"
"s_xor_b32 s43, s43, s22\n"
"s_xor_b32 s43, s43, 1302528\n"
"s_add_u32 s42, s65, 1\n"
"v_mov_b32 v3, s42\n"
"buffer_store_dword v3, v2, s[24:27], s43 offen sc1\n"
"Lpf_162:\n"
"Lar_161:\n"
"s_cmp_eq_u32 s21, 63\n"
"s_cbranch_scc1 LP3_end\n"
"LP3_next0:\n"
"s_add_u32 s59, s21, 1\n"
"s_cmp_eq_u32 s20, 3\n"
"s_cbranch_scc1 Lbk_163w\n"
"Lbk_163:\n"
"s_cmp_gt_i32 s70, s59\n"
"s_cbranch_scc1 Lbk_164\n"
"s_cmp_lg_u32 s20, 3\n"
"s_cbranch_scc1 Lbk_165\n"
"buffer_load_dword v10, v4, s[24:27], s71 offen sc1\n"
"Lbk_163w:\n"
"s_waitcnt vmcnt(0)\n"
"s_cmp_lg_u32 s20, 3\n"
"s_cbranch_scc1 Lav_166\n"
"v_cmp_ge_i32 vcc, s59, v10\n"
"s_cbranch_vccnz Lav_166\n"
"s_add_u32 s41, s59, 1\n"
"v_cmp_ge_i32 vcc, s41, v10\n"
"s_cbranch_vccnz Lav_167\n"
"s_add_u32 s41, s41, 1\n"
"Lav_167:\n"
"s_max_i32 s70, s70, s41\n"
"v_mov_b32 v251, s70\n"
"ds_write_b32 v15, v251\n"
"Lav_166:\n"
"s_cmp_gt_i32 s70, s59\n"
"s_cbranch_scc1 Lbk_164\n"
"s_sleep 1\n"
"s_memrealtime s[48:49]\n"
"s_waitcnt lgkmcnt(0)\n"
"s_sub_u32 s48, s48, s23\n"
"s_cmp_gt_i32 s48, 0\n"
"s_cbranch_scc1 LP3_end\n"
"s_branch Lbk_163\n"
"Lbk_165:\n"
"ds_read_b32 v251, v15\n"
"s_waitcnt lgkmcnt(0)\n"
"v_readfirstlane_b32 s70, v251\n"
"s_cmp_gt_i32 s70, s59\n"
"s_cbranch_scc1 Lbk_164\n"
"s_sleep 1\n"
"s_memrealtime s[48:49]\n"
"s_waitcnt lgkmcnt(0)\n"
"s_sub_u32 s48, s48, s23\n"
"s_cmp_gt_i32 s48, 0\n"
"s_cbranch_scc1 LP3_end\n"
"s_branch Lbk_165\n"
"Lbk_164:\n"
"s_lshl_b32 s62, s59, 17\n"
"s_add_u32 s62, s62, 157286400\n"
"s_lshr_b32 s43, s62, 8\n"
"s_xor_b32 s43, s43, s22\n"
"s_not_b32 s43, s43\n"
"s_and_b32 s43, s43, 8192\n"
"s_or_b32 s62, s62, s43\n"
"s_add_u32 s63, s62, 81920\n"
"buffer_load_dwordx4 v[168:171], v5, s[24:27], s62 offen sc1\n"
"s_add_u32 s62, s62, 4096\n"
"buffer_load_dwordx4 v[172:175], v5, s[24:27], s62 offen sc1\n"
"s_add_u32 s62, s62, 12288\n"
"buffer_load_dwordx4 v[176:179], v5, s[24:27], s62 offen sc1\n"
"s_add_u32 s62, s62, 4096\n"
"buffer_load_dwordx4 v[180:183], v5, s[24:27], s62 offen sc1\n"
"s_add_u32 s62, s62, 12288\n"
"buffer_load_dwordx4 v[204:207], v11, s[24:27], s63 offen sc1\n"
"buffer_load_dwordx4 v[208:211], v12, s[24:27], s63 offen sc1\n"
"buffer_load_dwordx4 v[184:187], v5, s[24:27], s62 offen sc1\n"
"s_add_u32 s62, s62, 4096\n"
"buffer_load_dwordx4 v[188:191], v5, s[24:27], s62 offen sc1\n"
"s_add_u32 s62, s62, 12288\n"
"buffer_load_dwordx4 v[192:195], v5, s[24:27], s62 offen sc1\n"
"s_add_u32 s62, s62, 4096\n"
"buffer_load_dwordx4 v[196:199], v5, s[24:27], s62 offen sc1\n"
"s_add_u32 s62, s62, 12288\n"
"buffer_load_dwordx4 v[200:203], v5, s[24:27], s62 offen sc1\n"
"s_add_u32 s62, s62, 4096\n"
"s_waitcnt vmcnt(10)\n"
"ds_write_b128 v8, v[168:171] offset:0\n"
"s_waitcnt vmcnt(9)\n"
"ds_write_b128 v8, v[172:175] offset:2048\n"
"s_waitcnt vmcnt(8)\n"
"ds_write_b128 v8, v[176:179] offset:4096\n"
"s_waitcnt vmcnt(7)\n"
"ds_write_b128 v8, v[180:183] offset:6144\n"
"s_waitcnt vmcnt(6)\n"
"ds_write_b128 v13, v[204:207]\n"
"s_waitcnt vmcnt(5)\n"
"s_mov_b64 exec, 0xff\n"
"ds_write_b128 v13, v[208:211] offset:1024\n"
"s_mov_b64 exec, -1\n"
"v_mov_b32 v9, v8\n"
"v_xor_b32 v6, 8192, v6\n"
"v_xor_b32 v13, 8192, v13\n"
"v_add_u32 v7, 36864, v7\n"
"v_add_u32 v8, -36864, v8\n"
"s_add_u32 s21, s21, 1\n"
"s_waitcnt lgkmcnt(0)\n"
"s_barrier\n"
"s_branch LP3_10\n"
"LP3_late1:\n"
"v_pk_fma_f32 v[112:113], v[242:243], v[48:49], v[112:113] op_sel:[0,0,0] op_sel_hi:[0,1,1] neg_lo:[1,0,0] neg_hi:[1,0,0]\n"
"v_pk_fma_f32 v[114:115], v[242:243], v[50:51], v[114:115] op_sel:[0,0,0] op_sel_hi:[0,1,1] neg_lo:[1,0,0] neg_hi:[1,0,0]\n"
"v_pk_fma_f32 v[128:129], v[242:243], v[64:65], v[128:129] op_sel:[0,0,0] op_sel_hi:[0,1,1] neg_lo:[1,0,0] neg_hi:[1,0,0]\n"
"v_pk_fma_f32 v[130:131], v[242:243], v[66:67], v[130:131] op_sel:[0,0,0] op_sel_hi:[0,1,1] neg_lo:[1,0,0] neg_hi:[1,0,0]\n"
"v_pk_mul_f32 v[238:239], v[242:243], v[112:113] op_sel:[1,0] op_sel_hi:[1,1]\n"
"v_pk_mul_f32 v[240:241], v[242:243], v[114:115] op_sel:[1,0] op_sel_hi:[1,1]\n"
"v_exp_f32 v238, v238\n"
"v_exp_f32 v240, v240\n"
"v_exp_f32 v239, v239\n"
"v_exp_f32 v241, v241\n"
"v_pk_mul_f32 v[112:113], v[112:113], v[128:129]\n"
"v_pk_mul_f32 v[114:115], v[114:115], v[130:131]\n"
"v_pk_fma_f32 v[238:239], v[244:245], v[238:239], v[244:245] op_sel:[0,0,0] op_sel_hi:[0,1,0]\n"
"v_pk_fma_f32 v[240:241], v[244:245], v[240:241], v[244:245] op_sel:[0,0,0] op_sel_hi:[0,1,0]\n"
"v_rcp_f32 v238, v238\n"
"v_rcp_f32 v240, v240\n"
"v_rcp_f32 v239, v239\n"
"v_rcp_f32 v241, v241\n"
"v_pk_mul_f32 v[112:113], v[112:113], v[238:239]\n"
"v_pk_mul_f32 v[114:115], v[114:115], v[240:241]\n"
"v_cvt_pk_bf16_f32 v230, v112, v113\n"
"v_cvt_pk_bf16_f32 v231, v114, v115\n"
"v_pk_fma_f32 v[116:117], v[242:243], v[52:53], v[116:117] op_sel:[0,0,0] op_sel_hi:[0,1,1] neg_lo:[1,0,0] neg_hi:[1,0,0]\n"
"v_pk_fma_f32 v[118:119], v[242:243], v[54:55], v[118:119] op_sel:[0,0,0] op_sel_hi:[0,1,1] neg_lo:[1,0,0] neg_hi:[1,0,0]\n"
"v_pk_fma_f32 v[132:133], v[242:243], v[68:69], v[132:133] op_sel:[0,0,0] op_sel_hi:[0,1,1] neg_lo:[1,0,0] neg_hi:[1,0,0]\n"
"v_pk_fma_f32 v[134:135], v[242:243], v[70:71], v[134:135] op_sel:[0,0,0] op_sel_hi:[0,1,1] neg_lo:[1,0,0] neg_hi:[1,0,0]\n"
"v_pk_mul_f32 v[238:239], v[242:243], v[116:117] op_sel:[1,0] op_sel_hi:[1,1]\n"
"v_pk_mul_f32 v[240:241], v[242:243], v[118:119] op_sel:[1,0] op_sel_hi:[1,1]\n"
"v_exp_f32 v238, v238\n"
"v_exp_f32 v240, v240\n"
"v_exp_f32 v239, v239\n"
"v_exp_f32 v241, v241\n"
"v_pk_mul_f32 v[116:117], v[116:117], v[132:133]\n"
"v_pk_mul_f32 v[118:119], v[118:119], v[134:135]\n"
"v_pk_fma_f32 v[238:239], v[244:245], v[238:239], v[244:245] op_sel:[0,0,0] op_sel_hi:[0,1,0]\n"
"v_pk_fma_f32 v[240:241], v[244:245], v[240:241], v[244:245] op_sel:[0,0,0] op_sel_hi:[0,1,0]\n"
"v_rcp_f32 v238, v238\n"
"v_rcp_f32 v240, v240\n"
"v_rcp_f32 v239, v239\n"
"v_rcp_f32 v241, v241\n"
"v_pk_mul_f32 v[116:117], v[116:117], v[238:239]\n"
"v_pk_mul_f32 v[118:119], v[118:119], v[240:241]\n"
"v_cvt_pk_bf16_f32 v232, v116, v117\n"
"v_cvt_pk_bf16_f32 v233, v118, v119\n"
"buffer_store_dwordx4 v[230:233], v14, s[24:27], s56 offen offset:0 sc1\n"
"v_pk_fma_f32 v[120:121], v[242:243], v[56:57], v[120:121] op_sel:[0,0,0] op_sel_hi:[0,1,1] neg_lo:[1,0,0] neg_hi:[1,0,0]\n"
"v_pk_fma_f32 v[122:123], v[242:243], v[58:59], v[122:123] op_sel:[0,0,0] op_sel_hi:[0,1,1] neg_lo:[1,0,0] neg_hi:[1,0,0]\n"
"v_pk_fma_f32 v[136:137], v[242:243], v[72:73], v[136:137] op_sel:[0,0,0] op_sel_hi:[0,1,1] neg_lo:[1,0,0] neg_hi:[1,0,0]\n"
"v_pk_fma_f32 v[138:139], v[242:243], v[74:75], v[138:139] op_sel:[0,0,0] op_sel_hi:[0,1,1] neg_lo:[1,0,0] neg_hi:[1,0,0]\n"
"s_waitcnt vmcnt(6)\n"
"v_pk_mul_f32 v[238:239], v[242:243], v[120:121] op_sel:[1,0] op_sel_hi:[1,1]\n"
"ds_write_b128 v8, v[168:171] offset:0\n"
"v_pk_mul_f32 v[240:241], v[242:243], v[122:123] op_sel:[1,0] op_sel_hi:[1,1]\n"
"v_exp_f32 v238, v238\n"
"v_exp_f32 v240, v240\n"
"v_exp_f32 v239, v239\n"
"v_exp_f32 v241, v241\n"
"v_pk_mul_f32 v[120:121], v[120:121], v[136:137]\n"
"ds_write_b128 v8, v[172:175] offset:2048\n"
"v_pk_mul_f32 v[122:123], v[122:123], v[138:139]\n"
"v_pk_fma_f32 v[238:239], v[244:245], v[238:239], v[244:245] op_sel:[0,0,0] op_sel_hi:[0,1,0]\n"
"v_pk_fma_f32 v[240:241], v[244:245], v[240:241], v[244:245] op_sel:[0,0,0] op_sel_hi:[0,1,0]\n"
"v_rcp_f32 v238, v238\n"
"v_rcp_f32 v240, v240\n"
"v_rcp_f32 v239, v239\n"
"ds_write_b128 v8, v[176:179] offset:4096\n"
"v_rcp_f32 v241, v241\n"
"v_pk_mul_f32 v[120:121], v[120:121], v[238:239]\n"
"v_pk_mul_f32 v[122:123], v[122:123], v[240:241]\n"
"v_cvt_pk_bf16_f32 v234, v120, v121\n"
"v_cvt_pk_bf16_f32 v235, v122, v123\n"
"v_pk_fma_f32 v[124:125], v[242:243], v[60:61], v[124:125] op_sel:[0,0,0] op_sel_hi:[0,1,1] neg_lo:[1,0,0] neg_hi:[1,0,0]\n"
"ds_write_b128 v8, v[180:183] offset:6144\n"
"v_pk_fma_f32 v[126:127], v[242:243], v[62:63], v[126:127] op_sel:[0,0,0] op_sel_hi:[0,1,1] neg_lo:[1,0,0] neg_hi:[1,0,0]\n"
"v_pk_fma_f32 v[140:141], v[242:243], v[76:77], v[140:141] op_sel:[0,0,0] op_sel_hi:[0,1,1] neg_lo:[1,0,0] neg_hi:[1,0,0]\n"
"v_pk_fma_f32 v[142:143], v[242:243], v[78:79], v[142:143] op_sel:[0,0,0] op_sel_hi:[0,1,1] neg_lo:[1,0,0] neg_hi:[1,0,0]\n"
"v_pk_mul_f32 v[238:239], v[242:243], v[124:125] op_sel:[1,0] op_sel_hi:[1,1]\n"
"v_pk_mul_f32 v[240:241], v[242:243], v[126:127] op_sel:[1,0] op_sel_hi:[1,1]\n"
"v_exp_f32 v238, v238\n"
"ds_write_b128 v13, v[204:207]\n"
"v_exp_f32 v240, v240\n"
"v_exp_f32 v239, v239\n"
"v_exp_f32 v241, v241\n"
"v_pk_mul_f32 v[124:125], v[124:125], v[140:141]\n"
"v_pk_mul_f32 v[126:127], v[126:127], v[142:143]\n"
"v_pk_fma_f32 v[238:239], v[244:245], v[238:239], v[244:245] op_sel:[0,0,0] op_sel_hi:[0,1,0]\n"
"s_mov_b64 exec, 0xff\n"
"ds_write_b128 v13, v[208:211] offset:1024\n"
"s_mov_b64 exec, -1\n"
"v_pk_fma_f32 v[240:241], v[244:245], v[240:241], v[244:245] op_sel:[0,0,0] op_sel_hi:[0,1,0]\n"
"v_rcp_f32 v238, v238\n"
"v_rcp_f32 v240, v240\n"
"v_rcp_f32 v239, v239\n"
"v_rcp_f32 v241, v241\n"
"v_pk_mul_f32 v[124:125], v[124:125], v[238:239]\n"
"v_pk_mul_f32 v[126:127], v[126:127], v[240:241]\n"
"v_cvt_pk_bf16_f32 v236, v124, v125\n"
"v_cvt_pk_bf16_f32 v237, v126, v127\n"
"buffer_store_dwordx4 v[234:237], v14, s[24:27], s56 offen offset:256 sc1\n"
"s_mov_b32 s68, 1\n"
"v_mov_b32 v9, v8\n"
"v_xor_b32 v6, 8192, v6\n"
"v_xor_b32 v13, 8192, v13\n"
"v_add_u32 v7, -36864, v7\n"
"v_add_u32 v8, 36864, v8\n"
"s_add_u32 s21, s21, 1\n"
"s_waitcnt lgkmcnt(0)\n"
"s_barrier\n"
"s_branch LP3_00\n"
"LP3_fin1:\n"
"v_pk_fma_f32 v[112:113], v[242:243], v[48:49], v[112:113] op_sel:[0,0,0] op_sel_hi:[0,1,1] neg_lo:[1,0,0] neg_hi:[1,0,0]\n"
"v_pk_fma_f32 v[114:115], v[242:243], v[50:51], v[114:115] op_sel:[0,0,0] op_sel_hi:[0,1,1] neg_lo:[1,0,0] neg_hi:[1,0,0]\n"
"v_pk_fma_f32 v[128:129], v[242:243], v[64:65], v[128:129] op_sel:[0,0,0] op_sel_hi:[0,1,1] neg_lo:[1,0,0] neg_hi:[1,0,0]\n"
"v_pk_fma_f32 v[130:131], v[242:243], v[66:67], v[130:131] op_sel:[0,0,0] op_sel_hi:[0,1,1] neg_lo:[1,0,0] neg_hi:[1,0,0]\n"
"v_pk_mul_f32 v[238:239], v[242:243], v[112:113] op_sel:[1,0] op_sel_hi:[1,1]\n"
"v_pk_mul_f32 v[240:241], v[242:243], v[114:115] op_sel:[1,0] op_sel_hi:[1,1]\n"
"v_exp_f32 v238, v238\n"
"v_exp_f32 v240, v240\n"
"v_exp_f32 v239, v239\n"
"v_exp_f32 v241, v241\n"
"v_pk_mul_f32 v[112:113], v[112:113], v[128:129]\n"
"v_pk_mul_f32 v[114:115], v[114:115], v[130:131]\n"
"v_pk_fma_f32 v[238:239], v[244:245], v[238:239], v[244:245] op_sel:[0,0,0] op_sel_hi:[0,1,0]\n"
"v_pk_fma_f32 v[240:241], v[244:245], v[240:241], v[244:245] op_sel:[0,0,0] op_sel_hi:[0,1,0]\n"
"v_rcp_f32 v238, v238\n"
"v_rcp_f32 v240, v240\n"
"v_rcp_f32 v239, v239\n"
"v_rcp_f32 v241, v241\n"
"v_pk_mul_f32 v[112:113], v[112:113], v[238:239]\n"
"v_pk_mul_f32 v[114:115], v[114:115], v[240:241]\n"
"v_cvt_pk_bf16_f32 v230, v112, v113\n"
"v_cvt_pk_bf16_f32 v231, v114, v115\n"
"v_pk_fma_f32 v[116:117], v[242:243], v[52:53], v[116:117] op_sel:[0,0,0] op_sel_hi:[0,1,1] neg_lo:[1,0,0] neg_hi:[1,0,0]\n"
"v_pk_fma_f32 v[118:119], v[242:243], v[54:55], v[118:119] op_sel:[0,0,0] op_sel_hi:[0,1,1] neg_lo:[1,0,0] neg_hi:[1,0,0]\n"
"v_pk_fma_f32 v[132:133], v[242:243], v[68:69], v[132:133] op_sel:[0,0,0] op_sel_hi:[0,1,1] neg_lo:[1,0,0] neg_hi:[1,0,0]\n"
"v_pk_fma_f32 v[134:135], v[242:243], v[70:71], v[134:135] op_sel:[0,0,0] op_sel_hi:[0,1,1] neg_lo:[1,0,0] neg_hi:[1,0,0]\n"
"v_pk_mul_f32 v[238:239], v[242:243], v[116:117] op_sel:[1,0] op_sel_hi:[1,1]\n"
"v_pk_mul_f32 v[240:241], v[242:243], v[118:119] op_sel:[1,0] op_sel_hi:[1,1]\n"
"v_exp_f32 v238, v238\n"
"v_exp_f32 v240, v240\n"
"v_exp_f32 v239, v239\n"
"v_exp_f32 v241, v241\n"
"v_pk_mul_f32 v[116:117], v[116:117], v[132:133]\n"
"v_pk_mul_f32 v[118:119], v[118:119], v[134:135]\n"
"v_pk_fma_f32 v[238:239], v[244:245], v[238:239], v[244:245] op_sel:[0,0,0] op_sel_hi:[0,1,0]\n"
"v_pk_fma_f32 v[240:241], v[244:245], v[240:241], v[244:245] op_sel:[0,0,0] op_sel_hi:[0,1,0]\n"
"v_rcp_f32 v238, v238\n"
"v_rcp_f32 v240, v240\n"
"v_rcp_f32 v239, v239\n"
"v_rcp_f32 v241, v241\n"
"v_pk_mul_f32 v[116:117], v[116:117], v[238:239]\n"
"v_pk_mul_f32 v[118:119], v[118:119], v[240:241]\n"
"v_cvt_pk_bf16_f32 v232, v116, v117\n"
"v_cvt_pk_bf16_f32 v233, v118, v119\n"
"buffer_store_dwordx4 v[230:233], v14, s[24:27], s56 offen offset:0 sc1\n"
"v_pk_fma_f32 v[120:121], v[242:243], v[56:57], v[120:121] op_sel:[0,0,0] op_sel_hi:[0,1,1] neg_lo:[1,0,0] neg_hi:[1,0,0]\n"
"v_pk_fma_f32 v[122:123], v[242:243], v[58:59], v[122:123] op_sel:[0,0,0] op_sel_hi:[0,1,1] neg_lo:[1,0,0] neg_hi:[1,0,0]\n"
"v_pk_fma_f32 v[136:137], v[242:243], v[72:73], v[136:137] op_sel:[0,0,0] op_sel_hi:[0,1,1] neg_lo:[1,0,0] neg_hi:[1,0,0]\n"
"v_pk_fma_f32 v[138:139], v[242:243], v[74:75], v[138:139] op_sel:[0,0,0] op_sel_hi:[0,1,1] neg_lo:[1,0,0] neg_hi:[1,0,0]\n"
"v_pk_mul_f32 v[238:239], v[242:243], v[120:121] op_sel:[1,0] op_sel_hi:[1,1]\n"
"v_pk_mul_f32 v[240:241], v[242:243], v[122:123] op_sel:[1,0] op_sel_hi:[1,1]\n"
"v_exp_f32 v238, v238\n"
"v_exp_f32 v240, v240\n"
"v_exp_f32 v239, v239\n"
"v_exp_f32 v241, v241\n"
"v_pk_mul_f32 v[120:121], v[120:121], v[136:137]\n"
"v_pk_mul_f32 v[122:123], v[122:123], v[138:139]\n"
"v_pk_fma_f32 v[238:239], v[244:245], v[238:239], v[244:245] op_sel:[0,0,0] op_sel_hi:[0,1,0]\n"
"v_pk_fma_f32 v[240:241], v[244:245], v[240:241], v[244:245] op_sel:[0,0,0] op_sel_hi:[0,1,0]\n"
"v_rcp_f32 v238, v238\n"
"v_rcp_f32 v240, v240\n"
"v_rcp_f32 v239, v239\n"
"v_rcp_f32 v241, v241\n"
"v_pk_mul_f32 v[120:121], v[120:121], v[238:239]\n"
"v_pk_mul_f32 v[122:123], v[122:123], v[240:241]\n"
"v_cvt_pk_bf16_f32 v234, v120, v121\n"
"v_cvt_pk_bf16_f32 v235, v122, v123\n"
"v_pk_fma_f32 v[124:125], v[242:243], v[60:61], v[124:125] op_sel:[0,0,0] op_sel_hi:[0,1,1] neg_lo:[1,0,0] neg_hi:[1,0,0]\n"
"v_pk_fma_f32 v[126:127], v[242:243], v[62:63], v[126:127] op_sel:[0,0,0] op_sel_hi:[0,1,1] neg_lo:[1,0,0] neg_hi:[1,0,0]\n"
"v_pk_fma_f32 v[140:141], v[242:243], v[76:77], v[140:141] op_sel:[0,0,0] op_sel_hi:[0,1,1] neg_lo:[1,0,0] neg_hi:[1,0,0]\n"
"v_pk_fma_f32 v[142:143], v[242:243], v[78:79], v[142:143] op_sel:[0,0,0] op_sel_hi:[0,1,1] neg_lo:[1,0,0] neg_hi:[1,0,0]\n"
"v_pk_mul_f32 v[238:239], v[242:243], v[124:125] op_sel:[1,0] op_sel_hi:[1,1]\n"
"v_pk_mul_f32 v[240:241], v[242:243], v[126:127] op_sel:[1,0] op_sel_hi:[1,1]\n"
"v_exp_f32 v238, v238\n"
"v_exp_f32 v240, v240\n"
"v_exp_f32 v239, v239\n"
"v_exp_f32 v241, v241\n"
"v_pk_mul_f32 v[124:125], v[124:125], v[140:141]\n"
"v_pk_mul_f32 v[126:127], v[126:127], v[142:143]\n"
"v_pk_fma_f32 v[238:239], v[244:245], v[238:239], v[244:245] op_sel:[0,0,0] op_sel_hi:[0,1,0]\n"
"v_pk_fma_f32 v[240:241], v[244:245], v[240:241], v[244:245] op_sel:[0,0,0] op_sel_hi:[0,1,0]\n"
"v_rcp_f32 v238, v238\n"
"v_rcp_f32 v240, v240\n"
"v_rcp_f32 v239, v239\n"
"v_rcp_f32 v241, v241\n"
"v_pk_mul_f32 v[124:125], v[124:125], v[238:239]\n"
"v_pk_mul_f32 v[126:127], v[126:127], v[240:241]\n"
"v_cvt_pk_bf16_f32 v236, v124, v125\n"
"v_cvt_pk_bf16_f32 v237, v126, v127\n"
"buffer_store_dwordx4 v[234:237], v14, s[24:27], s56 offen offset:256 sc1\n"
"s_waitcnt vmcnt(0)\n"
"s_add_u32 s65, s21, 0\n"
"s_and_b32 s41, s65, 1\n"
"s_lshl_b32 s41, s41, 2\n"
"s_add_u32 s41, s41, 98240\n"
"v_mov_b32 v248, s41\n"
"v_mov_b32 v250, 1\n"
"s_mov_b64 exec, 1\n"
"ds_add_rtn_u32 v249, v248, v250\n"
"s_mov_b64 exec, -1\n"
"s_waitcnt lgkmcnt(0)\n"
"v_readfirstlane_b32 s41, v249\n"
"s_and_b32 s41, s41, 3\n"
"s_cmp_lg_u32 s41, 3\n"
"s_cbranch_scc1 Lar_168\n"
"s_lshl_b32 s43, s19, 7\n"
"s_xor_b32 s43, s43, s22\n"
"s_xor_b32 s43, s43, 1302528\n"
"s_add_u32 s42, s65, 1\n"
"v_mov_b32 v3, s42\n"
"buffer_store_dword v3, v2, s[24:27], s43 offen sc1\n"
"Lpf_169:\n"
"Lar_168:\n"
"s_cmp_eq_u32 s21, 63\n"
"s_cbranch_scc1 LP3_end\n"
"LP3_next1:\n"
"s_add_u32 s59, s21, 1\n"
"s_cmp_eq_u32 s20, 3\n"
"s_cbranch_scc1 Lbk_170w\n"
"Lbk_170:\n"
"s_cmp_gt_i32 s70, s59\n"
"s_cbranch_scc1 Lbk_171\n"
"s_cmp_lg_u32 s20, 3\n"
"s_cbranch_scc1 Lbk_172\n"
"buffer_load_dword v10, v4, s[24:27], s71 offen sc1\n"
"Lbk_170w:\n"
"s_waitcnt vmcnt(0)\n"
"s_cmp_lg_u32 s20, 3\n"
"s_cbranch_scc1 Lav_173\n"
"v_cmp_ge_i32 vcc, s59, v10\n"
"s_cbranch_vccnz Lav_173\n"
"s_add_u32 s41, s59, 1\n"
"v_cmp_ge_i32 vcc, s41, v10\n"
"s_cbranch_vccnz Lav_174\n"
"s_add_u32 s41, s41, 1\n"
"Lav_174:\n"
"s_max_i32 s70, s70, s41\n"
"v_mov_b32 v251, s70\n"
"ds_write_b32 v15, v251\n"
"Lav_173:\n"
"s_cmp_gt_i32 s70, s59\n"
"s_cbranch_scc1 Lbk_171\n"
"s_sleep 1\n"
"s_memrealtime s[48:49]\n"
"s_waitcnt lgkmcnt(0)\n"
"s_sub_u32 s48, s48, s23\n"
"s_cmp_gt_i32 s48, 0\n"
"s_cbranch_scc1 LP3_end\n"
"s_branch Lbk_170\n"
"Lbk_172:\n"
"ds_read_b32 v251, v15\n"
"s_waitcnt lgkmcnt(0)\n"
"v_readfirstlane_b32 s70, v251\n"
"s_cmp_gt_i32 s70, s59\n"
"s_cbranch_scc1 Lbk_171\n"
"s_sleep 1\n"
"s_memrealtime s[48:49]\n"
"s_waitcnt lgkmcnt(0)\n"
"s_sub_u32 s48, s48, s23\n"
"s_cmp_gt_i32 s48, 0\n"
"s_cbranch_scc1 LP3_end\n"
"s_branch Lbk_172\n"
"Lbk_171:\n"
"s_lshl_b32 s62, s59, 17\n"
"s_add_u32 s62, s62, 157286400\n"
"s_lshr_b32 s43, s62, 8\n"
"s_xor_b32 s43, s43, s22\n"
"s_not_b32 s43, s43\n"
"s_and_b32 s43, s43, 8192\n"
"s_or_b32 s62, s62, s43\n"
"s_add_u32 s63, s62, 81920\n"
"buffer_load_dwordx4 v[168:171], v5, s[24:27], s62 offen sc1\n"
"s_add_u32 s62, s62, 4096\n"
"buffer_load_dwordx4 v[172:175], v5, s[24:27], s62 offen sc1\n"
"s_add_u32 s62, s62, 12288\n"
"buffer_load_dwordx4 v[176:179], v5, s[24:27], s62 offen sc1\n"
"s_add_u32 s62, s62, 4096\n"
"buffer_load_dwordx4 v[180:183], v5, s[24:27], s62 offen sc1\n"
"s_add_u32 s62, s62, 12288\n"
"buffer_load_dwordx4 v[204:207], v11, s[24:27], s63 offen sc1\n"
"buffer_load_dwordx4 v[208:211], v12, s[24:27], s63 offen sc1\n"
"buffer_load_dwordx4 v[184:187], v5, s[24:27], s62 offen sc1\n"
"s_add_u32 s62, s62, 4096\n"
"buffer_load_dwordx4 v[188:191], v5, s[24:27], s62 offen sc1\n"
"s_add_u32 s62, s62, 12288\n"
"buffer_load_dwordx4 v[192:195], v5, s[24:27], s62 offen sc1\n"
"s_add_u32 s62, s62, 4096\n"
"buffer_load_dwordx4 v[196:199], v5, s[24:27], s62 offen sc1\n"
"s_add_u32 s62, s62, 12288\n"
"buffer_load_dwordx4 v[200:203], v5, s[24:27], s62 offen sc1\n"
"s_add_u32 s62, s62, 4096\n"
"s_waitcnt vmcnt(10)\n"
"ds_write_b128 v8, v[168:171] offset:0\n"
"s_waitcnt vmcnt(9)\n"
"ds_write_b128 v8, v[172:175] offset:2048\n"
"s_waitcnt vmcnt(8)\n"
"ds_write_b128 v8, v[176:179] offset:4096\n"
"s_waitcnt vmcnt(7)\n"
"ds_write_b128 v8, v[180:183] offset:6144\n"
"s_waitcnt vmcnt(6)\n"
"ds_write_b128 v13, v[204:207]\n"
"s_waitcnt vmcnt(5)\n"
"s_mov_b64 exec, 0xff\n"
"ds_write_b128 v13, v[208:211] offset:1024\n"
"s_mov_b64 exec, -1\n"
"v_mov_b32 v9, v8\n"
"v_xor_b32 v6, 8192, v6\n"
"v_xor_b32 v13, 8192, v13\n"
"v_add_u32 v7, -36864, v7\n"
"v_add_u32 v8, 36864, v8\n"
"s_add_u32 s21, s21, 1\n"
"s_waitcnt lgkmcnt(0)\n"
"s_barrier\n"
"s_branch LP3_00\n"
"LP3_end:\n"
"s_endpgm\n"
"LP4:\n"
"v_mov_b32 v2, 2146435072\n"
"v_cmp_eq_u32 vcc, 0, v0\n"
"s_nop 1\n"
"v_cndmask_b32 v2, v2, 0, vcc\n"
"v_min_u32 v5, 35, v0\n"
"v_lshlrev_b32 v5, 7, v5\n"
"s_and_b32 s53, s19, 1\n"
"s_lshr_b32 s54, s19, 1\n"
"s_mov_b32 s55, 0xffff0000\n"
"v_lshlrev_b32 v1, 4, v0\n"
"s_lshl_b32 s36, s20, 11\n"
"v_add_u32 v6, s36, v1\n"
"v_mov_b32 v9, v1\n"
"v_and_b32 v3, 15, v0\n"
"v_mul_u32_u24 v7, 1152, v3\n"
"v_lshrrev_b32 v4, 4, v0\n"
"v_lshl_add_u32 v7, v4, 4, v7\n"
"s_mul_i32 s36, s53, 18432\n"
"s_lshl_b32 s37, s54, 6\n"
"s_add_u32 s36, s36, s37\n"
"v_add_u32 v7, s36, v7\n"
"v_lshlrev_b32 v8, 3, v3\n"
"v_mov_b32 v3, 2146435072\n"
"v_cmp_gt_u32 vcc, 16, v0\n"
"s_nop 1\n"
"v_cndmask_b32 v8, v3, v8, vcc\n"
"s_sleep 32\n"
"s_sleep 32\n"
"s_sleep 32\n"
"s_sleep 32\n"
"s_sleep 32\n"
"s_sleep 32\n"
"s_lshl_b32 s36, s20, 12\n"
"s_add_u32 s36, s36, 16384\n"
"v_lshlrev_b32 v61, 4, v0\n"
"v_add_u32 v61, s36, v61\n"
"v_lshrrev_b32 v60, 5, v0\n"
"v_lshlrev_b32 v62, 10, v60\n"
"v_bfe_u32 v60, v0, 3, 1\n"
"v_lshl_add_u32 v62, v60, 9, v62\n"
"v_bfe_u32 v60, v0, 4, 1\n"
"v_lshl_add_u32 v62, v60, 8, v62\n"
"v_bfe_u32 v60, v0, 2, 1\n"
"v_lshl_add_u32 v62, v60, 6, v62\n"
"v_and_b32 v60, 3, v0\n"
"v_lshl_add_u32 v62, v60, 4, v62\n"
"v_add_u32 v62, s36, v62\n"
"v_lshrrev_b32 v60, 4, v0\n"
"v_lshlrev_b32 v63, 9, v60\n"
"v_bfe_u32 v60, v0, 2, 2\n"
"v_lshl_add_u32 v63, v60, 6, v63\n"
"v_and_b32 v60, 3, v0\n"
"v_lshl_add_u32 v63, v60, 4, v63\n"
"v_add_u32 v63, s36, v63\n"
"v_lshrrev_b32 v65, 2, v0\n"
"v_mul_u32_u24 v64, 1152, v65\n"
"v_and_b32 v65, 3, v0\n"
"v_lshl_add_u32 v64, v65, 4, v64\n"
"s_load_dwordx2 s[28:29], s[16:17], 64\n"
"s_mov_b32 s30, 5308416\n"
"s_mov_b32 s31, 0x00020000\n"
"s_waitcnt lgkmcnt(0)\n"
"s_and_b32 s29, s29, 0xffff\n"
"s_load_dwordx2 s[32:33], s[16:17], 72\n"
"s_mov_b32 s34, 2359296\n"
"s_mov_b32 s35, 0x00020000\n"
"s_waitcnt lgkmcnt(0)\n"
"s_and_b32 s33, s33, 0xffff\n"
"s_mul_i32 s60, s20, 1327104\n"
"s_lshl_b32 s36, s54, 6\n"
"s_add_u32 s60, s60, s36\n"
"buffer_load_dwordx4 a[0:3], v64, s[28:31], s60 offen nt\n"
"s_add_u32 s60, s60, 18432\n"
"buffer_load_dwordx4 a[4:7], v64, s[28:31], s60 offen nt\n"
"s_add_u32 s60, s60, 18432\n"
"buffer_load_dwordx4 a[8:11], v64, s[28:31], s60 offen nt\n"
"s_add_u32 s60, s60, 18432\n"
"buffer_load_dwordx4 a[12:15], v64, s[28:31], s60 offen nt\n"
"s_add_u32 s60, s60, 18432\n"
"buffer_load_dwordx4 a[16:19], v64, s[28:31], s60 offen nt\n"
"s_add_u32 s60, s60, 18432\n"
"buffer_load_dwordx4 a[20:23], v64, s[28:31], s60 offen nt\n"
"s_add_u32 s60, s60, 18432\n"
"buffer_load_dwordx4 a[24:27], v64, s[28:31], s60 offen nt\n"
"s_add_u32 s60, s60, 18432\n"
"buffer_load_dwordx4 a[28:31], v64, s[28:31], s60 offen nt\n"
"s_add_u32 s60, s60, 18432\n"
"buffer_load_dwordx4 a[32:35], v64, s[28:31], s60 offen nt\n"
"s_add_u32 s60, s60, 18432\n"
"buffer_load_dwordx4 a[36:39], v64, s[28:31], s60 offen nt\n"
"s_add_u32 s60, s60, 18432\n"
"buffer_load_dwordx4 a[40:43], v64, s[28:31], s60 offen nt\n"
"s_add_u32 s60, s60, 18432\n"
"buffer_load_dwordx4 a[44:47], v64, s[28:31], s60 offen nt\n"
"s_add_u32 s60, s60, 18432\n"
"buffer_load_dwordx4 a[48:51], v64, s[28:31], s60 offen nt\n"
"s_add_u32 s60, s60, 18432\n"
"buffer_load_dwordx4 a[52:55], v64, s[28:31], s60 offen nt\n"
"s_add_u32 s60, s60, 18432\n"
"buffer_load_dwordx4 a[56:59], v64, s[28:31], s60 offen nt\n"
"s_add_u32 s60, s60, 18432\n"
"buffer_load_dwordx4 a[60:63], v64, s[28:31], s60 offen nt\n"
"s_add_u32 s60, s60, 18432\n"
"buffer_load_dwordx4 a[64:67], v64, s[28:31], s60 offen nt\n"
"s_add_u32 s60, s60, 18432\n"
"buffer_load_dwordx4 a[68:71], v64, s[28:31], s60 offen nt\n"
"s_add_u32 s60, s60, 18432\n"
"buffer_load_dwordx4 a[72:75], v64, s[28:31], s60 offen nt\n"
"s_add_u32 s60, s60, 18432\n"
"buffer_load_dwordx4 a[76:79], v64, s[28:31], s60 offen nt\n"
"s_add_u32 s60, s60, 18432\n"
"buffer_load_dwordx4 a[80:83], v64, s[28:31], s60 offen nt\n"
"s_add_u32 s60, s60, 18432\n"
"buffer_load_dwordx4 a[84:87], v64, s[28:31], s60 offen nt\n"
"s_add_u32 s60, s60, 18432\n"
"buffer_load_dwordx4 a[88:91], v64, s[28:31], s60 offen nt\n"
"s_add_u32 s60, s60, 18432\n"
"buffer_load_dwordx4 a[92:95], v64, s[28:31], s60 offen nt\n"
"s_add_u32 s60, s60, 18432\n"
"s_waitcnt vmcnt(22)\n"
"ds_write_b128 v61, a[0:3] offset:0\n"
"ds_write_b128 v61, a[4:7] offset:1024\n"
"ds_read_b64_tr_b16 a[0:1], v63 offset:0\n"
"ds_read_b64_tr_b16 a[2:3], v63 offset:256\n"
"ds_read_b64_tr_b16 a[4:5], v63 offset:8\n"
"ds_read_b64_tr_b16 a[6:7], v63 offset:264\n"
"buffer_load_dwordx4 a[96:99], v64, s[28:31], s60 offen nt\n"
"s_add_u32 s60, s60, 18432\n"
"buffer_load_dwordx4 a[100:103], v64, s[28:31], s60 offen nt\n"
"s_add_u32 s60, s60, 18432\n"
"s_waitcnt vmcnt(22)\n"
"ds_write_b128 v61, a[8:11] offset:0\n"
"ds_write_b128 v61, a[12:15] offset:1024\n"
"ds_read_b64_tr_b16 a[8:9], v63 offset:0\n"
"ds_read_b64_tr_b16 a[10:11], v63 offset:256\n"
"ds_read_b64_tr_b16 a[12:13], v63 offset:8\n"
"ds_read_b64_tr_b16 a[14:15], v63 offset:264\n"
"buffer_load_dwordx4 a[104:107], v64, s[28:31], s60 offen nt\n"
"s_add_u32 s60, s60, 18432\n"
"buffer_load_dwordx4 a[108:111], v64, s[28:31], s60 offen nt\n"
"s_add_u32 s60, s60, 18432\n"
"s_waitcnt vmcnt(22)\n"
"ds_write_b128 v61, a[16:19] offset:0\n"
"ds_write_b128 v61, a[20:23] offset:1024\n"
"ds_read_b64_tr_b16 a[16:17], v63 offset:0\n"
"ds_read_b64_tr_b16 a[18:19], v63 offset:256\n"
"ds_read_b64_tr_b16 a[20:21], v63 offset:8\n"
"ds_read_b64_tr_b16 a[22:23], v63 offset:264\n"
"buffer_load_dwordx4 a[112:115], v64, s[28:31], s60 offen nt\n"
"s_add_u32 s60, s60, 18432\n"
"buffer_load_dwordx4 a[116:119], v64, s[28:31], s60 offen nt\n"
"s_add_u32 s60, s60, 18432\n"
"s_waitcnt vmcnt(22)\n"
"ds_write_b128 v61, a[24:27] offset:0\n"
"ds_write_b128 v61, a[28:31] offset:1024\n"
"ds_read_b64_tr_b16 a[24:25], v63 offset:0\n"
"ds_read_b64_tr_b16 a[26:27], v63 offset:256\n"
"ds_read_b64_tr_b16 a[28:29], v63 offset:8\n"
"ds_read_b64_tr_b16 a[30:31], v63 offset:264\n"
"buffer_load_dwordx4 a[120:123], v64, s[28:31], s60 offen nt\n"
"s_add_u32 s60, s60, 18432\n"
"buffer_load_dwordx4 a[124:127], v64, s[28:31], s60 offen nt\n"
"s_add_u32 s60, s60, 18432\n"
"s_waitcnt vmcnt(22)\n"
"ds_write_b128 v61, a[32:35] offset:0\n"
"ds_write_b128 v61, a[36:39] offset:1024\n"
"ds_read_b64_tr_b16 a[32:33], v63 offset:0\n"
"ds_read_b64_tr_b16 a[34:35], v63 offset:256\n"
"ds_read_b64_tr_b16 a[36:37], v63 offset:8\n"
"ds_read_b64_tr_b16 a[38:39], v63 offset:264\n"
"buffer_load_dwordx4 a[128:131], v64, s[28:31], s60 offen nt\n"
"s_add_u32 s60, s60, 18432\n"
"buffer_load_dwordx4 a[132:135], v64, s[28:31], s60 offen nt\n"
"s_add_u32 s60, s60, 18432\n"
"s_waitcnt vmcnt(22)\n"
"ds_write_b128 v61, a[40:43] offset:0\n"
"ds_write_b128 v61, a[44:47] offset:1024\n"
"ds_read_b64_tr_b16 a[40:41], v63 offset:0\n"
"ds_read_b64_tr_b16 a[42:43], v63 offset:256\n"
"ds_read_b64_tr_b16 a[44:45], v63 offset:8\n"
"ds_read_b64_tr_b16 a[46:47], v63 offset:264\n"
"buffer_load_dwordx4 a[136:139], v64, s[28:31], s60 offen nt\n"
"s_add_u32 s60, s60, 18432\n"
"buffer_load_dwordx4 a[140:143], v64, s[28:31], s60 offen nt\n"
"s_add_u32 s60, s60, 18432\n"
"s_waitcnt vmcnt(22)\n"
"ds_write_b128 v61, a[48:51] offset:0\n"
"ds_write_b128 v61, a[52:55] offset:1024\n"
"ds_read_b64_tr_b16 a[48:49], v63 offset:0\n"
"ds_read_b64_tr_b16 a[50:51], v63 offset:256\n"
"ds_read_b64_tr_b16 a[52:53], v63 offset:8\n"
"ds_read_b64_tr_b16 a[54:55], v63 offset:264\n"
"buffer_load_dwordx4 a[144:147], v64, s[28:31], s60 offen nt\n"
"s_add_u32 s60, s60, 18432\n"
"buffer_load_dwordx4 a[148:151], v64, s[28:31], s60 offen nt\n"
"s_add_u32 s60, s60, 18432\n"
"s_waitcnt vmcnt(22)\n"
"ds_write_b128 v61, a[56:59] offset:0\n"
"ds_write_b128 v61, a[60:63] offset:1024\n"
"ds_read_b64_tr_b16 a[56:57], v63 offset:0\n"
"ds_read_b64_tr_b16 a[58:59], v63 offset:256\n"
"ds_read_b64_tr_b16 a[60:61], v63 offset:8\n"
"ds_read_b64_tr_b16 a[62:63], v63 offset:264\n"
"buffer_load_dwordx4 a[152:155], v64, s[28:31], s60 offen nt\n"
"s_add_u32 s60, s60, 18432\n"
"buffer_load_dwordx4 a[156:159], v64, s[28:31], s60 offen nt\n"
"s_add_u32 s60, s60, 18432\n"
"s_waitcnt vmcnt(22)\n"
"ds_write_b128 v61, a[64:67] offset:0\n"
"ds_write_b128 v61, a[68:71] offset:1024\n"
"ds_read_b64_tr_b16 a[64:65], v63 offset:0\n"
"ds_read_b64_tr_b16 a[66:67], v63 offset:256\n"
"ds_read_b64_tr_b16 a[68:69], v63 offset:8\n"
"ds_read_b64_tr_b16 a[70:71], v63 offset:264\n"
"buffer_load_dwordx4 a[160:163], v64, s[28:31], s60 offen nt\n"
"s_add_u32 s60, s60, 18432\n"
"buffer_load_dwordx4 a[164:167], v64, s[28:31], s60 offen nt\n"
"s_add_u32 s60, s60, 18432\n"
"s_waitcnt vmcnt(22)\n"
"ds_write_b128 v61, a[72:75] offset:0\n"
"ds_write_b128 v61, a[76:79] offset:1024\n"
"ds_read_b64_tr_b16 a[72:73], v63 offset:0\n"
"ds_read_b64_tr_b16 a[74:75], v63 offset:256\n"
"ds_read_b64_tr_b16 a[76:77], v63 offset:8\n"
"ds_read_b64_tr_b16 a[78:79], v63 offset:264\n"
"buffer_load_dwordx4 a[168:171], v64, s[28:31], s60 offen nt\n"
"s_add_u32 s60, s60, 18432\n"
"buffer_load_dwordx4 a[172:175], v64, s[28:31], s60 offen nt\n"
"s_add_u32 s60, s60, 18432\n"
"s_waitcnt vmcnt(22)\n"
"ds_write_b128 v61, a[80:83] offset:0\n"
"ds_write_b128 v61, a[84:87] offset:1024\n"
"ds_read_b64_tr_b16 a[80:81], v63 offset:0\n"
"ds_read_b64_tr_b16 a[82:83], v63 offset:256\n"
"ds_read_b64_tr_b16 a[84:85], v63 offset:8\n"
"ds_read_b64_tr_b16 a[86:87], v63 offset:264\n"
"buffer_load_dwordx4 a[176:179], v64, s[28:31], s60 offen nt\n"
"s_add_u32 s60, s60, 18432\n"
"buffer_load_dwordx4 a[180:183], v64, s[28:31], s60 offen nt\n"
"s_add_u32 s60, s60, 18432\n"
"s_waitcnt vmcnt(22)\n"
"ds_write_b128 v61, a[88:91] offset:0\n"
"ds_write_b128 v61, a[92:95] offset:1024\n"
"ds_read_b64_tr_b16 a[88:89], v63 offset:0\n"
"ds_read_b64_tr_b16 a[90:91], v63 offset:256\n"
"ds_read_b64_tr_b16 a[92:93], v63 offset:8\n"
"ds_read_b64_tr_b16 a[94:95], v63 offset:264\n"
"buffer_load_dwordx4 a[184:187], v64, s[28:31], s60 offen nt\n"
"s_add_u32 s60, s60, 18432\n"
"buffer_load_dwordx4 a[188:191], v64, s[28:31], s60 offen nt\n"
"s_add_u32 s60, s60, 18432\n"
"s_waitcnt vmcnt(22)\n"
"ds_write_b128 v61, a[96:99] offset:0\n"
"ds_write_b128 v61, a[100:103] offset:1024\n"
"ds_read_b64_tr_b16 a[96:97], v63 offset:0\n"
"ds_read_b64_tr_b16 a[98:99], v63 offset:256\n"
"ds_read_b64_tr_b16 a[100:101], v63 offset:8\n"
"ds_read_b64_tr_b16 a[102:103], v63 offset:264\n"
"buffer_load_dwordx4 a[192:195], v64, s[28:31], s60 offen nt\n"
"s_add_u32 s60, s60, 18432\n"
"buffer_load_dwordx4 a[196:199], v64, s[28:31], s60 offen nt\n"
"s_add_u32 s60, s60, 18432\n"
"s_waitcnt vmcnt(22)\n"
"ds_write_b128 v61, a[104:107] offset:0\n"
"ds_write_b128 v61, a[108:111] offset:1024\n"
"ds_read_b64_tr_b16 a[104:105], v63 offset:0\n"
"ds_read_b64_tr_b16 a[106:107], v63 offset:256\n"
"ds_read_b64_tr_b16 a[108:109], v63 offset:8\n"
"ds_read_b64_tr_b16 a[110:111], v63 offset:264\n"
"buffer_load_dwordx4 a[200:203], v64, s[28:31], s60 offen nt\n"
"s_add_u32 s60, s60, 18432\n"
"buffer_load_dwordx4 a[204:207], v64, s[28:31], s60 offen nt\n"
"s_add_u32 s60, s60, 18432\n"
"s_waitcnt vmcnt(22)\n"
"ds_write_b128 v61, a[112:115] offset:0\n"
"ds_write_b128 v61, a[116:119] offset:1024\n"
"ds_read_b64_tr_b16 a[112:113], v63 offset:0\n"
"ds_read_b64_tr_b16 a[114:115], v63 offset:256\n"
"ds_read_b64_tr_b16 a[116:117], v63 offset:8\n"
"ds_read_b64_tr_b16 a[118:119], v63 offset:264\n"
"buffer_load_dwordx4 a[208:211], v64, s[28:31], s60 offen nt\n"
"s_add_u32 s60, s60, 18432\n"
"buffer_load_dwordx4 a[212:215], v64, s[28:31], s60 offen nt\n"
"s_add_u32 s60, s60, 18432\n"
"s_waitcnt vmcnt(22)\n"
"ds_write_b128 v61, a[120:123] offset:0\n"
"ds_write_b128 v61, a[124:127] offset:1024\n"
"ds_read_b64_tr_b16 a[120:121], v63 offset:0\n"
"ds_read_b64_tr_b16 a[122:123], v63 offset:256\n"
"ds_read_b64_tr_b16 a[124:125], v63 offset:8\n"
"ds_read_b64_tr_b16 a[126:127], v63 offset:264\n"
"buffer_load_dwordx4 a[216:219], v64, s[28:31], s60 offen nt\n"
"s_add_u32 s60, s60, 18432\n"
"buffer_load_dwordx4 a[220:223], v64, s[28:31], s60 offen nt\n"
"s_add_u32 s60, s60, 18432\n"
"s_waitcnt vmcnt(22)\n"
"ds_write_b128 v61, a[128:131] offset:0\n"
"ds_write_b128 v61, a[132:135] offset:1024\n"
"ds_read_b64_tr_b16 a[128:129], v63 offset:0\n"
"ds_read_b64_tr_b16 a[130:131], v63 offset:256\n"
"ds_read_b64_tr_b16 a[132:133], v63 offset:8\n"
"ds_read_b64_tr_b16 a[134:135], v63 offset:264\n"
"buffer_load_dwordx4 a[224:227], v64, s[28:31], s60 offen nt\n"
"s_add_u32 s60, s60, 18432\n"
"buffer_load_dwordx4 a[228:231], v64, s[28:31], s60 offen nt\n"
"s_add_u32 s60, s60, 18432\n"
"s_waitcnt vmcnt(22)\n"
"ds_write_b128 v61, a[136:139] offset:0\n"
"ds_write_b128 v61, a[140:143] offset:1024\n"
"ds_read_b64_tr_b16 a[136:137], v63 offset:0\n"
"ds_read_b64_tr_b16 a[138:139], v63 offset:256\n"
"ds_read_b64_tr_b16 a[140:141], v63 offset:8\n"
"ds_read_b64_tr_b16 a[142:143], v63 offset:264\n"
"buffer_load_dwordx4 a[232:235], v64, s[28:31], s60 offen nt\n"
"s_add_u32 s60, s60, 18432\n"
"buffer_load_dwordx4 a[236:239], v64, s[28:31], s60 offen nt\n"
"s_add_u32 s60, s60, 18432\n"
"s_waitcnt vmcnt(22)\n"
"ds_write_b128 v61, a[144:147] offset:0\n"
"ds_write_b128 v61, a[148:151] offset:1024\n"
"ds_read_b64_tr_b16 a[144:145], v63 offset:0\n"
"ds_read_b64_tr_b16 a[146:147], v63 offset:256\n"
"ds_read_b64_tr_b16 a[148:149], v63 offset:8\n"
"ds_read_b64_tr_b16 a[150:151], v63 offset:264\n"
"buffer_load_dwordx4 a[240:243], v64, s[28:31], s60 offen nt\n"
"s_add_u32 s60, s60, 18432\n"
"buffer_load_dwordx4 a[244:247], v64, s[28:31], s60 offen nt\n"
"s_add_u32 s60, s60, 18432\n"
"s_waitcnt vmcnt(22)\n"
"ds_write_b128 v61, a[152:155] offset:0\n"
"ds_write_b128 v61, a[156:159] offset:1024\n"
"ds_read_b64_tr_b16 a[152:153], v63 offset:0\n"
"ds_read_b64_tr_b16 a[154:155], v63 offset:256\n"
"ds_read_b64_tr_b16 a[156:157], v63 offset:8\n"
"ds_read_b64_tr_b16 a[158:159], v63 offset:264\n"
"buffer_load_dwordx4 a[248:251], v64, s[28:31], s60 offen nt\n"
"s_add_u32 s60, s60, 18432\n"
"buffer_load_dwordx4 a[252:255], v64, s[28:31], s60 offen nt\n"
"s_add_u32 s60, s60, 18432\n"
"s_waitcnt vmcnt(22)\n"
"ds_write_b128 v61, a[160:163] offset:0\n"
"ds_write_b128 v61, a[164:167] offset:1024\n"
"ds_read_b64_tr_b16 a[160:161], v63 offset:0\n"
"ds_read_b64_tr_b16 a[162:163], v63 offset:256\n"
"ds_read_b64_tr_b16 a[164:165], v63 offset:8\n"
"ds_read_b64_tr_b16 a[166:167], v63 offset:264\n"
"buffer_load_dwordx4 v[16:19], v64, s[28:31], s60 offen nt\n"
"s_add_u32 s60, s60, 18432\n"
"buffer_load_dwordx4 v[20:23], v64, s[28:31], s60 offen nt\n"
"s_add_u32 s60, s60, 18432\n"
"s_waitcnt vmcnt(22)\n"
"ds_write_b128 v61, a[168:171] offset:0\n"
"ds_write_b128 v61, a[172:175] offset:1024\n"
"ds_read_b64_tr_b16 a[168:169], v63 offset:0\n"
"ds_read_b64_tr_b16 a[170:171], v63 offset:256\n"
"ds_read_b64_tr_b16 a[172:173], v63 offset:8\n"
"ds_read_b64_tr_b16 a[174:175], v63 offset:264\n"
"buffer_load_dwordx4 v[24:27], v64, s[28:31], s60 offen nt\n"
"s_add_u32 s60, s60, 18432\n"
"buffer_load_dwordx4 v[28:31], v64, s[28:31], s60 offen nt\n"
"s_add_u32 s60, s60, 18432\n"
"s_waitcnt vmcnt(22)\n"
"ds_write_b128 v61, a[176:179] offset:0\n"
"ds_write_b128 v61, a[180:183] offset:1024\n"
"ds_read_b64_tr_b16 a[176:177], v63 offset:0\n"
"ds_read_b64_tr_b16 a[178:179], v63 offset:256\n"
"ds_read_b64_tr_b16 a[180:181], v63 offset:8\n"
"ds_read_b64_tr_b16 a[182:183], v63 offset:264\n"
"buffer_load_dwordx4 v[32:35], v64, s[28:31], s60 offen nt\n"
"s_add_u32 s60, s60, 18432\n"
"buffer_load_dwordx4 v[36:39], v64, s[28:31], s60 offen nt\n"
"s_add_u32 s60, s60, 18432\n"
"s_waitcnt vmcnt(22)\n"
"ds_write_b128 v61, a[184:187] offset:0\n"
"ds_write_b128 v61, a[188:191] offset:1024\n"
"ds_read_b64_tr_b16 a[184:185], v63 offset:0\n"
"ds_read_b64_tr_b16 a[186:187], v63 offset:256\n"
"ds_read_b64_tr_b16 a[188:189], v63 offset:8\n"
"ds_read_b64_tr_b16 a[190:191], v63 offset:264\n"
"buffer_load_dwordx4 v[40:43], v64, s[28:31], s60 offen nt\n"
"s_add_u32 s60, s60, 18432\n"
"buffer_load_dwordx4 v[44:47], v64, s[28:31], s60 offen nt\n"
"s_add_u32 s60, s60, 18432\n"
"s_waitcnt vmcnt(22)\n"
"ds_write_b128 v61, a[192:195] offset:0\n"
"ds_write_b128 v61, a[196:199] offset:1024\n"
"ds_read_b64_tr_b16 a[192:193], v63 offset:0\n"
"ds_read_b64_tr_b16 a[194:195], v63 offset:256\n"
"ds_read_b64_tr_b16 a[196:197], v63 offset:8\n"
"ds_read_b64_tr_b16 a[198:199], v63 offset:264\n"
"s_waitcnt vmcnt(20)\n"
"ds_write_b128 v61, a[200:203] offset:0\n"
"ds_write_b128 v61, a[204:207] offset:1024\n"
"ds_read_b64_tr_b16 a[200:201], v63 offset:0\n"
"ds_read_b64_tr_b16 a[202:203], v63 offset:256\n"
"ds_read_b64_tr_b16 a[204:205], v63 offset:8\n"
"ds_read_b64_tr_b16 a[206:207], v63 offset:264\n"
"s_waitcnt vmcnt(18)\n"
"ds_write_b128 v61, a[208:211] offset:0\n"
"ds_write_b128 v61, a[212:215] offset:1024\n"
"ds_read_b64_tr_b16 a[208:209], v63 offset:0\n"
"ds_read_b64_tr_b16 a[210:211], v63 offset:256\n"
"ds_read_b64_tr_b16 a[212:213], v63 offset:8\n"
"ds_read_b64_tr_b16 a[214:215], v63 offset:264\n"
"s_waitcnt vmcnt(16)\n"
"ds_write_b128 v61, a[216:219] offset:0\n"
"ds_write_b128 v61, a[220:223] offset:1024\n"
"ds_read_b64_tr_b16 a[216:217], v63 offset:0\n"
"ds_read_b64_tr_b16 a[218:219], v63 offset:256\n"
"ds_read_b64_tr_b16 a[220:221], v63 offset:8\n"
"ds_read_b64_tr_b16 a[222:223], v63 offset:264\n"
"s_waitcnt vmcnt(14)\n"
"ds_write_b128 v61, a[224:227] offset:0\n"
"ds_write_b128 v61, a[228:231] offset:1024\n"
"ds_read_b64_tr_b16 a[224:225], v63 offset:0\n"
"ds_read_b64_tr_b16 a[226:227], v63 offset:256\n"
"ds_read_b64_tr_b16 a[228:229], v63 offset:8\n"
"ds_read_b64_tr_b16 a[230:231], v63 offset:264\n"
"s_waitcnt vmcnt(12)\n"
"ds_write_b128 v61, a[232:235] offset:0\n"
"ds_write_b128 v61, a[236:239] offset:1024\n"
"ds_read_b64_tr_b16 a[232:233], v63 offset:0\n"
"ds_read_b64_tr_b16 a[234:235], v63 offset:256\n"
"ds_read_b64_tr_b16 a[236:237], v63 offset:8\n"
"ds_read_b64_tr_b16 a[238:239], v63 offset:264\n"
"s_waitcnt vmcnt(10)\n"
"ds_write_b128 v61, a[240:243] offset:0\n"
"ds_write_b128 v61, a[244:247] offset:1024\n"
"ds_read_b64_tr_b16 a[240:241], v63 offset:0\n"
"ds_read_b64_tr_b16 a[242:243], v63 offset:256\n"
"ds_read_b64_tr_b16 a[244:245], v63 offset:8\n"
"ds_read_b64_tr_b16 a[246:247], v63 offset:264\n"
"s_waitcnt vmcnt(8)\n"
"ds_write_b128 v61, a[248:251] offset:0\n"
"ds_write_b128 v61, a[252:255] offset:1024\n"
"ds_read_b64_tr_b16 a[248:249], v63 offset:0\n"
"ds_read_b64_tr_b16 a[250:251], v63 offset:256\n"
"ds_read_b64_tr_b16 a[252:253], v63 offset:8\n"
"ds_read_b64_tr_b16 a[254:255], v63 offset:264\n"
"s_waitcnt vmcnt(6)\n"
"ds_write_b128 v61, v[16:19] offset:0\n"
"ds_write_b128 v61, v[20:23] offset:1024\n"
"ds_read_b64_tr_b16 v[16:17], v63 offset:0\n"
"ds_read_b64_tr_b16 v[18:19], v63 offset:256\n"
"ds_read_b64_tr_b16 v[20:21], v63 offset:8\n"
"ds_read_b64_tr_b16 v[22:23], v63 offset:264\n"
"s_waitcnt vmcnt(4)\n"
"ds_write_b128 v61, v[24:27] offset:0\n"
"ds_write_b128 v61, v[28:31] offset:1024\n"
"ds_read_b64_tr_b16 v[24:25], v63 offset:0\n"
"ds_read_b64_tr_b16 v[26:27], v63 offset:256\n"
"ds_read_b64_tr_b16 v[28:29], v63 offset:8\n"
"ds_read_b64_tr_b16 v[30:31], v63 offset:264\n"
"s_waitcnt vmcnt(2)\n"
"ds_write_b128 v61, v[32:35] offset:0\n"
"ds_write_b128 v61, v[36:39] offset:1024\n"
"ds_read_b64_tr_b16 v[32:33], v63 offset:0\n"
"ds_read_b64_tr_b16 v[34:35], v63 offset:256\n"
"ds_read_b64_tr_b16 v[36:37], v63 offset:8\n"
"ds_read_b64_tr_b16 v[38:39], v63 offset:264\n"
"s_waitcnt vmcnt(0)\n"
"ds_write_b128 v61, v[40:43] offset:0\n"
"ds_write_b128 v61, v[44:47] offset:1024\n"
"ds_read_b64_tr_b16 v[40:41], v63 offset:0\n"
"ds_read_b64_tr_b16 v[42:43], v63 offset:256\n"
"ds_read_b64_tr_b16 v[44:45], v63 offset:8\n"
"ds_read_b64_tr_b16 v[46:47], v63 offset:264\n"
"s_waitcnt lgkmcnt(0)\n"
"s_lshr_b32 s36, s19, 3\n"
"s_lshl_b32 s36, s36, 14\n"
"s_and_b32 s72, s19, 7\n"
"s_lshl_b32 s72, s72, 10\n"
"s_add_u32 s72, s72, s36\n"
"s_mul_i32 s73, s53, 18\n"
"s_add_u32 s73, s73, s54\n"
"s_lshl_b32 s73, s73, 7\n"
"s_add_u32 s73, s73, 81920\n"
"s_lshl_b32 s68, s53, 2\n"
"s_add_u32 s68, s68, s20\n"
"s_mul_i32 s68, s68, 81920\n"
"v_mov_b32 v11, 32768\n"
"v_mov_b32 v4, 0\n"
"v_mov_b32 v10, -1\n"
"ds_write_b32 v11, v4\n"
"s_mov_b32 s70, 0\n"
"s_waitcnt lgkmcnt(0)\n"
"s_barrier\n"
"s_xor_b32 s71, s22, 1302528\n"
"s_cmp_eq_u32 s20, 3\n"
"s_cselect_b32 s71, s71, 2146435072\n"
"s_mov_b32 s21, 0\n"
"s_mov_b32 s64, 0\n"
"LP4_block:\n"
"s_cmp_eq_u32 s20, 3\n"
"s_cbranch_scc1 Lbk_175w\n"
"Lbk_175:\n"
"s_cmp_gt_i32 s70, s21\n"
"s_cbranch_scc1 Lbk_176\n"
"s_cmp_lg_u32 s20, 3\n"
"s_cbranch_scc1 Lbk_177\n"
"buffer_load_dword v10, v5, s[24:27], s71 offen sc1\n"
"Lbk_175w:\n"
"s_waitcnt vmcnt(0)\n"
"s_cmp_lg_u32 s20, 3\n"
"s_cbranch_scc1 Lav_178\n"
"v_cmp_ge_i32 vcc, s21, v10\n"
"s_cbranch_vccnz Lav_178\n"
"s_add_u32 s41, s21, 1\n"
"v_cmp_ge_i32 vcc, s41, v10\n"
"s_cbranch_vccnz Lav_179\n"
"s_add_u32 s41, s41, 1\n"
"Lav_179:\n"
"s_max_i32 s70, s70, s41\n"
"v_mov_b32 v4, s70\n"
"ds_write_b32 v11, v4\n"
"Lav_178:\n"
"s_cmp_gt_i32 s70, s21\n"
"s_cbranch_scc1 Lbk_176\n"
"s_sleep 1\n"
"s_memrealtime s[48:49]\n"
"s_waitcnt lgkmcnt(0)\n"
"s_sub_u32 s48, s48, s23\n"
"s_cmp_gt_i32 s48, 0\n"
"s_cbranch_scc1 LP4_end\n"
"s_branch Lbk_175\n"
"Lbk_177:\n"
"ds_read_b32 v4, v11\n"
"s_waitcnt lgkmcnt(0)\n"
"v_readfirstlane_b32 s70, v4\n"
"s_cmp_gt_i32 s70, s21\n"
"s_cbranch_scc1 Lbk_176\n"
"s_sleep 1\n"
"s_memrealtime s[48:49]\n"
"s_waitcnt lgkmcnt(0)\n"
"s_sub_u32 s48, s48, s23\n"
"s_cmp_gt_i32 s48, 0\n"
"s_cbranch_scc1 LP4_end\n"
"s_branch Lbk_177\n"
"Lbk_176:\n"
"s_mul_i32 s42, s21, 86\n"
"s_lshr_b32 s42, s42, 8\n"
"s_mul_i32 s56, s21, 655360\n"
"s_mul_i32 s42, s42, 131072\n"
"s_add_u32 s56, s56, s42\n"
"s_add_u32 s56, s56, 102760448\n"
"s_add_u32 s56, s56, s68\n"
"s_lshr_b32 s43, s56, 8\n"
"s_xor_b32 s43, s43, s22\n"
"s_not_b32 s43, s43\n"
"s_and_b32 s43, s43, 8192\n"
"s_or_b32 s56, s56, s43\n"
"s_lshl_b32 s65, s21, 17\n"
"s_add_u32 s65, s65, 157286400\n"
"s_mul_i32 s67, s21, 36864\n"
"s_lshr_b32 s43, s65, 8\n"
"s_xor_b32 s43, s43, s22\n"
"s_not_b32 s43, s43\n"
"s_and_b32 s43, s43, 8192\n"
"s_or_b32 s65, s65, s43\n"
"s_add_u32 s66, s65, s73\n"
"s_add_u32 s65, s65, s72\n"
"buffer_load_dwordx4 v[96:99], v1, s[24:27], s56 offen offset:0 sc1\n"
"s_nop 5\n"
"buffer_load_dwordx4 v[100:103], v1, s[24:27], s56 offen offset:1024 sc1\n"
"s_nop 5\n"
"buffer_load_dwordx4 v[104:107], v1, s[24:27], s56 offen offset:2048 sc1\n"
"s_nop 5\n"
"buffer_load_dwordx4 v[108:111], v1, s[24:27], s56 offen offset:3072 sc1\n"
"s_add_u32 s56, s56, 4096\n"
"s_nop 5\n"
"buffer_load_dwordx4 v[112:115], v1, s[24:27], s56 offen offset:0 sc1\n"
"s_nop 5\n"
"buffer_load_dwordx4 v[116:119], v1, s[24:27], s56 offen offset:1024 sc1\n"
"s_nop 5\n"
"buffer_load_dwordx4 v[120:123], v1, s[24:27], s56 offen offset:2048 sc1\n"
"s_nop 5\n"
"buffer_load_dwordx4 v[124:127], v1, s[24:27], s56 offen offset:3072 sc1\n"
"s_add_u32 s56, s56, 12288\n"
"s_nop 5\n"
"buffer_load_dwordx4 v[128:131], v1, s[24:27], s56 offen offset:0 sc1\n"
"s_nop 5\n"
"buffer_load_dwordx4 v[132:135], v1, s[24:27], s56 offen offset:1024 sc1\n"
"s_nop 5\n"
"buffer_load_dwordx4 v[136:139], v1, s[24:27], s56 offen offset:2048 sc1\n"
"s_nop 5\n"
"buffer_load_dwordx4 v[140:143], v1, s[24:27], s56 offen offset:3072 sc1\n"
"s_add_u32 s56, s56, 4096\n"
"s_nop 5\n"
"buffer_load_dwordx4 v[144:147], v1, s[24:27], s56 offen offset:0 sc1\n"
"s_nop 5\n"
"buffer_load_dwordx4 v[148:151], v1, s[24:27], s56 offen offset:1024 sc1\n"
"s_nop 5\n"
"buffer_load_dwordx4 v[152:155], v1, s[24:27], s56 offen offset:2048 sc1\n"
"s_nop 5\n"
"buffer_load_dwordx4 v[156:159], v1, s[24:27], s56 offen offset:3072 sc1\n"
"s_add_u32 s56, s56, 12288\n"
"s_nop 5\n"
"buffer_load_dwordx4 v[160:163], v1, s[24:27], s56 offen offset:0 sc1\n"
"s_nop 5\n"
"buffer_load_dwordx4 v[164:167], v1, s[24:27], s56 offen offset:1024 sc1\n"
"s_nop 5\n"
"s_add_u32 s63, s21, 1\n"
"s_mov_b32 s59, s65\n"
"s_mov_b32 s58, s66\n"
"s_mov_b32 s61, s67\n"
"buffer_load_dword v10, v5, s[24:27], s71 offen sc1\n"
"s_waitcnt vmcnt(18)\n"
"v_mfma_f32_16x16x32_bf16 v[48:51], a[0:3], v[96:99], 0\n"
"buffer_load_dwordx4 v[168:171], v1, s[24:27], s56 offen offset:2048 sc1\n"
"v_mfma_f32_16x16x32_bf16 v[52:55], a[4:7], v[96:99], 0\n"
"s_waitcnt vmcnt(18)\n"
"v_mfma_f32_16x16x32_bf16 v[48:51], a[8:11], v[100:103], v[48:51]\n"
"buffer_load_dwordx4 v[172:175], v1, s[24:27], s56 offen offset:3072 sc1\n"
"s_add_u32 s56, s56, 4096\n"
"v_mfma_f32_16x16x32_bf16 v[52:55], a[12:15], v[100:103], v[52:55]\n"
"s_waitcnt vmcnt(18)\n"
"v_mfma_f32_16x16x32_bf16 v[48:51], a[16:19], v[104:107], v[48:51]\n"
"buffer_load_dwordx4 v[176:179], v1, s[24:27], s56 offen offset:0 sc1\n"
"v_mfma_f32_16x16x32_bf16 v[52:55], a[20:23], v[104:107], v[52:55]\n"
"s_waitcnt vmcnt(18)\n"
"v_mfma_f32_16x16x32_bf16 v[48:51], a[24:27], v[108:111], v[48:51]\n"
"buffer_load_dwordx4 v[180:183], v1, s[24:27], s56 offen offset:1024 sc1\n"
"v_mfma_f32_16x16x32_bf16 v[52:55], a[28:31], v[108:111], v[52:55]\n"
"s_waitcnt vmcnt(18)\n"
"v_mfma_f32_16x16x32_bf16 v[48:51], a[32:35], v[112:115], v[48:51]\n"
"buffer_load_dwordx4 v[184:187], v1, s[24:27], s56 offen offset:2048 sc1\n"
"v_mfma_f32_16x16x32_bf16 v[52:55], a[36:39], v[112:115], v[52:55]\n"
"s_waitcnt vmcnt(18)\n"
"v_mfma_f32_16x16x32_bf16 v[48:51], a[40:43], v[116:119], v[48:51]\n"
"buffer_load_dwordx4 v[188:191], v1, s[24:27], s56 offen offset:3072 sc1\n"
"s_add_u32 s56, s56, 12288\n"
"v_mfma_f32_16x16x32_bf16 v[52:55], a[44:47], v[116:119], v[52:55]\n"
"s_waitcnt vmcnt(18)\n"
"v_mfma_f32_16x16x32_bf16 v[48:51], a[48:51], v[120:123], v[48:51]\n"
"buffer_load_dwordx4 v[192:195], v1, s[24:27], s56 offen offset:0 sc1\n"
"v_mfma_f32_16x16x32_bf16 v[52:55], a[52:55], v[120:123], v[52:55]\n"
"s_waitcnt vmcnt(18)\n"
"v_mfma_f32_16x16x32_bf16 v[48:51], a[56:59], v[124:127], v[48:51]\n"
"buffer_load_dwordx4 v[196:199], v1, s[24:27], s56 offen offset:1024 sc1\n"
"v_mfma_f32_16x16x32_bf16 v[52:55], a[60:63], v[124:127], v[52:55]\n"
"s_waitcnt vmcnt(18)\n"
"v_mfma_f32_16x16x32_bf16 v[48:51], a[64:67], v[128:131], v[48:51]\n"
"buffer_load_dwordx4 v[200:203], v1, s[24:27], s56 offen offset:2048 sc1\n"
"v_mfma_f32_16x16x32_bf16 v[52:55], a[68:71], v[128:131], v[52:55]\n"
"s_waitcnt vmcnt(18)\n"
"v_mfma_f32_16x16x32_bf16 v[48:51], a[72:75], v[132:135], v[48:51]\n"
"buffer_load_dwordx4 v[204:207], v1, s[24:27], s56 offen offset:3072 sc1\n"
"s_add_u32 s56, s56, 4096\n"
"v_mfma_f32_16x16x32_bf16 v[52:55], a[76:79], v[132:135], v[52:55]\n"
"s_waitcnt vmcnt(18)\n"
"v_mfma_f32_16x16x32_bf16 v[48:51], a[80:83], v[136:139], v[48:51]\n"
"buffer_load_dwordx4 v[208:211], v1, s[24:27], s56 offen offset:0 sc1\n"
"v_mfma_f32_16x16x32_bf16 v[52:55], a[84:87], v[136:139], v[52:55]\n"
"s_waitcnt vmcnt(18)\n"
"v_mfma_f32_16x16x32_bf16 v[48:51], a[88:91], v[140:143], v[48:51]\n"
"buffer_load_dwordx4 v[212:215], v1, s[24:27], s56 offen offset:1024 sc1\n"
"v_mfma_f32_16x16x32_bf16 v[52:55], a[92:95], v[140:143], v[52:55]\n"
"s_waitcnt vmcnt(18)\n"
"v_mfma_f32_16x16x32_bf16 v[48:51], a[96:99], v[144:147], v[48:51]\n"
"buffer_load_dwordx4 v[216:219], v1, s[24:27], s56 offen offset:2048 sc1\n"
"v_mfma_f32_16x16x32_bf16 v[52:55], a[100:103], v[144:147], v[52:55]\n"
"s_waitcnt vmcnt(18)\n"
"v_mfma_f32_16x16x32_bf16 v[48:51], a[104:107], v[148:151], v[48:51]\n"
"buffer_load_dwordx4 v[220:223], v1, s[24:27], s56 offen offset:3072 sc1\n"
"s_add_u32 s56, s56, 12288\n"
"v_mfma_f32_16x16x32_bf16 v[52:55], a[108:111], v[148:151], v[52:55]\n"
"s_waitcnt vmcnt(18)\n"
"v_mfma_f32_16x16x32_bf16 v[48:51], a[112:115], v[152:155], v[48:51]\n"
"buffer_load_dwordx4 v[224:227], v1, s[24:27], s56 offen offset:0 sc1\n"
"v_mfma_f32_16x16x32_bf16 v[52:55], a[116:119], v[152:155], v[52:55]\n"
"s_waitcnt vmcnt(18)\n"
"v_mfma_f32_16x16x32_bf16 v[48:51], a[120:123], v[156:159], v[48:51]\n"
"buffer_load_dwordx4 v[228:231], v1, s[24:27], s56 offen offset:1024 sc1\n"
"v_mfma_f32_16x16x32_bf16 v[52:55], a[124:127], v[156:159], v[52:55]\n"
"s_waitcnt vmcnt(18)\n"
"v_mfma_f32_16x16x32_bf16 v[48:51], a[128:131], v[160:163], v[48:51]\n"
"buffer_load_dwordx4 v[232:235], v1, s[24:27], s56 offen offset:2048 sc1\n"
"v_mfma_f32_16x16x32_bf16 v[52:55], a[132:135], v[160:163], v[52:55]\n"
"s_waitcnt vmcnt(18)\n"
"v_mfma_f32_16x16x32_bf16 v[48:51], a[136:139], v[164:167], v[48:51]\n"
"buffer_load_dwordx4 v[236:239], v1, s[24:27], s56 offen offset:3072 sc1\n"
"s_add_u32 s56, s56, 4096\n"
"v_mfma_f32_16x16x32_bf16 v[52:55], a[140:143], v[164:167], v[52:55]\n"
"s_branch LP4_mid\n"
"LP4_loop:\n"
"s_add_u32 s63, s21, 1\n"
"s_mov_b32 s59, s65\n"
"s_mov_b32 s58, s66\n"
"s_mov_b32 s61, s67\n"
"s_waitcnt vmcnt(36)\n"
"v_mfma_f32_16x16x32_bf16 v[48:51], a[0:3], v[96:99], 0\n"
"v_mfma_f32_16x16x32_bf16 v[52:55], a[4:7], v[96:99], 0\n"
"s_waitcnt vmcnt(35)\n"
"v_mfma_f32_16x16x32_bf16 v[48:51], a[8:11], v[100:103], v[48:51]\n"
"v_mfma_f32_16x16x32_bf16 v[52:55], a[12:15], v[100:103], v[52:55]\n"
"s_waitcnt vmcnt(34)\n"
"v_mfma_f32_16x16x32_bf16 v[48:51], a[16:19], v[104:107], v[48:51]\n"
"v_mfma_f32_16x16x32_bf16 v[52:55], a[20:23], v[104:107], v[52:55]\n"
"s_waitcnt vmcnt(33)\n"
"v_mfma_f32_16x16x32_bf16 v[48:51], a[24:27], v[108:111], v[48:51]\n"
"v_mfma_f32_16x16x32_bf16 v[52:55], a[28:31], v[108:111], v[52:55]\n"
"s_waitcnt vmcnt(32)\n"
"v_mfma_f32_16x16x32_bf16 v[48:51], a[32:35], v[112:115], v[48:51]\n"
"v_mfma_f32_16x16x32_bf16 v[52:55], a[36:39], v[112:115], v[52:55]\n"
"s_waitcnt vmcnt(31)\n"
"v_mfma_f32_16x16x32_bf16 v[48:51], a[40:43], v[116:119], v[48:51]\n"
"v_mfma_f32_16x16x32_bf16 v[52:55], a[44:47], v[116:119], v[52:55]\n"
"s_waitcnt vmcnt(30)\n"
"v_mfma_f32_16x16x32_bf16 v[48:51], a[48:51], v[120:123], v[48:51]\n"
"v_mfma_f32_16x16x32_bf16 v[52:55], a[52:55], v[120:123], v[52:55]\n"
"s_waitcnt vmcnt(29)\n"
"v_mfma_f32_16x16x32_bf16 v[48:51], a[56:59], v[124:127], v[48:51]\n"
"v_mfma_f32_16x16x32_bf16 v[52:55], a[60:63], v[124:127], v[52:55]\n"
"s_waitcnt vmcnt(28)\n"
"v_mfma_f32_16x16x32_bf16 v[48:51], a[64:67], v[128:131], v[48:51]\n"
"v_mfma_f32_16x16x32_bf16 v[52:55], a[68:71], v[128:131], v[52:55]\n"
"s_waitcnt vmcnt(27)\n"
"v_mfma_f32_16x16x32_bf16 v[48:51], a[72:75], v[132:135], v[48:51]\n"
"v_mfma_f32_16x16x32_bf16 v[52:55], a[76:79], v[132:135], v[52:55]\n"
"s_waitcnt vmcnt(26)\n"
"v_mfma_f32_16x16x32_bf16 v[48:51], a[80:83], v[136:139], v[48:51]\n"
"v_mfma_f32_16x16x32_bf16 v[52:55], a[84:87], v[136:139], v[52:55]\n"
"s_waitcnt vmcnt(25)\n"
"v_mfma_f32_16x16x32_bf16 v[48:51], a[88:91], v[140:143], v[48:51]\n"
"v_mfma_f32_16x16x32_bf16 v[52:55], a[92:95], v[140:143], v[52:55]\n"
"s_waitcnt vmcnt(24)\n"
"v_mfma_f32_16x16x32_bf16 v[48:51], a[96:99], v[144:147], v[48:51]\n"
"v_mfma_f32_16x16x32_bf16 v[52:55], a[100:103], v[144:147], v[52:55]\n"
"s_waitcnt vmcnt(23)\n"
"v_mfma_f32_16x16x32_bf16 v[48:51], a[104:107], v[148:151], v[48:51]\n"
"v_mfma_f32_16x16x32_bf16 v[52:55], a[108:111], v[148:151], v[52:55]\n"
"s_waitcnt vmcnt(22)\n"
"v_mfma_f32_16x16x32_bf16 v[48:51], a[112:115], v[152:155], v[48:51]\n"
"v_mfma_f32_16x16x32_bf16 v[52:55], a[116:119], v[152:155], v[52:55]\n"
"s_waitcnt vmcnt(21)\n"
"v_mfma_f32_16x16x32_bf16 v[48:51], a[120:123], v[156:159], v[48:51]\n"
"v_mfma_f32_16x16x32_bf16 v[52:55], a[124:127], v[156:159], v[52:55]\n"
"s_waitcnt vmcnt(20)\n"
"v_mfma_f32_16x16x32_bf16 v[48:51], a[128:131], v[160:163], v[48:51]\n"
"v_mfma_f32_16x16x32_bf16 v[52:55], a[132:135], v[160:163], v[52:55]\n"
"s_waitcnt vmcnt(19)\n"
"v_mfma_f32_16x16x32_bf16 v[48:51], a[136:139], v[164:167], v[48:51]\n"
"v_mfma_f32_16x16x32_bf16 v[52:55], a[140:143], v[164:167], v[52:55]\n"
"LP4_mid:\n"
"s_waitcnt vmcnt(18)\n"
"s_cmp_eq_u32 s20, 0\n"
"s_cselect_b32 s36, s65, 2146435072\n"
"buffer_load_dwordx4 v[56:59], v1, s[24:27], s36 offen sc1\n"
"s_cmp_lg_u32 s20, 3\n"
"s_cbranch_scc1 Lav_182\n"
"v_cmp_ge_i32 vcc, s63, v10\n"
"s_cbranch_vccnz Lav_182\n"
"s_add_u32 s41, s63, 1\n"
"v_cmp_ge_i32 vcc, s41, v10\n"
"s_cbranch_vccnz Lav_183\n"
"s_add_u32 s41, s41, 1\n"
"Lav_183:\n"
"s_max_i32 s70, s70, s41\n"
"v_mov_b32 v4, s70\n"
"ds_write_b32 v11, v4\n"
"Lav_182:\n"
"ds_read_b32 v4, v11\n"
"s_waitcnt lgkmcnt(0)\n"
"v_readfirstlane_b32 s70, v4\n"
"s_add_u32 s36, s21, 1\n"
"s_cmp_gt_i32 s70, s36\n"
"s_cselect_b32 s62, 1, 0\n"
"s_cbranch_scc0 Lno_180\n"
"s_mul_i32 s42, s63, 86\n"
"s_lshr_b32 s42, s42, 8\n"
"s_mul_i32 s56, s63, 655360\n"
"s_mul_i32 s42, s42, 131072\n"
"s_add_u32 s56, s56, s42\n"
"s_add_u32 s56, s56, 102760448\n"
"s_add_u32 s56, s56, s68\n"
"s_lshr_b32 s43, s56, 8\n"
"s_xor_b32 s43, s43, s22\n"
"s_not_b32 s43, s43\n"
"s_and_b32 s43, s43, 8192\n"
"s_or_b32 s56, s56, s43\n"
"s_lshl_b32 s65, s63, 17\n"
"s_add_u32 s65, s65, 157286400\n"
"s_mul_i32 s67, s63, 36864\n"
"s_lshr_b32 s43, s65, 8\n"
"s_xor_b32 s43, s43, s22\n"
"s_not_b32 s43, s43\n"
"s_and_b32 s43, s43, 8192\n"
"s_or_b32 s65, s65, s43\n"
"s_add_u32 s66, s65, s73\n"
"s_add_u32 s65, s65, s72\n"
"s_waitcnt vmcnt(18)\n"
"v_mfma_f32_16x16x32_bf16 v[48:51], a[144:147], v[168:171], v[48:51]\n"
"buffer_load_dwordx4 v[96:99], v1, s[24:27], s56 offen offset:0 sc1\n"
"v_mfma_f32_16x16x32_bf16 v[52:55], a[148:151], v[168:171], v[52:55]\n"
"s_waitcnt vmcnt(18)\n"
"v_mfma_f32_16x16x32_bf16 v[48:51], a[152:155], v[172:175], v[48:51]\n"
"buffer_load_dwordx4 v[100:103], v1, s[24:27], s56 offen offset:1024 sc1\n"
"v_mfma_f32_16x16x32_bf16 v[52:55], a[156:159], v[172:175], v[52:55]\n"
"s_waitcnt vmcnt(18)\n"
"v_mfma_f32_16x16x32_bf16 v[48:51], a[160:163], v[176:179], v[48:51]\n"
"buffer_load_dwordx4 v[104:107], v1, s[24:27], s56 offen offset:2048 sc1\n"
"v_mfma_f32_16x16x32_bf16 v[52:55], a[164:167], v[176:179], v[52:55]\n"
"s_waitcnt vmcnt(18)\n"
"v_mfma_f32_16x16x32_bf16 v[48:51], a[168:171], v[180:183], v[48:51]\n"
"buffer_load_dwordx4 v[108:111], v1, s[24:27], s56 offen offset:3072 sc1\n"
"s_add_u32 s56, s56, 4096\n"
"v_mfma_f32_16x16x32_bf16 v[52:55], a[172:175], v[180:183], v[52:55]\n"
"s_waitcnt vmcnt(18)\n"
"v_mfma_f32_16x16x32_bf16 v[48:51], a[176:179], v[184:187], v[48:51]\n"
"buffer_load_dwordx4 v[112:115], v1, s[24:27], s56 offen offset:0 sc1\n"
"v_mfma_f32_16x16x32_bf16 v[52:55], a[180:183], v[184:187], v[52:55]\n"
"s_waitcnt vmcnt(18)\n"
"v_mfma_f32_16x16x32_bf16 v[48:51], a[184:187], v[188:191], v[48:51]\n"
"buffer_load_dwordx4 v[116:119], v1, s[24:27], s56 offen offset:1024 sc1\n"
"v_mfma_f32_16x16x32_bf16 v[52:55], a[188:191], v[188:191], v[52:55]\n"
"s_waitcnt vmcnt(18)\n"
"v_mfma_f32_16x16x32_bf16 v[48:51], a[192:195], v[192:195], v[48:51]\n"
"buffer_load_dwordx4 v[120:123], v1, s[24:27], s56 offen offset:2048 sc1\n"
"v_mfma_f32_16x16x32_bf16 v[52:55], a[196:199], v[192:195], v[52:55]\n"
"s_waitcnt vmcnt(18)\n"
"v_mfma_f32_16x16x32_bf16 v[48:51], a[200:203], v[196:199], v[48:51]\n"
"buffer_load_dwordx4 v[124:127], v1, s[24:27], s56 offen offset:3072 sc1\n"
"s_add_u32 s56, s56, 12288\n"
"v_mfma_f32_16x16x32_bf16 v[52:55], a[204:207], v[196:199], v[52:55]\n"
"s_waitcnt vmcnt(18)\n"
"v_mfma_f32_16x16x32_bf16 v[48:51], a[208:211], v[200:203], v[48:51]\n"
"buffer_load_dwordx4 v[128:131], v1, s[24:27], s56 offen offset:0 sc1\n"
"v_mfma_f32_16x16x32_bf16 v[52:55], a[212:215], v[200:203], v[52:55]\n"
"s_waitcnt vmcnt(18)\n"
"v_mfma_f32_16x16x32_bf16 v[48:51], a[216:219], v[204:207], v[48:51]\n"
"buffer_load_dwordx4 v[132:135], v1, s[24:27], s56 offen offset:1024 sc1\n"
"v_mfma_f32_16x16x32_bf16 v[52:55], a[220:223], v[204:207], v[52:55]\n"
"s_waitcnt vmcnt(18)\n"
"v_mfma_f32_16x16x32_bf16 v[48:51], a[224:227], v[208:211], v[48:51]\n"
"buffer_load_dwordx4 v[136:139], v1, s[24:27], s56 offen offset:2048 sc1\n"
"v_mfma_f32_16x16x32_bf16 v[52:55], a[228:231], v[208:211], v[52:55]\n"
"s_waitcnt vmcnt(18)\n"
"v_mfma_f32_16x16x32_bf16 v[48:51], a[232:235], v[212:215], v[48:51]\n"
"buffer_load_dwordx4 v[140:143], v1, s[24:27], s56 offen offset:3072 sc1\n"
"s_add_u32 s56, s56, 4096\n"
"v_mfma_f32_16x16x32_bf16 v[52:55], a[236:239], v[212:215], v[52:55]\n"
"s_waitcnt vmcnt(18)\n"
"v_mfma_f32_16x16x32_bf16 v[48:51], a[240:243], v[216:219], v[48:51]\n"
"buffer_load_dwordx4 v[144:147], v1, s[24:27], s56 offen offset:0 sc1\n"
"v_mfma_f32_16x16x32_bf16 v[52:55], a[244:247], v[216:219], v[52:55]\n"
"s_waitcnt vmcnt(18)\n"
"v_mfma_f32_16x16x32_bf16 v[48:51], a[248:251], v[220:223], v[48:51]\n"
"buffer_load_dwordx4 v[148:151], v1, s[24:27], s56 offen offset:1024 sc1\n"
"v_mfma_f32_16x16x32_bf16 v[52:55], a[252:255], v[220:223], v[52:55]\n"
"s_waitcnt vmcnt(18)\n"
"v_mfma_f32_16x16x32_bf16 v[48:51], v[16:19], v[224:227], v[48:51]\n"
"buffer_load_dwordx4 v[152:155], v1, s[24:27], s56 offen offset:2048 sc1\n"
"v_mfma_f32_16x16x32_bf16 v[52:55], v[20:23], v[224:227], v[52:55]\n"
"s_waitcnt vmcnt(18)\n"
"v_mfma_f32_16x16x32_bf16 v[48:51], v[24:27], v[228:231], v[48:51]\n"
"buffer_load_dwordx4 v[156:159], v1, s[24:27], s56 offen offset:3072 sc1\n"
"s_add_u32 s56, s56, 12288\n"
"v_mfma_f32_16x16x32_bf16 v[52:55], v[28:31], v[228:231], v[52:55]\n"
"s_waitcnt vmcnt(18)\n"
"v_mfma_f32_16x16x32_bf16 v[48:51], v[32:35], v[232:235], v[48:51]\n"
"buffer_load_dwordx4 v[160:163], v1, s[24:27], s56 offen offset:0 sc1\n"
"v_mfma_f32_16x16x32_bf16 v[52:55], v[36:39], v[232:235], v[52:55]\n"
"s_waitcnt vmcnt(18)\n"
"v_mfma_f32_16x16x32_bf16 v[48:51], v[40:43], v[236:239], v[48:51]\n"
"buffer_load_dwordx4 v[164:167], v1, s[24:27], s56 offen offset:1024 sc1\n"
"v_mfma_f32_16x16x32_bf16 v[52:55], v[44:47], v[236:239], v[52:55]\n"
"s_branch Lj_181\n"
"Lno_180:\n"
"s_waitcnt vmcnt(18)\n"
"v_mfma_f32_16x16x32_bf16 v[48:51], a[144:147], v[168:171], v[48:51]\n"
"v_mfma_f32_16x16x32_bf16 v[52:55], a[148:151], v[168:171], v[52:55]\n"
"s_waitcnt vmcnt(17)\n"
"v_mfma_f32_16x16x32_bf16 v[48:51], a[152:155], v[172:175], v[48:51]\n"
"v_mfma_f32_16x16x32_bf16 v[52:55], a[156:159], v[172:175], v[52:55]\n"
"s_waitcnt vmcnt(16)\n"
"v_mfma_f32_16x16x32_bf16 v[48:51], a[160:163], v[176:179], v[48:51]\n"
"v_mfma_f32_16x16x32_bf16 v[52:55], a[164:167], v[176:179], v[52:55]\n"
"s_waitcnt vmcnt(15)\n"
"v_mfma_f32_16x16x32_bf16 v[48:51], a[168:171], v[180:183], v[48:51]\n"
"v_mfma_f32_16x16x32_bf16 v[52:55], a[172:175], v[180:183], v[52:55]\n"
"s_waitcnt vmcnt(14)\n"
"v_mfma_f32_16x16x32_bf16 v[48:51], a[176:179], v[184:187], v[48:51]\n"
"v_mfma_f32_16x16x32_bf16 v[52:55], a[180:183], v[184:187], v[52:55]\n"
"s_waitcnt vmcnt(13)\n"
"v_mfma_f32_16x16x32_bf16 v[48:51], a[184:187], v[188:191], v[48:51]\n"
"v_mfma_f32_16x16x32_bf16 v[52:55], a[188:191], v[188:191], v[52:55]\n"
"s_waitcnt vmcnt(12)\n"
"v_mfma_f32_16x16x32_bf16 v[48:51], a[192:195], v[192:195], v[48:51]\n"
"buffer_load_dword v10, v5, s[24:27], s71 offen sc1\n"
"v_mfma_f32_16x16x32_bf16 v[52:55], a[196:199], v[192:195], v[52:55]\n"
"s_waitcnt vmcnt(12)\n"
"v_mfma_f32_16x16x32_bf16 v[48:51], a[200:203], v[196:199], v[48:51]\n"
"v_mfma_f32_16x16x32_bf16 v[52:55], a[204:207], v[196:199], v[52:55]\n"
"s_waitcnt vmcnt(11)\n"
"v_mfma_f32_16x16x32_bf16 v[48:51], a[208:211], v[200:203], v[48:51]\n"
"v_mfma_f32_16x16x32_bf16 v[52:55], a[212:215], v[200:203], v[52:55]\n"
"s_waitcnt vmcnt(10)\n"
"v_mfma_f32_16x16x32_bf16 v[48:51], a[216:219], v[204:207], v[48:51]\n"
"v_mfma_f32_16x16x32_bf16 v[52:55], a[220:223], v[204:207], v[52:55]\n"
"s_waitcnt vmcnt(9)\n"
"v_mfma_f32_16x16x32_bf16 v[48:51], a[224:227], v[208:211], v[48:51]\n"
"v_mfma_f32_16x16x32_bf16 v[52:55], a[228:231], v[208:211], v[52:55]\n"
"s_waitcnt vmcnt(8)\n"
"v_mfma_f32_16x16x32_bf16 v[48:51], a[232:235], v[212:215], v[48:51]\n"
"v_mfma_f32_16x16x32_bf16 v[52:55], a[236:239], v[212:215], v[52:55]\n"
"s_waitcnt vmcnt(7)\n"
"v_mfma_f32_16x16x32_bf16 v[48:51], a[240:243], v[216:219], v[48:51]\n"
"v_mfma_f32_16x16x32_bf16 v[52:55], a[244:247], v[216:219], v[52:55]\n"
"s_waitcnt vmcnt(6)\n"
"v_mfma_f32_16x16x32_bf16 v[48:51], a[248:251], v[220:223], v[48:51]\n"
"v_mfma_f32_16x16x32_bf16 v[52:55], a[252:255], v[220:223], v[52:55]\n"
"s_waitcnt vmcnt(5)\n"
"v_mfma_f32_16x16x32_bf16 v[48:51], v[16:19], v[224:227], v[48:51]\n"
"v_mfma_f32_16x16x32_bf16 v[52:55], v[20:23], v[224:227], v[52:55]\n"
"s_waitcnt vmcnt(4)\n"
"v_mfma_f32_16x16x32_bf16 v[48:51], v[24:27], v[228:231], v[48:51]\n"
"v_mfma_f32_16x16x32_bf16 v[52:55], v[28:31], v[228:231], v[52:55]\n"
"s_waitcnt vmcnt(3)\n"
"v_mfma_f32_16x16x32_bf16 v[48:51], v[32:35], v[232:235], v[48:51]\n"
"v_mfma_f32_16x16x32_bf16 v[52:55], v[36:39], v[232:235], v[52:55]\n"
"s_waitcnt vmcnt(2)\n"
"v_mfma_f32_16x16x32_bf16 v[48:51], v[40:43], v[236:239], v[48:51]\n"
"v_mfma_f32_16x16x32_bf16 v[52:55], v[44:47], v[236:239], v[52:55]\n"
"Lj_181:\n"
"s_nop 6\n"
"ds_write_b128 v6, v[48:51] offset:0\n"
"ds_write_b128 v6, v[52:55] offset:1024\n"
"s_waitcnt lgkmcnt(0)\n"
"s_barrier\n"
"s_cmp_lg_u32 s20, 0\n"
"s_cbranch_scc1 Lw0_184\n"
"s_cmp_eq_u32 s62, 1\n"
"s_cbranch_scc1 Leh_186\n"
"s_waitcnt vmcnt(1)\n"
"ds_read_b128 v[60:63], v9 offset:0\n"
"ds_read_b128 v[64:67], v9 offset:1024\n"
"ds_read_b128 v[68:71], v9 offset:2048\n"
"ds_read_b128 v[72:75], v9 offset:3072\n"
"ds_read_b128 v[76:79], v9 offset:4096\n"
"ds_read_b128 v[80:83], v9 offset:5120\n"
"ds_read_b128 v[84:87], v9 offset:6144\n"
"ds_read_b128 v[88:91], v9 offset:7168\n"
"s_waitcnt lgkmcnt(4)\n"
"v_pk_add_f32 v[60:61], v[60:61], v[68:69]\n"
"v_pk_add_f32 v[62:63], v[62:63], v[70:71]\n"
"v_pk_add_f32 v[64:65], v[64:65], v[72:73]\n"
"v_pk_add_f32 v[66:67], v[66:67], v[74:75]\n"
"s_waitcnt lgkmcnt(2)\n"
"v_pk_add_f32 v[60:61], v[60:61], v[76:77]\n"
"v_pk_add_f32 v[62:63], v[62:63], v[78:79]\n"
"v_pk_add_f32 v[64:65], v[64:65], v[80:81]\n"
"v_pk_add_f32 v[66:67], v[66:67], v[82:83]\n"
"s_waitcnt lgkmcnt(0)\n"
"v_pk_add_f32 v[60:61], v[60:61], v[84:85]\n"
"v_pk_add_f32 v[62:63], v[62:63], v[86:87]\n"
"v_pk_add_f32 v[64:65], v[64:65], v[88:89]\n"
"v_pk_add_f32 v[66:67], v[66:67], v[90:91]\n"
"v_cvt_pk_bf16_f32 v3, v60, v61\n"
"v_lshlrev_b32 v76, 16, v56\n"
"v_and_b32 v77, s55, v56\n"
"v_lshlrev_b32 v60, 16, v3\n"
"v_and_b32 v61, s55, v3\n"
"v_pk_add_f32 v[60:61], v[60:61], v[76:77]\n"
"v_cvt_pk_bf16_f32 v68, v60, v61\n"
"v_cvt_pk_bf16_f32 v3, v62, v63\n"
"v_lshlrev_b32 v76, 16, v57\n"
"v_and_b32 v77, s55, v57\n"
"v_lshlrev_b32 v62, 16, v3\n"
"v_and_b32 v63, s55, v3\n"
"v_pk_add_f32 v[62:63], v[62:63], v[76:77]\n"
"v_cvt_pk_bf16_f32 v69, v62, v63\n"
"v_cvt_pk_bf16_f32 v3, v64, v65\n"
"v_lshlrev_b32 v76, 16, v58\n"
"v_and_b32 v77, s55, v58\n"
"v_lshlrev_b32 v64, 16, v3\n"
"v_and_b32 v65, s55, v3\n"
"v_pk_add_f32 v[64:65], v[64:65], v[76:77]\n"
"v_cvt_pk_bf16_f32 v70, v64, v65\n"
"v_cvt_pk_bf16_f32 v3, v66, v67\n"
"v_lshlrev_b32 v76, 16, v59\n"
"v_and_b32 v77, s55, v59\n"
"v_lshlrev_b32 v66, 16, v3\n"
"v_and_b32 v67, s55, v3\n"
"v_pk_add_f32 v[66:67], v[66:67], v[76:77]\n"
"v_cvt_pk_bf16_f32 v71, v66, v67\n"
"buffer_store_dwordx4 v[68:71], v7, s[32:35], s61 offen\n"
"s_branch Lw0_185\n"
"Leh_186:\n"
"s_waitcnt vmcnt(18)\n"
"buffer_load_dword v10, v5, s[24:27], s71 offen sc1\n"
"ds_read_b128 v[60:63], v9 offset:0\n"
"ds_read_b128 v[64:67], v9 offset:1024\n"
"ds_read_b128 v[68:71], v9 offset:2048\n"
"ds_read_b128 v[72:75], v9 offset:3072\n"
"ds_read_b128 v[76:79], v9 offset:4096\n"
"ds_read_b128 v[80:83], v9 offset:5120\n"
"ds_read_b128 v[84:87], v9 offset:6144\n"
"ds_read_b128 v[88:91], v9 offset:7168\n"
"s_waitcnt lgkmcnt(4)\n"
"buffer_load_dwordx4 v[168:171], v1, s[24:27], s56 offen offset:2048 sc1\n"
"v_pk_add_f32 v[60:61], v[60:61], v[68:69]\n"
"v_pk_add_f32 v[62:63], v[62:63], v[70:71]\n"
"buffer_load_dwordx4 v[172:175], v1, s[24:27], s56 offen offset:3072 sc1\n"
"s_add_u32 s56, s56, 4096\n"
"v_pk_add_f32 v[64:65], v[64:65], v[72:73]\n"
"v_pk_add_f32 v[66:67], v[66:67], v[74:75]\n"
"buffer_load_dwordx4 v[176:179], v1, s[24:27], s56 offen offset:0 sc1\n"
"s_waitcnt lgkmcnt(2)\n"
"v_pk_add_f32 v[60:61], v[60:61], v[76:77]\n"
"buffer_load_dwordx4 v[180:183], v1, s[24:27], s56 offen offset:1024 sc1\n"
"v_pk_add_f32 v[62:63], v[62:63], v[78:79]\n"
"v_pk_add_f32 v[64:65], v[64:65], v[80:81]\n"
"buffer_load_dwordx4 v[184:187], v1, s[24:27], s56 offen offset:2048 sc1\n"
"v_pk_add_f32 v[66:67], v[66:67], v[82:83]\n"
"s_waitcnt lgkmcnt(0)\n"
"buffer_load_dwordx4 v[188:191], v1, s[24:27], s56 offen offset:3072 sc1\n"
"s_add_u32 s56, s56, 12288\n"
"v_pk_add_f32 v[60:61], v[60:61], v[84:85]\n"
"v_pk_add_f32 v[62:63], v[62:63], v[86:87]\n"
"buffer_load_dwordx4 v[192:195], v1, s[24:27], s56 offen offset:0 sc1\n"
"v_pk_add_f32 v[64:65], v[64:65], v[88:89]\n"
"v_pk_add_f32 v[66:67], v[66:67], v[90:91]\n"
"buffer_load_dwordx4 v[196:199], v1, s[24:27], s56 offen offset:1024 sc1\n"
"v_cvt_pk_bf16_f32 v3, v60, v61\n"
"v_lshlrev_b32 v76, 16, v56\n"
"buffer_load_dwordx4 v[200:203], v1, s[24:27], s56 offen offset:2048 sc1\n"
"v_and_b32 v77, s55, v56\n"
"v_lshlrev_b32 v60, 16, v3\n"
"buffer_load_dwordx4 v[204:207], v1, s[24:27], s56 offen offset:3072 sc1\n"
"s_add_u32 s56, s56, 4096\n"
"v_and_b32 v61, s55, v3\n"
"v_pk_add_f32 v[60:61], v[60:61], v[76:77]\n"
"buffer_load_dwordx4 v[208:211], v1, s[24:27], s56 offen offset:0 sc1\n"
"v_cvt_pk_bf16_f32 v68, v60, v61\n"
"v_cvt_pk_bf16_f32 v3, v62, v63\n"
"buffer_load_dwordx4 v[212:215], v1, s[24:27], s56 offen offset:1024 sc1\n"
"v_lshlrev_b32 v76, 16, v57\n"
"v_and_b32 v77, s55, v57\n"
"buffer_load_dwordx4 v[216:219], v1, s[24:27], s56 offen offset:2048 sc1\n"
"v_lshlrev_b32 v62, 16, v3\n"
"v_and_b32 v63, s55, v3\n"
"buffer_load_dwordx4 v[220:223], v1, s[24:27], s56 offen offset:3072 sc1\n"
"s_add_u32 s56, s56, 12288\n"
"v_pk_add_f32 v[62:63], v[62:63], v[76:77]\n"
"v_cvt_pk_bf16_f32 v69, v62, v63\n"
"buffer_load_dwordx4 v[224:227], v1, s[24:27], s56 offen offset:0 sc1\n"
"v_cvt_pk_bf16_f32 v3, v64, v65\n"
"v_lshlrev_b32 v76, 16, v58\n"
"buffer_load_dwordx4 v[228:231], v1, s[24:27], s56 offen offset:1024 sc1\n"
"v_and_b32 v77, s55, v58\n"
"v_lshlrev_b32 v64, 16, v3\n"
"buffer_load_dwordx4 v[232:235], v1, s[24:27], s56 offen offset:2048 sc1\n"
"v_and_b32 v65, s55, v3\n"
"v_pk_add_f32 v[64:65], v[64:65], v[76:77]\n"
"buffer_load_dwordx4 v[236:239], v1, s[24:27], s56 offen offset:3072 sc1\n"
"s_add_u32 s56, s56, 4096\n"
"v_cvt_pk_bf16_f32 v70, v64, v65\n"
"v_cvt_pk_bf16_f32 v3, v66, v67\n"
"v_lshlrev_b32 v76, 16, v59\n"
"v_and_b32 v77, s55, v59\n"
"v_lshlrev_b32 v66, 16, v3\n"
"v_and_b32 v67, s55, v3\n"
"v_pk_add_f32 v[66:67], v[66:67], v[76:77]\n"
"v_cvt_pk_bf16_f32 v71, v66, v67\n"
"buffer_store_dwordx4 v[68:71], v7, s[32:35], s61 offen\n"
"Lw0_185:\n"
"s_branch Lw0_185x\n"
"Lw0_184:\n"
"s_cmp_eq_u32 s62, 0\n"
"s_cbranch_scc1 Lw0_185x\n"
"buffer_load_dword v10, v5, s[24:27], s71 offen sc1\n"
"buffer_load_dwordx4 v[168:171], v1, s[24:27], s56 offen offset:2048 sc1\n"
"s_nop 9\n"
"buffer_load_dwordx4 v[172:175], v1, s[24:27], s56 offen offset:3072 sc1\n"
"s_add_u32 s56, s56, 4096\n"
"s_nop 9\n"
"buffer_load_dwordx4 v[176:179], v1, s[24:27], s56 offen offset:0 sc1\n"
"s_nop 9\n"
"buffer_load_dwordx4 v[180:183], v1, s[24:27], s56 offen offset:1024 sc1\n"
"s_nop 9\n"
"buffer_load_dwordx4 v[184:187], v1, s[24:27], s56 offen offset:2048 sc1\n"
"s_nop 9\n"
"buffer_load_dwordx4 v[188:191], v1, s[24:27], s56 offen offset:3072 sc1\n"
"s_add_u32 s56, s56, 12288\n"
"s_nop 9\n"
"buffer_load_dwordx4 v[192:195], v1, s[24:27], s56 offen offset:0 sc1\n"
"s_nop 9\n"
"buffer_load_dwordx4 v[196:199], v1, s[24:27], s56 offen offset:1024 sc1\n"
"s_nop 9\n"
"buffer_load_dwordx4 v[200:203], v1, s[24:27], s56 offen offset:2048 sc1\n"
"s_nop 9\n"
"buffer_load_dwordx4 v[204:207], v1, s[24:27], s56 offen offset:3072 sc1\n"
"s_add_u32 s56, s56, 4096\n"
"s_nop 9\n"
"buffer_load_dwordx4 v[208:211], v1, s[24:27], s56 offen offset:0 sc1\n"
"s_nop 9\n"
"buffer_load_dwordx4 v[212:215], v1, s[24:27], s56 offen offset:1024 sc1\n"
"s_nop 9\n"
"buffer_load_dwordx4 v[216:219], v1, s[24:27], s56 offen offset:2048 sc1\n"
"s_nop 9\n"
"buffer_load_dwordx4 v[220:223], v1, s[24:27], s56 offen offset:3072 sc1\n"
"s_add_u32 s56, s56, 12288\n"
"s_nop 9\n"
"buffer_load_dwordx4 v[224:227], v1, s[24:27], s56 offen offset:0 sc1\n"
"s_nop 9\n"
"buffer_load_dwordx4 v[228:231], v1, s[24:27], s56 offen offset:1024 sc1\n"
"s_nop 9\n"
"buffer_load_dwordx4 v[232:235], v1, s[24:27], s56 offen offset:2048 sc1\n"
"s_nop 9\n"
"buffer_load_dwordx4 v[236:239], v1, s[24:27], s56 offen offset:3072 sc1\n"
"s_add_u32 s56, s56, 4096\n"
"s_nop 9\n"
"Lw0_185x:\n"
"v_xor_b32 v6, 8192, v6\n"
"v_xor_b32 v9, 8192, v9\n"
"s_add_u32 s21, s21, 1\n"
"s_cmp_eq_u32 s21, 64\n"
"s_cbranch_scc1 LP4_fin\n"
"s_cmp_eq_u32 s62, 0\n"
"s_cbranch_scc1 LP4_block\n"
"s_branch LP4_loop\n"
"LP4_fin:\n"
"LP4_end:\n"
"s_endpgm\n"
"Lcode1:\n"
  : : "s"(__builtin_amdgcn_kernarg_segment_ptr()), "s"(role), "s"(idx), "s"(b), "v"(tid)
  : "v1", "v2", "v3", "v4", "v5", "v6", "v7", "v8", "v9", "v10", "v11", "v12", "v13", "v14", "v15", "v16", "v17", "v18", "v19", "v20", "v21", "v22", "v23", "v24", "v25", "v26", "v27", "v28", "v29", "v30", "v31", "v32", "v33", "v34", "v35", "v36", "v37", "v38", "v39", "v40", "v41", "v42", "v43", "v44", "v45", "v46", "v47", "v48", "v49", "v50", "v51", "v52", "v53", "v54", "v55", "v56", "v57", "v58", "v59", "v60", "v61", "v62", "v63", "v64", "v65", "v66", "v67", "v68", "v69", "v70", "v71", "v72", "v73", "v74", "v75", "v76", "v77", "v78", "v79", "v80", "v81", "v82", "v83", "v84", "v85", "v86", "v87", "v88", "v89", "v90", "v91", "v92", "v93", "v94", "v95", "v96", "v97", "v98", "v99", "v100", "v101", "v102", "v103", "v104", "v105", "v106", "v107", "v108", "v109", "v110", "v111", "v112", "v113", "v114", "v115", "v116", "v117", "v118", "v119", "v120", "v121", "v122", "v123", "v124", "v125", "v126", "v127", "v128", "v129", "v130", "v131", "v132", "v133", "v134", "v135", "v136", "v137", "v138", "v139", "v140", "v141", "v142", "v143", "v144", "v145", "v146", "v147", "v148", "v149", "v150", "v151", "v152", "v153", "v154", "v155", "v156", "v157", "v158", "v159", "v160", "v161", "v162", "v163", "v164", "v165", "v166", "v167", "v168", "v169", "v170", "v171", "v172", "v173", "v174", "v175", "v176", "v177", "v178", "v179", "v180", "v181", "v182", "v183", "v184", "v185", "v186", "v187", "v188", "v189", "v190", "v191", "v192", "v193", "v194", "v195", "v196", "v197", "v198", "v199", "v200", "v201", "v202", "v203", "v204", "v205", "v206", "v207", "v208", "v209", "v210", "v211", "v212", "v213", "v214", "v215", "v216", "v217", "v218", "v219", "v220", "v221", "v222", "v223", "v224", "v225", "v226", "v227", "v228", "v229", "v230", "v231", "v232", "v233", "v234", "v235", "v236", "v237", "v238", "v239", "v240", "v241", "v242", "v243", "v244", "v245", "v246", "v247", "v248", "v249", "v250", "v251", "v252", "v253", "v254", "v255", "a0", "a1", "a2", "a3", "a4", "a5", "a6", "a7", "a8", "a9", "a10", "a11", "a12", "a13", "a14", "a15", "a16", "a17", "a18", "a19", "a20", "a21", "a22", "a23", "a24", "a25", "a26", "a27", "a28", "a29", "a30", "a31", "a32", "a33", "a34", "a35", "a36", "a37", "a38", "a39", "a40", "a41", "a42", "a43", "a44", "a45", "a46", "a47", "a48", "a49", "a50", "a51", "a52", "a53", "a54", "a55", "a56", "a57", "a58", "a59", "a60", "a61", "a62", "a63", "a64", "a65", "a66", "a67", "a68", "a69", "a70", "a71", "a72", "a73", "a74", "a75", "a76", "a77", "a78", "a79", "a80", "a81", "a82", "a83", "a84", "a85", "a86", "a87", "a88", "a89", "a90", "a91", "a92", "a93", "a94", "a95", "a96", "a97", "a98", "a99", "a100", "a101", "a102", "a103", "a104", "a105", "a106", "a107", "a108", "a109", "a110", "a111", "a112", "a113", "a114", "a115", "a116", "a117", "a118", "a119", "a120", "a121", "a122", "a123", "a124", "a125", "a126", "a127", "a128", "a129", "a130", "a131", "a132", "a133", "a134", "a135", "a136", "a137", "a138", "a139", "a140", "a141", "a142", "a143", "a144", "a145", "a146", "a147", "a148", "a149", "a150", "a151", "a152", "a153", "a154", "a155", "a156", "a157", "a158", "a159", "a160", "a161", "a162", "a163", "a164", "a165", "a166", "a167", "a168", "a169", "a170", "a171", "a172", "a173", "a174", "a175", "a176", "a177", "a178", "a179", "a180", "a181", "a182", "a183", "a184", "a185", "a186", "a187", "a188", "a189", "a190", "a191", "a192", "a193", "a194", "a195", "a196", "a197", "a198", "a199", "a200", "a201", "a202", "a203", "a204", "a205", "a206", "a207", "a208", "a209", "a210", "a211", "a212", "a213", "a214", "a215", "a216", "a217", "a218", "a219", "a220", "a221", "a222", "a223", "a224", "a225", "a226", "a227", "a228", "a229", "a230", "a231", "a232", "a233", "a234", "a235", "a236", "a237", "a238", "a239", "a240", "a241", "a242", "a243", "a244", "a245", "a246", "a247", "a248", "a249", "a250", "a251", "a252", "a253", "a254", "a255", "s16", "s17", "s18", "s19", "s20", "s21", "s22", "s23", "s24", "s25", "s26", "s27", "s28", "s29", "s30", "s31", "s33", "s34", "s35", "s36", "s37", "s38", "s39", "s40", "s41", "s42", "s43", "s44", "s45", "s46", "s47", "s48", "s49", "s50", "s51", "s52", "s53", "s54", "s55", "s56", "s57", "s58", "s59", "s60", "s61", "s62", "s63", "s64", "s65", "s66", "s67", "s68", "s69", "s70", "s71", "s72", "s73", "s74", "s75", "s76", "s77", "s78", "s79", "s80", "s81", "s82", "s83", "s84", "s85", "s86", "s87", "s88", "s89", "s90", "s91", "s92", "s93", "s94", "s95", "s96", "s97", "s98", "s99", "vcc", "scc", "memory");
}

extern "C" void kernel_launch(void* const* d_in, const int* in_sizes, int n_in, void* d_out, int out_size, void* d_ws, size_t ws_size, hipStream_t stream) {
  char* wsb = (char*)(((uintptr_t)d_ws + ((2u << 20) - 1)) & ~(uintptr_t)((2u << 20) - 1));
  (void)hipFuncSetAttribute((const void*)rmt_main, hipFuncAttributeMaxDynamicSharedMemorySize, 160 * 1024);
  rmt_init<<<(1327104 / 16 + 255) / 256 + 32, 256, 0, stream>>>(wsb);
  rmt_main<<<256, 256, 160 * 1024, stream>>>(d_in[0], d_in[1], d_in[2], d_in[3], d_in[4], d_in[5], d_in[8], d_in[9], d_in[10], d_out, wsb);
}
```
